```python
import torch

def _softmax(x, axis=-1):
    return torch.softmax(x, dim=axis)
def _gelu(x):
    return torch.nn.functional.gelu(x, approximate="tanh")
def _silu(x):
    return torch.nn.functional.silu(x)
def _sigmoid(x):
    return torch.sigmoid(x)
def _layernorm(x, axis=-1, eps=1e-5):
    m = x.mean(dim=axis, keepdim=True)
    v = x.var(dim=axis, keepdim=True, unbiased=False)
    return (x - m) / torch.sqrt(v + eps)
def _scatter_add(tgt, idx, src, axis=0):
    return tgt.clone().index_add_(axis, idx, src)
def _segment_sum(src, seg, n):
    out = torch.zeros((n,) + src.shape[1:], dtype=src.dtype)
    return out.index_add_(0, seg, src)
def _pad(x, pw):
    flat = tuple(v for pair in reversed(pw) for v in pair)
    return torch.nn.functional.pad(x, flat)

kernel_name = 'parallel_sum__compressed_kv_cla__dense_activation_silu__prompt'

def setup_inputs():

    _w = (lambda a: (lambda t: t.to(torch.bfloat16) if t.is_floating_point() else t)(torch.as_tensor(a)))
    def _A_setup():
        B, H, Sq, Sk, D, M = 1, 16, 2048, 2048, 256, 448
        Dc = 4 * D
        return {
            "x": _w((lambda *s: torch.randn(*s, dtype=torch.bfloat16) * 0.05)(B, Sq, M)),
            "c_kv": _w((lambda *s: torch.randn(*s, dtype=torch.bfloat16) * 0.4)(B, Sk, Dc)),
            "w_q": _w((lambda *s: torch.randn(*s, dtype=torch.bfloat16) * 3.2)(M, H, D) * (M ** -0.5)),
            "w_uk": _w((lambda *s: torch.randn(*s, dtype=torch.bfloat16) * 3.2)(Dc, H, D) * (Dc ** -0.5)),
            "w_uv": _w((lambda *s: torch.randn(*s, dtype=torch.bfloat16) * 0.4)(Dc, H, D) * (Dc ** -0.5)),
            "w_o": _w((lambda *s: torch.randn(*s, dtype=torch.bfloat16) * 0.4)(H, D, M) * ((H * D) ** -0.5)),
        }
    _A_in = _A_setup()

    def _B_setup():
        B, S, D, F = 1, 2048, 448, 1792
        return {
            "x": _w((lambda *s: torch.randn(*s, dtype=torch.bfloat16) * 0.05)(B, S, D)),
            "w1": _w((lambda *s: torch.randn(*s, dtype=torch.bfloat16) * 0.8)(D, F) * (D ** -0.5)),
            "w2": _w((lambda *s: torch.randn(*s, dtype=torch.bfloat16) * 0.1)(F, D) * (F ** -0.5)),
        }
    _B_in = _B_setup()

    return {
        "A_x": _A_in["x"],
        "A_c_kv": _A_in["c_kv"],
        "A_w_q": _A_in["w_q"],
        "A_w_uk": _A_in["w_uk"],
        "A_w_uv": _A_in["w_uv"],
        "A_w_o": _A_in["w_o"],
        "B_x": _B_in["x"],
        "B_w1": _B_in["w1"],
        "B_w2": _B_in["w2"],
    }

def reference(A_x, A_c_kv, A_w_q, A_w_uk, A_w_uv, A_w_o, B_x, B_w1, B_w2):
    def _A_ref(x, c_kv, w_q, w_uk, w_uv, w_o):
        B, Sq, M = x.shape[0], x.shape[1], x.shape[2]
        Sk = c_kv.shape[1]
        H, D = w_q.shape[1], w_q.shape[2]
        q = torch.einsum("bqm,mhd->bhqd", x, w_q)
        k = torch.einsum("bkc,chd->bhkd", c_kv, w_uk)
        v = torch.einsum("bkc,chd->bhkd", c_kv, w_uv)
        scores = torch.einsum("bhqd,bhkd->bhqk", q, k) * (D ** -0.5)
        off = Sk - Sq
        qi = torch.arange(Sq)[:, None]
        kj = torch.arange(Sk)[None, :]
        cmask = kj <= (qi + off)
        scores = torch.where(cmask[None, None], scores, -1e9)
        p = _softmax(scores, axis=-1)
        ctx = torch.einsum("bhqk,bhkd->bhqd", p, v)
        return torch.einsum("bhqd,hdm->bqm", ctx, w_o)

    def _B_ref(x, w1, w2):
        h = torch.einsum("bsd,df->bsf", x, w1)
        h = _silu(h)
        out = torch.einsum("bsf,fd->bsd", h, w2)
        return out

    x = A_x
    xn = _layernorm(x, axis=-1)
    return x + _A_ref(xn, A_c_kv, A_w_q, A_w_uk, A_w_uv, A_w_o) + _B_ref(xn, B_w1, B_w2)
```

```cpp
#include <hip/hip_runtime.h>
#include <hip/hip_bf16.h>
#include <utility>

typedef unsigned short u16;
using bf16x8 = __attribute__((ext_vector_type(8))) short;
using s16x4  = __attribute__((ext_vector_type(4))) short;
using f32x4  = __attribute__((ext_vector_type(4))) float;
using f32x2  = __attribute__((ext_vector_type(2))) float;
using f32x16 = __attribute__((ext_vector_type(16))) float;
using u32x4  = __attribute__((ext_vector_type(4))) unsigned;
using u32x2  = __attribute__((ext_vector_type(2))) unsigned;
typedef __bf16 bf16x2_t __attribute__((ext_vector_type(2)));
typedef short v4i16_t __attribute__((ext_vector_type(4)));
typedef __attribute__((address_space(3))) char* lds_ptr;
typedef __attribute__((address_space(3))) const char* lds_cptr;
typedef __attribute__((address_space(1))) unsigned gu32;
typedef __amdgpu_buffer_rsrc_t rsrc_t;

#define DI __device__ __forceinline__
#define SBAR() __builtin_amdgcn_sched_barrier(0)
#define PIN(x) asm volatile("" : "+v"(x))
#define BAR() __builtin_amdgcn_s_barrier()
#define WAIT_V(n) asm volatile("s_waitcnt vmcnt(" #n ")" ::: "memory")
#define WAIT_L(n) asm volatile("s_waitcnt lgkmcnt(" #n ")" ::: "memory")
#define MFMA32(a, b, c) __builtin_amdgcn_mfma_f32_32x32x16_bf16((a), (b), (c), 0, 0, 0)
#define MFMA16(a, b, c) __builtin_amdgcn_mfma_f32_16x16x32_bf16((a), (b), (c), 0, 0, 0)
#define LAMBDA __attribute__((always_inline))

template <int... I, class F> DI void sfor_impl(std::integer_sequence<int, I...>, F&& f) { (f(std::integral_constant<int, I>{}), ...); }
template <int N, class F> DI void sfor(F&& f) { sfor_impl(std::make_integer_sequence<int, N>{}, f); }
template <int N> DI void wait_vm() { asm volatile("s_waitcnt vmcnt(%0)" :: "i"(N) : "memory"); }
template <int N> DI void wait_vm_bar() { asm volatile("s_waitcnt vmcnt(%0)\n\ts_barrier" :: "i"(N) : "memory"); }

#define CL1 "memory", "vcc", "scc", "v16", "v17", "v18", "v19", "v20", "v21", "v22", "v23", "v24", "v25", "v26", "v27", "v28", "v29", "v30", "v31", "v32", "v33", "v34", "v35", "v36", "v37", "v38", "v39", "v40", "v41", "v42", "v43", "v44", "v45", "v46", "v47", "v48", "v49", "v50", "v51", "v52", "v53", "v54", "v55", "v56", "v57", "v58", "v59", "v60", "v61", "v62", "v63", "v64", "v65", "v66", "v67", "v68", "v69", "v70", "v71", "v72", "v73", "v74", "v75", "v76", "v77", "v78", "v79", "v80", "v81", "v82", "v83", "v84", "v85", "v86", "v87", "v88", "v89", "v90", "v91", "v92", "v93", "v94", "v95", "v96", "v97", "v98", "v99", "v100", "v101", "v102", "v103", "v104", "v105", "v106", "v107", "v108", "v109", "v110", "v111", "v112", "v113", "v114", "v115", "v116", "v117", "v118", "v119", "v120", "v121", "v122", "v123", "v124", "v125", "v126", "v127", "v128", "v129", "v130", "v131", "v132", "v133", "v134", "v135", "v136", "v137", "v138", "v139", "v140", "v141", "v142", "v143", "v144", "v145", "v146", "v147", "v148", "v149", "v150", "v151", "v152", "v153", "v154", "v155", "v156", "v157", "v158", "v159", "v160", "v161", "v162", "v163", "v164", "v165", "v166", "v167", "v168", "v169", "v170", "v171", "v172", "v173", "v174", "v175", "v176", "v177", "v178", "v179", "v180", "v181", "v182", "v183", "v184", "v185", "v186", "v187", "v188", "v189", "v190", "v191", "v192", "v193", "v194", "v195", "v196", "v197", "v198", "v199", "v200", "v201", "v202", "v203", "v204", "v205", "v206", "v207", "v208", "v209", "v210", "v211", "v212", "v213", "v214", "v215", "v216", "v217", "v218", "v219", "v220", "v221", "v222", "v223", "v224", "v225", "v226", "v227", "v228", "v229", "v230", "v231", "v232", "v233", "v234", "v235", "v236", "v237", "v238", "v239", "v240", "v241", "v242", "v243", "v244", "v245", "v246", "v247", "v248", "v249", "v250", "v251", "v252", "v253", "v254", "v255", "a0", "a1", "a2", "a3", "a4", "a5", "a6", "a7", "a8", "a9", "a10", "a11", "a12", "a13", "a14", "a15", "a16", "a17", "a18", "a19", "a20", "a21", "a22", "a23", "a24", "a25", "a26", "a27", "a28", "a29", "a30", "a31", "a32", "a33", "a34", "a35", "a36", "a37", "a38", "a39", "a40", "a41", "a42", "a43", "a44", "a45", "a46", "a47", "a48", "a49", "a50", "a51", "a52", "a53", "a54", "a55", "a56", "a57", "a58", "a59", "a60", "a61", "a62", "a63", "a64", "a65", "a66", "a67", "a68", "a69", "a70", "a71", "a72", "a73", "a74", "a75", "a76", "a77", "a78", "a79", "a80", "a81", "a82", "a83", "a84", "a85", "a86", "a87", "a88", "a89", "a90", "a91", "a92", "a93", "a94", "a95", "a96", "a97", "a98", "a99", "a100", "a101", "a102", "a103", "a104", "a105", "a106", "a107", "a108", "a109", "a110", "a111", "a112", "a113", "a114", "a115", "a116", "a117", "a118", "a119", "a120", "a121", "a122", "a123", "a124", "a125", "a126", "a127", "a128", "a129", "a130", "a131", "a132", "a133", "a134", "a135", "a136", "a137", "a138", "a139", "a140", "a141", "a142", "a143", "a144", "a145", "a146", "a147", "a148", "a149", "a150", "a151", "a152", "a153", "a154", "a155", "a156", "a157", "a158", "a159", "a160", "a161", "a162", "a163", "a164", "a165", "a166", "a167", "a168", "a169", "a170", "a171", "a172", "a173", "a174", "a175", "a176", "a177", "a178", "a179", "a180", "a181", "a182", "a183", "a184", "a185", "a186", "a187", "a188", "a189", "a190", "a191", "a192", "a193", "a194", "a195", "a196", "a197", "a198", "a199", "a200", "a201", "a202", "a203", "a204", "a205", "a206", "a207", "a208", "a209", "a210", "a211", "a212", "a213", "a214", "a215", "a216", "a217", "a218", "a219", "a220", "a221", "a222", "a223", "a224", "a225", "a226", "a227", "a228", "a229", "a230", "a231", "a232", "a233", "a234", "a235", "a236", "a237", "a238", "a239", "s64", "s65", "s66", "s67", "s68", "s69", "s70", "s71", "s72", "s73", "s74", "s75", "s76", "s77", "s78", "s79", "s80", "s81", "s82", "s83", "s84", "s85", "s86", "s87", "s88", "s89", "s90", "s91", "s92", "s93", "s94", "s95", "s96", "s97", "s98", "s99"
#define CL2 "memory", "vcc", "scc", "v0", "v1", "v2", "v3", "v4", "v5", "v6", "v7", "v8", "v9", "v10", "v11", "v12", "v13", "v14", "v15", "v16", "v17", "v18", "v19", "v20", "v21", "v22", "v23", "v24", "v25", "v26", "v27", "v28", "v29", "v30", "v31", "v32", "v33", "v34", "v35", "v36", "v37", "v38", "v39", "v40", "v41", "v42", "v43", "v44", "v45", "v46", "v47", "v48", "v49", "v50", "v51", "v52", "v53", "v54", "v55", "v56", "v57", "v58", "v59", "v60", "v61", "v62", "v63", "v64", "v65", "v66", "v67", "v68", "v69", "v70", "v71", "v72", "v73", "v74", "v75", "v76", "v77", "v78", "v79", "v80", "v81", "v82", "v83", "v84", "v85", "v86", "v87", "v88", "v89", "v90", "v91", "v92", "v93", "v94", "v95", "v96", "v97", "v98", "v99", "v100", "v101", "v102", "v103", "v104", "v105", "v106", "v107", "v108", "v109", "v110", "v111", "v112", "v113", "v114", "v115", "v116", "v117", "v118", "v119", "v120", "v121", "v122", "v123", "v124", "v125", "v126", "v127", "v128", "v129", "v130", "v131", "v132", "v133", "v134", "v135", "v136", "v137", "v138", "v139", "v140", "v141", "v142", "v143", "v144", "v145", "v146", "v147", "v148", "v149", "v150", "v151", "v152", "v153", "v154", "v155", "v156", "v157", "v158", "v159", "v160", "v161", "v162", "v163", "v164", "v165", "v166", "v167", "v168", "v169", "v170", "v171", "v172", "v173", "v174", "v175", "v176", "v177", "v178", "v179", "v180", "v181", "v182", "v183", "v184", "v185", "v186", "v187", "v188", "v189", "v190", "v191", "v192", "v193", "v194", "v195", "v196", "v197", "v198", "v199", "v200", "v201", "v202", "v203", "v204", "v205", "v206", "v207", "v208", "v209", "v210", "v211", "v212", "v213", "v214", "v215", "v216", "v217", "v218", "v219", "v220", "v221", "v222", "v223", "v224", "v225", "v226", "v227", "v228", "v229", "v230", "v231", "v232", "v233", "v234", "v235", "v236", "v237", "v238", "v239", "v240", "v241", "v242", "v243", "v244", "v245", "v246", "v247", "v248", "v249", "v250", "v251", "v252", "v253", "v254", "v255", "a0", "a1", "a2", "a3", "a4", "a5", "a6", "a7", "a8", "a9", "a10", "a11", "a12", "a13", "a14", "a15", "a16", "a17", "a18", "a19", "a20", "a21", "a22", "a23", "a24", "a25", "a26", "a27", "a28", "a29", "a30", "a31", "a32", "a33", "a34", "a35", "a36", "a37", "a38", "a39", "a40", "a41", "a42", "a43", "a44", "a45", "a46", "a47", "a48", "a49", "a50", "a51", "a52", "a53", "a54", "a55", "a56", "a57", "a58", "a59", "a60", "a61", "a62", "a63", "a64", "a65", "a66", "a67", "a68", "a69", "a70", "a71", "a72", "a73", "a74", "a75", "a76", "a77", "a78", "a79", "a80", "a81", "a82", "a83", "a84", "a85", "a86", "a87", "a88", "a89", "a90", "a91", "a92", "a93", "a94", "a95", "a96", "a97", "a98", "a99", "a100", "a101", "a102", "a103", "a104", "a105", "a106", "a107", "a108", "a109", "a110", "a111", "a112", "a113", "a114", "a115", "a116", "a117", "a118", "a119", "a120", "a121", "a122", "a123", "a124", "a125", "a126", "a127", "a128", "a129", "a130", "a131", "a132", "a133", "a134", "a135", "a136", "a137", "a138", "a139", "a140", "a141", "a142", "a143", "a144", "a145", "a146", "a147", "a148", "a149", "a150", "a151", "a152", "a153", "a154", "a155", "a156", "a157", "a158", "a159", "a160", "a161", "a162", "a163", "a164", "a165", "a166", "a167", "a168", "a169", "a170", "a171", "a172", "a173", "a174", "a175", "a176", "a177", "a178", "a179", "a180", "a181", "a182", "a183", "a184", "a185", "a186", "a187", "a188", "a189", "a190", "a191", "a192", "a193", "a194", "a195", "a196", "a197", "a198", "a199", "a200", "a201", "a202", "a203", "a204", "a205", "a206", "a207", "a208", "a209", "a210", "a211", "a212", "a213", "a214", "a215", "a216", "a217", "a218", "a219", "a220", "a221", "a222", "a223", "a224", "a225", "a226", "a227", "a228", "a229", "a230", "a231", "a232", "a233", "a234", "a235", "a236", "a237", "a238", "a239", "s60", "s61", "s62", "s63", "s64", "s65", "s66", "s67", "s68", "s69", "s70", "s71", "s72", "s73", "s74", "s75", "s76", "s77", "s78", "s79", "s80", "s81", "s82", "s83", "s84", "s85", "s86", "s87", "s88", "s89", "s90", "s91", "s92", "s93", "s94", "s95", "s96", "s97", "s98", "s99"
#define CL3 "memory", "vcc", "scc", "v16", "v17", "v18", "v19", "v20", "v21", "v22", "v23", "v24", "v25", "v26", "v27", "v28", "v29", "v30", "v31", "v32", "v33", "v34", "v35", "v36", "v37", "v38", "v39", "v40", "v41", "v42", "v43", "v44", "v45", "v46", "v47", "v48", "v49", "v50", "v51", "v52", "v53", "v54", "v55", "v56", "v57", "v58", "v59", "v60", "v61", "v62", "v63", "v64", "v65", "v66", "v67", "v68", "v69", "v70", "v71", "v72", "v73", "v74", "v75", "v76", "v77", "v78", "v79", "v80", "v81", "v82", "v83", "v84", "v85", "v86", "v87", "v88", "v89", "v90", "v91", "v92", "v93", "v94", "v95", "v96", "v97", "v98", "v99", "v100", "v101", "v102", "v103", "v104", "v105", "v106", "v107", "v108", "v109", "v110", "v111", "v112", "v113", "v114", "v115", "v116", "v117", "v118", "v119", "v120", "v121", "v122", "v123", "v124", "v125", "v126", "v127", "v128", "v129", "v130", "v131", "v132", "v133", "v134", "v135", "v136", "v137", "v138", "v139", "v140", "v141", "v142", "v143", "v144", "v145", "v146", "v147", "v148", "v149", "v150", "v151", "v152", "v153", "v154", "v155", "v156", "v157", "v158", "v159", "v160", "v161", "v162", "v163", "v164", "v165", "v166", "v167", "v168", "v169", "v170", "v171", "v172", "v173", "v174", "v175", "v176", "v177", "v178", "v179", "v180", "v181", "v182", "v183", "v184", "v185", "v186", "v187", "v188", "v189", "v190", "v191", "v192", "v193", "v194", "v195", "v196", "v197", "v198", "v199", "v200", "v201", "v202", "v203", "v204", "v205", "v206", "v207", "v208", "v209", "v210", "v211", "v212", "v213", "v214", "v215", "v216", "v217", "v218", "v219", "v220", "v221", "v222", "v223", "v224", "v225", "v226", "v227", "v228", "v229", "v230", "v231", "v232", "v233", "v234", "v235", "v236", "v237", "v238", "v239", "v240", "v241", "v242", "v243", "v244", "v245", "v246", "v247", "v248", "v249", "v250", "v251", "v252", "v253", "v254", "v255", "a0", "a1", "a2", "a3", "a4", "a5", "a6", "a7", "a8", "a9", "a10", "a11", "a12", "a13", "a14", "a15", "a16", "a17", "a18", "a19", "a20", "a21", "a22", "a23", "a24", "a25", "a26", "a27", "a28", "a29", "a30", "a31", "a32", "a33", "a34", "a35", "a36", "a37", "a38", "a39", "a40", "a41", "a42", "a43", "a44", "a45", "a46", "a47", "a48", "a49", "a50", "a51", "a52", "a53", "a54", "a55", "a56", "a57", "a58", "a59", "a60", "a61", "a62", "a63", "a64", "a65", "a66", "a67", "a68", "a69", "a70", "a71", "a72", "a73", "a74", "a75", "a76", "a77", "a78", "a79", "a80", "a81", "a82", "a83", "a84", "a85", "a86", "a87", "a88", "a89", "a90", "a91", "a92", "a93", "a94", "a95", "a96", "a97", "a98", "a99", "a100", "a101", "a102", "a103", "a104", "a105", "a106", "a107", "a108", "a109", "a110", "a111", "a112", "a113", "a114", "a115", "a116", "a117", "a118", "a119", "a120", "a121", "a122", "a123", "a124", "a125", "a126", "a127", "a128", "a129", "a130", "a131", "a132", "a133", "a134", "a135", "a136", "a137", "a138", "a139", "a140", "a141", "a142", "a143", "a144", "a145", "a146", "a147", "a148", "a149", "a150", "a151", "a152", "a153", "a154", "a155", "a156", "a157", "a158", "a159", "a160", "a161", "a162", "a163", "a164", "a165", "a166", "a167", "a168", "a169", "a170", "a171", "a172", "a173", "a174", "a175", "a176", "a177", "a178", "a179", "a180", "a181", "a182", "a183", "s64", "s65", "s66", "s67", "s68", "s69", "s70", "s71", "s72", "s73", "s74", "s75", "s76", "s77", "s78", "s79", "s80", "s81", "s82", "s83", "s84", "s85", "s86", "s87", "s88", "s89", "s90", "s91", "s92", "s93", "s94", "s95", "s96", "s97", "s98", "s99"

constexpr int S = 2048, M = 448, DC = 1024, H = 16, D = 256, F = 1792;
constexpr int KS3 = 368;
constexpr size_t WS_XF = 32768;
constexpr size_t WS_KF = WS_XF + (size_t)64 * 28 * 1024;
constexpr size_t WS_VF = WS_KF + (size_t)H * S * D * 2;
constexpr size_t WS_AF = WS_VF + (size_t)H * S * D * 2;
constexpr size_t WS_PO = WS_AF + (size_t)64 * KS3 * 1024;
constexpr size_t PO_STRIDE = (size_t)4 * 33 * 1024;
constexpr size_t WS_P3 = WS_PO + 128 * PO_STRIDE;
constexpr size_t P3_TILE = (size_t)64 * 7 * 1024;
constexpr size_t WS_DUMMY = WS_P3 + (size_t)32 * P3_TILE;
constexpr unsigned WS_EPOCH = 0x5F00000, WS_LNF = WS_EPOCH + 0x2000, WS_KVF = WS_EPOCH + 0x4000, WS_PRF = WS_EPOCH + 0x8000, WS_K3F = WS_EPOCH + 0xC000, WS_AFF = WS_EPOCH + 0x10000;
constexpr size_t WS_STAMP = 0x6000000;
static_assert(WS_DUMMY + 8192 <= WS_EPOCH, "layout");
constexpr size_t WS_END = WS_STAMP + 786432;

DI unsigned cvtpk(float lo, float hi) { f32x2 v = {lo, hi}; bf16x2_t b = __builtin_convertvector(v, bf16x2_t); return __builtin_bit_cast(unsigned, b); }
DI float bf2f(u16 v) { return __uint_as_float((unsigned)v << 16); }
DI float rbf(float v) { return __uint_as_float(cvtpk(v, 0.f) << 16); }
DI rsrc_t mkrsrc(const void* p, unsigned bytes) { return __builtin_amdgcn_make_buffer_rsrc((void*)p, 0, (int)bytes, 0x00020000); }
template <int IMM> DI void dma16(rsrc_t rs, lds_ptr dst, unsigned voff, unsigned soff) {
  __builtin_amdgcn_raw_ptr_buffer_load_lds(rs, dst, 16, (int)voff, (int)soff, IMM, 0);
}
DI s16x4 tr_rd(lds_cptr p) { return __builtin_bit_cast(s16x4, __builtin_amdgcn_ds_read_tr16_b64_v4i16((__attribute__((address_space(3))) v4i16_t*)p)); }
DI bf16x8 cat8(s16x4 lo, s16x4 hi) { return __builtin_shufflevector(lo, hi, 0, 1, 2, 3, 4, 5, 6, 7); }
DI bf16x8 lds_rd16(lds_cptr p) { return *(const __attribute__((address_space(3))) bf16x8*)p; }
DI float swap_max(float m) { auto rr = __builtin_amdgcn_permlane32_swap(__float_as_uint(m), __float_as_uint(m), false, false);
  return __builtin_fmaxf(__uint_as_float(rr[0]), __uint_as_float(rr[1])); }
DI float swap_add(float m) { auto rr = __builtin_amdgcn_permlane32_swap(__float_as_uint(m), __float_as_uint(m), false, false);
  return __uint_as_float(rr[0]) + __uint_as_float(rr[1]); }

template <int C> DI float dpp_add(float v) { return v + __int_as_float(__builtin_amdgcn_update_dpp(0, __float_as_int(v), C, 0xf, 0xf, true)); }
DI float wave_sum(float v) {
  v = dpp_add<0xB1>(v); v = dpp_add<0x4E>(v); v = dpp_add<0x141>(v); v = dpp_add<0x140>(v);
  auto r = __builtin_amdgcn_permlane16_swap(__float_as_uint(v), __float_as_uint(v), false, false);
  return swap_add(__uint_as_float(r[0]) + __uint_as_float(r[1]));
}

constexpr int LDS_BYTES = 163840;
constexpr int NA = 19;
struct Args { const u16* x; const u16* ckv; const u16* wq; const u16* wuk; const u16* wuv; const u16* wo; const u16* w1; const u16* w2; char* ws; u16* out; };
typedef const __attribute__((address_space(4))) Args* ArgP;
DI int lane_id() { int l; asm volatile("v_mbcnt_lo_u32_b32 %0, -1, 0\n\tv_mbcnt_hi_u32_b32 %0, -1, %0" : "=v"(l)); return l; }

__global__ __launch_bounds__(256, 1)
void fused(Args args_) {
  ArgP ap = (ArgP)__builtin_amdgcn_kernarg_segment_ptr();
  int b = blockIdx.x, w = __builtin_amdgcn_readfirstlane(threadIdx.x >> 6);
  unsigned long long tag;
  unsigned vo[7], va, vl, vf, tbe, tbo, vx;
  {
  const u16* x = ap->x; const u16* ckv = ap->ckv; const u16* wuk = ap->wuk; const u16* wuv = ap->wuv; char* ws = ap->ws;
  const int xcd = b & 7, j = b >> 3, pm = j & 7, pn = j >> 3, h = 2 * xcd + (pn & 1), vt = pn >> 1;
  const int lane = threadIdx.x & 63, wr = w >> 1, wc = w & 1, fr = lane & 15, g = lane >> 4;
  const rsrc_t rsC = mkrsrc(ckv, S * DC * 2), rsB = mkrsrc(vt ? wuv : wuk, DC * H * D * 2), rsW = mkrsrc(ws, (unsigned)WS_END), rsX = mkrsrc(x, S * M * 2);
  unsigned vA[4], vB[4];
#pragma unroll
  for (int p = 0; p < 4; ++p) {
    vA[p] = (8 * p + (lane >> 3)) * (DC * 2) + ((lane & 7) ^ (4 * (p & 1) + g)) * 16 - p * 1024;
    const int hi = lane >> 5, f = ((2 * p + hi) & 3) | ((w & 1) << 2);
    vB[p] = (2 * p + hi) * 8192 + ((lane & 31) ^ (2 * f)) * 16 - p * 1024;
  }
  const unsigned rb0 = 32 * (w >> 1) + 8 * (w & 1);
  const unsigned ca = (pm * 256 + w * 64) * (DC * 2), cb = rb0 * 8192 + h * 512, wa = w * 8192, wb = rb0 * 512;
  const int row = b * 8 + w * 2;
  u32x4 xv[2]; unsigned ev;
  { const unsigned xo = (lane < 56 ? lane : 55) * 16, so = row * (M * 2), eo = WS_EPOCH;
    asm volatile("buffer_load_dwordx4 %0, %3, %4, %5 offen\n\tbuffer_load_dwordx4 %1, %3, %4, %5 offen offset:896\n\tbuffer_load_dword %2, off, %6, %7 sc1"
                 : "=&v"(xv[0]), "=&v"(xv[1]), "=&v"(ev) : "v"(xo), "s"(rsX), "s"(so), "s"(rsW), "s"(eo) : "memory"); }
  asm volatile(
"s_add_u32 m0, %[wb], 0\n"
"s_add_u32 s70, %[cb], 0\n"
"buffer_load_dwordx4 %[vB0], %[rsB], s70 offen lds\n"
"buffer_load_dwordx4 %[vB1], %[rsB], s70 offen offset:1024 lds\n"
"buffer_load_dwordx4 %[vB2], %[rsB], s70 offen offset:2048 lds\n"
"buffer_load_dwordx4 %[vB3], %[rsB], s70 offen offset:3072 lds\n"
"s_add_u32 m0, %[wb], 0x2000\n"
"s_add_u32 s70, %[cb], 0x20000\n"
"buffer_load_dwordx4 %[vB0], %[rsB], s70 offen lds\n"
"buffer_load_dwordx4 %[vB1], %[rsB], s70 offen offset:1024 lds\n"
"buffer_load_dwordx4 %[vB2], %[rsB], s70 offen offset:2048 lds\n"
"buffer_load_dwordx4 %[vB3], %[rsB], s70 offen offset:3072 lds\n"
"s_add_u32 m0, %[wa], 0x8000\n"
"s_add_u32 s70, %[ca], 0\n"
"buffer_load_dwordx4 %[vA0], %[rsC], s70 offen lds\n"
"buffer_load_dwordx4 %[vA1], %[rsC], s70 offen offset:1024 lds\n"
"buffer_load_dwordx4 %[vA2], %[rsC], s70 offen offset:2048 lds\n"
"buffer_load_dwordx4 %[vA3], %[rsC], s70 offen offset:3072 lds\n"
"s_add_u32 m0, %[wa], 0x9000\n"
"s_add_u32 s70, %[ca], 0x10000\n"
"buffer_load_dwordx4 %[vA0], %[rsC], s70 offen lds\n"
"buffer_load_dwordx4 %[vA1], %[rsC], s70 offen offset:1024 lds\n"
"buffer_load_dwordx4 %[vA2], %[rsC], s70 offen offset:2048 lds\n"
"buffer_load_dwordx4 %[vA3], %[rsC], s70 offen offset:3072 lds\n"
"s_add_u32 m0, %[wb], 0x10000\n"
"s_add_u32 s70, %[cb], 0x80000\n"
"buffer_load_dwordx4 %[vB0], %[rsB], s70 offen lds\n"
"buffer_load_dwordx4 %[vB1], %[rsB], s70 offen offset:1024 lds\n"
"buffer_load_dwordx4 %[vB2], %[rsB], s70 offen offset:2048 lds\n"
"buffer_load_dwordx4 %[vB3], %[rsB], s70 offen offset:3072 lds\n"
"s_add_u32 m0, %[wb], 0x12000\n"
"s_add_u32 s70, %[cb], 0xa0000\n"
"buffer_load_dwordx4 %[vB0], %[rsB], s70 offen lds\n"
"buffer_load_dwordx4 %[vB1], %[rsB], s70 offen offset:1024 lds\n"
"buffer_load_dwordx4 %[vB2], %[rsB], s70 offen offset:2048 lds\n"
"buffer_load_dwordx4 %[vB3], %[rsB], s70 offen offset:3072 lds\n"
"s_add_u32 m0, %[wa], 0x18000\n"
"s_add_u32 s70, %[ca], 0x80\n"
"buffer_load_dwordx4 %[vA0], %[rsC], s70 offen lds\n"
"buffer_load_dwordx4 %[vA1], %[rsC], s70 offen offset:1024 lds\n"
"buffer_load_dwordx4 %[vA2], %[rsC], s70 offen offset:2048 lds\n"
"buffer_load_dwordx4 %[vA3], %[rsC], s70 offen offset:3072 lds\n"
"s_add_u32 m0, %[wa], 0x19000\n"
"s_add_u32 s70, %[ca], 0x10080\n"
"buffer_load_dwordx4 %[vA0], %[rsC], s70 offen lds\n"
"buffer_load_dwordx4 %[vA1], %[rsC], s70 offen offset:1024 lds\n"
"buffer_load_dwordx4 %[vA2], %[rsC], s70 offen offset:2048 lds\n"
"buffer_load_dwordx4 %[vA3], %[rsC], s70 offen offset:3072 lds\n"
"s_add_u32 m0, %[wb], 0x20000\n"
"s_add_u32 s70, %[cb], 0x100000\n"
"buffer_load_dwordx4 %[vB0], %[rsB], s70 offen lds\n"
"buffer_load_dwordx4 %[vB1], %[rsB], s70 offen offset:1024 lds\n"
"buffer_load_dwordx4 %[vB2], %[rsB], s70 offen offset:2048 lds\n"
"buffer_load_dwordx4 %[vB3], %[rsB], s70 offen offset:3072 lds\n"
"s_add_u32 m0, %[wb], 0x22000\n"
"s_add_u32 s70, %[cb], 0x120000\n"
"buffer_load_dwordx4 %[vB0], %[rsB], s70 offen lds\n"
"buffer_load_dwordx4 %[vB1], %[rsB], s70 offen offset:1024 lds\n"
"buffer_load_dwordx4 %[vB2], %[rsB], s70 offen offset:2048 lds\n"
"buffer_load_dwordx4 %[vB3], %[rsB], s70 offen offset:3072 lds\n"
    :
    : [rsC] "s"(rsC), [rsB] "s"(rsB), [ca] "s"(ca), [cb] "s"(cb), [wa] "s"(wa), [wb] "s"(wb),
      [vA0] "v"(vA[0]), [vA1] "v"(vA[1]), [vA2] "v"(vA[2]), [vA3] "v"(vA[3]), [vB0] "v"(vB[0]), [vB1] "v"(vB[1]), [vB2] "v"(vB[2]), [vB3] "v"(vB[3])
    : "memory", "scc", "s70");
#pragma unroll
  for (int i = 0; i < 7; ++i) { const int c = (w * 7 + i) * 64 + lane, row = c / 28; vo[i] = row * (M * 2) + ((c % 28) ^ ((row >> 1) & 2)) * 16 - (i & 3) * 1024; }
  va = ((g >> 1) * 64 + 32 * (g & 1) + fr) * 16; vl = lane * 16; vf = (lane & 7) * 8;
  const unsigned trow = (16 * (g >> 1) + 4 * (g & 1) + (fr >> 2)) * 448 + (lane & 3) * 8; tbe = trow + 32 * (g & 1); tbo = trow + 32 - 32 * (g & 1);
  vx = fr * (M * 2) + (16 * (g & 1) + 8 * (g >> 1)) * 2;
  asm volatile("" : "+v"(vo[0]), "+v"(vo[1]), "+v"(vo[2]), "+v"(vo[3]), "+v"(vo[4]), "+v"(vo[5]), "+v"(vo[6]), "+v"(va), "+v"(vl), "+v"(vf), "+v"(tbe), "+v"(tbo), "+v"(vx));
  asm volatile("s_waitcnt vmcnt(40)" : "+v"(xv[0]), "+v"(xv[1]), "+v"(ev) :: "memory");
  { const unsigned e = __builtin_amdgcn_readfirstlane(ev), t0 = e * 0x9E3779B1u + 0x7F4A7C15u, t1 = (e ^ (e >> 15)) * 0x85EBCA6Bu + 0x2545F491u;
    tag = (unsigned long long)t0 | ((unsigned long long)t1 << 32); }
  { float v[2][8], mean[2], q[2];
#pragma unroll
    for (int r = 0; r < 2; ++r) {
#pragma unroll
      for (int i = 0; i < 4; ++i) { v[r][2 * i] = __uint_as_float(xv[r][i] << 16); v[r][2 * i + 1] = __uint_as_float(xv[r][i] & 0xffff0000u); }
      const float s = ((v[r][0] + v[r][1]) + (v[r][2] + v[r][3])) + ((v[r][4] + v[r][5]) + (v[r][6] + v[r][7]));
      mean[r] = wave_sum(lane < 56 ? s : 0.f) * (1.f / M);
    }
#pragma unroll
    for (int r = 0; r < 2; ++r) {
      float s = 0.f;
#pragma unroll
      for (int i = 0; i < 8; ++i) { const float d = v[r][i] - mean[r]; s += d * d; }
      q[r] = wave_sum(lane < 56 ? s : 0.f);
    }
#pragma unroll
    for (int r = 0; r < 2; ++r) {
      const float mb = rbf(mean[r]), den = rbf(__builtin_sqrtf(rbf(rbf(q[r] * (1.f / M)) + 1e-5f))), inv = 1.f / den;
      u32x4 o4;
#pragma unroll
      for (int i = 0; i < 4; ++i) o4[i] = cvtpk(rbf(v[r][2 * i] - mb) * inv, rbf(v[r][2 * i + 1] - mb) * inv);
      const int rw = row + r;
      if (lane < 56) __builtin_amdgcn_raw_buffer_store_b128(o4, rsW, (int)WS_XF + (((rw >> 5) * 28 + (lane >> 1)) * 64 + (rw & 31) + 32 * (lane & 1)) * 16, 0, 16);
    }
  }
  const unsigned lA = (wr * 128 + fr) * 128 + (g ^ (fr >> 1)) * 16;
  const unsigned lB = (8 * g + (fr >> 2)) * 512 + wc * 256 + (((fr >> 2) | ((g & 1) << 2)) * 32) + (lane & 3) * 8;
  const unsigned vst = vt ? ((g >> 1) * 512 + fr + 32 * (g & 1)) * 16 : ((g >> 1) * 64 + (fr & 7) + 16 * (fr >> 3) + 32 * (g & 1)) * 16;
  const unsigned out = (unsigned)(vt ? WS_VF : WS_KF) + h * (S * D * 2) + (pm * 8 + wr * 4) * 16384 + wc * (vt ? 4096 : 8192);
  const unsigned stoff = (unsigned)WS_STAMP + (b * 4 + w) * 128;
  const int h2 = 2 * xcd + (j >> 4), pi = (j & 15) >> 1, role = j & 1, help = (role ^ 1) & (int)((unsigned)(2 * pi + NA + 1 - 32) >> 31), qb = (role == 1 || help) ? 15 - pi : pi, qb2 = role == 1 ? 15 - pi : pi;
  const rsrc_t rsP = mkrsrc(ap->w1, M * F * 2), rsQ = mkrsrc(ap->wq, M * H * D * 2);
  const unsigned q0 = h2 * 512 + w * 16 * 8192;
  const unsigned p0 = ((h2 >> 1) * 7 + (h2 & 1) * 3) * 64 + w * 16 * (F * 2), xoff = (unsigned)WS_XF + (qb2 * 4 + w) * 28 * 1024;
  const int iq = min((w & 1) * 64 + lane, 111), i1 = min(lane, 34);
  const unsigned vpw = w < 2 ? (14 * j + (iq >> 3)) * 8192 + xcd * 1024 + (iq & 7) * 128 : (14 * j + 7 * (w & 1) + i1 / 5) * (F * 2) + xcd * 448 + min((i1 % 5) * 128, 444);
  const unsigned vpz = w < 2 ? xcd * 458752 + (j * 112 + iq) * 128 : (w == 2 ? xcd * 200704 + (j * 49 + min(lane, 48)) * 128 : (j * 64 + lane) * 128);
  const rsrc_t rsZ = mkrsrc(w < 2 ? ap->wo : ap->w2, w < 2 ? H * D * M * 2 : F * M * 2);
  const unsigned lnf = WS_LNF + (b >> 2) * 128 + ((b & 3) * 4 + w) * 8, vpl = WS_LNF + (((lane & 16) ? qb2 : qb) * 4 + w) * 128 + (lane & 15) * 8;
  asm volatile(
"s_mov_b32 s65, 0\n"
"s_mov_b32 s64, 0x8000\n"
"s_add_u32 s68, %[ca], 0x100\n"
"s_add_u32 s69, %[cb], 0x180000\n"
"s_mov_b32 s71, 0\n"
"s_mov_b32 s73, 0x3db8aa3b\n"
"v_mov_b32 v30, 0x3db8aa3b\n"
"v_mov_b32 v31, 0x3db8aa3b\n"
"v_add_u32 v16, s64, %[lA]\n"
"v_add_u32 v18, s65, %[lB]\n"
"v_xor_b32 v17, 64, v16\n"
"v_xor_b32 v19, 32, v18\n"
"v_xor_b32 v20, 64, v18\n"
"v_xor_b32 v21, 0x60, v18\n"
"v_xor_b32 v22, 0x80, v18\n"
"v_xor_b32 v23, 0xa0, v18\n"
"v_xor_b32 v24, 0xc0, v18\n"
"v_xor_b32 v25, 0xe0, v18\n"
"v_mov_b64 v[28:29], %[tag]\n"
"s_lshr_b32 s74, %[wa], 1\n"
"s_waitcnt vmcnt(24)\n"
"s_barrier\n"
"s_cmp_lg_u32 %[vt], 0\n"
"s_cbranch_scc1 K1V%=\n"
"ds_read_b128 a[32:35], v16\n"
"ds_read_b64_tr_b16 a[64:65], v18\n"
"ds_read_b64_tr_b16 a[66:67], v18 offset:2048\n"
"ds_read_b64_tr_b16 a[68:69], v19\n"
"ds_read_b64_tr_b16 a[70:71], v19 offset:2048\n"
"ds_read_b64_tr_b16 a[72:73], v20\n"
"ds_read_b64_tr_b16 a[74:75], v20 offset:2048\n"
"ds_read_b64_tr_b16 a[76:77], v21\n"
"ds_read_b64_tr_b16 a[78:79], v21 offset:2048\n"
"ds_read_b64_tr_b16 a[80:81], v22\n"
"ds_read_b64_tr_b16 a[82:83], v22 offset:2048\n"
"ds_read_b64_tr_b16 a[84:85], v23\n"
"ds_read_b64_tr_b16 a[86:87], v23 offset:2048\n"
"ds_read_b64_tr_b16 a[88:89], v24\n"
"ds_read_b64_tr_b16 a[90:91], v24 offset:2048\n"
"ds_read_b64_tr_b16 a[92:93], v25\n"
"ds_read_b64_tr_b16 a[94:95], v25 offset:2048\n"
"ds_read_b128 a[36:39], v16 offset:2048\n"
"ds_read_b128 a[40:43], v16 offset:4096\n"
"ds_read_b128 a[44:47], v16 offset:6144\n"
"ds_read_b128 a[48:51], v16 offset:8192\n"
"ds_read_b128 a[52:55], v16 offset:10240\n"
"ds_read_b128 a[56:59], v16 offset:12288\n"
"ds_read_b128 a[60:63], v16 offset:14336\n"
"s_waitcnt lgkmcnt(15)\n"
"v_mfma_f32_16x16x32_bf16 v[32:35], a[64:67], a[32:35], 0\n"
"ds_read_b128 a[96:99], v17\n"
"v_mfma_f32_16x16x32_bf16 v[36:39], a[68:71], a[32:35], 0\n"
"ds_read_b64_tr_b16 a[128:129], v18 offset:16384\n"
"v_mfma_f32_16x16x32_bf16 v[40:43], a[72:75], a[32:35], 0\n"
"ds_read_b64_tr_b16 a[130:131], v18 offset:18432\n"
"v_mfma_f32_16x16x32_bf16 v[44:47], a[76:79], a[32:35], 0\n"
"ds_read_b64_tr_b16 a[132:133], v19 offset:16384\n"
"s_waitcnt lgkmcnt(15)\n"
"v_mfma_f32_16x16x32_bf16 v[48:51], a[80:83], a[32:35], 0\n"
"ds_read_b64_tr_b16 a[134:135], v19 offset:18432\n"
"v_mfma_f32_16x16x32_bf16 v[52:55], a[84:87], a[32:35], 0\n"
"ds_read_b64_tr_b16 a[136:137], v20 offset:16384\n"
"s_waitcnt lgkmcnt(15)\n"
"v_mfma_f32_16x16x32_bf16 v[56:59], a[88:91], a[32:35], 0\n"
"ds_read_b64_tr_b16 a[138:139], v20 offset:18432\n"
"s_waitcnt lgkmcnt(14)\n"
"v_mfma_f32_16x16x32_bf16 v[60:63], a[92:95], a[32:35], 0\n"
"ds_read_b64_tr_b16 a[140:141], v21 offset:16384\n"
"s_waitcnt lgkmcnt(14)\n"
"v_mfma_f32_16x16x32_bf16 v[64:67], a[64:67], a[36:39], 0\n"
"ds_read_b64_tr_b16 a[142:143], v21 offset:18432\n"
"v_mfma_f32_16x16x32_bf16 v[68:71], a[68:71], a[36:39], 0\n"
"ds_read_b64_tr_b16 a[144:145], v22 offset:16384\n"
"v_mfma_f32_16x16x32_bf16 v[72:75], a[72:75], a[36:39], 0\n"
"ds_read_b64_tr_b16 a[146:147], v22 offset:18432\n"
"v_mfma_f32_16x16x32_bf16 v[76:79], a[76:79], a[36:39], 0\n"
"ds_read_b64_tr_b16 a[148:149], v23 offset:16384\n"
"v_mfma_f32_16x16x32_bf16 v[80:83], a[80:83], a[36:39], 0\n"
"ds_read_b64_tr_b16 a[150:151], v23 offset:18432\n"
"v_mfma_f32_16x16x32_bf16 v[84:87], a[84:87], a[36:39], 0\n"
"ds_read_b64_tr_b16 a[152:153], v24 offset:16384\n"
"v_mfma_f32_16x16x32_bf16 v[88:91], a[88:91], a[36:39], 0\n"
"ds_read_b64_tr_b16 a[154:155], v24 offset:18432\n"
"v_mfma_f32_16x16x32_bf16 v[92:95], a[92:95], a[36:39], 0\n"
"ds_read_b64_tr_b16 a[156:157], v25 offset:16384\n"
"s_waitcnt lgkmcnt(15)\n"
"v_mfma_f32_16x16x32_bf16 v[96:99], a[64:67], a[40:43], 0\n"
"ds_read_b64_tr_b16 a[158:159], v25 offset:18432\n"
"v_mfma_f32_16x16x32_bf16 v[100:103], a[68:71], a[40:43], 0\n"
"ds_read_b128 a[100:103], v17 offset:2048\n"
"v_mfma_f32_16x16x32_bf16 v[104:107], a[72:75], a[40:43], 0\n"
"ds_read_b128 a[104:107], v17 offset:4096\n"
"v_mfma_f32_16x16x32_bf16 v[108:111], a[76:79], a[40:43], 0\n"
"ds_read_b128 a[108:111], v17 offset:6144\n"
"v_mfma_f32_16x16x32_bf16 v[112:115], a[80:83], a[40:43], 0\n"
"ds_read_b128 a[112:115], v17 offset:8192\n"
"v_mfma_f32_16x16x32_bf16 v[116:119], a[84:87], a[40:43], 0\n"
"ds_read_b128 a[116:119], v17 offset:10240\n"
"v_mfma_f32_16x16x32_bf16 v[120:123], a[88:91], a[40:43], 0\n"
"ds_read_b128 a[120:123], v17 offset:12288\n"
"v_mfma_f32_16x16x32_bf16 v[124:127], a[92:95], a[40:43], 0\n"
"ds_read_b128 a[124:127], v17 offset:14336\n"
"v_mfma_f32_16x16x32_bf16 v[128:131], a[64:67], a[44:47], 0\n"
"v_mfma_f32_16x16x32_bf16 v[132:135], a[68:71], a[44:47], 0\n"
"v_mfma_f32_16x16x32_bf16 v[136:139], a[72:75], a[44:47], 0\n"
"v_mfma_f32_16x16x32_bf16 v[140:143], a[76:79], a[44:47], 0\n"
"v_mfma_f32_16x16x32_bf16 v[144:147], a[80:83], a[44:47], 0\n"
"v_mfma_f32_16x16x32_bf16 v[148:151], a[84:87], a[44:47], 0\n"
"v_mfma_f32_16x16x32_bf16 v[152:155], a[88:91], a[44:47], 0\n"
"v_mfma_f32_16x16x32_bf16 v[156:159], a[92:95], a[44:47], 0\n"
"v_mfma_f32_16x16x32_bf16 v[160:163], a[64:67], a[48:51], 0\n"
"v_mfma_f32_16x16x32_bf16 v[164:167], a[68:71], a[48:51], 0\n"
"v_mfma_f32_16x16x32_bf16 v[168:171], a[72:75], a[48:51], 0\n"
"v_mfma_f32_16x16x32_bf16 v[172:175], a[76:79], a[48:51], 0\n"
"v_mfma_f32_16x16x32_bf16 v[176:179], a[80:83], a[48:51], 0\n"
"v_mfma_f32_16x16x32_bf16 v[180:183], a[84:87], a[48:51], 0\n"
"v_mfma_f32_16x16x32_bf16 v[184:187], a[88:91], a[48:51], 0\n"
"v_mfma_f32_16x16x32_bf16 v[188:191], a[92:95], a[48:51], 0\n"
"v_mfma_f32_16x16x32_bf16 v[192:195], a[64:67], a[52:55], 0\n"
"v_mfma_f32_16x16x32_bf16 v[196:199], a[68:71], a[52:55], 0\n"
"v_mfma_f32_16x16x32_bf16 v[200:203], a[72:75], a[52:55], 0\n"
"v_mfma_f32_16x16x32_bf16 v[204:207], a[76:79], a[52:55], 0\n"
"v_mfma_f32_16x16x32_bf16 v[208:211], a[80:83], a[52:55], 0\n"
"s_mov_b32 s66, s65\n"
"v_mfma_f32_16x16x32_bf16 v[212:215], a[84:87], a[52:55], 0\n"
"s_mov_b32 s67, s64\n"
"v_mfma_f32_16x16x32_bf16 v[216:219], a[88:91], a[52:55], 0\n"
"s_add_u32 s65, s64, 0x8000\n"
"v_mfma_f32_16x16x32_bf16 v[220:223], a[92:95], a[52:55], 0\n"
"s_cmp_eq_u32 s65, 0x28000\n"
"v_mfma_f32_16x16x32_bf16 v[224:227], a[64:67], a[56:59], 0\n"
"s_cselect_b32 s65, 0, s65\n"
"v_mfma_f32_16x16x32_bf16 v[228:231], a[68:71], a[56:59], 0\n"
"s_add_u32 s64, s65, 0x8000\n"
"v_mfma_f32_16x16x32_bf16 v[232:235], a[72:75], a[56:59], 0\n"
"s_cmp_eq_u32 s64, 0x28000\n"
"v_mfma_f32_16x16x32_bf16 v[236:239], a[76:79], a[56:59], 0\n"
"s_cselect_b32 s64, 0, s64\n"
"v_mfma_f32_16x16x32_bf16 v[240:243], a[80:83], a[56:59], 0\n"
"v_add_u32 v16, s64, %[lA]\n"
"v_mfma_f32_16x16x32_bf16 v[244:247], a[84:87], a[56:59], 0\n"
"v_add_u32 v18, s65, %[lB]\n"
"v_mfma_f32_16x16x32_bf16 v[248:251], a[88:91], a[56:59], 0\n"
"v_xor_b32 v17, 64, v16\n"
"v_mfma_f32_16x16x32_bf16 v[252:255], a[92:95], a[56:59], 0\n"
"v_xor_b32 v19, 32, v18\n"
"v_mfma_f32_16x16x32_bf16 a[0:3], a[64:67], a[60:63], 0\n"
"v_xor_b32 v20, 64, v18\n"
"v_mfma_f32_16x16x32_bf16 a[4:7], a[68:71], a[60:63], 0\n"
"v_xor_b32 v21, 0x60, v18\n"
"v_mfma_f32_16x16x32_bf16 a[8:11], a[72:75], a[60:63], 0\n"
"v_xor_b32 v22, 0x80, v18\n"
"v_mfma_f32_16x16x32_bf16 a[12:15], a[76:79], a[60:63], 0\n"
"v_xor_b32 v23, 0xa0, v18\n"
"v_mfma_f32_16x16x32_bf16 a[16:19], a[80:83], a[60:63], 0\n"
"v_xor_b32 v24, 0xc0, v18\n"
"v_mfma_f32_16x16x32_bf16 a[20:23], a[84:87], a[60:63], 0\n"
"v_xor_b32 v25, 0xe0, v18\n"
"v_mfma_f32_16x16x32_bf16 a[24:27], a[88:91], a[60:63], 0\n"
"v_mfma_f32_16x16x32_bf16 a[28:31], a[92:95], a[60:63], 0\n"
"K1Lk%=:\n"
"s_waitcnt lgkmcnt(0)\n"
"s_waitcnt vmcnt(8)\n"
"s_barrier\n"
"v_mfma_f32_16x16x32_bf16 v[32:35], a[128:131], a[96:99], v[32:35]\n"
"ds_read_b128 a[32:35], v16\n"
"v_mfma_f32_16x16x32_bf16 v[36:39], a[132:135], a[96:99], v[36:39]\n"
"ds_read_b64_tr_b16 a[64:65], v18\n"
"s_add_u32 m0, s66, %[wa]\n"
"s_cmp_eq_u32 s71, 1\n"
"s_cbranch_scc1 LNFk%=\n"
"LNBk%=:\n"
"v_mfma_f32_16x16x32_bf16 v[40:43], a[136:139], a[96:99], v[40:43]\n"
"ds_read_b64_tr_b16 a[66:67], v18 offset:2048\n"
"s_mov_b32 s70, s68\n"
"v_mfma_f32_16x16x32_bf16 v[44:47], a[140:143], a[96:99], v[44:47]\n"
"ds_read_b64_tr_b16 a[68:69], v19\n"
"buffer_load_dwordx4 %[vA0], %[rsC], s70 offen lds\n"
"v_mfma_f32_16x16x32_bf16 v[48:51], a[144:147], a[96:99], v[48:51]\n"
"ds_read_b64_tr_b16 a[70:71], v19 offset:2048\n"
"v_mfma_f32_16x16x32_bf16 v[52:55], a[148:151], a[96:99], v[52:55]\n"
"ds_read_b64_tr_b16 a[72:73], v20\n"
"v_mfma_f32_16x16x32_bf16 v[56:59], a[152:155], a[96:99], v[56:59]\n"
"ds_read_b64_tr_b16 a[74:75], v20 offset:2048\n"
"v_mfma_f32_16x16x32_bf16 v[60:63], a[156:159], a[96:99], v[60:63]\n"
"ds_read_b64_tr_b16 a[76:77], v21\n"
"buffer_load_dwordx4 %[vA1], %[rsC], s70 offen offset:1024 lds\n"
"v_mfma_f32_16x16x32_bf16 v[64:67], a[128:131], a[100:103], v[64:67]\n"
"ds_read_b64_tr_b16 a[78:79], v21 offset:2048\n"
"v_mfma_f32_16x16x32_bf16 v[68:71], a[132:135], a[100:103], v[68:71]\n"
"ds_read_b64_tr_b16 a[80:81], v22\n"
"v_mfma_f32_16x16x32_bf16 v[72:75], a[136:139], a[100:103], v[72:75]\n"
"ds_read_b64_tr_b16 a[82:83], v22 offset:2048\n"
"v_mfma_f32_16x16x32_bf16 v[76:79], a[140:143], a[100:103], v[76:79]\n"
"ds_read_b64_tr_b16 a[84:85], v23\n"
"buffer_load_dwordx4 %[vA2], %[rsC], s70 offen offset:2048 lds\n"
"v_mfma_f32_16x16x32_bf16 v[80:83], a[144:147], a[100:103], v[80:83]\n"
"ds_read_b64_tr_b16 a[86:87], v23 offset:2048\n"
"v_mfma_f32_16x16x32_bf16 v[84:87], a[148:151], a[100:103], v[84:87]\n"
"ds_read_b64_tr_b16 a[88:89], v24\n"
"v_mfma_f32_16x16x32_bf16 v[88:91], a[152:155], a[100:103], v[88:91]\n"
"ds_read_b64_tr_b16 a[90:91], v24 offset:2048\n"
"v_mfma_f32_16x16x32_bf16 v[92:95], a[156:159], a[100:103], v[92:95]\n"
"ds_read_b64_tr_b16 a[92:93], v25\n"
"buffer_load_dwordx4 %[vA3], %[rsC], s70 offen offset:3072 lds\n"
"v_mfma_f32_16x16x32_bf16 v[96:99], a[128:131], a[104:107], v[96:99]\n"
"ds_read_b64_tr_b16 a[94:95], v25 offset:2048\n"
"v_mfma_f32_16x16x32_bf16 v[100:103], a[132:135], a[104:107], v[100:103]\n"
"ds_read_b128 a[36:39], v16 offset:2048\n"
"s_add_u32 m0, m0, 0x1000\n"
"v_mfma_f32_16x16x32_bf16 v[104:107], a[136:139], a[104:107], v[104:107]\n"
"ds_read_b128 a[40:43], v16 offset:4096\n"
"s_add_u32 s70, s70, 0x10000\n"
"v_mfma_f32_16x16x32_bf16 v[108:111], a[140:143], a[104:107], v[108:111]\n"
"ds_read_b128 a[44:47], v16 offset:6144\n"
"buffer_load_dwordx4 %[vA0], %[rsC], s70 offen lds\n"
"v_mfma_f32_16x16x32_bf16 v[112:115], a[144:147], a[104:107], v[112:115]\n"
"ds_read_b128 a[48:51], v16 offset:8192\n"
"v_mfma_f32_16x16x32_bf16 v[116:119], a[148:151], a[104:107], v[116:119]\n"
"ds_read_b128 a[52:55], v16 offset:10240\n"
"v_mfma_f32_16x16x32_bf16 v[120:123], a[152:155], a[104:107], v[120:123]\n"
"ds_read_b128 a[56:59], v16 offset:12288\n"
"v_mfma_f32_16x16x32_bf16 v[124:127], a[156:159], a[104:107], v[124:127]\n"
"ds_read_b128 a[60:63], v16 offset:14336\n"
"buffer_load_dwordx4 %[vA1], %[rsC], s70 offen offset:1024 lds\n"
"v_mfma_f32_16x16x32_bf16 v[128:131], a[128:131], a[108:111], v[128:131]\n"
"v_mfma_f32_16x16x32_bf16 v[132:135], a[132:135], a[108:111], v[132:135]\n"
"v_mfma_f32_16x16x32_bf16 v[136:139], a[136:139], a[108:111], v[136:139]\n"
"v_mfma_f32_16x16x32_bf16 v[140:143], a[140:143], a[108:111], v[140:143]\n"
"buffer_load_dwordx4 %[vA2], %[rsC], s70 offen offset:2048 lds\n"
"v_mfma_f32_16x16x32_bf16 v[144:147], a[144:147], a[108:111], v[144:147]\n"
"v_mfma_f32_16x16x32_bf16 v[148:151], a[148:151], a[108:111], v[148:151]\n"
"v_mfma_f32_16x16x32_bf16 v[152:155], a[152:155], a[108:111], v[152:155]\n"
"v_mfma_f32_16x16x32_bf16 v[156:159], a[156:159], a[108:111], v[156:159]\n"
"buffer_load_dwordx4 %[vA3], %[rsC], s70 offen offset:3072 lds\n"
"v_mfma_f32_16x16x32_bf16 v[160:163], a[128:131], a[112:115], v[160:163]\n"
"v_mfma_f32_16x16x32_bf16 v[164:167], a[132:135], a[112:115], v[164:167]\n"
"v_mfma_f32_16x16x32_bf16 v[168:171], a[136:139], a[112:115], v[168:171]\n"
"v_mfma_f32_16x16x32_bf16 v[172:175], a[140:143], a[112:115], v[172:175]\n"
"v_mfma_f32_16x16x32_bf16 v[176:179], a[144:147], a[112:115], v[176:179]\n"
"v_mfma_f32_16x16x32_bf16 v[180:183], a[148:151], a[112:115], v[180:183]\n"
"s_add_u32 s68, s68, 0x80\n"
"v_mfma_f32_16x16x32_bf16 v[184:187], a[152:155], a[112:115], v[184:187]\n"
"s_add_u32 m0, s67, %[wb]\n"
"v_mfma_f32_16x16x32_bf16 v[188:191], a[156:159], a[112:115], v[188:191]\n"
"s_mov_b32 s70, s69\n"
"v_mfma_f32_16x16x32_bf16 v[192:195], a[128:131], a[116:119], v[192:195]\n"
"buffer_load_dwordx4 %[vB0], %[rsB], s70 offen lds\n"
"v_mfma_f32_16x16x32_bf16 v[196:199], a[132:135], a[116:119], v[196:199]\n"
"v_mfma_f32_16x16x32_bf16 v[200:203], a[136:139], a[116:119], v[200:203]\n"
"v_mfma_f32_16x16x32_bf16 v[204:207], a[140:143], a[116:119], v[204:207]\n"
"v_mfma_f32_16x16x32_bf16 v[208:211], a[144:147], a[116:119], v[208:211]\n"
"v_mfma_f32_16x16x32_bf16 v[212:215], a[148:151], a[116:119], v[212:215]\n"
"v_mfma_f32_16x16x32_bf16 v[216:219], a[152:155], a[116:119], v[216:219]\n"
"v_mfma_f32_16x16x32_bf16 v[220:223], a[156:159], a[116:119], v[220:223]\n"
"v_mfma_f32_16x16x32_bf16 v[224:227], a[128:131], a[120:123], v[224:227]\n"
"buffer_load_dwordx4 %[vB1], %[rsB], s70 offen offset:1024 lds\n"
"v_mfma_f32_16x16x32_bf16 v[228:231], a[132:135], a[120:123], v[228:231]\n"
"v_mfma_f32_16x16x32_bf16 v[232:235], a[136:139], a[120:123], v[232:235]\n"
"v_mfma_f32_16x16x32_bf16 v[236:239], a[140:143], a[120:123], v[236:239]\n"
"v_mfma_f32_16x16x32_bf16 v[240:243], a[144:147], a[120:123], v[240:243]\n"
"v_mfma_f32_16x16x32_bf16 v[244:247], a[148:151], a[120:123], v[244:247]\n"
"v_mfma_f32_16x16x32_bf16 v[248:251], a[152:155], a[120:123], v[248:251]\n"
"v_mfma_f32_16x16x32_bf16 v[252:255], a[156:159], a[120:123], v[252:255]\n"
"v_mfma_f32_16x16x32_bf16 a[0:3], a[128:131], a[124:127], a[0:3]\n"
"buffer_load_dwordx4 %[vB2], %[rsB], s70 offen offset:2048 lds\n"
"v_mfma_f32_16x16x32_bf16 a[4:7], a[132:135], a[124:127], a[4:7]\n"
"v_mfma_f32_16x16x32_bf16 a[8:11], a[136:139], a[124:127], a[8:11]\n"
"v_mfma_f32_16x16x32_bf16 a[12:15], a[140:143], a[124:127], a[12:15]\n"
"v_mfma_f32_16x16x32_bf16 a[16:19], a[144:147], a[124:127], a[16:19]\n"
"v_mfma_f32_16x16x32_bf16 a[20:23], a[148:151], a[124:127], a[20:23]\n"
"v_mfma_f32_16x16x32_bf16 a[24:27], a[152:155], a[124:127], a[24:27]\n"
"v_mfma_f32_16x16x32_bf16 a[28:31], a[156:159], a[124:127], a[28:31]\n"
"s_waitcnt lgkmcnt(15)\n"
"v_mfma_f32_16x16x32_bf16 v[32:35], a[64:67], a[32:35], v[32:35]\n"
"ds_read_b128 a[96:99], v17\n"
"buffer_load_dwordx4 %[vB3], %[rsB], s70 offen offset:3072 lds\n"
"v_mfma_f32_16x16x32_bf16 v[36:39], a[68:71], a[32:35], v[36:39]\n"
"ds_read_b64_tr_b16 a[128:129], v18 offset:16384\n"
"v_mfma_f32_16x16x32_bf16 v[40:43], a[72:75], a[32:35], v[40:43]\n"
"ds_read_b64_tr_b16 a[130:131], v18 offset:18432\n"
"v_mfma_f32_16x16x32_bf16 v[44:47], a[76:79], a[32:35], v[44:47]\n"
"ds_read_b64_tr_b16 a[132:133], v19 offset:16384\n"
"s_waitcnt lgkmcnt(15)\n"
"v_mfma_f32_16x16x32_bf16 v[48:51], a[80:83], a[32:35], v[48:51]\n"
"ds_read_b64_tr_b16 a[134:135], v19 offset:18432\n"
"v_mfma_f32_16x16x32_bf16 v[52:55], a[84:87], a[32:35], v[52:55]\n"
"ds_read_b64_tr_b16 a[136:137], v20 offset:16384\n"
"s_waitcnt lgkmcnt(15)\n"
"v_mfma_f32_16x16x32_bf16 v[56:59], a[88:91], a[32:35], v[56:59]\n"
"ds_read_b64_tr_b16 a[138:139], v20 offset:18432\n"
"s_add_u32 m0, m0, 0x2000\n"
"s_waitcnt lgkmcnt(14)\n"
"v_mfma_f32_16x16x32_bf16 v[60:63], a[92:95], a[32:35], v[60:63]\n"
"ds_read_b64_tr_b16 a[140:141], v21 offset:16384\n"
"s_add_u32 s70, s70, 0x20000\n"
"s_waitcnt lgkmcnt(14)\n"
"v_mfma_f32_16x16x32_bf16 v[64:67], a[64:67], a[36:39], v[64:67]\n"
"ds_read_b64_tr_b16 a[142:143], v21 offset:18432\n"
"buffer_load_dwordx4 %[vB0], %[rsB], s70 offen lds\n"
"v_mfma_f32_16x16x32_bf16 v[68:71], a[68:71], a[36:39], v[68:71]\n"
"ds_read_b64_tr_b16 a[144:145], v22 offset:16384\n"
"v_mfma_f32_16x16x32_bf16 v[72:75], a[72:75], a[36:39], v[72:75]\n"
"ds_read_b64_tr_b16 a[146:147], v22 offset:18432\n"
"v_mfma_f32_16x16x32_bf16 v[76:79], a[76:79], a[36:39], v[76:79]\n"
"ds_read_b64_tr_b16 a[148:149], v23 offset:16384\n"
"v_mfma_f32_16x16x32_bf16 v[80:83], a[80:83], a[36:39], v[80:83]\n"
"ds_read_b64_tr_b16 a[150:151], v23 offset:18432\n"
"v_mfma_f32_16x16x32_bf16 v[84:87], a[84:87], a[36:39], v[84:87]\n"
"ds_read_b64_tr_b16 a[152:153], v24 offset:16384\n"
"v_mfma_f32_16x16x32_bf16 v[88:91], a[88:91], a[36:39], v[88:91]\n"
"ds_read_b64_tr_b16 a[154:155], v24 offset:18432\n"
"v_mfma_f32_16x16x32_bf16 v[92:95], a[92:95], a[36:39], v[92:95]\n"
"ds_read_b64_tr_b16 a[156:157], v25 offset:16384\n"
"s_waitcnt lgkmcnt(15)\n"
"v_mfma_f32_16x16x32_bf16 v[96:99], a[64:67], a[40:43], v[96:99]\n"
"ds_read_b64_tr_b16 a[158:159], v25 offset:18432\n"
"buffer_load_dwordx4 %[vB1], %[rsB], s70 offen offset:1024 lds\n"
"v_mfma_f32_16x16x32_bf16 v[100:103], a[68:71], a[40:43], v[100:103]\n"
"ds_read_b128 a[100:103], v17 offset:2048\n"
"v_mfma_f32_16x16x32_bf16 v[104:107], a[72:75], a[40:43], v[104:107]\n"
"ds_read_b128 a[104:107], v17 offset:4096\n"
"v_mfma_f32_16x16x32_bf16 v[108:111], a[76:79], a[40:43], v[108:111]\n"
"ds_read_b128 a[108:111], v17 offset:6144\n"
"v_mfma_f32_16x16x32_bf16 v[112:115], a[80:83], a[40:43], v[112:115]\n"
"ds_read_b128 a[112:115], v17 offset:8192\n"
"v_mfma_f32_16x16x32_bf16 v[116:119], a[84:87], a[40:43], v[116:119]\n"
"ds_read_b128 a[116:119], v17 offset:10240\n"
"v_mfma_f32_16x16x32_bf16 v[120:123], a[88:91], a[40:43], v[120:123]\n"
"ds_read_b128 a[120:123], v17 offset:12288\n"
"v_mfma_f32_16x16x32_bf16 v[124:127], a[92:95], a[40:43], v[124:127]\n"
"ds_read_b128 a[124:127], v17 offset:14336\n"
"v_mfma_f32_16x16x32_bf16 v[128:131], a[64:67], a[44:47], v[128:131]\n"
"buffer_load_dwordx4 %[vB2], %[rsB], s70 offen offset:2048 lds\n"
"v_mfma_f32_16x16x32_bf16 v[132:135], a[68:71], a[44:47], v[132:135]\n"
"v_mfma_f32_16x16x32_bf16 v[136:139], a[72:75], a[44:47], v[136:139]\n"
"v_mfma_f32_16x16x32_bf16 v[140:143], a[76:79], a[44:47], v[140:143]\n"
"v_mfma_f32_16x16x32_bf16 v[144:147], a[80:83], a[44:47], v[144:147]\n"
"v_mfma_f32_16x16x32_bf16 v[148:151], a[84:87], a[44:47], v[148:151]\n"
"v_mfma_f32_16x16x32_bf16 v[152:155], a[88:91], a[44:47], v[152:155]\n"
"v_mfma_f32_16x16x32_bf16 v[156:159], a[92:95], a[44:47], v[156:159]\n"
"v_mfma_f32_16x16x32_bf16 v[160:163], a[64:67], a[48:51], v[160:163]\n"
"buffer_load_dwordx4 %[vB3], %[rsB], s70 offen offset:3072 lds\n"
"v_mfma_f32_16x16x32_bf16 v[164:167], a[68:71], a[48:51], v[164:167]\n"
"s_add_u32 s69, s69, 0x80000\n"
"v_mfma_f32_16x16x32_bf16 v[168:171], a[72:75], a[48:51], v[168:171]\n"
"v_mfma_f32_16x16x32_bf16 v[172:175], a[76:79], a[48:51], v[172:175]\n"
"v_mfma_f32_16x16x32_bf16 v[176:179], a[80:83], a[48:51], v[176:179]\n"
"v_mfma_f32_16x16x32_bf16 v[180:183], a[84:87], a[48:51], v[180:183]\n"
"v_mfma_f32_16x16x32_bf16 v[184:187], a[88:91], a[48:51], v[184:187]\n"
"v_mfma_f32_16x16x32_bf16 v[188:191], a[92:95], a[48:51], v[188:191]\n"
"v_mfma_f32_16x16x32_bf16 v[192:195], a[64:67], a[52:55], v[192:195]\n"
"s_cmp_eq_u32 s71, 9\n"
"s_cbranch_scc1 PFWk%=\n"
"PFBk%=:\n"
"v_mfma_f32_16x16x32_bf16 v[196:199], a[68:71], a[52:55], v[196:199]\n"
"v_mfma_f32_16x16x32_bf16 v[200:203], a[72:75], a[52:55], v[200:203]\n"
"v_mfma_f32_16x16x32_bf16 v[204:207], a[76:79], a[52:55], v[204:207]\n"
"v_mfma_f32_16x16x32_bf16 v[208:211], a[80:83], a[52:55], v[208:211]\n"
"s_mov_b32 s66, s65\n"
"v_mfma_f32_16x16x32_bf16 v[212:215], a[84:87], a[52:55], v[212:215]\n"
"s_mov_b32 s67, s64\n"
"v_mfma_f32_16x16x32_bf16 v[216:219], a[88:91], a[52:55], v[216:219]\n"
"s_add_u32 s65, s64, 0x8000\n"
"v_mfma_f32_16x16x32_bf16 v[220:223], a[92:95], a[52:55], v[220:223]\n"
"s_cmp_eq_u32 s65, 0x28000\n"
"v_mfma_f32_16x16x32_bf16 v[224:227], a[64:67], a[56:59], v[224:227]\n"
"s_cselect_b32 s65, 0, s65\n"
"v_mfma_f32_16x16x32_bf16 v[228:231], a[68:71], a[56:59], v[228:231]\n"
"s_add_u32 s64, s65, 0x8000\n"
"v_mfma_f32_16x16x32_bf16 v[232:235], a[72:75], a[56:59], v[232:235]\n"
"s_cmp_eq_u32 s64, 0x28000\n"
"v_mfma_f32_16x16x32_bf16 v[236:239], a[76:79], a[56:59], v[236:239]\n"
"s_cselect_b32 s64, 0, s64\n"
"v_mfma_f32_16x16x32_bf16 v[240:243], a[80:83], a[56:59], v[240:243]\n"
"v_add_u32 v16, s64, %[lA]\n"
"v_mfma_f32_16x16x32_bf16 v[244:247], a[84:87], a[56:59], v[244:247]\n"
"v_add_u32 v18, s65, %[lB]\n"
"v_mfma_f32_16x16x32_bf16 v[248:251], a[88:91], a[56:59], v[248:251]\n"
"v_xor_b32 v17, 64, v16\n"
"v_mfma_f32_16x16x32_bf16 v[252:255], a[92:95], a[56:59], v[252:255]\n"
"v_xor_b32 v19, 32, v18\n"
"v_mfma_f32_16x16x32_bf16 a[0:3], a[64:67], a[60:63], a[0:3]\n"
"v_xor_b32 v20, 64, v18\n"
"v_mfma_f32_16x16x32_bf16 a[4:7], a[68:71], a[60:63], a[4:7]\n"
"v_xor_b32 v21, 0x60, v18\n"
"v_mfma_f32_16x16x32_bf16 a[8:11], a[72:75], a[60:63], a[8:11]\n"
"v_xor_b32 v22, 0x80, v18\n"
"v_mfma_f32_16x16x32_bf16 a[12:15], a[76:79], a[60:63], a[12:15]\n"
"v_xor_b32 v23, 0xa0, v18\n"
"v_mfma_f32_16x16x32_bf16 a[16:19], a[80:83], a[60:63], a[16:19]\n"
"v_xor_b32 v24, 0xc0, v18\n"
"v_mfma_f32_16x16x32_bf16 a[20:23], a[84:87], a[60:63], a[20:23]\n"
"v_xor_b32 v25, 0xe0, v18\n"
"v_mfma_f32_16x16x32_bf16 a[24:27], a[88:91], a[60:63], a[24:27]\n"
"s_add_u32 s71, s71, 1\n"
"v_mfma_f32_16x16x32_bf16 a[28:31], a[92:95], a[60:63], a[28:31]\n"
"s_cmp_lt_u32 s71, 13\n"
"s_cbranch_scc1 K1Lk%=\n"
"s_waitcnt lgkmcnt(0)\n"
"s_waitcnt vmcnt(8)\n"
"s_barrier\n"
"v_mfma_f32_16x16x32_bf16 v[32:35], a[128:131], a[96:99], v[32:35]\n"
"ds_read_b128 a[32:35], v16\n"
"v_mfma_f32_16x16x32_bf16 v[36:39], a[132:135], a[96:99], v[36:39]\n"
"ds_read_b64_tr_b16 a[64:65], v18\n"
"s_add_u32 m0, s66, %[wa]\n"
"v_mfma_f32_16x16x32_bf16 v[40:43], a[136:139], a[96:99], v[40:43]\n"
"ds_read_b64_tr_b16 a[66:67], v18 offset:2048\n"
"s_mov_b32 s70, s68\n"
"v_mfma_f32_16x16x32_bf16 v[44:47], a[140:143], a[96:99], v[44:47]\n"
"ds_read_b64_tr_b16 a[68:69], v19\n"
"buffer_load_dwordx4 %[vA0], %[rsC], s70 offen lds\n"
"v_mfma_f32_16x16x32_bf16 v[48:51], a[144:147], a[96:99], v[48:51]\n"
"ds_read_b64_tr_b16 a[70:71], v19 offset:2048\n"
"v_mfma_f32_16x16x32_bf16 v[52:55], a[148:151], a[96:99], v[52:55]\n"
"ds_read_b64_tr_b16 a[72:73], v20\n"
"v_mfma_f32_16x16x32_bf16 v[56:59], a[152:155], a[96:99], v[56:59]\n"
"ds_read_b64_tr_b16 a[74:75], v20 offset:2048\n"
"v_mfma_f32_16x16x32_bf16 v[60:63], a[156:159], a[96:99], v[60:63]\n"
"ds_read_b64_tr_b16 a[76:77], v21\n"
"buffer_load_dwordx4 %[vA1], %[rsC], s70 offen offset:1024 lds\n"
"v_mfma_f32_16x16x32_bf16 v[64:67], a[128:131], a[100:103], v[64:67]\n"
"ds_read_b64_tr_b16 a[78:79], v21 offset:2048\n"
"v_mfma_f32_16x16x32_bf16 v[68:71], a[132:135], a[100:103], v[68:71]\n"
"ds_read_b64_tr_b16 a[80:81], v22\n"
"v_mfma_f32_16x16x32_bf16 v[72:75], a[136:139], a[100:103], v[72:75]\n"
"ds_read_b64_tr_b16 a[82:83], v22 offset:2048\n"
"v_mfma_f32_16x16x32_bf16 v[76:79], a[140:143], a[100:103], v[76:79]\n"
"ds_read_b64_tr_b16 a[84:85], v23\n"
"buffer_load_dwordx4 %[vA2], %[rsC], s70 offen offset:2048 lds\n"
"v_mfma_f32_16x16x32_bf16 v[80:83], a[144:147], a[100:103], v[80:83]\n"
"ds_read_b64_tr_b16 a[86:87], v23 offset:2048\n"
"v_mfma_f32_16x16x32_bf16 v[84:87], a[148:151], a[100:103], v[84:87]\n"
"ds_read_b64_tr_b16 a[88:89], v24\n"
"v_mfma_f32_16x16x32_bf16 v[88:91], a[152:155], a[100:103], v[88:91]\n"
"ds_read_b64_tr_b16 a[90:91], v24 offset:2048\n"
"v_mfma_f32_16x16x32_bf16 v[92:95], a[156:159], a[100:103], v[92:95]\n"
"ds_read_b64_tr_b16 a[92:93], v25\n"
"buffer_load_dwordx4 %[vA3], %[rsC], s70 offen offset:3072 lds\n"
"v_mfma_f32_16x16x32_bf16 v[96:99], a[128:131], a[104:107], v[96:99]\n"
"ds_read_b64_tr_b16 a[94:95], v25 offset:2048\n"
"v_mfma_f32_16x16x32_bf16 v[100:103], a[132:135], a[104:107], v[100:103]\n"
"ds_read_b128 a[36:39], v16 offset:2048\n"
"s_add_u32 m0, m0, 0x1000\n"
"v_mfma_f32_16x16x32_bf16 v[104:107], a[136:139], a[104:107], v[104:107]\n"
"ds_read_b128 a[40:43], v16 offset:4096\n"
"s_add_u32 s70, s70, 0x10000\n"
"v_mfma_f32_16x16x32_bf16 v[108:111], a[140:143], a[104:107], v[108:111]\n"
"ds_read_b128 a[44:47], v16 offset:6144\n"
"buffer_load_dwordx4 %[vA0], %[rsC], s70 offen lds\n"
"v_mfma_f32_16x16x32_bf16 v[112:115], a[144:147], a[104:107], v[112:115]\n"
"ds_read_b128 a[48:51], v16 offset:8192\n"
"v_mfma_f32_16x16x32_bf16 v[116:119], a[148:151], a[104:107], v[116:119]\n"
"ds_read_b128 a[52:55], v16 offset:10240\n"
"v_mfma_f32_16x16x32_bf16 v[120:123], a[152:155], a[104:107], v[120:123]\n"
"ds_read_b128 a[56:59], v16 offset:12288\n"
"v_mfma_f32_16x16x32_bf16 v[124:127], a[156:159], a[104:107], v[124:127]\n"
"ds_read_b128 a[60:63], v16 offset:14336\n"
"buffer_load_dwordx4 %[vA1], %[rsC], s70 offen offset:1024 lds\n"
"v_mfma_f32_16x16x32_bf16 v[128:131], a[128:131], a[108:111], v[128:131]\n"
"v_mfma_f32_16x16x32_bf16 v[132:135], a[132:135], a[108:111], v[132:135]\n"
"v_mfma_f32_16x16x32_bf16 v[136:139], a[136:139], a[108:111], v[136:139]\n"
"v_mfma_f32_16x16x32_bf16 v[140:143], a[140:143], a[108:111], v[140:143]\n"
"buffer_load_dwordx4 %[vA2], %[rsC], s70 offen offset:2048 lds\n"
"v_mfma_f32_16x16x32_bf16 v[144:147], a[144:147], a[108:111], v[144:147]\n"
"v_mfma_f32_16x16x32_bf16 v[148:151], a[148:151], a[108:111], v[148:151]\n"
"v_mfma_f32_16x16x32_bf16 v[152:155], a[152:155], a[108:111], v[152:155]\n"
"v_mfma_f32_16x16x32_bf16 v[156:159], a[156:159], a[108:111], v[156:159]\n"
"buffer_load_dwordx4 %[vA3], %[rsC], s70 offen offset:3072 lds\n"
"v_mfma_f32_16x16x32_bf16 v[160:163], a[128:131], a[112:115], v[160:163]\n"
"s_add_u32 s68, s68, 0x80\n"
"v_mfma_f32_16x16x32_bf16 v[164:167], a[132:135], a[112:115], v[164:167]\n"
"v_mfma_f32_16x16x32_bf16 v[168:171], a[136:139], a[112:115], v[168:171]\n"
"v_mfma_f32_16x16x32_bf16 v[172:175], a[140:143], a[112:115], v[172:175]\n"
"v_mfma_f32_16x16x32_bf16 v[176:179], a[144:147], a[112:115], v[176:179]\n"
"v_mfma_f32_16x16x32_bf16 v[180:183], a[148:151], a[112:115], v[180:183]\n"
"v_mfma_f32_16x16x32_bf16 v[184:187], a[152:155], a[112:115], v[184:187]\n"
"v_mfma_f32_16x16x32_bf16 v[188:191], a[156:159], a[112:115], v[188:191]\n"
"v_mfma_f32_16x16x32_bf16 v[192:195], a[128:131], a[116:119], v[192:195]\n"
"v_mfma_f32_16x16x32_bf16 v[196:199], a[132:135], a[116:119], v[196:199]\n"
"v_mfma_f32_16x16x32_bf16 v[200:203], a[136:139], a[116:119], v[200:203]\n"
"v_mfma_f32_16x16x32_bf16 v[204:207], a[140:143], a[116:119], v[204:207]\n"
"v_mfma_f32_16x16x32_bf16 v[208:211], a[144:147], a[116:119], v[208:211]\n"
"v_mfma_f32_16x16x32_bf16 v[212:215], a[148:151], a[116:119], v[212:215]\n"
"v_mfma_f32_16x16x32_bf16 v[216:219], a[152:155], a[116:119], v[216:219]\n"
"v_mfma_f32_16x16x32_bf16 v[220:223], a[156:159], a[116:119], v[220:223]\n"
"v_mfma_f32_16x16x32_bf16 v[224:227], a[128:131], a[120:123], v[224:227]\n"
"v_mfma_f32_16x16x32_bf16 v[228:231], a[132:135], a[120:123], v[228:231]\n"
"v_mfma_f32_16x16x32_bf16 v[232:235], a[136:139], a[120:123], v[232:235]\n"
"v_mfma_f32_16x16x32_bf16 v[236:239], a[140:143], a[120:123], v[236:239]\n"
"v_mfma_f32_16x16x32_bf16 v[240:243], a[144:147], a[120:123], v[240:243]\n"
"v_mfma_f32_16x16x32_bf16 v[244:247], a[148:151], a[120:123], v[244:247]\n"
"v_mfma_f32_16x16x32_bf16 v[248:251], a[152:155], a[120:123], v[248:251]\n"
"v_mfma_f32_16x16x32_bf16 v[252:255], a[156:159], a[120:123], v[252:255]\n"
"v_mfma_f32_16x16x32_bf16 a[0:3], a[128:131], a[124:127], a[0:3]\n"
"v_mfma_f32_16x16x32_bf16 a[4:7], a[132:135], a[124:127], a[4:7]\n"
"v_mfma_f32_16x16x32_bf16 a[8:11], a[136:139], a[124:127], a[8:11]\n"
"v_mfma_f32_16x16x32_bf16 a[12:15], a[140:143], a[124:127], a[12:15]\n"
"v_mfma_f32_16x16x32_bf16 a[16:19], a[144:147], a[124:127], a[16:19]\n"
"v_mfma_f32_16x16x32_bf16 a[20:23], a[148:151], a[124:127], a[20:23]\n"
"v_mfma_f32_16x16x32_bf16 a[24:27], a[152:155], a[124:127], a[24:27]\n"
"v_mfma_f32_16x16x32_bf16 a[28:31], a[156:159], a[124:127], a[28:31]\n"
"s_waitcnt lgkmcnt(15)\n"
"v_mfma_f32_16x16x32_bf16 v[32:35], a[64:67], a[32:35], v[32:35]\n"
"ds_read_b128 a[96:99], v17\n"
"v_mfma_f32_16x16x32_bf16 v[36:39], a[68:71], a[32:35], v[36:39]\n"
"ds_read_b64_tr_b16 a[128:129], v18 offset:16384\n"
"v_mfma_f32_16x16x32_bf16 v[40:43], a[72:75], a[32:35], v[40:43]\n"
"ds_read_b64_tr_b16 a[130:131], v18 offset:18432\n"
"v_mfma_f32_16x16x32_bf16 v[44:47], a[76:79], a[32:35], v[44:47]\n"
"ds_read_b64_tr_b16 a[132:133], v19 offset:16384\n"
"s_waitcnt lgkmcnt(15)\n"
"v_mfma_f32_16x16x32_bf16 v[48:51], a[80:83], a[32:35], v[48:51]\n"
"ds_read_b64_tr_b16 a[134:135], v19 offset:18432\n"
"v_mfma_f32_16x16x32_bf16 v[52:55], a[84:87], a[32:35], v[52:55]\n"
"ds_read_b64_tr_b16 a[136:137], v20 offset:16384\n"
"s_waitcnt lgkmcnt(15)\n"
"v_mfma_f32_16x16x32_bf16 v[56:59], a[88:91], a[32:35], v[56:59]\n"
"ds_read_b64_tr_b16 a[138:139], v20 offset:18432\n"
"s_waitcnt lgkmcnt(14)\n"
"v_mfma_f32_16x16x32_bf16 v[60:63], a[92:95], a[32:35], v[60:63]\n"
"ds_read_b64_tr_b16 a[140:141], v21 offset:16384\n"
"s_waitcnt lgkmcnt(14)\n"
"v_mfma_f32_16x16x32_bf16 v[64:67], a[64:67], a[36:39], v[64:67]\n"
"ds_read_b64_tr_b16 a[142:143], v21 offset:18432\n"
"v_mfma_f32_16x16x32_bf16 v[68:71], a[68:71], a[36:39], v[68:71]\n"
"ds_read_b64_tr_b16 a[144:145], v22 offset:16384\n"
"v_mfma_f32_16x16x32_bf16 v[72:75], a[72:75], a[36:39], v[72:75]\n"
"ds_read_b64_tr_b16 a[146:147], v22 offset:18432\n"
"v_mfma_f32_16x16x32_bf16 v[76:79], a[76:79], a[36:39], v[76:79]\n"
"ds_read_b64_tr_b16 a[148:149], v23 offset:16384\n"
"v_mfma_f32_16x16x32_bf16 v[80:83], a[80:83], a[36:39], v[80:83]\n"
"ds_read_b64_tr_b16 a[150:151], v23 offset:18432\n"
"v_mfma_f32_16x16x32_bf16 v[84:87], a[84:87], a[36:39], v[84:87]\n"
"ds_read_b64_tr_b16 a[152:153], v24 offset:16384\n"
"v_mfma_f32_16x16x32_bf16 v[88:91], a[88:91], a[36:39], v[88:91]\n"
"ds_read_b64_tr_b16 a[154:155], v24 offset:18432\n"
"v_mfma_f32_16x16x32_bf16 v[92:95], a[92:95], a[36:39], v[92:95]\n"
"ds_read_b64_tr_b16 a[156:157], v25 offset:16384\n"
"s_waitcnt lgkmcnt(15)\n"
"v_mfma_f32_16x16x32_bf16 v[96:99], a[64:67], a[40:43], v[96:99]\n"
"ds_read_b64_tr_b16 a[158:159], v25 offset:18432\n"
"v_mfma_f32_16x16x32_bf16 v[100:103], a[68:71], a[40:43], v[100:103]\n"
"ds_read_b128 a[100:103], v17 offset:2048\n"
"v_mfma_f32_16x16x32_bf16 v[104:107], a[72:75], a[40:43], v[104:107]\n"
"ds_read_b128 a[104:107], v17 offset:4096\n"
"v_mfma_f32_16x16x32_bf16 v[108:111], a[76:79], a[40:43], v[108:111]\n"
"ds_read_b128 a[108:111], v17 offset:6144\n"
"v_mfma_f32_16x16x32_bf16 v[112:115], a[80:83], a[40:43], v[112:115]\n"
"ds_read_b128 a[112:115], v17 offset:8192\n"
"v_mfma_f32_16x16x32_bf16 v[116:119], a[84:87], a[40:43], v[116:119]\n"
"ds_read_b128 a[116:119], v17 offset:10240\n"
"v_mfma_f32_16x16x32_bf16 v[120:123], a[88:91], a[40:43], v[120:123]\n"
"ds_read_b128 a[120:123], v17 offset:12288\n"
"v_mfma_f32_16x16x32_bf16 v[124:127], a[92:95], a[40:43], v[124:127]\n"
"ds_read_b128 a[124:127], v17 offset:14336\n"
"v_mfma_f32_16x16x32_bf16 v[128:131], a[64:67], a[44:47], v[128:131]\n"
"v_mfma_f32_16x16x32_bf16 v[132:135], a[68:71], a[44:47], v[132:135]\n"
"v_mfma_f32_16x16x32_bf16 v[136:139], a[72:75], a[44:47], v[136:139]\n"
"v_mfma_f32_16x16x32_bf16 v[140:143], a[76:79], a[44:47], v[140:143]\n"
"v_mfma_f32_16x16x32_bf16 v[144:147], a[80:83], a[44:47], v[144:147]\n"
"v_mfma_f32_16x16x32_bf16 v[148:151], a[84:87], a[44:47], v[148:151]\n"
"v_mfma_f32_16x16x32_bf16 v[152:155], a[88:91], a[44:47], v[152:155]\n"
"v_mfma_f32_16x16x32_bf16 v[156:159], a[92:95], a[44:47], v[156:159]\n"
"v_mfma_f32_16x16x32_bf16 v[160:163], a[64:67], a[48:51], v[160:163]\n"
"v_mfma_f32_16x16x32_bf16 v[164:167], a[68:71], a[48:51], v[164:167]\n"
"v_mfma_f32_16x16x32_bf16 v[168:171], a[72:75], a[48:51], v[168:171]\n"
"v_mfma_f32_16x16x32_bf16 v[172:175], a[76:79], a[48:51], v[172:175]\n"
"v_mfma_f32_16x16x32_bf16 v[176:179], a[80:83], a[48:51], v[176:179]\n"
"v_mfma_f32_16x16x32_bf16 v[180:183], a[84:87], a[48:51], v[180:183]\n"
"v_mfma_f32_16x16x32_bf16 v[184:187], a[88:91], a[48:51], v[184:187]\n"
"v_mfma_f32_16x16x32_bf16 v[188:191], a[92:95], a[48:51], v[188:191]\n"
"v_mfma_f32_16x16x32_bf16 v[192:195], a[64:67], a[52:55], v[192:195]\n"
"v_mfma_f32_16x16x32_bf16 v[196:199], a[68:71], a[52:55], v[196:199]\n"
"v_mfma_f32_16x16x32_bf16 v[200:203], a[72:75], a[52:55], v[200:203]\n"
"v_mfma_f32_16x16x32_bf16 v[204:207], a[76:79], a[52:55], v[204:207]\n"
"v_mfma_f32_16x16x32_bf16 v[208:211], a[80:83], a[52:55], v[208:211]\n"
"s_mov_b32 s66, s65\n"
"v_mfma_f32_16x16x32_bf16 v[212:215], a[84:87], a[52:55], v[212:215]\n"
"s_mov_b32 s67, s64\n"
"v_mfma_f32_16x16x32_bf16 v[216:219], a[88:91], a[52:55], v[216:219]\n"
"s_add_u32 s65, s64, 0x8000\n"
"v_mfma_f32_16x16x32_bf16 v[220:223], a[92:95], a[52:55], v[220:223]\n"
"s_cmp_eq_u32 s65, 0x28000\n"
"v_mfma_f32_16x16x32_bf16 v[224:227], a[64:67], a[56:59], v[224:227]\n"
"s_cselect_b32 s65, 0, s65\n"
"v_mfma_f32_16x16x32_bf16 v[228:231], a[68:71], a[56:59], v[228:231]\n"
"s_add_u32 s64, s65, 0x8000\n"
"v_mfma_f32_16x16x32_bf16 v[232:235], a[72:75], a[56:59], v[232:235]\n"
"s_cmp_eq_u32 s64, 0x28000\n"
"v_mfma_f32_16x16x32_bf16 v[236:239], a[76:79], a[56:59], v[236:239]\n"
"s_cselect_b32 s64, 0, s64\n"
"v_mfma_f32_16x16x32_bf16 v[240:243], a[80:83], a[56:59], v[240:243]\n"
"v_add_u32 v16, s64, %[lA]\n"
"v_mfma_f32_16x16x32_bf16 v[244:247], a[84:87], a[56:59], v[244:247]\n"
"v_add_u32 v18, s65, %[lB]\n"
"v_mfma_f32_16x16x32_bf16 v[248:251], a[88:91], a[56:59], v[248:251]\n"
"v_xor_b32 v17, 64, v16\n"
"v_mfma_f32_16x16x32_bf16 v[252:255], a[92:95], a[56:59], v[252:255]\n"
"v_xor_b32 v19, 32, v18\n"
"v_mfma_f32_16x16x32_bf16 a[0:3], a[64:67], a[60:63], a[0:3]\n"
"v_xor_b32 v20, 64, v18\n"
"v_mfma_f32_16x16x32_bf16 a[4:7], a[68:71], a[60:63], a[4:7]\n"
"v_xor_b32 v21, 0x60, v18\n"
"v_mfma_f32_16x16x32_bf16 a[8:11], a[72:75], a[60:63], a[8:11]\n"
"v_xor_b32 v22, 0x80, v18\n"
"v_mfma_f32_16x16x32_bf16 a[12:15], a[76:79], a[60:63], a[12:15]\n"
"v_xor_b32 v23, 0xa0, v18\n"
"v_mfma_f32_16x16x32_bf16 a[16:19], a[80:83], a[60:63], a[16:19]\n"
"v_xor_b32 v24, 0xc0, v18\n"
"v_mfma_f32_16x16x32_bf16 a[20:23], a[84:87], a[60:63], a[20:23]\n"
"v_xor_b32 v25, 0xe0, v18\n"
"v_mfma_f32_16x16x32_bf16 a[24:27], a[88:91], a[60:63], a[24:27]\n"
"v_mfma_f32_16x16x32_bf16 a[28:31], a[92:95], a[60:63], a[28:31]\n"
"s_waitcnt lgkmcnt(0)\n"
"s_waitcnt vmcnt(0)\n"
"s_barrier\n"
"v_mfma_f32_16x16x32_bf16 v[32:35], a[128:131], a[96:99], v[32:35]\n"
"ds_read_b128 a[32:35], v16\n"
"v_mfma_f32_16x16x32_bf16 v[36:39], a[132:135], a[96:99], v[36:39]\n"
"ds_read_b64_tr_b16 a[64:65], v18\n"
"v_mfma_f32_16x16x32_bf16 v[40:43], a[136:139], a[96:99], v[40:43]\n"
"ds_read_b64_tr_b16 a[66:67], v18 offset:2048\n"
"v_mfma_f32_16x16x32_bf16 v[44:47], a[140:143], a[96:99], v[44:47]\n"
"ds_read_b64_tr_b16 a[68:69], v19\n"
"v_mfma_f32_16x16x32_bf16 v[48:51], a[144:147], a[96:99], v[48:51]\n"
"ds_read_b64_tr_b16 a[70:71], v19 offset:2048\n"
"v_mfma_f32_16x16x32_bf16 v[52:55], a[148:151], a[96:99], v[52:55]\n"
"ds_read_b64_tr_b16 a[72:73], v20\n"
"v_mfma_f32_16x16x32_bf16 v[56:59], a[152:155], a[96:99], v[56:59]\n"
"ds_read_b64_tr_b16 a[74:75], v20 offset:2048\n"
"v_mfma_f32_16x16x32_bf16 v[60:63], a[156:159], a[96:99], v[60:63]\n"
"ds_read_b64_tr_b16 a[76:77], v21\n"
"v_mfma_f32_16x16x32_bf16 v[64:67], a[128:131], a[100:103], v[64:67]\n"
"ds_read_b64_tr_b16 a[78:79], v21 offset:2048\n"
"v_mfma_f32_16x16x32_bf16 v[68:71], a[132:135], a[100:103], v[68:71]\n"
"ds_read_b64_tr_b16 a[80:81], v22\n"
"v_mfma_f32_16x16x32_bf16 v[72:75], a[136:139], a[100:103], v[72:75]\n"
"ds_read_b64_tr_b16 a[82:83], v22 offset:2048\n"
"v_mfma_f32_16x16x32_bf16 v[76:79], a[140:143], a[100:103], v[76:79]\n"
"ds_read_b64_tr_b16 a[84:85], v23\n"
"v_mfma_f32_16x16x32_bf16 v[80:83], a[144:147], a[100:103], v[80:83]\n"
"ds_read_b64_tr_b16 a[86:87], v23 offset:2048\n"
"v_mfma_f32_16x16x32_bf16 v[84:87], a[148:151], a[100:103], v[84:87]\n"
"ds_read_b64_tr_b16 a[88:89], v24\n"
"v_mfma_f32_16x16x32_bf16 v[88:91], a[152:155], a[100:103], v[88:91]\n"
"ds_read_b64_tr_b16 a[90:91], v24 offset:2048\n"
"v_mfma_f32_16x16x32_bf16 v[92:95], a[156:159], a[100:103], v[92:95]\n"
"ds_read_b64_tr_b16 a[92:93], v25\n"
"v_mfma_f32_16x16x32_bf16 v[96:99], a[128:131], a[104:107], v[96:99]\n"
"ds_read_b64_tr_b16 a[94:95], v25 offset:2048\n"
"v_mfma_f32_16x16x32_bf16 v[100:103], a[132:135], a[104:107], v[100:103]\n"
"ds_read_b128 a[36:39], v16 offset:2048\n"
"v_mfma_f32_16x16x32_bf16 v[104:107], a[136:139], a[104:107], v[104:107]\n"
"ds_read_b128 a[40:43], v16 offset:4096\n"
"v_mfma_f32_16x16x32_bf16 v[108:111], a[140:143], a[104:107], v[108:111]\n"
"ds_read_b128 a[44:47], v16 offset:6144\n"
"v_mfma_f32_16x16x32_bf16 v[112:115], a[144:147], a[104:107], v[112:115]\n"
"ds_read_b128 a[48:51], v16 offset:8192\n"
"v_mfma_f32_16x16x32_bf16 v[116:119], a[148:151], a[104:107], v[116:119]\n"
"ds_read_b128 a[52:55], v16 offset:10240\n"
"v_mfma_f32_16x16x32_bf16 v[120:123], a[152:155], a[104:107], v[120:123]\n"
"ds_read_b128 a[56:59], v16 offset:12288\n"
"v_mfma_f32_16x16x32_bf16 v[124:127], a[156:159], a[104:107], v[124:127]\n"
"ds_read_b128 a[60:63], v16 offset:14336\n"
"v_mfma_f32_16x16x32_bf16 v[128:131], a[128:131], a[108:111], v[128:131]\n"
"v_mfma_f32_16x16x32_bf16 v[132:135], a[132:135], a[108:111], v[132:135]\n"
"v_mfma_f32_16x16x32_bf16 v[136:139], a[136:139], a[108:111], v[136:139]\n"
"v_mfma_f32_16x16x32_bf16 v[140:143], a[140:143], a[108:111], v[140:143]\n"
"v_mfma_f32_16x16x32_bf16 v[144:147], a[144:147], a[108:111], v[144:147]\n"
"v_mfma_f32_16x16x32_bf16 v[148:151], a[148:151], a[108:111], v[148:151]\n"
"v_mfma_f32_16x16x32_bf16 v[152:155], a[152:155], a[108:111], v[152:155]\n"
"buffer_load_dwordx2 v[26:27], %[vpl], %[rsW], 0 offen sc1\n"
"v_mfma_f32_16x16x32_bf16 v[156:159], a[156:159], a[108:111], v[156:159]\n"
"v_mfma_f32_16x16x32_bf16 v[160:163], a[128:131], a[112:115], v[160:163]\n"
"v_mfma_f32_16x16x32_bf16 v[164:167], a[132:135], a[112:115], v[164:167]\n"
"v_mfma_f32_16x16x32_bf16 v[168:171], a[136:139], a[112:115], v[168:171]\n"
"v_mfma_f32_16x16x32_bf16 v[172:175], a[140:143], a[112:115], v[172:175]\n"
"v_mfma_f32_16x16x32_bf16 v[176:179], a[144:147], a[112:115], v[176:179]\n"
"v_mfma_f32_16x16x32_bf16 v[180:183], a[148:151], a[112:115], v[180:183]\n"
"v_mfma_f32_16x16x32_bf16 v[184:187], a[152:155], a[112:115], v[184:187]\n"
"v_mfma_f32_16x16x32_bf16 v[188:191], a[156:159], a[112:115], v[188:191]\n"
"v_mfma_f32_16x16x32_bf16 v[192:195], a[128:131], a[116:119], v[192:195]\n"
"v_mfma_f32_16x16x32_bf16 v[196:199], a[132:135], a[116:119], v[196:199]\n"
"v_mfma_f32_16x16x32_bf16 v[200:203], a[136:139], a[116:119], v[200:203]\n"
"v_mfma_f32_16x16x32_bf16 v[204:207], a[140:143], a[116:119], v[204:207]\n"
"v_mfma_f32_16x16x32_bf16 v[208:211], a[144:147], a[116:119], v[208:211]\n"
"v_mfma_f32_16x16x32_bf16 v[212:215], a[148:151], a[116:119], v[212:215]\n"
"v_mfma_f32_16x16x32_bf16 v[216:219], a[152:155], a[116:119], v[216:219]\n"
"v_mfma_f32_16x16x32_bf16 v[220:223], a[156:159], a[116:119], v[220:223]\n"
"v_mfma_f32_16x16x32_bf16 v[224:227], a[128:131], a[120:123], v[224:227]\n"
"v_mfma_f32_16x16x32_bf16 v[228:231], a[132:135], a[120:123], v[228:231]\n"
"v_mfma_f32_16x16x32_bf16 v[232:235], a[136:139], a[120:123], v[232:235]\n"
"v_mfma_f32_16x16x32_bf16 v[236:239], a[140:143], a[120:123], v[236:239]\n"
"v_mfma_f32_16x16x32_bf16 v[240:243], a[144:147], a[120:123], v[240:243]\n"
"v_mfma_f32_16x16x32_bf16 v[244:247], a[148:151], a[120:123], v[244:247]\n"
"v_mfma_f32_16x16x32_bf16 v[248:251], a[152:155], a[120:123], v[248:251]\n"
"v_mfma_f32_16x16x32_bf16 v[252:255], a[156:159], a[120:123], v[252:255]\n"
"v_mfma_f32_16x16x32_bf16 a[0:3], a[128:131], a[124:127], a[0:3]\n"
"v_mfma_f32_16x16x32_bf16 a[4:7], a[132:135], a[124:127], a[4:7]\n"
"v_mfma_f32_16x16x32_bf16 a[8:11], a[136:139], a[124:127], a[8:11]\n"
"v_mfma_f32_16x16x32_bf16 a[12:15], a[140:143], a[124:127], a[12:15]\n"
"v_mfma_f32_16x16x32_bf16 a[16:19], a[144:147], a[124:127], a[16:19]\n"
"v_mfma_f32_16x16x32_bf16 a[20:23], a[148:151], a[124:127], a[20:23]\n"
"v_mfma_f32_16x16x32_bf16 a[24:27], a[152:155], a[124:127], a[24:27]\n"
"v_mfma_f32_16x16x32_bf16 a[28:31], a[156:159], a[124:127], a[28:31]\n"
"s_waitcnt lgkmcnt(15)\n"
"v_mfma_f32_16x16x32_bf16 v[32:35], a[64:67], a[32:35], v[32:35]\n"
"ds_read_b128 a[96:99], v17\n"
"v_mfma_f32_16x16x32_bf16 v[36:39], a[68:71], a[32:35], v[36:39]\n"
"ds_read_b64_tr_b16 a[128:129], v18 offset:16384\n"
"v_mfma_f32_16x16x32_bf16 v[40:43], a[72:75], a[32:35], v[40:43]\n"
"ds_read_b64_tr_b16 a[130:131], v18 offset:18432\n"
"v_mfma_f32_16x16x32_bf16 v[44:47], a[76:79], a[32:35], v[44:47]\n"
"ds_read_b64_tr_b16 a[132:133], v19 offset:16384\n"
"s_waitcnt lgkmcnt(15)\n"
"v_mfma_f32_16x16x32_bf16 v[48:51], a[80:83], a[32:35], v[48:51]\n"
"ds_read_b64_tr_b16 a[134:135], v19 offset:18432\n"
"v_mfma_f32_16x16x32_bf16 v[52:55], a[84:87], a[32:35], v[52:55]\n"
"ds_read_b64_tr_b16 a[136:137], v20 offset:16384\n"
"s_waitcnt lgkmcnt(15)\n"
"v_mfma_f32_16x16x32_bf16 v[56:59], a[88:91], a[32:35], v[56:59]\n"
"ds_read_b64_tr_b16 a[138:139], v20 offset:18432\n"
"s_waitcnt lgkmcnt(14)\n"
"v_mfma_f32_16x16x32_bf16 v[60:63], a[92:95], a[32:35], v[60:63]\n"
"ds_read_b64_tr_b16 a[140:141], v21 offset:16384\n"
"s_waitcnt lgkmcnt(14)\n"
"v_mfma_f32_16x16x32_bf16 v[64:67], a[64:67], a[36:39], v[64:67]\n"
"ds_read_b64_tr_b16 a[142:143], v21 offset:18432\n"
"v_mfma_f32_16x16x32_bf16 v[68:71], a[68:71], a[36:39], v[68:71]\n"
"ds_read_b64_tr_b16 a[144:145], v22 offset:16384\n"
"v_mfma_f32_16x16x32_bf16 v[72:75], a[72:75], a[36:39], v[72:75]\n"
"ds_read_b64_tr_b16 a[146:147], v22 offset:18432\n"
"v_mfma_f32_16x16x32_bf16 v[76:79], a[76:79], a[36:39], v[76:79]\n"
"ds_read_b64_tr_b16 a[148:149], v23 offset:16384\n"
"v_mfma_f32_16x16x32_bf16 v[80:83], a[80:83], a[36:39], v[80:83]\n"
"ds_read_b64_tr_b16 a[150:151], v23 offset:18432\n"
"v_mfma_f32_16x16x32_bf16 v[84:87], a[84:87], a[36:39], v[84:87]\n"
"ds_read_b64_tr_b16 a[152:153], v24 offset:16384\n"
"v_mfma_f32_16x16x32_bf16 v[88:91], a[88:91], a[36:39], v[88:91]\n"
"ds_read_b64_tr_b16 a[154:155], v24 offset:18432\n"
"v_mfma_f32_16x16x32_bf16 v[92:95], a[92:95], a[36:39], v[92:95]\n"
"ds_read_b64_tr_b16 a[156:157], v25 offset:16384\n"
"s_waitcnt lgkmcnt(15)\n"
"v_mfma_f32_16x16x32_bf16 v[96:99], a[64:67], a[40:43], v[96:99]\n"
"ds_read_b64_tr_b16 a[158:159], v25 offset:18432\n"
"v_mfma_f32_16x16x32_bf16 v[100:103], a[68:71], a[40:43], v[100:103]\n"
"ds_read_b128 a[100:103], v17 offset:2048\n"
"v_mfma_f32_16x16x32_bf16 v[104:107], a[72:75], a[40:43], v[104:107]\n"
"ds_read_b128 a[104:107], v17 offset:4096\n"
"v_mfma_f32_16x16x32_bf16 v[108:111], a[76:79], a[40:43], v[108:111]\n"
"ds_read_b128 a[108:111], v17 offset:6144\n"
"v_mfma_f32_16x16x32_bf16 v[112:115], a[80:83], a[40:43], v[112:115]\n"
"ds_read_b128 a[112:115], v17 offset:8192\n"
"v_mfma_f32_16x16x32_bf16 v[116:119], a[84:87], a[40:43], v[116:119]\n"
"ds_read_b128 a[116:119], v17 offset:10240\n"
"v_mfma_f32_16x16x32_bf16 v[120:123], a[88:91], a[40:43], v[120:123]\n"
"ds_read_b128 a[120:123], v17 offset:12288\n"
"v_mfma_f32_16x16x32_bf16 v[124:127], a[92:95], a[40:43], v[124:127]\n"
"ds_read_b128 a[124:127], v17 offset:14336\n"
"v_mfma_f32_16x16x32_bf16 v[128:131], a[64:67], a[44:47], v[128:131]\n"
"v_mfma_f32_16x16x32_bf16 v[132:135], a[68:71], a[44:47], v[132:135]\n"
"v_mfma_f32_16x16x32_bf16 v[136:139], a[72:75], a[44:47], v[136:139]\n"
"v_mfma_f32_16x16x32_bf16 v[140:143], a[76:79], a[44:47], v[140:143]\n"
"v_mfma_f32_16x16x32_bf16 v[144:147], a[80:83], a[44:47], v[144:147]\n"
"v_mfma_f32_16x16x32_bf16 v[148:151], a[84:87], a[44:47], v[148:151]\n"
"v_mfma_f32_16x16x32_bf16 v[152:155], a[88:91], a[44:47], v[152:155]\n"
"v_mfma_f32_16x16x32_bf16 v[156:159], a[92:95], a[44:47], v[156:159]\n"
"v_mfma_f32_16x16x32_bf16 v[160:163], a[64:67], a[48:51], v[160:163]\n"
"v_mfma_f32_16x16x32_bf16 v[164:167], a[68:71], a[48:51], v[164:167]\n"
"v_mfma_f32_16x16x32_bf16 v[168:171], a[72:75], a[48:51], v[168:171]\n"
"v_mfma_f32_16x16x32_bf16 v[172:175], a[76:79], a[48:51], v[172:175]\n"
"v_mfma_f32_16x16x32_bf16 v[176:179], a[80:83], a[48:51], v[176:179]\n"
"v_mfma_f32_16x16x32_bf16 v[180:183], a[84:87], a[48:51], v[180:183]\n"
"v_mfma_f32_16x16x32_bf16 v[184:187], a[88:91], a[48:51], v[184:187]\n"
"v_mfma_f32_16x16x32_bf16 v[188:191], a[92:95], a[48:51], v[188:191]\n"
"v_mfma_f32_16x16x32_bf16 v[192:195], a[64:67], a[52:55], v[192:195]\n"
"v_mfma_f32_16x16x32_bf16 v[196:199], a[68:71], a[52:55], v[196:199]\n"
"v_mfma_f32_16x16x32_bf16 v[200:203], a[72:75], a[52:55], v[200:203]\n"
"v_mfma_f32_16x16x32_bf16 v[204:207], a[76:79], a[52:55], v[204:207]\n"
"v_mfma_f32_16x16x32_bf16 v[208:211], a[80:83], a[52:55], v[208:211]\n"
"s_mov_b32 s66, s65\n"
"v_mfma_f32_16x16x32_bf16 v[212:215], a[84:87], a[52:55], v[212:215]\n"
"s_mov_b32 s67, s64\n"
"v_mfma_f32_16x16x32_bf16 v[216:219], a[88:91], a[52:55], v[216:219]\n"
"s_add_u32 s65, s64, 0x8000\n"
"v_mfma_f32_16x16x32_bf16 v[220:223], a[92:95], a[52:55], v[220:223]\n"
"s_cmp_eq_u32 s65, 0x28000\n"
"v_mfma_f32_16x16x32_bf16 v[224:227], a[64:67], a[56:59], v[224:227]\n"
"s_cselect_b32 s65, 0, s65\n"
"v_mfma_f32_16x16x32_bf16 v[228:231], a[68:71], a[56:59], v[228:231]\n"
"s_add_u32 s64, s65, 0x8000\n"
"v_mfma_f32_16x16x32_bf16 v[232:235], a[72:75], a[56:59], v[232:235]\n"
"s_cmp_eq_u32 s64, 0x28000\n"
"v_mfma_f32_16x16x32_bf16 v[236:239], a[76:79], a[56:59], v[236:239]\n"
"s_cselect_b32 s64, 0, s64\n"
"v_mfma_f32_16x16x32_bf16 v[240:243], a[80:83], a[56:59], v[240:243]\n"
"v_add_u32 v16, s64, %[lA]\n"
"v_mfma_f32_16x16x32_bf16 v[244:247], a[84:87], a[56:59], v[244:247]\n"
"v_add_u32 v18, s65, %[lB]\n"
"v_mfma_f32_16x16x32_bf16 v[248:251], a[88:91], a[56:59], v[248:251]\n"
"v_xor_b32 v17, 64, v16\n"
"v_mfma_f32_16x16x32_bf16 v[252:255], a[92:95], a[56:59], v[252:255]\n"
"v_xor_b32 v19, 32, v18\n"
"v_mfma_f32_16x16x32_bf16 a[0:3], a[64:67], a[60:63], a[0:3]\n"
"v_xor_b32 v20, 64, v18\n"
"v_mfma_f32_16x16x32_bf16 a[4:7], a[68:71], a[60:63], a[4:7]\n"
"v_xor_b32 v21, 0x60, v18\n"
"v_mfma_f32_16x16x32_bf16 a[8:11], a[72:75], a[60:63], a[8:11]\n"
"v_xor_b32 v22, 0x80, v18\n"
"v_mfma_f32_16x16x32_bf16 a[12:15], a[76:79], a[60:63], a[12:15]\n"
"v_xor_b32 v23, 0xa0, v18\n"
"v_mfma_f32_16x16x32_bf16 a[16:19], a[80:83], a[60:63], a[16:19]\n"
"v_xor_b32 v24, 0xc0, v18\n"
"v_mfma_f32_16x16x32_bf16 a[20:23], a[84:87], a[60:63], a[20:23]\n"
"v_xor_b32 v25, 0xe0, v18\n"
"v_mfma_f32_16x16x32_bf16 a[24:27], a[88:91], a[60:63], a[24:27]\n"
"v_mfma_f32_16x16x32_bf16 a[28:31], a[92:95], a[60:63], a[28:31]\n"
"s_waitcnt lgkmcnt(0)\n"
"s_barrier\n"
"v_mbcnt_lo_u32_b32 v16, -1, 0\n"
"v_mbcnt_hi_u32_b32 v16, -1, v16\n"
"v_lshlrev_b32 v17, 4, v16\n"
"s_waitcnt vmcnt(0)\n"
"v_cmp_ne_u64 vcc, %[tag], v[26:27]\n"
"s_cbranch_vccnz LNWk%=\n"
"LNGk%=:\n"
"v_lshrrev_b32 v21, 4, v16\n"
"v_and_b32 v20, 15, v16\n"
"v_lshlrev_b32 v19, 2, v21\n"
"v_xor_b32 v20, v20, v19\n"
"v_lshlrev_b32 v20, 4, v20\n"
"v_mul_u32_u24 v21, 0xe00, v21\n"
"v_add_u32 v18, v20, v21\n"
"v_lshrrev_b32 v21, 5, v16\n"
"v_and_b32 v20, 31, v16\n"
"v_lshlrev_b32 v23, 2, v21\n"
"v_xor_b32 v20, v20, v23\n"
"v_lshlrev_b32 v20, 4, v20\n"
"v_lshl_add_u32 v22, v21, 13, v20\n"
"v_xor_b32 v23, 0x80, v22\n"
"v_mfma_f32_16x16x32_bf16 v[32:35], a[128:131], a[96:99], v[32:35]\n"
"s_cmp_lg_u32 %[help], 0\n"
"s_cselect_b32 s72, 2, 6\n"
"s_lshl_b32 s72, s72, 14\n"
"s_add_u32 m0, s74, s72\n"
"s_add_u32 s75, %[p0], 0\n"
"buffer_load_dwordx4 v18, %[rsP], s75 offen lds\n"
"v_mfma_f32_16x16x32_bf16 v[36:39], a[132:135], a[96:99], v[36:39]\n"
"v_mfma_f32_16x16x32_bf16 v[64:67], a[128:131], a[100:103], v[64:67]\n"
"s_add_u32 s75, %[p0], 0x3400\n"
"buffer_load_dwordx4 v18, %[rsP], s75 offen offset:1024 lds\n"
"v_mfma_f32_16x16x32_bf16 v[68:71], a[132:135], a[100:103], v[68:71]\n"
"v_mfma_f32_16x16x32_bf16 v[96:99], a[128:131], a[104:107], v[96:99]\n"
"s_add_u32 s75, %[p0], 0x6800\n"
"buffer_load_dwordx4 v18, %[rsP], s75 offen offset:2048 lds\n"
"v_mfma_f32_16x16x32_bf16 v[100:103], a[132:135], a[104:107], v[100:103]\n"
"v_mfma_f32_16x16x32_bf16 v[128:131], a[128:131], a[108:111], v[128:131]\n"
"s_add_u32 s75, %[p0], 0x9c00\n"
"buffer_load_dwordx4 v18, %[rsP], s75 offen offset:3072 lds\n"
"v_mfma_f32_16x16x32_bf16 v[132:135], a[132:135], a[108:111], v[132:135]\n"
"v_mfma_f32_16x16x32_bf16 v[160:163], a[128:131], a[112:115], v[160:163]\n"
"s_add_u32 s75, %[xoff], 0\n"
"buffer_load_dwordx4 a[64:67], v17, %[rsW], s75 offen sc1\n"
"v_mfma_f32_16x16x32_bf16 v[164:167], a[132:135], a[112:115], v[164:167]\n"
"v_mul_f32 v32, s73, v32\n"
"v_mul_f32 v33, s73, v33\n"
"v_mfma_f32_16x16x32_bf16 v[192:195], a[128:131], a[116:119], v[192:195]\n"
"buffer_load_dwordx4 a[68:71], v17, %[rsW], s75 offen offset:1024 sc1\n"
"v_mul_f32 v34, s73, v34\n"
"v_mfma_f32_16x16x32_bf16 v[196:199], a[132:135], a[116:119], v[196:199]\n"
"v_mul_f32 v35, s73, v35\n"
"v_mul_f32 v36, s73, v36\n"
"v_mfma_f32_16x16x32_bf16 v[224:227], a[128:131], a[120:123], v[224:227]\n"
"buffer_load_dwordx4 a[72:75], v17, %[rsW], s75 offen offset:2048 sc1\n"
"v_mul_f32 v37, s73, v37\n"
"v_mfma_f32_16x16x32_bf16 v[228:231], a[132:135], a[120:123], v[228:231]\n"
"v_mul_f32 v38, s73, v38\n"
"v_mul_f32 v39, s73, v39\n"
"v_mfma_f32_16x16x32_bf16 a[0:3], a[128:131], a[124:127], a[0:3]\n"
"buffer_load_dwordx4 a[76:79], v17, %[rsW], s75 offen offset:3072 sc1\n"
"v_cvt_pk_bf16_f32 v32, v32, v33\n"
"v_mfma_f32_16x16x32_bf16 a[4:7], a[132:135], a[124:127], a[4:7]\n"
"v_cvt_pk_bf16_f32 v33, v34, v35\n"
"v_cvt_pk_bf16_f32 v34, v36, v37\n"
"v_mfma_f32_16x16x32_bf16 v[40:43], a[136:139], a[96:99], v[40:43]\n"
"s_add_u32 s75, %[xoff], 0x1000\n"
"buffer_load_dwordx4 a[80:83], v17, %[rsW], s75 offen sc1\n"
"v_cvt_pk_bf16_f32 v35, v38, v39\n"
"v_mfma_f32_16x16x32_bf16 v[44:47], a[140:143], a[96:99], v[44:47]\n"
"v_mul_f32 v64, s73, v64\n"
"v_mul_f32 v65, s73, v65\n"
"v_mfma_f32_16x16x32_bf16 v[72:75], a[136:139], a[100:103], v[72:75]\n"
"buffer_load_dwordx4 a[84:87], v17, %[rsW], s75 offen offset:1024 sc1\n"
"v_mul_f32 v66, s73, v66\n"
"v_mfma_f32_16x16x32_bf16 v[76:79], a[140:143], a[100:103], v[76:79]\n"
"v_mul_f32 v67, s73, v67\n"
"v_mul_f32 v68, s73, v68\n"
"v_mfma_f32_16x16x32_bf16 v[104:107], a[136:139], a[104:107], v[104:107]\n"
"buffer_load_dwordx4 a[88:91], v17, %[rsW], s75 offen offset:2048 sc1\n"
"v_mul_f32 v69, s73, v69\n"
"v_mfma_f32_16x16x32_bf16 v[108:111], a[140:143], a[104:107], v[108:111]\n"
"v_mul_f32 v70, s73, v70\n"
"v_mul_f32 v71, s73, v71\n"
"v_mfma_f32_16x16x32_bf16 v[136:139], a[136:139], a[108:111], v[136:139]\n"
"buffer_load_dwordx4 a[92:95], v17, %[rsW], s75 offen offset:3072 sc1\n"
"v_cvt_pk_bf16_f32 v64, v64, v65\n"
"v_mfma_f32_16x16x32_bf16 v[140:143], a[140:143], a[108:111], v[140:143]\n"
"v_cvt_pk_bf16_f32 v65, v66, v67\n"
"v_cvt_pk_bf16_f32 v66, v68, v69\n"
"v_mfma_f32_16x16x32_bf16 v[168:171], a[136:139], a[112:115], v[168:171]\n"
"s_cmp_lg_u32 %[help], 0\n"
"s_cselect_b32 s72, 3, 7\n"
"s_lshl_b32 s72, s72, 14\n"
"s_add_u32 m0, s74, s72\n"
"s_add_u32 s75, %[p0], 0x38000\n"
"buffer_load_dwordx4 v18, %[rsP], s75 offen lds\n"
"v_cvt_pk_bf16_f32 v67, v70, v71\n"
"v_mfma_f32_16x16x32_bf16 v[172:175], a[140:143], a[112:115], v[172:175]\n"
"v_mul_f32 v96, s73, v96\n"
"v_mul_f32 v97, s73, v97\n"
"v_mfma_f32_16x16x32_bf16 v[200:203], a[136:139], a[116:119], v[200:203]\n"
"s_add_u32 s75, %[p0], 0x3b400\n"
"buffer_load_dwordx4 v18, %[rsP], s75 offen offset:1024 lds\n"
"v_mul_f32 v98, s73, v98\n"
"v_mfma_f32_16x16x32_bf16 v[204:207], a[140:143], a[116:119], v[204:207]\n"
"v_mul_f32 v99, s73, v99\n"
"v_mul_f32 v100, s73, v100\n"
"v_mfma_f32_16x16x32_bf16 v[232:235], a[136:139], a[120:123], v[232:235]\n"
"s_add_u32 s75, %[p0], 0x3e800\n"
"buffer_load_dwordx4 v18, %[rsP], s75 offen offset:2048 lds\n"
"v_mul_f32 v101, s73, v101\n"
"v_mfma_f32_16x16x32_bf16 v[236:239], a[140:143], a[120:123], v[236:239]\n"
"v_mul_f32 v102, s73, v102\n"
"v_mul_f32 v103, s73, v103\n"
"v_mfma_f32_16x16x32_bf16 a[8:11], a[136:139], a[124:127], a[8:11]\n"
"s_add_u32 s75, %[p0], 0x41c00\n"
"buffer_load_dwordx4 v18, %[rsP], s75 offen offset:3072 lds\n"
"v_cvt_pk_bf16_f32 v96, v96, v97\n"
"v_mfma_f32_16x16x32_bf16 a[12:15], a[140:143], a[124:127], a[12:15]\n"
"v_cvt_pk_bf16_f32 v97, v98, v99\n"
"v_cvt_pk_bf16_f32 v98, v100, v101\n"
"v_mfma_f32_16x16x32_bf16 v[48:51], a[144:147], a[96:99], v[48:51]\n"
"s_add_u32 s75, %[xoff], 0x6000\n"
"buffer_load_dwordx4 a[160:163], v17, %[rsW], s75 offen sc1\n"
"v_cvt_pk_bf16_f32 v99, v102, v103\n"
"v_mfma_f32_16x16x32_bf16 v[52:55], a[148:151], a[96:99], v[52:55]\n"
"v_mul_f32 v128, s73, v128\n"
"v_mul_f32 v129, s73, v129\n"
"v_mfma_f32_16x16x32_bf16 v[80:83], a[144:147], a[100:103], v[80:83]\n"
"buffer_load_dwordx4 a[164:167], v17, %[rsW], s75 offen offset:1024 sc1\n"
"v_mul_f32 v130, s73, v130\n"
"v_mfma_f32_16x16x32_bf16 v[84:87], a[148:151], a[100:103], v[84:87]\n"
"v_mul_f32 v131, s73, v131\n"
"v_mul_f32 v132, s73, v132\n"
"v_mfma_f32_16x16x32_bf16 v[112:115], a[144:147], a[104:107], v[112:115]\n"
"buffer_load_dwordx4 a[168:171], v17, %[rsW], s75 offen offset:2048 sc1\n"
"v_mul_f32 v133, s73, v133\n"
"v_mfma_f32_16x16x32_bf16 v[116:119], a[148:151], a[104:107], v[116:119]\n"
"v_mul_f32 v134, s73, v134\n"
"v_mul_f32 v135, s73, v135\n"
"v_mfma_f32_16x16x32_bf16 v[144:147], a[144:147], a[108:111], v[144:147]\n"
"buffer_load_dwordx4 a[172:175], v17, %[rsW], s75 offen offset:3072 sc1\n"
"v_cvt_pk_bf16_f32 v128, v128, v129\n"
"v_mfma_f32_16x16x32_bf16 v[148:151], a[148:151], a[108:111], v[148:151]\n"
"v_cvt_pk_bf16_f32 v129, v130, v131\n"
"v_cvt_pk_bf16_f32 v130, v132, v133\n"
"v_mfma_f32_16x16x32_bf16 v[176:179], a[144:147], a[112:115], v[176:179]\n"
"s_cmp_lg_u32 %[help], 0\n"
"s_cselect_b32 s72, 4, 8\n"
"s_lshl_b32 s72, s72, 14\n"
"s_add_u32 m0, s74, s72\n"
"s_add_u32 s75, %[p0], 0x70000\n"
"buffer_load_dwordx4 v18, %[rsP], s75 offen lds\n"
"v_cvt_pk_bf16_f32 v131, v134, v135\n"
"v_mfma_f32_16x16x32_bf16 v[180:183], a[148:151], a[112:115], v[180:183]\n"
"v_mul_f32 v160, s73, v160\n"
"v_mul_f32 v161, s73, v161\n"
"v_mfma_f32_16x16x32_bf16 v[208:211], a[144:147], a[116:119], v[208:211]\n"
"s_add_u32 s75, %[p0], 0x73400\n"
"buffer_load_dwordx4 v18, %[rsP], s75 offen offset:1024 lds\n"
"v_mul_f32 v162, s73, v162\n"
"v_mfma_f32_16x16x32_bf16 v[212:215], a[148:151], a[116:119], v[212:215]\n"
"v_mul_f32 v163, s73, v163\n"
"v_mul_f32 v164, s73, v164\n"
"v_mfma_f32_16x16x32_bf16 v[240:243], a[144:147], a[120:123], v[240:243]\n"
"s_add_u32 s75, %[p0], 0x76800\n"
"buffer_load_dwordx4 v18, %[rsP], s75 offen offset:2048 lds\n"
"v_mul_f32 v165, s73, v165\n"
"v_mfma_f32_16x16x32_bf16 v[244:247], a[148:151], a[120:123], v[244:247]\n"
"v_mul_f32 v166, s73, v166\n"
"v_mul_f32 v167, s73, v167\n"
"v_mfma_f32_16x16x32_bf16 a[16:19], a[144:147], a[124:127], a[16:19]\n"
"s_add_u32 s75, %[p0], 0x79c00\n"
"buffer_load_dwordx4 v18, %[rsP], s75 offen offset:3072 lds\n"
"v_cvt_pk_bf16_f32 v160, v160, v161\n"
"v_mfma_f32_16x16x32_bf16 a[20:23], a[148:151], a[124:127], a[20:23]\n"
"v_cvt_pk_bf16_f32 v161, v162, v163\n"
"v_cvt_pk_bf16_f32 v162, v164, v165\n"
"v_mfma_f32_16x16x32_bf16 v[56:59], a[152:155], a[96:99], v[56:59]\n"
"s_cmp_lg_u32 %[help], 0\n"
"s_cselect_b32 s72, 5, 9\n"
"s_lshl_b32 s72, s72, 14\n"
"s_add_u32 m0, s74, s72\n"
"s_add_u32 s75, %[p0], 0xa8000\n"
"buffer_load_dwordx4 v18, %[rsP], s75 offen lds\n"
"v_cvt_pk_bf16_f32 v163, v166, v167\n"
"v_mfma_f32_16x16x32_bf16 v[60:63], a[156:159], a[96:99], v[60:63]\n"
"v_mul_f32 v192, s73, v192\n"
"v_mul_f32 v193, s73, v193\n"
"v_mfma_f32_16x16x32_bf16 v[88:91], a[152:155], a[100:103], v[88:91]\n"
"s_add_u32 s75, %[p0], 0xab400\n"
"buffer_load_dwordx4 v18, %[rsP], s75 offen offset:1024 lds\n"
"v_mul_f32 v194, s73, v194\n"
"v_mfma_f32_16x16x32_bf16 v[92:95], a[156:159], a[100:103], v[92:95]\n"
"v_mul_f32 v195, s73, v195\n"
"v_mul_f32 v196, s73, v196\n"
"v_mfma_f32_16x16x32_bf16 v[120:123], a[152:155], a[104:107], v[120:123]\n"
"s_add_u32 s75, %[p0], 0xae800\n"
"buffer_load_dwordx4 v18, %[rsP], s75 offen offset:2048 lds\n"
"v_mul_f32 v197, s73, v197\n"
"v_mfma_f32_16x16x32_bf16 v[124:127], a[156:159], a[104:107], v[124:127]\n"
"v_mul_f32 v198, s73, v198\n"
"v_mul_f32 v199, s73, v199\n"
"v_mfma_f32_16x16x32_bf16 v[152:155], a[152:155], a[108:111], v[152:155]\n"
"s_add_u32 s75, %[p0], 0xb1c00\n"
"buffer_load_dwordx4 v18, %[rsP], s75 offen offset:3072 lds\n"
"v_cvt_pk_bf16_f32 v192, v192, v193\n"
"v_mfma_f32_16x16x32_bf16 v[156:159], a[156:159], a[108:111], v[156:159]\n"
"v_cvt_pk_bf16_f32 v193, v194, v195\n"
"v_cvt_pk_bf16_f32 v194, v196, v197\n"
"v_mfma_f32_16x16x32_bf16 v[184:187], a[152:155], a[112:115], v[184:187]\n"
"s_cmp_lg_u32 %[help], 0\n"
"s_cselect_b32 s72, 6, 0\n"
"s_lshl_b32 s72, s72, 14\n"
"s_add_u32 m0, s74, s72\n"
"s_add_u32 s75, %[p0], 0xe0000\n"
"buffer_load_dwordx4 v18, %[rsP], s75 offen lds\n"
"v_cvt_pk_bf16_f32 v195, v198, v199\n"
"v_mfma_f32_16x16x32_bf16 v[188:191], a[156:159], a[112:115], v[188:191]\n"
"v_pk_mul_f32 v[224:225], v[224:225], v[30:31]\n"
"v_pk_mul_f32 v[226:227], v[226:227], v[30:31]\n"
"v_mfma_f32_16x16x32_bf16 v[216:219], a[152:155], a[116:119], v[216:219]\n"
"s_add_u32 s75, %[p0], 0xe3400\n"
"buffer_load_dwordx4 v18, %[rsP], s75 offen offset:1024 lds\n"
"v_pk_mul_f32 v[228:229], v[228:229], v[30:31]\n"
"v_mfma_f32_16x16x32_bf16 v[220:223], a[156:159], a[116:119], v[220:223]\n"
"v_pk_mul_f32 v[230:231], v[230:231], v[30:31]\n"
"v_cvt_pk_bf16_f32 v224, v224, v225\n"
"v_mfma_f32_16x16x32_bf16 v[248:251], a[152:155], a[120:123], v[248:251]\n"
"s_add_u32 s75, %[p0], 0xe6800\n"
"buffer_load_dwordx4 v18, %[rsP], s75 offen offset:2048 lds\n"
"v_cvt_pk_bf16_f32 v225, v226, v227\n"
"v_mfma_f32_16x16x32_bf16 v[252:255], a[156:159], a[120:123], v[252:255]\n"
"v_cvt_pk_bf16_f32 v226, v228, v229\n"
"v_cvt_pk_bf16_f32 v227, v230, v231\n"
"v_mfma_f32_16x16x32_bf16 a[24:27], a[152:155], a[124:127], a[24:27]\n"
"s_add_u32 s75, %[p0], 0xe9c00\n"
"buffer_load_dwordx4 v18, %[rsP], s75 offen offset:3072 lds\n"
"v_accvgpr_read_b32 v36, a4\n"
"v_mfma_f32_16x16x32_bf16 a[28:31], a[156:159], a[124:127], a[28:31]\n"
"v_accvgpr_read_b32 v37, a5\n"
"v_accvgpr_read_b32 v38, a6\n"
"s_cmp_lg_u32 %[help], 0\n"
"s_cselect_b32 s72, 7, 1\n"
"s_lshl_b32 s72, s72, 14\n"
"s_add_u32 m0, s74, s72\n"
"s_add_u32 s75, %[p0], 0x118000\n"
"buffer_load_dwordx4 v18, %[rsP], s75 offen lds\n"
"v_accvgpr_read_b32 v39, a7\n"
"v_accvgpr_read_b32 v68, a0\n"
"v_accvgpr_read_b32 v69, a1\n"
"v_accvgpr_read_b32 v70, a2\n"
"v_accvgpr_read_b32 v71, a3\n"
"v_pk_mul_f32 v[68:69], v[68:69], v[30:31]\n"
"v_pk_mul_f32 v[70:71], v[70:71], v[30:31]\n"
"v_pk_mul_f32 v[36:37], v[36:37], v[30:31]\n"
"v_pk_mul_f32 v[38:39], v[38:39], v[30:31]\n"
"s_add_u32 s75, %[p0], 0x11b400\n"
"buffer_load_dwordx4 v18, %[rsP], s75 offen offset:1024 lds\n"
"v_cvt_pk_bf16_f32 v68, v68, v69\n"
"v_cvt_pk_bf16_f32 v69, v70, v71\n"
"v_cvt_pk_bf16_f32 v70, v36, v37\n"
"v_cvt_pk_bf16_f32 v71, v38, v39\n"
"v_pk_mul_f32 v[40:41], v[40:41], v[30:31]\n"
"v_pk_mul_f32 v[42:43], v[42:43], v[30:31]\n"
"v_pk_mul_f32 v[44:45], v[44:45], v[30:31]\n"
"v_pk_mul_f32 v[46:47], v[46:47], v[30:31]\n"
"v_cvt_pk_bf16_f32 v40, v40, v41\n"
"s_add_u32 s75, %[p0], 0x11e800\n"
"buffer_load_dwordx4 v18, %[rsP], s75 offen offset:2048 lds\n"
"v_cvt_pk_bf16_f32 v41, v42, v43\n"
"v_cvt_pk_bf16_f32 v42, v44, v45\n"
"v_cvt_pk_bf16_f32 v43, v46, v47\n"
"v_accvgpr_write_b32 a176, v40\n"
"v_accvgpr_write_b32 a177, v41\n"
"v_accvgpr_write_b32 a178, v42\n"
"v_accvgpr_write_b32 a179, v43\n"
"v_pk_mul_f32 v[72:73], v[72:73], v[30:31]\n"
"v_pk_mul_f32 v[74:75], v[74:75], v[30:31]\n"
"s_add_u32 s75, %[p0], 0x121c00\n"
"buffer_load_dwordx4 v18, %[rsP], s75 offen offset:3072 lds\n"
"v_pk_mul_f32 v[76:77], v[76:77], v[30:31]\n"
"v_pk_mul_f32 v[78:79], v[78:79], v[30:31]\n"
"v_cvt_pk_bf16_f32 v72, v72, v73\n"
"v_cvt_pk_bf16_f32 v73, v74, v75\n"
"v_cvt_pk_bf16_f32 v74, v76, v77\n"
"v_cvt_pk_bf16_f32 v75, v78, v79\n"
"v_accvgpr_write_b32 a192, v72\n"
"v_accvgpr_write_b32 a193, v73\n"
"v_accvgpr_write_b32 a194, v74\n"
"s_cmp_lg_u32 %[help], 0\n"
"s_cselect_b32 s72, 8, 2\n"
"s_lshl_b32 s72, s72, 14\n"
"s_add_u32 m0, s74, s72\n"
"s_add_u32 s75, %[p0], 0x150000\n"
"buffer_load_dwordx4 v18, %[rsP], s75 offen lds\n"
"v_accvgpr_write_b32 a195, v75\n"
"v_pk_mul_f32 v[104:105], v[104:105], v[30:31]\n"
"v_pk_mul_f32 v[106:107], v[106:107], v[30:31]\n"
"v_pk_mul_f32 v[108:109], v[108:109], v[30:31]\n"
"v_pk_mul_f32 v[110:111], v[110:111], v[30:31]\n"
"v_cvt_pk_bf16_f32 v104, v104, v105\n"
"v_cvt_pk_bf16_f32 v105, v106, v107\n"
"v_cvt_pk_bf16_f32 v106, v108, v109\n"
"v_cvt_pk_bf16_f32 v107, v110, v111\n"
"s_add_u32 s75, %[p0], 0x153400\n"
"buffer_load_dwordx4 v18, %[rsP], s75 offen offset:1024 lds\n"
"v_accvgpr_write_b32 a180, v104\n"
"v_accvgpr_write_b32 a181, v105\n"
"v_accvgpr_write_b32 a182, v106\n"
"v_accvgpr_write_b32 a183, v107\n"
"v_pk_mul_f32 v[136:137], v[136:137], v[30:31]\n"
"v_pk_mul_f32 v[138:139], v[138:139], v[30:31]\n"
"v_pk_mul_f32 v[140:141], v[140:141], v[30:31]\n"
"v_pk_mul_f32 v[142:143], v[142:143], v[30:31]\n"
"v_cvt_pk_bf16_f32 v136, v136, v137\n"
"s_add_u32 s75, %[p0], 0x156800\n"
"buffer_load_dwordx4 v18, %[rsP], s75 offen offset:2048 lds\n"
"v_cvt_pk_bf16_f32 v137, v138, v139\n"
"v_cvt_pk_bf16_f32 v138, v140, v141\n"
"v_cvt_pk_bf16_f32 v139, v142, v143\n"
"v_accvgpr_write_b32 a196, v136\n"
"v_accvgpr_write_b32 a197, v137\n"
"v_accvgpr_write_b32 a198, v138\n"
"v_accvgpr_write_b32 a199, v139\n"
"v_pk_mul_f32 v[168:169], v[168:169], v[30:31]\n"
"v_pk_mul_f32 v[170:171], v[170:171], v[30:31]\n"
"s_add_u32 s75, %[p0], 0x159c00\n"
"buffer_load_dwordx4 v18, %[rsP], s75 offen offset:3072 lds\n"
"v_pk_mul_f32 v[172:173], v[172:173], v[30:31]\n"
"v_pk_mul_f32 v[174:175], v[174:175], v[30:31]\n"
"v_cvt_pk_bf16_f32 v168, v168, v169\n"
"v_cvt_pk_bf16_f32 v169, v170, v171\n"
"v_cvt_pk_bf16_f32 v170, v172, v173\n"
"v_cvt_pk_bf16_f32 v171, v174, v175\n"
"v_accvgpr_write_b32 a184, v168\n"
"v_accvgpr_write_b32 a185, v169\n"
"v_accvgpr_write_b32 a186, v170\n"
"s_cmp_lg_u32 %[help], 0\n"
"s_cselect_b32 s72, 0, 4\n"
"s_lshl_b32 s72, s72, 14\n"
"s_add_u32 m0, %[wa], s72\n"
"s_add_u32 s75, %[q0], 0\n"
"buffer_load_dwordx4 v22, %[rsQ], s75 offen lds\n"
"v_accvgpr_write_b32 a187, v171\n"
"v_pk_mul_f32 v[200:201], v[200:201], v[30:31]\n"
"v_pk_mul_f32 v[202:203], v[202:203], v[30:31]\n"
"v_pk_mul_f32 v[204:205], v[204:205], v[30:31]\n"
"v_pk_mul_f32 v[206:207], v[206:207], v[30:31]\n"
"v_cvt_pk_bf16_f32 v200, v200, v201\n"
"v_cvt_pk_bf16_f32 v201, v202, v203\n"
"v_cvt_pk_bf16_f32 v202, v204, v205\n"
"v_cvt_pk_bf16_f32 v203, v206, v207\n"
"s_add_u32 s75, %[q0], 0x3c00\n"
"buffer_load_dwordx4 v23, %[rsQ], s75 offen offset:1024 lds\n"
"v_accvgpr_write_b32 a200, v200\n"
"v_accvgpr_write_b32 a201, v201\n"
"v_accvgpr_write_b32 a202, v202\n"
"v_accvgpr_write_b32 a203, v203\n"
"v_pk_mul_f32 v[232:233], v[232:233], v[30:31]\n"
"v_pk_mul_f32 v[234:235], v[234:235], v[30:31]\n"
"v_pk_mul_f32 v[236:237], v[236:237], v[30:31]\n"
"v_pk_mul_f32 v[238:239], v[238:239], v[30:31]\n"
"v_cvt_pk_bf16_f32 v232, v232, v233\n"
"s_add_u32 s75, %[q0], 0x7800\n"
"buffer_load_dwordx4 v22, %[rsQ], s75 offen offset:2048 lds\n"
"v_cvt_pk_bf16_f32 v233, v234, v235\n"
"v_cvt_pk_bf16_f32 v234, v236, v237\n"
"v_cvt_pk_bf16_f32 v235, v238, v239\n"
"v_accvgpr_write_b32 a188, v232\n"
"v_accvgpr_write_b32 a189, v233\n"
"v_accvgpr_write_b32 a190, v234\n"
"v_accvgpr_write_b32 a191, v235\n"
"v_accvgpr_read_b32 v100, a12\n"
"v_accvgpr_read_b32 v101, a13\n"
"s_add_u32 s75, %[q0], 0xb400\n"
"buffer_load_dwordx4 v23, %[rsQ], s75 offen offset:3072 lds\n"
"v_accvgpr_read_b32 v102, a14\n"
"v_accvgpr_read_b32 v103, a15\n"
"v_accvgpr_read_b32 v132, a8\n"
"v_accvgpr_read_b32 v133, a9\n"
"v_accvgpr_read_b32 v134, a10\n"
"v_accvgpr_read_b32 v135, a11\n"
"v_pk_mul_f32 v[132:133], v[132:133], v[30:31]\n"
"v_pk_mul_f32 v[134:135], v[134:135], v[30:31]\n"
"v_pk_mul_f32 v[100:101], v[100:101], v[30:31]\n"
"s_add_u32 m0, m0, 0x1000\n"
"s_add_u32 s75, %[q0], 0x10000\n"
"buffer_load_dwordx4 v22, %[rsQ], s75 offen lds\n"
"v_pk_mul_f32 v[102:103], v[102:103], v[30:31]\n"
"v_cvt_pk_bf16_f32 v132, v132, v133\n"
"v_cvt_pk_bf16_f32 v133, v134, v135\n"
"v_cvt_pk_bf16_f32 v134, v100, v101\n"
"v_cvt_pk_bf16_f32 v135, v102, v103\n"
"v_accvgpr_write_b32 a204, v132\n"
"v_accvgpr_write_b32 a205, v133\n"
"v_accvgpr_write_b32 a206, v134\n"
"v_accvgpr_write_b32 a207, v135\n"
"s_add_u32 s75, %[q0], 0x13c00\n"
"buffer_load_dwordx4 v23, %[rsQ], s75 offen offset:1024 lds\n"
"v_pk_mul_f32 v[48:49], v[48:49], v[30:31]\n"
"v_pk_mul_f32 v[50:51], v[50:51], v[30:31]\n"
"v_pk_mul_f32 v[52:53], v[52:53], v[30:31]\n"
"v_pk_mul_f32 v[54:55], v[54:55], v[30:31]\n"
"v_cvt_pk_bf16_f32 v48, v48, v49\n"
"v_cvt_pk_bf16_f32 v49, v50, v51\n"
"v_cvt_pk_bf16_f32 v50, v52, v53\n"
"v_cvt_pk_bf16_f32 v51, v54, v55\n"
"v_accvgpr_write_b32 a208, v48\n"
"s_add_u32 s75, %[q0], 0x17800\n"
"buffer_load_dwordx4 v22, %[rsQ], s75 offen offset:2048 lds\n"
"v_accvgpr_write_b32 a209, v49\n"
"v_accvgpr_write_b32 a210, v50\n"
"v_accvgpr_write_b32 a211, v51\n"
"v_pk_mul_f32 v[80:81], v[80:81], v[30:31]\n"
"v_pk_mul_f32 v[82:83], v[82:83], v[30:31]\n"
"v_pk_mul_f32 v[84:85], v[84:85], v[30:31]\n"
"v_pk_mul_f32 v[86:87], v[86:87], v[30:31]\n"
"v_cvt_pk_bf16_f32 v80, v80, v81\n"
"v_cvt_pk_bf16_f32 v81, v82, v83\n"
"s_add_u32 s75, %[q0], 0x1b400\n"
"buffer_load_dwordx4 v23, %[rsQ], s75 offen offset:3072 lds\n"
"v_cvt_pk_bf16_f32 v82, v84, v85\n"
"v_cvt_pk_bf16_f32 v83, v86, v87\n"
"v_accvgpr_write_b32 a224, v80\n"
"v_accvgpr_write_b32 a225, v81\n"
"v_accvgpr_write_b32 a226, v82\n"
"v_accvgpr_write_b32 a227, v83\n"
"v_pk_mul_f32 v[112:113], v[112:113], v[30:31]\n"
"v_pk_mul_f32 v[114:115], v[114:115], v[30:31]\n"
"v_pk_mul_f32 v[116:117], v[116:117], v[30:31]\n"
"s_add_u32 s75, %[xoff], 0x2000\n"
"buffer_load_dwordx4 a[96:99], v17, %[rsW], s75 offen sc1\n"
"v_pk_mul_f32 v[118:119], v[118:119], v[30:31]\n"
"v_cvt_pk_bf16_f32 v112, v112, v113\n"
"v_cvt_pk_bf16_f32 v113, v114, v115\n"
"v_cvt_pk_bf16_f32 v114, v116, v117\n"
"v_cvt_pk_bf16_f32 v115, v118, v119\n"
"v_accvgpr_write_b32 a212, v112\n"
"v_accvgpr_write_b32 a213, v113\n"
"v_accvgpr_write_b32 a214, v114\n"
"v_accvgpr_write_b32 a215, v115\n"
"buffer_load_dwordx4 a[100:103], v17, %[rsW], s75 offen offset:1024 sc1\n"
"v_pk_mul_f32 v[144:145], v[144:145], v[30:31]\n"
"v_pk_mul_f32 v[146:147], v[146:147], v[30:31]\n"
"v_pk_mul_f32 v[148:149], v[148:149], v[30:31]\n"
"v_pk_mul_f32 v[150:151], v[150:151], v[30:31]\n"
"v_cvt_pk_bf16_f32 v144, v144, v145\n"
"v_cvt_pk_bf16_f32 v145, v146, v147\n"
"v_cvt_pk_bf16_f32 v146, v148, v149\n"
"v_cvt_pk_bf16_f32 v147, v150, v151\n"
"v_accvgpr_write_b32 a228, v144\n"
"buffer_load_dwordx4 a[104:107], v17, %[rsW], s75 offen offset:2048 sc1\n"
"v_accvgpr_write_b32 a229, v145\n"
"v_accvgpr_write_b32 a230, v146\n"
"v_accvgpr_write_b32 a231, v147\n"
"v_pk_mul_f32 v[176:177], v[176:177], v[30:31]\n"
"v_pk_mul_f32 v[178:179], v[178:179], v[30:31]\n"
"v_pk_mul_f32 v[180:181], v[180:181], v[30:31]\n"
"v_pk_mul_f32 v[182:183], v[182:183], v[30:31]\n"
"v_cvt_pk_bf16_f32 v176, v176, v177\n"
"v_cvt_pk_bf16_f32 v177, v178, v179\n"
"buffer_load_dwordx4 a[108:111], v17, %[rsW], s75 offen offset:3072 sc1\n"
"v_cvt_pk_bf16_f32 v178, v180, v181\n"
"v_cvt_pk_bf16_f32 v179, v182, v183\n"
"v_accvgpr_write_b32 a216, v176\n"
"v_accvgpr_write_b32 a217, v177\n"
"v_accvgpr_write_b32 a218, v178\n"
"v_accvgpr_write_b32 a219, v179\n"
"v_pk_mul_f32 v[208:209], v[208:209], v[30:31]\n"
"v_pk_mul_f32 v[210:211], v[210:211], v[30:31]\n"
"v_pk_mul_f32 v[212:213], v[212:213], v[30:31]\n"
"s_add_u32 s75, %[xoff], 0x3000\n"
"buffer_load_dwordx4 a[112:115], v17, %[rsW], s75 offen sc1\n"
"v_pk_mul_f32 v[214:215], v[214:215], v[30:31]\n"
"v_cvt_pk_bf16_f32 v208, v208, v209\n"
"v_cvt_pk_bf16_f32 v209, v210, v211\n"
"v_cvt_pk_bf16_f32 v210, v212, v213\n"
"v_cvt_pk_bf16_f32 v211, v214, v215\n"
"v_accvgpr_write_b32 a232, v208\n"
"v_accvgpr_write_b32 a233, v209\n"
"v_accvgpr_write_b32 a234, v210\n"
"v_accvgpr_write_b32 a235, v211\n"
"buffer_load_dwordx4 a[116:119], v17, %[rsW], s75 offen offset:1024 sc1\n"
"v_pk_mul_f32 v[240:241], v[240:241], v[30:31]\n"
"v_pk_mul_f32 v[242:243], v[242:243], v[30:31]\n"
"v_pk_mul_f32 v[244:245], v[244:245], v[30:31]\n"
"v_pk_mul_f32 v[246:247], v[246:247], v[30:31]\n"
"v_cvt_pk_bf16_f32 v240, v240, v241\n"
"v_cvt_pk_bf16_f32 v241, v242, v243\n"
"v_cvt_pk_bf16_f32 v242, v244, v245\n"
"v_cvt_pk_bf16_f32 v243, v246, v247\n"
"v_accvgpr_write_b32 a220, v240\n"
"buffer_load_dwordx4 a[120:123], v17, %[rsW], s75 offen offset:2048 sc1\n"
"v_accvgpr_write_b32 a221, v241\n"
"v_accvgpr_write_b32 a222, v242\n"
"v_accvgpr_write_b32 a223, v243\n"
"v_accvgpr_read_b32 v164, a20\n"
"v_accvgpr_read_b32 v165, a21\n"
"v_accvgpr_read_b32 v166, a22\n"
"v_accvgpr_read_b32 v167, a23\n"
"v_accvgpr_read_b32 v196, a16\n"
"v_accvgpr_read_b32 v197, a17\n"
"buffer_load_dwordx4 a[124:127], v17, %[rsW], s75 offen offset:3072 sc1\n"
"v_accvgpr_read_b32 v198, a18\n"
"v_accvgpr_read_b32 v199, a19\n"
"v_pk_mul_f32 v[196:197], v[196:197], v[30:31]\n"
"v_pk_mul_f32 v[198:199], v[198:199], v[30:31]\n"
"v_pk_mul_f32 v[164:165], v[164:165], v[30:31]\n"
"v_pk_mul_f32 v[166:167], v[166:167], v[30:31]\n"
"v_cvt_pk_bf16_f32 v196, v196, v197\n"
"v_cvt_pk_bf16_f32 v197, v198, v199\n"
"v_cvt_pk_bf16_f32 v198, v164, v165\n"
"s_add_u32 s75, %[xoff], 0x4000\n"
"buffer_load_dwordx4 a[128:131], v17, %[rsW], s75 offen sc1\n"
"v_cvt_pk_bf16_f32 v199, v166, v167\n"
"v_accvgpr_write_b32 a236, v196\n"
"v_accvgpr_write_b32 a237, v197\n"
"v_accvgpr_write_b32 a238, v198\n"
"v_accvgpr_write_b32 a239, v199\n"
"v_pk_mul_f32 v[56:57], v[56:57], v[30:31]\n"
"v_pk_mul_f32 v[58:59], v[58:59], v[30:31]\n"
"v_pk_mul_f32 v[60:61], v[60:61], v[30:31]\n"
"v_pk_mul_f32 v[62:63], v[62:63], v[30:31]\n"
"buffer_load_dwordx4 a[132:135], v17, %[rsW], s75 offen offset:1024 sc1\n"
"v_cvt_pk_bf16_f32 v56, v56, v57\n"
"v_cvt_pk_bf16_f32 v57, v58, v59\n"
"v_cvt_pk_bf16_f32 v58, v60, v61\n"
"v_cvt_pk_bf16_f32 v59, v62, v63\n"
"v_accvgpr_write_b32 a32, v56\n"
"v_accvgpr_write_b32 a33, v57\n"
"v_accvgpr_write_b32 a34, v58\n"
"v_accvgpr_write_b32 a35, v59\n"
"v_pk_mul_f32 v[88:89], v[88:89], v[30:31]\n"
"buffer_load_dwordx4 a[136:139], v17, %[rsW], s75 offen offset:2048 sc1\n"
"v_pk_mul_f32 v[90:91], v[90:91], v[30:31]\n"
"v_pk_mul_f32 v[92:93], v[92:93], v[30:31]\n"
"v_pk_mul_f32 v[94:95], v[94:95], v[30:31]\n"
"v_cvt_pk_bf16_f32 v88, v88, v89\n"
"v_cvt_pk_bf16_f32 v89, v90, v91\n"
"v_cvt_pk_bf16_f32 v90, v92, v93\n"
"v_cvt_pk_bf16_f32 v91, v94, v95\n"
"v_accvgpr_write_b32 a48, v88\n"
"v_accvgpr_write_b32 a49, v89\n"
"buffer_load_dwordx4 a[140:143], v17, %[rsW], s75 offen offset:3072 sc1\n"
"v_accvgpr_write_b32 a50, v90\n"
"v_accvgpr_write_b32 a51, v91\n"
"v_pk_mul_f32 v[120:121], v[120:121], v[30:31]\n"
"v_pk_mul_f32 v[122:123], v[122:123], v[30:31]\n"
"v_pk_mul_f32 v[124:125], v[124:125], v[30:31]\n"
"v_pk_mul_f32 v[126:127], v[126:127], v[30:31]\n"
"v_cvt_pk_bf16_f32 v120, v120, v121\n"
"v_cvt_pk_bf16_f32 v121, v122, v123\n"
"v_cvt_pk_bf16_f32 v122, v124, v125\n"
"s_add_u32 s75, %[xoff], 0x5000\n"
"buffer_load_dwordx4 a[144:147], v17, %[rsW], s75 offen sc1\n"
"v_cvt_pk_bf16_f32 v123, v126, v127\n"
"v_accvgpr_write_b32 a36, v120\n"
"v_accvgpr_write_b32 a37, v121\n"
"v_accvgpr_write_b32 a38, v122\n"
"v_accvgpr_write_b32 a39, v123\n"
"v_pk_mul_f32 v[152:153], v[152:153], v[30:31]\n"
"v_pk_mul_f32 v[154:155], v[154:155], v[30:31]\n"
"v_pk_mul_f32 v[156:157], v[156:157], v[30:31]\n"
"v_pk_mul_f32 v[158:159], v[158:159], v[30:31]\n"
"buffer_load_dwordx4 a[148:151], v17, %[rsW], s75 offen offset:1024 sc1\n"
"v_cvt_pk_bf16_f32 v152, v152, v153\n"
"v_cvt_pk_bf16_f32 v153, v154, v155\n"
"v_cvt_pk_bf16_f32 v154, v156, v157\n"
"v_cvt_pk_bf16_f32 v155, v158, v159\n"
"v_accvgpr_write_b32 a52, v152\n"
"v_accvgpr_write_b32 a53, v153\n"
"v_accvgpr_write_b32 a54, v154\n"
"v_accvgpr_write_b32 a55, v155\n"
"v_pk_mul_f32 v[184:185], v[184:185], v[30:31]\n"
"buffer_load_dwordx4 a[152:155], v17, %[rsW], s75 offen offset:2048 sc1\n"
"v_pk_mul_f32 v[186:187], v[186:187], v[30:31]\n"
"v_pk_mul_f32 v[188:189], v[188:189], v[30:31]\n"
"v_pk_mul_f32 v[190:191], v[190:191], v[30:31]\n"
"v_cvt_pk_bf16_f32 v184, v184, v185\n"
"v_cvt_pk_bf16_f32 v185, v186, v187\n"
"v_cvt_pk_bf16_f32 v186, v188, v189\n"
"v_cvt_pk_bf16_f32 v187, v190, v191\n"
"v_accvgpr_write_b32 a40, v184\n"
"v_accvgpr_write_b32 a41, v185\n"
"buffer_load_dwordx4 a[156:159], v17, %[rsW], s75 offen offset:3072 sc1\n"
"v_accvgpr_write_b32 a42, v186\n"
"v_accvgpr_write_b32 a43, v187\n"
"v_pk_mul_f32 v[216:217], v[216:217], v[30:31]\n"
"v_pk_mul_f32 v[218:219], v[218:219], v[30:31]\n"
"v_pk_mul_f32 v[220:221], v[220:221], v[30:31]\n"
"v_pk_mul_f32 v[222:223], v[222:223], v[30:31]\n"
"v_cvt_pk_bf16_f32 v216, v216, v217\n"
"v_cvt_pk_bf16_f32 v217, v218, v219\n"
"v_cvt_pk_bf16_f32 v218, v220, v221\n"
"s_add_u32 s70, %[out], 0\n"
"buffer_store_dwordx4 v[32:35], %[vst], %[rsW], s70 offen sc1\n"
"v_cvt_pk_bf16_f32 v219, v222, v223\n"
"v_accvgpr_write_b32 a56, v216\n"
"v_accvgpr_write_b32 a57, v217\n"
"v_accvgpr_write_b32 a58, v218\n"
"v_accvgpr_write_b32 a59, v219\n"
"v_pk_mul_f32 v[248:249], v[248:249], v[30:31]\n"
"v_pk_mul_f32 v[250:251], v[250:251], v[30:31]\n"
"v_pk_mul_f32 v[252:253], v[252:253], v[30:31]\n"
"v_pk_mul_f32 v[254:255], v[254:255], v[30:31]\n"
"s_add_u32 s70, %[out], 0\n"
"buffer_store_dwordx4 v[64:67], %[vst], %[rsW], s70 offen offset:128 sc1\n"
"v_cvt_pk_bf16_f32 v248, v248, v249\n"
"v_cvt_pk_bf16_f32 v249, v250, v251\n"
"v_cvt_pk_bf16_f32 v250, v252, v253\n"
"v_cvt_pk_bf16_f32 v251, v254, v255\n"
"v_accvgpr_write_b32 a44, v248\n"
"v_accvgpr_write_b32 a45, v249\n"
"v_accvgpr_write_b32 a46, v250\n"
"v_accvgpr_write_b32 a47, v251\n"
"v_accvgpr_read_b32 v228, a28\n"
"s_add_u32 s70, %[out], 0x4000\n"
"buffer_store_dwordx4 v[96:99], %[vst], %[rsW], s70 offen sc1\n"
"v_accvgpr_read_b32 v229, a29\n"
"v_accvgpr_read_b32 v230, a30\n"
"v_accvgpr_read_b32 v231, a31\n"
"v_accvgpr_read_b32 v36, a24\n"
"v_accvgpr_read_b32 v37, a25\n"
"v_accvgpr_read_b32 v38, a26\n"
"v_accvgpr_read_b32 v39, a27\n"
"v_pk_mul_f32 v[36:37], v[36:37], v[30:31]\n"
"v_pk_mul_f32 v[38:39], v[38:39], v[30:31]\n"
"s_add_u32 s70, %[out], 0x4000\n"
"buffer_store_dwordx4 v[128:131], %[vst], %[rsW], s70 offen offset:128 sc1\n"
"v_pk_mul_f32 v[228:229], v[228:229], v[30:31]\n"
"v_pk_mul_f32 v[230:231], v[230:231], v[30:31]\n"
"v_cvt_pk_bf16_f32 v36, v36, v37\n"
"v_cvt_pk_bf16_f32 v37, v38, v39\n"
"v_cvt_pk_bf16_f32 v38, v228, v229\n"
"v_cvt_pk_bf16_f32 v39, v230, v231\n"
"v_accvgpr_write_b32 a60, v36\n"
"v_accvgpr_write_b32 a61, v37\n"
"v_accvgpr_write_b32 a62, v38\n"
"s_add_u32 s70, %[out], 0x8000\n"
"buffer_store_dwordx4 v[160:163], %[vst], %[rsW], s70 offen sc1\n"
"v_accvgpr_write_b32 a63, v39\n"
"s_add_u32 s70, %[out], 0x8000\n"
"buffer_store_dwordx4 v[192:195], %[vst], %[rsW], s70 offen offset:128 sc1\n"
"s_add_u32 s70, %[out], 0xc000\n"
"buffer_store_dwordx4 v[224:227], %[vst], %[rsW], s70 offen sc1\n"
"s_add_u32 s70, %[out], 0xc000\n"
"buffer_store_dwordx4 v[68:71], %[vst], %[rsW], s70 offen offset:128 sc1\n"
"v_mov_b32 v92, %[vst]\n"
"v_add_u32 v93, 0x80, %[vst]\n"
"s_waitcnt vmcnt(8)\n"
"s_branch K1E%=\n"
"K1V%=:\n"
"ds_read_b64_tr_b16 a[64:65], v18\n"
"ds_read_b64_tr_b16 a[66:67], v18 offset:2048\n"
"ds_read_b128 a[32:35], v16\n"
"ds_read_b128 a[36:39], v16 offset:2048\n"
"ds_read_b128 a[40:43], v16 offset:4096\n"
"ds_read_b128 a[44:47], v16 offset:6144\n"
"ds_read_b128 a[48:51], v16 offset:8192\n"
"ds_read_b128 a[52:55], v16 offset:10240\n"
"ds_read_b128 a[56:59], v16 offset:12288\n"
"ds_read_b128 a[60:63], v16 offset:14336\n"
"ds_read_b64_tr_b16 a[68:69], v19\n"
"ds_read_b64_tr_b16 a[70:71], v19 offset:2048\n"
"ds_read_b64_tr_b16 a[72:73], v20\n"
"ds_read_b64_tr_b16 a[74:75], v20 offset:2048\n"
"ds_read_b64_tr_b16 a[76:77], v21\n"
"ds_read_b64_tr_b16 a[78:79], v21 offset:2048\n"
"ds_read_b64_tr_b16 a[80:81], v22\n"
"ds_read_b64_tr_b16 a[82:83], v22 offset:2048\n"
"ds_read_b64_tr_b16 a[84:85], v23\n"
"ds_read_b64_tr_b16 a[86:87], v23 offset:2048\n"
"ds_read_b64_tr_b16 a[88:89], v24\n"
"ds_read_b64_tr_b16 a[90:91], v24 offset:2048\n"
"ds_read_b64_tr_b16 a[92:93], v25\n"
"ds_read_b64_tr_b16 a[94:95], v25 offset:2048\n"
"s_waitcnt lgkmcnt(15)\n"
"v_mfma_f32_16x16x32_bf16 v[32:35], a[32:35], a[64:67], 0\n"
"ds_read_b64_tr_b16 a[128:129], v18 offset:16384\n"
"v_mfma_f32_16x16x32_bf16 v[64:67], a[36:39], a[64:67], 0\n"
"ds_read_b64_tr_b16 a[130:131], v18 offset:18432\n"
"v_mfma_f32_16x16x32_bf16 v[96:99], a[40:43], a[64:67], 0\n"
"ds_read_b128 a[96:99], v17\n"
"v_mfma_f32_16x16x32_bf16 v[128:131], a[44:47], a[64:67], 0\n"
"ds_read_b128 a[100:103], v17 offset:2048\n"
"v_mfma_f32_16x16x32_bf16 v[160:163], a[48:51], a[64:67], 0\n"
"ds_read_b128 a[104:107], v17 offset:4096\n"
"v_mfma_f32_16x16x32_bf16 v[192:195], a[52:55], a[64:67], 0\n"
"ds_read_b128 a[108:111], v17 offset:6144\n"
"v_mfma_f32_16x16x32_bf16 v[224:227], a[56:59], a[64:67], 0\n"
"ds_read_b128 a[112:115], v17 offset:8192\n"
"s_waitcnt lgkmcnt(15)\n"
"v_mfma_f32_16x16x32_bf16 a[0:3], a[60:63], a[64:67], 0\n"
"ds_read_b128 a[116:119], v17 offset:10240\n"
"v_mfma_f32_16x16x32_bf16 v[36:39], a[32:35], a[68:71], 0\n"
"ds_read_b128 a[120:123], v17 offset:12288\n"
"v_mfma_f32_16x16x32_bf16 v[68:71], a[36:39], a[68:71], 0\n"
"ds_read_b128 a[124:127], v17 offset:14336\n"
"v_mfma_f32_16x16x32_bf16 v[100:103], a[40:43], a[68:71], 0\n"
"ds_read_b64_tr_b16 a[132:133], v19 offset:16384\n"
"v_mfma_f32_16x16x32_bf16 v[132:135], a[44:47], a[68:71], 0\n"
"ds_read_b64_tr_b16 a[134:135], v19 offset:18432\n"
"v_mfma_f32_16x16x32_bf16 v[164:167], a[48:51], a[68:71], 0\n"
"ds_read_b64_tr_b16 a[136:137], v20 offset:16384\n"
"v_mfma_f32_16x16x32_bf16 v[196:199], a[52:55], a[68:71], 0\n"
"ds_read_b64_tr_b16 a[138:139], v20 offset:18432\n"
"v_mfma_f32_16x16x32_bf16 v[228:231], a[56:59], a[68:71], 0\n"
"ds_read_b64_tr_b16 a[140:141], v21 offset:16384\n"
"v_mfma_f32_16x16x32_bf16 a[4:7], a[60:63], a[68:71], 0\n"
"ds_read_b64_tr_b16 a[142:143], v21 offset:18432\n"
"v_mfma_f32_16x16x32_bf16 v[40:43], a[32:35], a[72:75], 0\n"
"ds_read_b64_tr_b16 a[144:145], v22 offset:16384\n"
"v_mfma_f32_16x16x32_bf16 v[72:75], a[36:39], a[72:75], 0\n"
"ds_read_b64_tr_b16 a[146:147], v22 offset:18432\n"
"v_mfma_f32_16x16x32_bf16 v[104:107], a[40:43], a[72:75], 0\n"
"ds_read_b64_tr_b16 a[148:149], v23 offset:16384\n"
"v_mfma_f32_16x16x32_bf16 v[136:139], a[44:47], a[72:75], 0\n"
"ds_read_b64_tr_b16 a[150:151], v23 offset:18432\n"
"v_mfma_f32_16x16x32_bf16 v[168:171], a[48:51], a[72:75], 0\n"
"ds_read_b64_tr_b16 a[152:153], v24 offset:16384\n"
"v_mfma_f32_16x16x32_bf16 v[200:203], a[52:55], a[72:75], 0\n"
"ds_read_b64_tr_b16 a[154:155], v24 offset:18432\n"
"v_mfma_f32_16x16x32_bf16 v[232:235], a[56:59], a[72:75], 0\n"
"ds_read_b64_tr_b16 a[156:157], v25 offset:16384\n"
"v_mfma_f32_16x16x32_bf16 a[8:11], a[60:63], a[72:75], 0\n"
"ds_read_b64_tr_b16 a[158:159], v25 offset:18432\n"
"v_mfma_f32_16x16x32_bf16 v[44:47], a[32:35], a[76:79], 0\n"
"v_mfma_f32_16x16x32_bf16 v[76:79], a[36:39], a[76:79], 0\n"
"v_mfma_f32_16x16x32_bf16 v[108:111], a[40:43], a[76:79], 0\n"
"v_mfma_f32_16x16x32_bf16 v[140:143], a[44:47], a[76:79], 0\n"
"v_mfma_f32_16x16x32_bf16 v[172:175], a[48:51], a[76:79], 0\n"
"v_mfma_f32_16x16x32_bf16 v[204:207], a[52:55], a[76:79], 0\n"
"v_mfma_f32_16x16x32_bf16 v[236:239], a[56:59], a[76:79], 0\n"
"v_mfma_f32_16x16x32_bf16 a[12:15], a[60:63], a[76:79], 0\n"
"s_waitcnt lgkmcnt(15)\n"
"v_mfma_f32_16x16x32_bf16 v[48:51], a[32:35], a[80:83], 0\n"
"v_mfma_f32_16x16x32_bf16 v[80:83], a[36:39], a[80:83], 0\n"
"v_mfma_f32_16x16x32_bf16 v[112:115], a[40:43], a[80:83], 0\n"
"v_mfma_f32_16x16x32_bf16 v[144:147], a[44:47], a[80:83], 0\n"
"v_mfma_f32_16x16x32_bf16 v[176:179], a[48:51], a[80:83], 0\n"
"v_mfma_f32_16x16x32_bf16 v[208:211], a[52:55], a[80:83], 0\n"
"v_mfma_f32_16x16x32_bf16 v[240:243], a[56:59], a[80:83], 0\n"
"v_mfma_f32_16x16x32_bf16 a[16:19], a[60:63], a[80:83], 0\n"
"v_mfma_f32_16x16x32_bf16 v[52:55], a[32:35], a[84:87], 0\n"
"v_mfma_f32_16x16x32_bf16 v[84:87], a[36:39], a[84:87], 0\n"
"v_mfma_f32_16x16x32_bf16 v[116:119], a[40:43], a[84:87], 0\n"
"v_mfma_f32_16x16x32_bf16 v[148:151], a[44:47], a[84:87], 0\n"
"v_mfma_f32_16x16x32_bf16 v[180:183], a[48:51], a[84:87], 0\n"
"s_mov_b32 s66, s65\n"
"v_mfma_f32_16x16x32_bf16 v[212:215], a[52:55], a[84:87], 0\n"
"s_mov_b32 s67, s64\n"
"v_mfma_f32_16x16x32_bf16 v[244:247], a[56:59], a[84:87], 0\n"
"s_add_u32 s65, s64, 0x8000\n"
"v_mfma_f32_16x16x32_bf16 a[20:23], a[60:63], a[84:87], 0\n"
"s_cmp_eq_u32 s65, 0x28000\n"
"v_mfma_f32_16x16x32_bf16 v[56:59], a[32:35], a[88:91], 0\n"
"s_cselect_b32 s65, 0, s65\n"
"v_mfma_f32_16x16x32_bf16 v[88:91], a[36:39], a[88:91], 0\n"
"s_add_u32 s64, s65, 0x8000\n"
"v_mfma_f32_16x16x32_bf16 v[120:123], a[40:43], a[88:91], 0\n"
"s_cmp_eq_u32 s64, 0x28000\n"
"v_mfma_f32_16x16x32_bf16 v[152:155], a[44:47], a[88:91], 0\n"
"s_cselect_b32 s64, 0, s64\n"
"v_mfma_f32_16x16x32_bf16 v[184:187], a[48:51], a[88:91], 0\n"
"v_add_u32 v16, s64, %[lA]\n"
"v_mfma_f32_16x16x32_bf16 v[216:219], a[52:55], a[88:91], 0\n"
"v_add_u32 v18, s65, %[lB]\n"
"v_mfma_f32_16x16x32_bf16 v[248:251], a[56:59], a[88:91], 0\n"
"v_xor_b32 v17, 64, v16\n"
"v_mfma_f32_16x16x32_bf16 a[24:27], a[60:63], a[88:91], 0\n"
"v_xor_b32 v19, 32, v18\n"
"v_mfma_f32_16x16x32_bf16 v[60:63], a[32:35], a[92:95], 0\n"
"v_xor_b32 v20, 64, v18\n"
"v_mfma_f32_16x16x32_bf16 v[92:95], a[36:39], a[92:95], 0\n"
"v_xor_b32 v21, 0x60, v18\n"
"v_mfma_f32_16x16x32_bf16 v[124:127], a[40:43], a[92:95], 0\n"
"v_xor_b32 v22, 0x80, v18\n"
"v_mfma_f32_16x16x32_bf16 v[156:159], a[44:47], a[92:95], 0\n"
"v_xor_b32 v23, 0xa0, v18\n"
"v_mfma_f32_16x16x32_bf16 v[188:191], a[48:51], a[92:95], 0\n"
"v_xor_b32 v24, 0xc0, v18\n"
"v_mfma_f32_16x16x32_bf16 v[220:223], a[52:55], a[92:95], 0\n"
"v_xor_b32 v25, 0xe0, v18\n"
"v_mfma_f32_16x16x32_bf16 v[252:255], a[56:59], a[92:95], 0\n"
"v_mfma_f32_16x16x32_bf16 a[28:31], a[60:63], a[92:95], 0\n"
"K1Lv%=:\n"
"s_waitcnt lgkmcnt(0)\n"
"s_waitcnt vmcnt(8)\n"
"s_barrier\n"
"v_mfma_f32_16x16x32_bf16 v[32:35], a[96:99], a[128:131], v[32:35]\n"
"ds_read_b64_tr_b16 a[64:65], v18\n"
"v_mfma_f32_16x16x32_bf16 v[64:67], a[100:103], a[128:131], v[64:67]\n"
"ds_read_b64_tr_b16 a[66:67], v18 offset:2048\n"
"s_add_u32 m0, s66, %[wa]\n"
"s_cmp_eq_u32 s71, 1\n"
"s_cbranch_scc1 LNFv%=\n"
"LNBv%=:\n"
"v_mfma_f32_16x16x32_bf16 v[96:99], a[104:107], a[128:131], v[96:99]\n"
"ds_read_b128 a[32:35], v16\n"
"s_mov_b32 s70, s68\n"
"v_mfma_f32_16x16x32_bf16 v[128:131], a[108:111], a[128:131], v[128:131]\n"
"ds_read_b128 a[36:39], v16 offset:2048\n"
"buffer_load_dwordx4 %[vA0], %[rsC], s70 offen lds\n"
"v_mfma_f32_16x16x32_bf16 v[160:163], a[112:115], a[128:131], v[160:163]\n"
"ds_read_b128 a[40:43], v16 offset:4096\n"
"v_mfma_f32_16x16x32_bf16 v[192:195], a[116:119], a[128:131], v[192:195]\n"
"ds_read_b128 a[44:47], v16 offset:6144\n"
"v_mfma_f32_16x16x32_bf16 v[224:227], a[120:123], a[128:131], v[224:227]\n"
"ds_read_b128 a[48:51], v16 offset:8192\n"
"v_mfma_f32_16x16x32_bf16 a[0:3], a[124:127], a[128:131], a[0:3]\n"
"ds_read_b128 a[52:55], v16 offset:10240\n"
"buffer_load_dwordx4 %[vA1], %[rsC], s70 offen offset:1024 lds\n"
"v_mfma_f32_16x16x32_bf16 v[36:39], a[96:99], a[132:135], v[36:39]\n"
"ds_read_b128 a[56:59], v16 offset:12288\n"
"v_mfma_f32_16x16x32_bf16 v[68:71], a[100:103], a[132:135], v[68:71]\n"
"ds_read_b128 a[60:63], v16 offset:14336\n"
"v_mfma_f32_16x16x32_bf16 v[100:103], a[104:107], a[132:135], v[100:103]\n"
"ds_read_b64_tr_b16 a[68:69], v19\n"
"v_mfma_f32_16x16x32_bf16 v[132:135], a[108:111], a[132:135], v[132:135]\n"
"ds_read_b64_tr_b16 a[70:71], v19 offset:2048\n"
"buffer_load_dwordx4 %[vA2], %[rsC], s70 offen offset:2048 lds\n"
"v_mfma_f32_16x16x32_bf16 v[164:167], a[112:115], a[132:135], v[164:167]\n"
"ds_read_b64_tr_b16 a[72:73], v20\n"
"v_mfma_f32_16x16x32_bf16 v[196:199], a[116:119], a[132:135], v[196:199]\n"
"ds_read_b64_tr_b16 a[74:75], v20 offset:2048\n"
"v_mfma_f32_16x16x32_bf16 v[228:231], a[120:123], a[132:135], v[228:231]\n"
"ds_read_b64_tr_b16 a[76:77], v21\n"
"v_mfma_f32_16x16x32_bf16 a[4:7], a[124:127], a[132:135], a[4:7]\n"
"ds_read_b64_tr_b16 a[78:79], v21 offset:2048\n"
"buffer_load_dwordx4 %[vA3], %[rsC], s70 offen offset:3072 lds\n"
"v_mfma_f32_16x16x32_bf16 v[40:43], a[96:99], a[136:139], v[40:43]\n"
"ds_read_b64_tr_b16 a[80:81], v22\n"
"v_mfma_f32_16x16x32_bf16 v[72:75], a[100:103], a[136:139], v[72:75]\n"
"ds_read_b64_tr_b16 a[82:83], v22 offset:2048\n"
"s_add_u32 m0, m0, 0x1000\n"
"v_mfma_f32_16x16x32_bf16 v[104:107], a[104:107], a[136:139], v[104:107]\n"
"ds_read_b64_tr_b16 a[84:85], v23\n"
"s_add_u32 s70, s70, 0x10000\n"
"v_mfma_f32_16x16x32_bf16 v[136:139], a[108:111], a[136:139], v[136:139]\n"
"ds_read_b64_tr_b16 a[86:87], v23 offset:2048\n"
"buffer_load_dwordx4 %[vA0], %[rsC], s70 offen lds\n"
"v_mfma_f32_16x16x32_bf16 v[168:171], a[112:115], a[136:139], v[168:171]\n"
"ds_read_b64_tr_b16 a[88:89], v24\n"
"v_mfma_f32_16x16x32_bf16 v[200:203], a[116:119], a[136:139], v[200:203]\n"
"ds_read_b64_tr_b16 a[90:91], v24 offset:2048\n"
"v_mfma_f32_16x16x32_bf16 v[232:235], a[120:123], a[136:139], v[232:235]\n"
"ds_read_b64_tr_b16 a[92:93], v25\n"
"v_mfma_f32_16x16x32_bf16 a[8:11], a[124:127], a[136:139], a[8:11]\n"
"ds_read_b64_tr_b16 a[94:95], v25 offset:2048\n"
"buffer_load_dwordx4 %[vA1], %[rsC], s70 offen offset:1024 lds\n"
"v_mfma_f32_16x16x32_bf16 v[44:47], a[96:99], a[140:143], v[44:47]\n"
"v_mfma_f32_16x16x32_bf16 v[76:79], a[100:103], a[140:143], v[76:79]\n"
"v_mfma_f32_16x16x32_bf16 v[108:111], a[104:107], a[140:143], v[108:111]\n"
"v_mfma_f32_16x16x32_bf16 v[140:143], a[108:111], a[140:143], v[140:143]\n"
"buffer_load_dwordx4 %[vA2], %[rsC], s70 offen offset:2048 lds\n"
"v_mfma_f32_16x16x32_bf16 v[172:175], a[112:115], a[140:143], v[172:175]\n"
"v_mfma_f32_16x16x32_bf16 v[204:207], a[116:119], a[140:143], v[204:207]\n"
"v_mfma_f32_16x16x32_bf16 v[236:239], a[120:123], a[140:143], v[236:239]\n"
"v_mfma_f32_16x16x32_bf16 a[12:15], a[124:127], a[140:143], a[12:15]\n"
"buffer_load_dwordx4 %[vA3], %[rsC], s70 offen offset:3072 lds\n"
"v_mfma_f32_16x16x32_bf16 v[48:51], a[96:99], a[144:147], v[48:51]\n"
"v_mfma_f32_16x16x32_bf16 v[80:83], a[100:103], a[144:147], v[80:83]\n"
"v_mfma_f32_16x16x32_bf16 v[112:115], a[104:107], a[144:147], v[112:115]\n"
"v_mfma_f32_16x16x32_bf16 v[144:147], a[108:111], a[144:147], v[144:147]\n"
"v_mfma_f32_16x16x32_bf16 v[176:179], a[112:115], a[144:147], v[176:179]\n"
"v_mfma_f32_16x16x32_bf16 v[208:211], a[116:119], a[144:147], v[208:211]\n"
"s_add_u32 s68, s68, 0x80\n"
"v_mfma_f32_16x16x32_bf16 v[240:243], a[120:123], a[144:147], v[240:243]\n"
"s_add_u32 m0, s67, %[wb]\n"
"v_mfma_f32_16x16x32_bf16 a[16:19], a[124:127], a[144:147], a[16:19]\n"
"s_mov_b32 s70, s69\n"
"v_mfma_f32_16x16x32_bf16 v[52:55], a[96:99], a[148:151], v[52:55]\n"
"buffer_load_dwordx4 %[vB0], %[rsB], s70 offen lds\n"
"v_mfma_f32_16x16x32_bf16 v[84:87], a[100:103], a[148:151], v[84:87]\n"
"v_mfma_f32_16x16x32_bf16 v[116:119], a[104:107], a[148:151], v[116:119]\n"
"v_mfma_f32_16x16x32_bf16 v[148:151], a[108:111], a[148:151], v[148:151]\n"
"v_mfma_f32_16x16x32_bf16 v[180:183], a[112:115], a[148:151], v[180:183]\n"
"v_mfma_f32_16x16x32_bf16 v[212:215], a[116:119], a[148:151], v[212:215]\n"
"v_mfma_f32_16x16x32_bf16 v[244:247], a[120:123], a[148:151], v[244:247]\n"
"v_mfma_f32_16x16x32_bf16 a[20:23], a[124:127], a[148:151], a[20:23]\n"
"v_mfma_f32_16x16x32_bf16 v[56:59], a[96:99], a[152:155], v[56:59]\n"
"buffer_load_dwordx4 %[vB1], %[rsB], s70 offen offset:1024 lds\n"
"v_mfma_f32_16x16x32_bf16 v[88:91], a[100:103], a[152:155], v[88:91]\n"
"v_mfma_f32_16x16x32_bf16 v[120:123], a[104:107], a[152:155], v[120:123]\n"
"v_mfma_f32_16x16x32_bf16 v[152:155], a[108:111], a[152:155], v[152:155]\n"
"v_mfma_f32_16x16x32_bf16 v[184:187], a[112:115], a[152:155], v[184:187]\n"
"v_mfma_f32_16x16x32_bf16 v[216:219], a[116:119], a[152:155], v[216:219]\n"
"v_mfma_f32_16x16x32_bf16 v[248:251], a[120:123], a[152:155], v[248:251]\n"
"v_mfma_f32_16x16x32_bf16 a[24:27], a[124:127], a[152:155], a[24:27]\n"
"v_mfma_f32_16x16x32_bf16 v[60:63], a[96:99], a[156:159], v[60:63]\n"
"buffer_load_dwordx4 %[vB2], %[rsB], s70 offen offset:2048 lds\n"
"v_mfma_f32_16x16x32_bf16 v[92:95], a[100:103], a[156:159], v[92:95]\n"
"v_mfma_f32_16x16x32_bf16 v[124:127], a[104:107], a[156:159], v[124:127]\n"
"v_mfma_f32_16x16x32_bf16 v[156:159], a[108:111], a[156:159], v[156:159]\n"
"v_mfma_f32_16x16x32_bf16 v[188:191], a[112:115], a[156:159], v[188:191]\n"
"v_mfma_f32_16x16x32_bf16 v[220:223], a[116:119], a[156:159], v[220:223]\n"
"v_mfma_f32_16x16x32_bf16 v[252:255], a[120:123], a[156:159], v[252:255]\n"
"v_mfma_f32_16x16x32_bf16 a[28:31], a[124:127], a[156:159], a[28:31]\n"
"s_waitcnt lgkmcnt(15)\n"
"v_mfma_f32_16x16x32_bf16 v[32:35], a[32:35], a[64:67], v[32:35]\n"
"ds_read_b64_tr_b16 a[128:129], v18 offset:16384\n"
"buffer_load_dwordx4 %[vB3], %[rsB], s70 offen offset:3072 lds\n"
"v_mfma_f32_16x16x32_bf16 v[64:67], a[36:39], a[64:67], v[64:67]\n"
"ds_read_b64_tr_b16 a[130:131], v18 offset:18432\n"
"v_mfma_f32_16x16x32_bf16 v[96:99], a[40:43], a[64:67], v[96:99]\n"
"ds_read_b128 a[96:99], v17\n"
"v_mfma_f32_16x16x32_bf16 v[128:131], a[44:47], a[64:67], v[128:131]\n"
"ds_read_b128 a[100:103], v17 offset:2048\n"
"v_mfma_f32_16x16x32_bf16 v[160:163], a[48:51], a[64:67], v[160:163]\n"
"ds_read_b128 a[104:107], v17 offset:4096\n"
"v_mfma_f32_16x16x32_bf16 v[192:195], a[52:55], a[64:67], v[192:195]\n"
"ds_read_b128 a[108:111], v17 offset:6144\n"
"v_mfma_f32_16x16x32_bf16 v[224:227], a[56:59], a[64:67], v[224:227]\n"
"ds_read_b128 a[112:115], v17 offset:8192\n"
"s_add_u32 m0, m0, 0x2000\n"
"s_waitcnt lgkmcnt(15)\n"
"v_mfma_f32_16x16x32_bf16 a[0:3], a[60:63], a[64:67], a[0:3]\n"
"ds_read_b128 a[116:119], v17 offset:10240\n"
"s_add_u32 s70, s70, 0x20000\n"
"v_mfma_f32_16x16x32_bf16 v[36:39], a[32:35], a[68:71], v[36:39]\n"
"ds_read_b128 a[120:123], v17 offset:12288\n"
"buffer_load_dwordx4 %[vB0], %[rsB], s70 offen lds\n"
"v_mfma_f32_16x16x32_bf16 v[68:71], a[36:39], a[68:71], v[68:71]\n"
"ds_read_b128 a[124:127], v17 offset:14336\n"
"v_mfma_f32_16x16x32_bf16 v[100:103], a[40:43], a[68:71], v[100:103]\n"
"ds_read_b64_tr_b16 a[132:133], v19 offset:16384\n"
"v_mfma_f32_16x16x32_bf16 v[132:135], a[44:47], a[68:71], v[132:135]\n"
"ds_read_b64_tr_b16 a[134:135], v19 offset:18432\n"
"v_mfma_f32_16x16x32_bf16 v[164:167], a[48:51], a[68:71], v[164:167]\n"
"ds_read_b64_tr_b16 a[136:137], v20 offset:16384\n"
"v_mfma_f32_16x16x32_bf16 v[196:199], a[52:55], a[68:71], v[196:199]\n"
"ds_read_b64_tr_b16 a[138:139], v20 offset:18432\n"
"v_mfma_f32_16x16x32_bf16 v[228:231], a[56:59], a[68:71], v[228:231]\n"
"ds_read_b64_tr_b16 a[140:141], v21 offset:16384\n"
"v_mfma_f32_16x16x32_bf16 a[4:7], a[60:63], a[68:71], a[4:7]\n"
"ds_read_b64_tr_b16 a[142:143], v21 offset:18432\n"
"v_mfma_f32_16x16x32_bf16 v[40:43], a[32:35], a[72:75], v[40:43]\n"
"ds_read_b64_tr_b16 a[144:145], v22 offset:16384\n"
"buffer_load_dwordx4 %[vB1], %[rsB], s70 offen offset:1024 lds\n"
"v_mfma_f32_16x16x32_bf16 v[72:75], a[36:39], a[72:75], v[72:75]\n"
"ds_read_b64_tr_b16 a[146:147], v22 offset:18432\n"
"v_mfma_f32_16x16x32_bf16 v[104:107], a[40:43], a[72:75], v[104:107]\n"
"ds_read_b64_tr_b16 a[148:149], v23 offset:16384\n"
"v_mfma_f32_16x16x32_bf16 v[136:139], a[44:47], a[72:75], v[136:139]\n"
"ds_read_b64_tr_b16 a[150:151], v23 offset:18432\n"
"v_mfma_f32_16x16x32_bf16 v[168:171], a[48:51], a[72:75], v[168:171]\n"
"ds_read_b64_tr_b16 a[152:153], v24 offset:16384\n"
"v_mfma_f32_16x16x32_bf16 v[200:203], a[52:55], a[72:75], v[200:203]\n"
"ds_read_b64_tr_b16 a[154:155], v24 offset:18432\n"
"v_mfma_f32_16x16x32_bf16 v[232:235], a[56:59], a[72:75], v[232:235]\n"
"ds_read_b64_tr_b16 a[156:157], v25 offset:16384\n"
"v_mfma_f32_16x16x32_bf16 a[8:11], a[60:63], a[72:75], a[8:11]\n"
"ds_read_b64_tr_b16 a[158:159], v25 offset:18432\n"
"v_mfma_f32_16x16x32_bf16 v[44:47], a[32:35], a[76:79], v[44:47]\n"
"buffer_load_dwordx4 %[vB2], %[rsB], s70 offen offset:2048 lds\n"
"v_mfma_f32_16x16x32_bf16 v[76:79], a[36:39], a[76:79], v[76:79]\n"
"v_mfma_f32_16x16x32_bf16 v[108:111], a[40:43], a[76:79], v[108:111]\n"
"v_mfma_f32_16x16x32_bf16 v[140:143], a[44:47], a[76:79], v[140:143]\n"
"v_mfma_f32_16x16x32_bf16 v[172:175], a[48:51], a[76:79], v[172:175]\n"
"v_mfma_f32_16x16x32_bf16 v[204:207], a[52:55], a[76:79], v[204:207]\n"
"v_mfma_f32_16x16x32_bf16 v[236:239], a[56:59], a[76:79], v[236:239]\n"
"v_mfma_f32_16x16x32_bf16 a[12:15], a[60:63], a[76:79], a[12:15]\n"
"s_waitcnt lgkmcnt(15)\n"
"v_mfma_f32_16x16x32_bf16 v[48:51], a[32:35], a[80:83], v[48:51]\n"
"buffer_load_dwordx4 %[vB3], %[rsB], s70 offen offset:3072 lds\n"
"v_mfma_f32_16x16x32_bf16 v[80:83], a[36:39], a[80:83], v[80:83]\n"
"s_add_u32 s69, s69, 0x80000\n"
"v_mfma_f32_16x16x32_bf16 v[112:115], a[40:43], a[80:83], v[112:115]\n"
"v_mfma_f32_16x16x32_bf16 v[144:147], a[44:47], a[80:83], v[144:147]\n"
"v_mfma_f32_16x16x32_bf16 v[176:179], a[48:51], a[80:83], v[176:179]\n"
"v_mfma_f32_16x16x32_bf16 v[208:211], a[52:55], a[80:83], v[208:211]\n"
"v_mfma_f32_16x16x32_bf16 v[240:243], a[56:59], a[80:83], v[240:243]\n"
"v_mfma_f32_16x16x32_bf16 a[16:19], a[60:63], a[80:83], a[16:19]\n"
"v_mfma_f32_16x16x32_bf16 v[52:55], a[32:35], a[84:87], v[52:55]\n"
"s_cmp_eq_u32 s71, 9\n"
"s_cbranch_scc1 PFWv%=\n"
"PFBv%=:\n"
"v_mfma_f32_16x16x32_bf16 v[84:87], a[36:39], a[84:87], v[84:87]\n"
"v_mfma_f32_16x16x32_bf16 v[116:119], a[40:43], a[84:87], v[116:119]\n"
"v_mfma_f32_16x16x32_bf16 v[148:151], a[44:47], a[84:87], v[148:151]\n"
"v_mfma_f32_16x16x32_bf16 v[180:183], a[48:51], a[84:87], v[180:183]\n"
"s_mov_b32 s66, s65\n"
"v_mfma_f32_16x16x32_bf16 v[212:215], a[52:55], a[84:87], v[212:215]\n"
"s_mov_b32 s67, s64\n"
"v_mfma_f32_16x16x32_bf16 v[244:247], a[56:59], a[84:87], v[244:247]\n"
"s_add_u32 s65, s64, 0x8000\n"
"v_mfma_f32_16x16x32_bf16 a[20:23], a[60:63], a[84:87], a[20:23]\n"
"s_cmp_eq_u32 s65, 0x28000\n"
"v_mfma_f32_16x16x32_bf16 v[56:59], a[32:35], a[88:91], v[56:59]\n"
"s_cselect_b32 s65, 0, s65\n"
"v_mfma_f32_16x16x32_bf16 v[88:91], a[36:39], a[88:91], v[88:91]\n"
"s_add_u32 s64, s65, 0x8000\n"
"v_mfma_f32_16x16x32_bf16 v[120:123], a[40:43], a[88:91], v[120:123]\n"
"s_cmp_eq_u32 s64, 0x28000\n"
"v_mfma_f32_16x16x32_bf16 v[152:155], a[44:47], a[88:91], v[152:155]\n"
"s_cselect_b32 s64, 0, s64\n"
"v_mfma_f32_16x16x32_bf16 v[184:187], a[48:51], a[88:91], v[184:187]\n"
"v_add_u32 v16, s64, %[lA]\n"
"v_mfma_f32_16x16x32_bf16 v[216:219], a[52:55], a[88:91], v[216:219]\n"
"v_add_u32 v18, s65, %[lB]\n"
"v_mfma_f32_16x16x32_bf16 v[248:251], a[56:59], a[88:91], v[248:251]\n"
"v_xor_b32 v17, 64, v16\n"
"v_mfma_f32_16x16x32_bf16 a[24:27], a[60:63], a[88:91], a[24:27]\n"
"v_xor_b32 v19, 32, v18\n"
"v_mfma_f32_16x16x32_bf16 v[60:63], a[32:35], a[92:95], v[60:63]\n"
"v_xor_b32 v20, 64, v18\n"
"v_mfma_f32_16x16x32_bf16 v[92:95], a[36:39], a[92:95], v[92:95]\n"
"v_xor_b32 v21, 0x60, v18\n"
"v_mfma_f32_16x16x32_bf16 v[124:127], a[40:43], a[92:95], v[124:127]\n"
"v_xor_b32 v22, 0x80, v18\n"
"v_mfma_f32_16x16x32_bf16 v[156:159], a[44:47], a[92:95], v[156:159]\n"
"v_xor_b32 v23, 0xa0, v18\n"
"v_mfma_f32_16x16x32_bf16 v[188:191], a[48:51], a[92:95], v[188:191]\n"
"v_xor_b32 v24, 0xc0, v18\n"
"v_mfma_f32_16x16x32_bf16 v[220:223], a[52:55], a[92:95], v[220:223]\n"
"v_xor_b32 v25, 0xe0, v18\n"
"v_mfma_f32_16x16x32_bf16 v[252:255], a[56:59], a[92:95], v[252:255]\n"
"s_add_u32 s71, s71, 1\n"
"v_mfma_f32_16x16x32_bf16 a[28:31], a[60:63], a[92:95], a[28:31]\n"
"s_cmp_lt_u32 s71, 13\n"
"s_cbranch_scc1 K1Lv%=\n"
"s_waitcnt lgkmcnt(0)\n"
"s_waitcnt vmcnt(8)\n"
"s_barrier\n"
"v_mfma_f32_16x16x32_bf16 v[32:35], a[96:99], a[128:131], v[32:35]\n"
"ds_read_b64_tr_b16 a[64:65], v18\n"
"v_mfma_f32_16x16x32_bf16 v[64:67], a[100:103], a[128:131], v[64:67]\n"
"ds_read_b64_tr_b16 a[66:67], v18 offset:2048\n"
"s_add_u32 m0, s66, %[wa]\n"
"v_mfma_f32_16x16x32_bf16 v[96:99], a[104:107], a[128:131], v[96:99]\n"
"ds_read_b128 a[32:35], v16\n"
"s_mov_b32 s70, s68\n"
"v_mfma_f32_16x16x32_bf16 v[128:131], a[108:111], a[128:131], v[128:131]\n"
"ds_read_b128 a[36:39], v16 offset:2048\n"
"buffer_load_dwordx4 %[vA0], %[rsC], s70 offen lds\n"
"v_mfma_f32_16x16x32_bf16 v[160:163], a[112:115], a[128:131], v[160:163]\n"
"ds_read_b128 a[40:43], v16 offset:4096\n"
"v_mfma_f32_16x16x32_bf16 v[192:195], a[116:119], a[128:131], v[192:195]\n"
"ds_read_b128 a[44:47], v16 offset:6144\n"
"v_mfma_f32_16x16x32_bf16 v[224:227], a[120:123], a[128:131], v[224:227]\n"
"ds_read_b128 a[48:51], v16 offset:8192\n"
"v_mfma_f32_16x16x32_bf16 a[0:3], a[124:127], a[128:131], a[0:3]\n"
"ds_read_b128 a[52:55], v16 offset:10240\n"
"buffer_load_dwordx4 %[vA1], %[rsC], s70 offen offset:1024 lds\n"
"v_mfma_f32_16x16x32_bf16 v[36:39], a[96:99], a[132:135], v[36:39]\n"
"ds_read_b128 a[56:59], v16 offset:12288\n"
"v_mfma_f32_16x16x32_bf16 v[68:71], a[100:103], a[132:135], v[68:71]\n"
"ds_read_b128 a[60:63], v16 offset:14336\n"
"v_mfma_f32_16x16x32_bf16 v[100:103], a[104:107], a[132:135], v[100:103]\n"
"ds_read_b64_tr_b16 a[68:69], v19\n"
"v_mfma_f32_16x16x32_bf16 v[132:135], a[108:111], a[132:135], v[132:135]\n"
"ds_read_b64_tr_b16 a[70:71], v19 offset:2048\n"
"buffer_load_dwordx4 %[vA2], %[rsC], s70 offen offset:2048 lds\n"
"v_mfma_f32_16x16x32_bf16 v[164:167], a[112:115], a[132:135], v[164:167]\n"
"ds_read_b64_tr_b16 a[72:73], v20\n"
"v_mfma_f32_16x16x32_bf16 v[196:199], a[116:119], a[132:135], v[196:199]\n"
"ds_read_b64_tr_b16 a[74:75], v20 offset:2048\n"
"v_mfma_f32_16x16x32_bf16 v[228:231], a[120:123], a[132:135], v[228:231]\n"
"ds_read_b64_tr_b16 a[76:77], v21\n"
"v_mfma_f32_16x16x32_bf16 a[4:7], a[124:127], a[132:135], a[4:7]\n"
"ds_read_b64_tr_b16 a[78:79], v21 offset:2048\n"
"buffer_load_dwordx4 %[vA3], %[rsC], s70 offen offset:3072 lds\n"
"v_mfma_f32_16x16x32_bf16 v[40:43], a[96:99], a[136:139], v[40:43]\n"
"ds_read_b64_tr_b16 a[80:81], v22\n"
"v_mfma_f32_16x16x32_bf16 v[72:75], a[100:103], a[136:139], v[72:75]\n"
"ds_read_b64_tr_b16 a[82:83], v22 offset:2048\n"
"s_add_u32 m0, m0, 0x1000\n"
"v_mfma_f32_16x16x32_bf16 v[104:107], a[104:107], a[136:139], v[104:107]\n"
"ds_read_b64_tr_b16 a[84:85], v23\n"
"s_add_u32 s70, s70, 0x10000\n"
"v_mfma_f32_16x16x32_bf16 v[136:139], a[108:111], a[136:139], v[136:139]\n"
"ds_read_b64_tr_b16 a[86:87], v23 offset:2048\n"
"buffer_load_dwordx4 %[vA0], %[rsC], s70 offen lds\n"
"v_mfma_f32_16x16x32_bf16 v[168:171], a[112:115], a[136:139], v[168:171]\n"
"ds_read_b64_tr_b16 a[88:89], v24\n"
"v_mfma_f32_16x16x32_bf16 v[200:203], a[116:119], a[136:139], v[200:203]\n"
"ds_read_b64_tr_b16 a[90:91], v24 offset:2048\n"
"v_mfma_f32_16x16x32_bf16 v[232:235], a[120:123], a[136:139], v[232:235]\n"
"ds_read_b64_tr_b16 a[92:93], v25\n"
"v_mfma_f32_16x16x32_bf16 a[8:11], a[124:127], a[136:139], a[8:11]\n"
"ds_read_b64_tr_b16 a[94:95], v25 offset:2048\n"
"buffer_load_dwordx4 %[vA1], %[rsC], s70 offen offset:1024 lds\n"
"v_mfma_f32_16x16x32_bf16 v[44:47], a[96:99], a[140:143], v[44:47]\n"
"v_mfma_f32_16x16x32_bf16 v[76:79], a[100:103], a[140:143], v[76:79]\n"
"v_mfma_f32_16x16x32_bf16 v[108:111], a[104:107], a[140:143], v[108:111]\n"
"v_mfma_f32_16x16x32_bf16 v[140:143], a[108:111], a[140:143], v[140:143]\n"
"buffer_load_dwordx4 %[vA2], %[rsC], s70 offen offset:2048 lds\n"
"v_mfma_f32_16x16x32_bf16 v[172:175], a[112:115], a[140:143], v[172:175]\n"
"v_mfma_f32_16x16x32_bf16 v[204:207], a[116:119], a[140:143], v[204:207]\n"
"v_mfma_f32_16x16x32_bf16 v[236:239], a[120:123], a[140:143], v[236:239]\n"
"v_mfma_f32_16x16x32_bf16 a[12:15], a[124:127], a[140:143], a[12:15]\n"
"buffer_load_dwordx4 %[vA3], %[rsC], s70 offen offset:3072 lds\n"
"v_mfma_f32_16x16x32_bf16 v[48:51], a[96:99], a[144:147], v[48:51]\n"
"s_add_u32 s68, s68, 0x80\n"
"v_mfma_f32_16x16x32_bf16 v[80:83], a[100:103], a[144:147], v[80:83]\n"
"v_mfma_f32_16x16x32_bf16 v[112:115], a[104:107], a[144:147], v[112:115]\n"
"v_mfma_f32_16x16x32_bf16 v[144:147], a[108:111], a[144:147], v[144:147]\n"
"v_mfma_f32_16x16x32_bf16 v[176:179], a[112:115], a[144:147], v[176:179]\n"
"v_mfma_f32_16x16x32_bf16 v[208:211], a[116:119], a[144:147], v[208:211]\n"
"v_mfma_f32_16x16x32_bf16 v[240:243], a[120:123], a[144:147], v[240:243]\n"
"v_mfma_f32_16x16x32_bf16 a[16:19], a[124:127], a[144:147], a[16:19]\n"
"v_mfma_f32_16x16x32_bf16 v[52:55], a[96:99], a[148:151], v[52:55]\n"
"v_mfma_f32_16x16x32_bf16 v[84:87], a[100:103], a[148:151], v[84:87]\n"
"v_mfma_f32_16x16x32_bf16 v[116:119], a[104:107], a[148:151], v[116:119]\n"
"v_mfma_f32_16x16x32_bf16 v[148:151], a[108:111], a[148:151], v[148:151]\n"
"v_mfma_f32_16x16x32_bf16 v[180:183], a[112:115], a[148:151], v[180:183]\n"
"v_mfma_f32_16x16x32_bf16 v[212:215], a[116:119], a[148:151], v[212:215]\n"
"v_mfma_f32_16x16x32_bf16 v[244:247], a[120:123], a[148:151], v[244:247]\n"
"v_mfma_f32_16x16x32_bf16 a[20:23], a[124:127], a[148:151], a[20:23]\n"
"v_mfma_f32_16x16x32_bf16 v[56:59], a[96:99], a[152:155], v[56:59]\n"
"v_mfma_f32_16x16x32_bf16 v[88:91], a[100:103], a[152:155], v[88:91]\n"
"v_mfma_f32_16x16x32_bf16 v[120:123], a[104:107], a[152:155], v[120:123]\n"
"v_mfma_f32_16x16x32_bf16 v[152:155], a[108:111], a[152:155], v[152:155]\n"
"v_mfma_f32_16x16x32_bf16 v[184:187], a[112:115], a[152:155], v[184:187]\n"
"v_mfma_f32_16x16x32_bf16 v[216:219], a[116:119], a[152:155], v[216:219]\n"
"v_mfma_f32_16x16x32_bf16 v[248:251], a[120:123], a[152:155], v[248:251]\n"
"v_mfma_f32_16x16x32_bf16 a[24:27], a[124:127], a[152:155], a[24:27]\n"
"v_mfma_f32_16x16x32_bf16 v[60:63], a[96:99], a[156:159], v[60:63]\n"
"v_mfma_f32_16x16x32_bf16 v[92:95], a[100:103], a[156:159], v[92:95]\n"
"v_mfma_f32_16x16x32_bf16 v[124:127], a[104:107], a[156:159], v[124:127]\n"
"v_mfma_f32_16x16x32_bf16 v[156:159], a[108:111], a[156:159], v[156:159]\n"
"v_mfma_f32_16x16x32_bf16 v[188:191], a[112:115], a[156:159], v[188:191]\n"
"v_mfma_f32_16x16x32_bf16 v[220:223], a[116:119], a[156:159], v[220:223]\n"
"v_mfma_f32_16x16x32_bf16 v[252:255], a[120:123], a[156:159], v[252:255]\n"
"v_mfma_f32_16x16x32_bf16 a[28:31], a[124:127], a[156:159], a[28:31]\n"
"s_waitcnt lgkmcnt(15)\n"
"v_mfma_f32_16x16x32_bf16 v[32:35], a[32:35], a[64:67], v[32:35]\n"
"ds_read_b64_tr_b16 a[128:129], v18 offset:16384\n"
"v_mfma_f32_16x16x32_bf16 v[64:67], a[36:39], a[64:67], v[64:67]\n"
"ds_read_b64_tr_b16 a[130:131], v18 offset:18432\n"
"v_mfma_f32_16x16x32_bf16 v[96:99], a[40:43], a[64:67], v[96:99]\n"
"ds_read_b128 a[96:99], v17\n"
"v_mfma_f32_16x16x32_bf16 v[128:131], a[44:47], a[64:67], v[128:131]\n"
"ds_read_b128 a[100:103], v17 offset:2048\n"
"v_mfma_f32_16x16x32_bf16 v[160:163], a[48:51], a[64:67], v[160:163]\n"
"ds_read_b128 a[104:107], v17 offset:4096\n"
"v_mfma_f32_16x16x32_bf16 v[192:195], a[52:55], a[64:67], v[192:195]\n"
"ds_read_b128 a[108:111], v17 offset:6144\n"
"v_mfma_f32_16x16x32_bf16 v[224:227], a[56:59], a[64:67], v[224:227]\n"
"ds_read_b128 a[112:115], v17 offset:8192\n"
"s_waitcnt lgkmcnt(15)\n"
"v_mfma_f32_16x16x32_bf16 a[0:3], a[60:63], a[64:67], a[0:3]\n"
"ds_read_b128 a[116:119], v17 offset:10240\n"
"v_mfma_f32_16x16x32_bf16 v[36:39], a[32:35], a[68:71], v[36:39]\n"
"ds_read_b128 a[120:123], v17 offset:12288\n"
"v_mfma_f32_16x16x32_bf16 v[68:71], a[36:39], a[68:71], v[68:71]\n"
"ds_read_b128 a[124:127], v17 offset:14336\n"
"v_mfma_f32_16x16x32_bf16 v[100:103], a[40:43], a[68:71], v[100:103]\n"
"ds_read_b64_tr_b16 a[132:133], v19 offset:16384\n"
"v_mfma_f32_16x16x32_bf16 v[132:135], a[44:47], a[68:71], v[132:135]\n"
"ds_read_b64_tr_b16 a[134:135], v19 offset:18432\n"
"v_mfma_f32_16x16x32_bf16 v[164:167], a[48:51], a[68:71], v[164:167]\n"
"ds_read_b64_tr_b16 a[136:137], v20 offset:16384\n"
"v_mfma_f32_16x16x32_bf16 v[196:199], a[52:55], a[68:71], v[196:199]\n"
"ds_read_b64_tr_b16 a[138:139], v20 offset:18432\n"
"v_mfma_f32_16x16x32_bf16 v[228:231], a[56:59], a[68:71], v[228:231]\n"
"ds_read_b64_tr_b16 a[140:141], v21 offset:16384\n"
"v_mfma_f32_16x16x32_bf16 a[4:7], a[60:63], a[68:71], a[4:7]\n"
"ds_read_b64_tr_b16 a[142:143], v21 offset:18432\n"
"v_mfma_f32_16x16x32_bf16 v[40:43], a[32:35], a[72:75], v[40:43]\n"
"ds_read_b64_tr_b16 a[144:145], v22 offset:16384\n"
"v_mfma_f32_16x16x32_bf16 v[72:75], a[36:39], a[72:75], v[72:75]\n"
"ds_read_b64_tr_b16 a[146:147], v22 offset:18432\n"
"v_mfma_f32_16x16x32_bf16 v[104:107], a[40:43], a[72:75], v[104:107]\n"
"ds_read_b64_tr_b16 a[148:149], v23 offset:16384\n"
"v_mfma_f32_16x16x32_bf16 v[136:139], a[44:47], a[72:75], v[136:139]\n"
"ds_read_b64_tr_b16 a[150:151], v23 offset:18432\n"
"v_mfma_f32_16x16x32_bf16 v[168:171], a[48:51], a[72:75], v[168:171]\n"
"ds_read_b64_tr_b16 a[152:153], v24 offset:16384\n"
"v_mfma_f32_16x16x32_bf16 v[200:203], a[52:55], a[72:75], v[200:203]\n"
"ds_read_b64_tr_b16 a[154:155], v24 offset:18432\n"
"v_mfma_f32_16x16x32_bf16 v[232:235], a[56:59], a[72:75], v[232:235]\n"
"ds_read_b64_tr_b16 a[156:157], v25 offset:16384\n"
"v_mfma_f32_16x16x32_bf16 a[8:11], a[60:63], a[72:75], a[8:11]\n"
"ds_read_b64_tr_b16 a[158:159], v25 offset:18432\n"
"v_mfma_f32_16x16x32_bf16 v[44:47], a[32:35], a[76:79], v[44:47]\n"
"v_mfma_f32_16x16x32_bf16 v[76:79], a[36:39], a[76:79], v[76:79]\n"
"v_mfma_f32_16x16x32_bf16 v[108:111], a[40:43], a[76:79], v[108:111]\n"
"v_mfma_f32_16x16x32_bf16 v[140:143], a[44:47], a[76:79], v[140:143]\n"
"v_mfma_f32_16x16x32_bf16 v[172:175], a[48:51], a[76:79], v[172:175]\n"
"v_mfma_f32_16x16x32_bf16 v[204:207], a[52:55], a[76:79], v[204:207]\n"
"v_mfma_f32_16x16x32_bf16 v[236:239], a[56:59], a[76:79], v[236:239]\n"
"v_mfma_f32_16x16x32_bf16 a[12:15], a[60:63], a[76:79], a[12:15]\n"
"s_waitcnt lgkmcnt(15)\n"
"v_mfma_f32_16x16x32_bf16 v[48:51], a[32:35], a[80:83], v[48:51]\n"
"v_mfma_f32_16x16x32_bf16 v[80:83], a[36:39], a[80:83], v[80:83]\n"
"v_mfma_f32_16x16x32_bf16 v[112:115], a[40:43], a[80:83], v[112:115]\n"
"v_mfma_f32_16x16x32_bf16 v[144:147], a[44:47], a[80:83], v[144:147]\n"
"v_mfma_f32_16x16x32_bf16 v[176:179], a[48:51], a[80:83], v[176:179]\n"
"v_mfma_f32_16x16x32_bf16 v[208:211], a[52:55], a[80:83], v[208:211]\n"
"v_mfma_f32_16x16x32_bf16 v[240:243], a[56:59], a[80:83], v[240:243]\n"
"v_mfma_f32_16x16x32_bf16 a[16:19], a[60:63], a[80:83], a[16:19]\n"
"v_mfma_f32_16x16x32_bf16 v[52:55], a[32:35], a[84:87], v[52:55]\n"
"v_mfma_f32_16x16x32_bf16 v[84:87], a[36:39], a[84:87], v[84:87]\n"
"v_mfma_f32_16x16x32_bf16 v[116:119], a[40:43], a[84:87], v[116:119]\n"
"v_mfma_f32_16x16x32_bf16 v[148:151], a[44:47], a[84:87], v[148:151]\n"
"v_mfma_f32_16x16x32_bf16 v[180:183], a[48:51], a[84:87], v[180:183]\n"
"s_mov_b32 s66, s65\n"
"v_mfma_f32_16x16x32_bf16 v[212:215], a[52:55], a[84:87], v[212:215]\n"
"s_mov_b32 s67, s64\n"
"v_mfma_f32_16x16x32_bf16 v[244:247], a[56:59], a[84:87], v[244:247]\n"
"s_add_u32 s65, s64, 0x8000\n"
"v_mfma_f32_16x16x32_bf16 a[20:23], a[60:63], a[84:87], a[20:23]\n"
"s_cmp_eq_u32 s65, 0x28000\n"
"v_mfma_f32_16x16x32_bf16 v[56:59], a[32:35], a[88:91], v[56:59]\n"
"s_cselect_b32 s65, 0, s65\n"
"v_mfma_f32_16x16x32_bf16 v[88:91], a[36:39], a[88:91], v[88:91]\n"
"s_add_u32 s64, s65, 0x8000\n"
"v_mfma_f32_16x16x32_bf16 v[120:123], a[40:43], a[88:91], v[120:123]\n"
"s_cmp_eq_u32 s64, 0x28000\n"
"v_mfma_f32_16x16x32_bf16 v[152:155], a[44:47], a[88:91], v[152:155]\n"
"s_cselect_b32 s64, 0, s64\n"
"v_mfma_f32_16x16x32_bf16 v[184:187], a[48:51], a[88:91], v[184:187]\n"
"v_add_u32 v16, s64, %[lA]\n"
"v_mfma_f32_16x16x32_bf16 v[216:219], a[52:55], a[88:91], v[216:219]\n"
"v_add_u32 v18, s65, %[lB]\n"
"v_mfma_f32_16x16x32_bf16 v[248:251], a[56:59], a[88:91], v[248:251]\n"
"v_xor_b32 v17, 64, v16\n"
"v_mfma_f32_16x16x32_bf16 a[24:27], a[60:63], a[88:91], a[24:27]\n"
"v_xor_b32 v19, 32, v18\n"
"v_mfma_f32_16x16x32_bf16 v[60:63], a[32:35], a[92:95], v[60:63]\n"
"v_xor_b32 v20, 64, v18\n"
"v_mfma_f32_16x16x32_bf16 v[92:95], a[36:39], a[92:95], v[92:95]\n"
"v_xor_b32 v21, 0x60, v18\n"
"v_mfma_f32_16x16x32_bf16 v[124:127], a[40:43], a[92:95], v[124:127]\n"
"v_xor_b32 v22, 0x80, v18\n"
"v_mfma_f32_16x16x32_bf16 v[156:159], a[44:47], a[92:95], v[156:159]\n"
"v_xor_b32 v23, 0xa0, v18\n"
"v_mfma_f32_16x16x32_bf16 v[188:191], a[48:51], a[92:95], v[188:191]\n"
"v_xor_b32 v24, 0xc0, v18\n"
"v_mfma_f32_16x16x32_bf16 v[220:223], a[52:55], a[92:95], v[220:223]\n"
"v_xor_b32 v25, 0xe0, v18\n"
"v_mfma_f32_16x16x32_bf16 v[252:255], a[56:59], a[92:95], v[252:255]\n"
"v_mfma_f32_16x16x32_bf16 a[28:31], a[60:63], a[92:95], a[28:31]\n"
"s_waitcnt lgkmcnt(0)\n"
"s_waitcnt vmcnt(0)\n"
"s_barrier\n"
"v_mfma_f32_16x16x32_bf16 v[32:35], a[96:99], a[128:131], v[32:35]\n"
"ds_read_b64_tr_b16 a[64:65], v18\n"
"v_mfma_f32_16x16x32_bf16 v[64:67], a[100:103], a[128:131], v[64:67]\n"
"ds_read_b64_tr_b16 a[66:67], v18 offset:2048\n"
"v_mfma_f32_16x16x32_bf16 v[96:99], a[104:107], a[128:131], v[96:99]\n"
"ds_read_b128 a[32:35], v16\n"
"v_mfma_f32_16x16x32_bf16 v[128:131], a[108:111], a[128:131], v[128:131]\n"
"ds_read_b128 a[36:39], v16 offset:2048\n"
"v_mfma_f32_16x16x32_bf16 v[160:163], a[112:115], a[128:131], v[160:163]\n"
"ds_read_b128 a[40:43], v16 offset:4096\n"
"v_mfma_f32_16x16x32_bf16 v[192:195], a[116:119], a[128:131], v[192:195]\n"
"ds_read_b128 a[44:47], v16 offset:6144\n"
"v_mfma_f32_16x16x32_bf16 v[224:227], a[120:123], a[128:131], v[224:227]\n"
"ds_read_b128 a[48:51], v16 offset:8192\n"
"v_mfma_f32_16x16x32_bf16 a[0:3], a[124:127], a[128:131], a[0:3]\n"
"ds_read_b128 a[52:55], v16 offset:10240\n"
"v_mfma_f32_16x16x32_bf16 v[36:39], a[96:99], a[132:135], v[36:39]\n"
"ds_read_b128 a[56:59], v16 offset:12288\n"
"v_mfma_f32_16x16x32_bf16 v[68:71], a[100:103], a[132:135], v[68:71]\n"
"ds_read_b128 a[60:63], v16 offset:14336\n"
"v_mfma_f32_16x16x32_bf16 v[100:103], a[104:107], a[132:135], v[100:103]\n"
"ds_read_b64_tr_b16 a[68:69], v19\n"
"v_mfma_f32_16x16x32_bf16 v[132:135], a[108:111], a[132:135], v[132:135]\n"
"ds_read_b64_tr_b16 a[70:71], v19 offset:2048\n"
"v_mfma_f32_16x16x32_bf16 v[164:167], a[112:115], a[132:135], v[164:167]\n"
"ds_read_b64_tr_b16 a[72:73], v20\n"
"v_mfma_f32_16x16x32_bf16 v[196:199], a[116:119], a[132:135], v[196:199]\n"
"ds_read_b64_tr_b16 a[74:75], v20 offset:2048\n"
"v_mfma_f32_16x16x32_bf16 v[228:231], a[120:123], a[132:135], v[228:231]\n"
"ds_read_b64_tr_b16 a[76:77], v21\n"
"v_mfma_f32_16x16x32_bf16 a[4:7], a[124:127], a[132:135], a[4:7]\n"
"ds_read_b64_tr_b16 a[78:79], v21 offset:2048\n"
"v_mfma_f32_16x16x32_bf16 v[40:43], a[96:99], a[136:139], v[40:43]\n"
"ds_read_b64_tr_b16 a[80:81], v22\n"
"v_mfma_f32_16x16x32_bf16 v[72:75], a[100:103], a[136:139], v[72:75]\n"
"ds_read_b64_tr_b16 a[82:83], v22 offset:2048\n"
"v_mfma_f32_16x16x32_bf16 v[104:107], a[104:107], a[136:139], v[104:107]\n"
"ds_read_b64_tr_b16 a[84:85], v23\n"
"v_mfma_f32_16x16x32_bf16 v[136:139], a[108:111], a[136:139], v[136:139]\n"
"ds_read_b64_tr_b16 a[86:87], v23 offset:2048\n"
"v_mfma_f32_16x16x32_bf16 v[168:171], a[112:115], a[136:139], v[168:171]\n"
"ds_read_b64_tr_b16 a[88:89], v24\n"
"v_mfma_f32_16x16x32_bf16 v[200:203], a[116:119], a[136:139], v[200:203]\n"
"ds_read_b64_tr_b16 a[90:91], v24 offset:2048\n"
"v_mfma_f32_16x16x32_bf16 v[232:235], a[120:123], a[136:139], v[232:235]\n"
"ds_read_b64_tr_b16 a[92:93], v25\n"
"v_mfma_f32_16x16x32_bf16 a[8:11], a[124:127], a[136:139], a[8:11]\n"
"ds_read_b64_tr_b16 a[94:95], v25 offset:2048\n"
"v_mfma_f32_16x16x32_bf16 v[44:47], a[96:99], a[140:143], v[44:47]\n"
"v_mfma_f32_16x16x32_bf16 v[76:79], a[100:103], a[140:143], v[76:79]\n"
"v_mfma_f32_16x16x32_bf16 v[108:111], a[104:107], a[140:143], v[108:111]\n"
"v_mfma_f32_16x16x32_bf16 v[140:143], a[108:111], a[140:143], v[140:143]\n"
"v_mfma_f32_16x16x32_bf16 v[172:175], a[112:115], a[140:143], v[172:175]\n"
"v_mfma_f32_16x16x32_bf16 v[204:207], a[116:119], a[140:143], v[204:207]\n"
"v_mfma_f32_16x16x32_bf16 v[236:239], a[120:123], a[140:143], v[236:239]\n"
"buffer_load_dwordx2 v[26:27], %[vpl], %[rsW], 0 offen sc1\n"
"v_mfma_f32_16x16x32_bf16 a[12:15], a[124:127], a[140:143], a[12:15]\n"
"v_mfma_f32_16x16x32_bf16 v[48:51], a[96:99], a[144:147], v[48:51]\n"
"v_mfma_f32_16x16x32_bf16 v[80:83], a[100:103], a[144:147], v[80:83]\n"
"v_mfma_f32_16x16x32_bf16 v[112:115], a[104:107], a[144:147], v[112:115]\n"
"v_mfma_f32_16x16x32_bf16 v[144:147], a[108:111], a[144:147], v[144:147]\n"
"v_mfma_f32_16x16x32_bf16 v[176:179], a[112:115], a[144:147], v[176:179]\n"
"v_mfma_f32_16x16x32_bf16 v[208:211], a[116:119], a[144:147], v[208:211]\n"
"v_mfma_f32_16x16x32_bf16 v[240:243], a[120:123], a[144:147], v[240:243]\n"
"v_mfma_f32_16x16x32_bf16 a[16:19], a[124:127], a[144:147], a[16:19]\n"
"v_mfma_f32_16x16x32_bf16 v[52:55], a[96:99], a[148:151], v[52:55]\n"
"v_mfma_f32_16x16x32_bf16 v[84:87], a[100:103], a[148:151], v[84:87]\n"
"v_mfma_f32_16x16x32_bf16 v[116:119], a[104:107], a[148:151], v[116:119]\n"
"v_mfma_f32_16x16x32_bf16 v[148:151], a[108:111], a[148:151], v[148:151]\n"
"v_mfma_f32_16x16x32_bf16 v[180:183], a[112:115], a[148:151], v[180:183]\n"
"v_mfma_f32_16x16x32_bf16 v[212:215], a[116:119], a[148:151], v[212:215]\n"
"v_mfma_f32_16x16x32_bf16 v[244:247], a[120:123], a[148:151], v[244:247]\n"
"v_mfma_f32_16x16x32_bf16 a[20:23], a[124:127], a[148:151], a[20:23]\n"
"v_mfma_f32_16x16x32_bf16 v[56:59], a[96:99], a[152:155], v[56:59]\n"
"v_mfma_f32_16x16x32_bf16 v[88:91], a[100:103], a[152:155], v[88:91]\n"
"v_mfma_f32_16x16x32_bf16 v[120:123], a[104:107], a[152:155], v[120:123]\n"
"v_mfma_f32_16x16x32_bf16 v[152:155], a[108:111], a[152:155], v[152:155]\n"
"v_mfma_f32_16x16x32_bf16 v[184:187], a[112:115], a[152:155], v[184:187]\n"
"v_mfma_f32_16x16x32_bf16 v[216:219], a[116:119], a[152:155], v[216:219]\n"
"v_mfma_f32_16x16x32_bf16 v[248:251], a[120:123], a[152:155], v[248:251]\n"
"v_mfma_f32_16x16x32_bf16 a[24:27], a[124:127], a[152:155], a[24:27]\n"
"v_mfma_f32_16x16x32_bf16 v[60:63], a[96:99], a[156:159], v[60:63]\n"
"v_mfma_f32_16x16x32_bf16 v[92:95], a[100:103], a[156:159], v[92:95]\n"
"v_mfma_f32_16x16x32_bf16 v[124:127], a[104:107], a[156:159], v[124:127]\n"
"v_mfma_f32_16x16x32_bf16 v[156:159], a[108:111], a[156:159], v[156:159]\n"
"v_mfma_f32_16x16x32_bf16 v[188:191], a[112:115], a[156:159], v[188:191]\n"
"v_mfma_f32_16x16x32_bf16 v[220:223], a[116:119], a[156:159], v[220:223]\n"
"v_mfma_f32_16x16x32_bf16 v[252:255], a[120:123], a[156:159], v[252:255]\n"
"v_mfma_f32_16x16x32_bf16 a[28:31], a[124:127], a[156:159], a[28:31]\n"
"s_waitcnt lgkmcnt(15)\n"
"v_mfma_f32_16x16x32_bf16 v[32:35], a[32:35], a[64:67], v[32:35]\n"
"ds_read_b64_tr_b16 a[128:129], v18 offset:16384\n"
"v_mfma_f32_16x16x32_bf16 v[64:67], a[36:39], a[64:67], v[64:67]\n"
"ds_read_b64_tr_b16 a[130:131], v18 offset:18432\n"
"v_mfma_f32_16x16x32_bf16 v[96:99], a[40:43], a[64:67], v[96:99]\n"
"ds_read_b128 a[96:99], v17\n"
"v_mfma_f32_16x16x32_bf16 v[128:131], a[44:47], a[64:67], v[128:131]\n"
"ds_read_b128 a[100:103], v17 offset:2048\n"
"v_mfma_f32_16x16x32_bf16 v[160:163], a[48:51], a[64:67], v[160:163]\n"
"ds_read_b128 a[104:107], v17 offset:4096\n"
"v_mfma_f32_16x16x32_bf16 v[192:195], a[52:55], a[64:67], v[192:195]\n"
"ds_read_b128 a[108:111], v17 offset:6144\n"
"v_mfma_f32_16x16x32_bf16 v[224:227], a[56:59], a[64:67], v[224:227]\n"
"ds_read_b128 a[112:115], v17 offset:8192\n"
"s_waitcnt lgkmcnt(15)\n"
"v_mfma_f32_16x16x32_bf16 a[0:3], a[60:63], a[64:67], a[0:3]\n"
"ds_read_b128 a[116:119], v17 offset:10240\n"
"v_mfma_f32_16x16x32_bf16 v[36:39], a[32:35], a[68:71], v[36:39]\n"
"ds_read_b128 a[120:123], v17 offset:12288\n"
"v_mfma_f32_16x16x32_bf16 v[68:71], a[36:39], a[68:71], v[68:71]\n"
"ds_read_b128 a[124:127], v17 offset:14336\n"
"v_mfma_f32_16x16x32_bf16 v[100:103], a[40:43], a[68:71], v[100:103]\n"
"ds_read_b64_tr_b16 a[132:133], v19 offset:16384\n"
"v_mfma_f32_16x16x32_bf16 v[132:135], a[44:47], a[68:71], v[132:135]\n"
"ds_read_b64_tr_b16 a[134:135], v19 offset:18432\n"
"v_mfma_f32_16x16x32_bf16 v[164:167], a[48:51], a[68:71], v[164:167]\n"
"ds_read_b64_tr_b16 a[136:137], v20 offset:16384\n"
"v_mfma_f32_16x16x32_bf16 v[196:199], a[52:55], a[68:71], v[196:199]\n"
"ds_read_b64_tr_b16 a[138:139], v20 offset:18432\n"
"v_mfma_f32_16x16x32_bf16 v[228:231], a[56:59], a[68:71], v[228:231]\n"
"ds_read_b64_tr_b16 a[140:141], v21 offset:16384\n"
"v_mfma_f32_16x16x32_bf16 a[4:7], a[60:63], a[68:71], a[4:7]\n"
"ds_read_b64_tr_b16 a[142:143], v21 offset:18432\n"
"v_mfma_f32_16x16x32_bf16 v[40:43], a[32:35], a[72:75], v[40:43]\n"
"ds_read_b64_tr_b16 a[144:145], v22 offset:16384\n"
"v_mfma_f32_16x16x32_bf16 v[72:75], a[36:39], a[72:75], v[72:75]\n"
"ds_read_b64_tr_b16 a[146:147], v22 offset:18432\n"
"v_mfma_f32_16x16x32_bf16 v[104:107], a[40:43], a[72:75], v[104:107]\n"
"ds_read_b64_tr_b16 a[148:149], v23 offset:16384\n"
"v_mfma_f32_16x16x32_bf16 v[136:139], a[44:47], a[72:75], v[136:139]\n"
"ds_read_b64_tr_b16 a[150:151], v23 offset:18432\n"
"v_mfma_f32_16x16x32_bf16 v[168:171], a[48:51], a[72:75], v[168:171]\n"
"ds_read_b64_tr_b16 a[152:153], v24 offset:16384\n"
"v_mfma_f32_16x16x32_bf16 v[200:203], a[52:55], a[72:75], v[200:203]\n"
"ds_read_b64_tr_b16 a[154:155], v24 offset:18432\n"
"v_mfma_f32_16x16x32_bf16 v[232:235], a[56:59], a[72:75], v[232:235]\n"
"ds_read_b64_tr_b16 a[156:157], v25 offset:16384\n"
"v_mfma_f32_16x16x32_bf16 a[8:11], a[60:63], a[72:75], a[8:11]\n"
"ds_read_b64_tr_b16 a[158:159], v25 offset:18432\n"
"v_mfma_f32_16x16x32_bf16 v[44:47], a[32:35], a[76:79], v[44:47]\n"
"v_mfma_f32_16x16x32_bf16 v[76:79], a[36:39], a[76:79], v[76:79]\n"
"v_mfma_f32_16x16x32_bf16 v[108:111], a[40:43], a[76:79], v[108:111]\n"
"v_mfma_f32_16x16x32_bf16 v[140:143], a[44:47], a[76:79], v[140:143]\n"
"v_mfma_f32_16x16x32_bf16 v[172:175], a[48:51], a[76:79], v[172:175]\n"
"v_mfma_f32_16x16x32_bf16 v[204:207], a[52:55], a[76:79], v[204:207]\n"
"v_mfma_f32_16x16x32_bf16 v[236:239], a[56:59], a[76:79], v[236:239]\n"
"v_mfma_f32_16x16x32_bf16 a[12:15], a[60:63], a[76:79], a[12:15]\n"
"s_waitcnt lgkmcnt(15)\n"
"v_mfma_f32_16x16x32_bf16 v[48:51], a[32:35], a[80:83], v[48:51]\n"
"v_mfma_f32_16x16x32_bf16 v[80:83], a[36:39], a[80:83], v[80:83]\n"
"v_mfma_f32_16x16x32_bf16 v[112:115], a[40:43], a[80:83], v[112:115]\n"
"v_mfma_f32_16x16x32_bf16 v[144:147], a[44:47], a[80:83], v[144:147]\n"
"v_mfma_f32_16x16x32_bf16 v[176:179], a[48:51], a[80:83], v[176:179]\n"
"v_mfma_f32_16x16x32_bf16 v[208:211], a[52:55], a[80:83], v[208:211]\n"
"v_mfma_f32_16x16x32_bf16 v[240:243], a[56:59], a[80:83], v[240:243]\n"
"v_mfma_f32_16x16x32_bf16 a[16:19], a[60:63], a[80:83], a[16:19]\n"
"v_mfma_f32_16x16x32_bf16 v[52:55], a[32:35], a[84:87], v[52:55]\n"
"v_mfma_f32_16x16x32_bf16 v[84:87], a[36:39], a[84:87], v[84:87]\n"
"v_mfma_f32_16x16x32_bf16 v[116:119], a[40:43], a[84:87], v[116:119]\n"
"v_mfma_f32_16x16x32_bf16 v[148:151], a[44:47], a[84:87], v[148:151]\n"
"v_mfma_f32_16x16x32_bf16 v[180:183], a[48:51], a[84:87], v[180:183]\n"
"s_mov_b32 s66, s65\n"
"v_mfma_f32_16x16x32_bf16 v[212:215], a[52:55], a[84:87], v[212:215]\n"
"s_mov_b32 s67, s64\n"
"v_mfma_f32_16x16x32_bf16 v[244:247], a[56:59], a[84:87], v[244:247]\n"
"s_add_u32 s65, s64, 0x8000\n"
"v_mfma_f32_16x16x32_bf16 a[20:23], a[60:63], a[84:87], a[20:23]\n"
"s_cmp_eq_u32 s65, 0x28000\n"
"v_mfma_f32_16x16x32_bf16 v[56:59], a[32:35], a[88:91], v[56:59]\n"
"s_cselect_b32 s65, 0, s65\n"
"v_mfma_f32_16x16x32_bf16 v[88:91], a[36:39], a[88:91], v[88:91]\n"
"s_add_u32 s64, s65, 0x8000\n"
"v_mfma_f32_16x16x32_bf16 v[120:123], a[40:43], a[88:91], v[120:123]\n"
"s_cmp_eq_u32 s64, 0x28000\n"
"v_mfma_f32_16x16x32_bf16 v[152:155], a[44:47], a[88:91], v[152:155]\n"
"s_cselect_b32 s64, 0, s64\n"
"v_mfma_f32_16x16x32_bf16 v[184:187], a[48:51], a[88:91], v[184:187]\n"
"v_add_u32 v16, s64, %[lA]\n"
"v_mfma_f32_16x16x32_bf16 v[216:219], a[52:55], a[88:91], v[216:219]\n"
"v_add_u32 v18, s65, %[lB]\n"
"v_mfma_f32_16x16x32_bf16 v[248:251], a[56:59], a[88:91], v[248:251]\n"
"v_xor_b32 v17, 64, v16\n"
"v_mfma_f32_16x16x32_bf16 a[24:27], a[60:63], a[88:91], a[24:27]\n"
"v_xor_b32 v19, 32, v18\n"
"v_mfma_f32_16x16x32_bf16 v[60:63], a[32:35], a[92:95], v[60:63]\n"
"v_xor_b32 v20, 64, v18\n"
"v_mfma_f32_16x16x32_bf16 v[92:95], a[36:39], a[92:95], v[92:95]\n"
"v_xor_b32 v21, 0x60, v18\n"
"v_mfma_f32_16x16x32_bf16 v[124:127], a[40:43], a[92:95], v[124:127]\n"
"v_xor_b32 v22, 0x80, v18\n"
"v_mfma_f32_16x16x32_bf16 v[156:159], a[44:47], a[92:95], v[156:159]\n"
"v_xor_b32 v23, 0xa0, v18\n"
"v_mfma_f32_16x16x32_bf16 v[188:191], a[48:51], a[92:95], v[188:191]\n"
"v_xor_b32 v24, 0xc0, v18\n"
"v_mfma_f32_16x16x32_bf16 v[220:223], a[52:55], a[92:95], v[220:223]\n"
"v_xor_b32 v25, 0xe0, v18\n"
"v_mfma_f32_16x16x32_bf16 v[252:255], a[56:59], a[92:95], v[252:255]\n"
"v_mfma_f32_16x16x32_bf16 a[28:31], a[60:63], a[92:95], a[28:31]\n"
"s_waitcnt lgkmcnt(0)\n"
"s_barrier\n"
"v_mbcnt_lo_u32_b32 v16, -1, 0\n"
"v_mbcnt_hi_u32_b32 v16, -1, v16\n"
"v_lshlrev_b32 v17, 4, v16\n"
"s_waitcnt vmcnt(0)\n"
"v_cmp_ne_u64 vcc, %[tag], v[26:27]\n"
"s_cbranch_vccnz LNWv%=\n"
"LNGv%=:\n"
"v_lshrrev_b32 v21, 4, v16\n"
"v_and_b32 v20, 15, v16\n"
"v_lshlrev_b32 v19, 2, v21\n"
"v_xor_b32 v20, v20, v19\n"
"v_lshlrev_b32 v20, 4, v20\n"
"v_mul_u32_u24 v21, 0xe00, v21\n"
"v_add_u32 v18, v20, v21\n"
"v_lshrrev_b32 v21, 5, v16\n"
"v_and_b32 v20, 31, v16\n"
"v_lshlrev_b32 v23, 2, v21\n"
"v_xor_b32 v20, v20, v23\n"
"v_lshlrev_b32 v20, 4, v20\n"
"v_lshl_add_u32 v22, v21, 13, v20\n"
"v_xor_b32 v23, 0x80, v22\n"
"v_mfma_f32_16x16x32_bf16 v[32:35], a[96:99], a[128:131], v[32:35]\n"
"s_cmp_lg_u32 %[help], 0\n"
"s_cselect_b32 s72, 2, 6\n"
"s_lshl_b32 s72, s72, 14\n"
"s_add_u32 m0, s74, s72\n"
"s_add_u32 s75, %[p0], 0\n"
"buffer_load_dwordx4 v18, %[rsP], s75 offen lds\n"
"v_mfma_f32_16x16x32_bf16 v[64:67], a[100:103], a[128:131], v[64:67]\n"
"v_mfma_f32_16x16x32_bf16 v[96:99], a[104:107], a[128:131], v[96:99]\n"
"s_add_u32 s75, %[p0], 0x3400\n"
"buffer_load_dwordx4 v18, %[rsP], s75 offen offset:1024 lds\n"
"v_mfma_f32_16x16x32_bf16 v[128:131], a[108:111], a[128:131], v[128:131]\n"
"v_mfma_f32_16x16x32_bf16 v[160:163], a[112:115], a[128:131], v[160:163]\n"
"s_add_u32 s75, %[p0], 0x6800\n"
"buffer_load_dwordx4 v18, %[rsP], s75 offen offset:2048 lds\n"
"v_mfma_f32_16x16x32_bf16 v[192:195], a[116:119], a[128:131], v[192:195]\n"
"v_mfma_f32_16x16x32_bf16 v[224:227], a[120:123], a[128:131], v[224:227]\n"
"s_add_u32 s75, %[p0], 0x9c00\n"
"buffer_load_dwordx4 v18, %[rsP], s75 offen offset:3072 lds\n"
"v_mfma_f32_16x16x32_bf16 a[0:3], a[124:127], a[128:131], a[0:3]\n"
"v_mfma_f32_16x16x32_bf16 v[36:39], a[96:99], a[132:135], v[36:39]\n"
"s_add_u32 s75, %[xoff], 0\n"
"buffer_load_dwordx4 a[64:67], v17, %[rsW], s75 offen sc1\n"
"v_mfma_f32_16x16x32_bf16 v[68:71], a[100:103], a[132:135], v[68:71]\n"
"v_cvt_pk_bf16_f32 v32, v32, v33\n"
"v_cvt_pk_bf16_f32 v33, v34, v35\n"
"v_mfma_f32_16x16x32_bf16 v[100:103], a[104:107], a[132:135], v[100:103]\n"
"buffer_load_dwordx4 a[68:71], v17, %[rsW], s75 offen offset:1024 sc1\n"
"v_cvt_pk_bf16_f32 v34, v64, v65\n"
"v_mfma_f32_16x16x32_bf16 v[132:135], a[108:111], a[132:135], v[132:135]\n"
"v_cvt_pk_bf16_f32 v35, v66, v67\n"
"v_cvt_pk_bf16_f32 v96, v96, v97\n"
"v_mfma_f32_16x16x32_bf16 v[164:167], a[112:115], a[132:135], v[164:167]\n"
"buffer_load_dwordx4 a[72:75], v17, %[rsW], s75 offen offset:2048 sc1\n"
"v_cvt_pk_bf16_f32 v97, v98, v99\n"
"v_mfma_f32_16x16x32_bf16 v[196:199], a[116:119], a[132:135], v[196:199]\n"
"v_cvt_pk_bf16_f32 v98, v128, v129\n"
"v_cvt_pk_bf16_f32 v99, v130, v131\n"
"v_mfma_f32_16x16x32_bf16 v[228:231], a[120:123], a[132:135], v[228:231]\n"
"buffer_load_dwordx4 a[76:79], v17, %[rsW], s75 offen offset:3072 sc1\n"
"v_cvt_pk_bf16_f32 v160, v160, v161\n"
"v_mfma_f32_16x16x32_bf16 a[4:7], a[124:127], a[132:135], a[4:7]\n"
"v_cvt_pk_bf16_f32 v161, v162, v163\n"
"v_cvt_pk_bf16_f32 v162, v192, v193\n"
"v_mfma_f32_16x16x32_bf16 v[40:43], a[96:99], a[136:139], v[40:43]\n"
"s_add_u32 s75, %[xoff], 0x1000\n"
"buffer_load_dwordx4 a[80:83], v17, %[rsW], s75 offen sc1\n"
"v_cvt_pk_bf16_f32 v163, v194, v195\n"
"v_mfma_f32_16x16x32_bf16 v[72:75], a[100:103], a[136:139], v[72:75]\n"
"v_accvgpr_read_b32 v64, a0\n"
"v_accvgpr_read_b32 v65, a1\n"
"v_mfma_f32_16x16x32_bf16 v[104:107], a[104:107], a[136:139], v[104:107]\n"
"buffer_load_dwordx4 a[84:87], v17, %[rsW], s75 offen offset:1024 sc1\n"
"v_accvgpr_read_b32 v66, a2\n"
"v_mfma_f32_16x16x32_bf16 v[136:139], a[108:111], a[136:139], v[136:139]\n"
"v_accvgpr_read_b32 v67, a3\n"
"v_cvt_pk_bf16_f32 v224, v224, v225\n"
"v_mfma_f32_16x16x32_bf16 v[168:171], a[112:115], a[136:139], v[168:171]\n"
"buffer_load_dwordx4 a[88:91], v17, %[rsW], s75 offen offset:2048 sc1\n"
"v_cvt_pk_bf16_f32 v225, v226, v227\n"
"v_mfma_f32_16x16x32_bf16 v[200:203], a[116:119], a[136:139], v[200:203]\n"
"v_cvt_pk_bf16_f32 v226, v64, v65\n"
"v_cvt_pk_bf16_f32 v227, v66, v67\n"
"v_mfma_f32_16x16x32_bf16 v[232:235], a[120:123], a[136:139], v[232:235]\n"
"buffer_load_dwordx4 a[92:95], v17, %[rsW], s75 offen offset:3072 sc1\n"
"v_cvt_pk_bf16_f32 v36, v36, v37\n"
"v_mfma_f32_16x16x32_bf16 a[8:11], a[124:127], a[136:139], a[8:11]\n"
"v_cvt_pk_bf16_f32 v37, v38, v39\n"
"v_cvt_pk_bf16_f32 v38, v68, v69\n"
"v_mfma_f32_16x16x32_bf16 v[44:47], a[96:99], a[140:143], v[44:47]\n"
"s_cmp_lg_u32 %[help], 0\n"
"s_cselect_b32 s72, 3, 7\n"
"s_lshl_b32 s72, s72, 14\n"
"s_add_u32 m0, s74, s72\n"
"s_add_u32 s75, %[p0], 0x38000\n"
"buffer_load_dwordx4 v18, %[rsP], s75 offen lds\n"
"v_cvt_pk_bf16_f32 v39, v70, v71\n"
"v_mfma_f32_16x16x32_bf16 v[76:79], a[100:103], a[140:143], v[76:79]\n"
"v_cvt_pk_bf16_f32 v100, v100, v101\n"
"v_cvt_pk_bf16_f32 v101, v102, v103\n"
"v_mfma_f32_16x16x32_bf16 v[108:111], a[104:107], a[140:143], v[108:111]\n"
"s_add_u32 s75, %[p0], 0x3b400\n"
"buffer_load_dwordx4 v18, %[rsP], s75 offen offset:1024 lds\n"
"v_cvt_pk_bf16_f32 v102, v132, v133\n"
"v_mfma_f32_16x16x32_bf16 v[140:143], a[108:111], a[140:143], v[140:143]\n"
"v_cvt_pk_bf16_f32 v103, v134, v135\n"
"v_cvt_pk_bf16_f32 v164, v164, v165\n"
"v_mfma_f32_16x16x32_bf16 v[172:175], a[112:115], a[140:143], v[172:175]\n"
"s_add_u32 s75, %[p0], 0x3e800\n"
"buffer_load_dwordx4 v18, %[rsP], s75 offen offset:2048 lds\n"
"v_cvt_pk_bf16_f32 v165, v166, v167\n"
"v_mfma_f32_16x16x32_bf16 v[204:207], a[116:119], a[140:143], v[204:207]\n"
"v_cvt_pk_bf16_f32 v166, v196, v197\n"
"v_cvt_pk_bf16_f32 v167, v198, v199\n"
"v_mfma_f32_16x16x32_bf16 v[236:239], a[120:123], a[140:143], v[236:239]\n"
"s_add_u32 s75, %[p0], 0x41c00\n"
"buffer_load_dwordx4 v18, %[rsP], s75 offen offset:3072 lds\n"
"v_accvgpr_read_b32 v128, a4\n"
"v_mfma_f32_16x16x32_bf16 a[12:15], a[124:127], a[140:143], a[12:15]\n"
"v_accvgpr_read_b32 v129, a5\n"
"v_accvgpr_read_b32 v130, a6\n"
"v_mfma_f32_16x16x32_bf16 v[48:51], a[96:99], a[144:147], v[48:51]\n"
"s_add_u32 s75, %[xoff], 0x6000\n"
"buffer_load_dwordx4 a[160:163], v17, %[rsW], s75 offen sc1\n"
"v_accvgpr_read_b32 v131, a7\n"
"v_mfma_f32_16x16x32_bf16 v[80:83], a[100:103], a[144:147], v[80:83]\n"
"v_cvt_pk_bf16_f32 v228, v228, v229\n"
"v_cvt_pk_bf16_f32 v229, v230, v231\n"
"v_mfma_f32_16x16x32_bf16 v[112:115], a[104:107], a[144:147], v[112:115]\n"
"buffer_load_dwordx4 a[164:167], v17, %[rsW], s75 offen offset:1024 sc1\n"
"v_cvt_pk_bf16_f32 v230, v128, v129\n"
"v_mfma_f32_16x16x32_bf16 v[144:147], a[108:111], a[144:147], v[144:147]\n"
"v_cvt_pk_bf16_f32 v231, v130, v131\n"
"v_cvt_pk_bf16_f32 v40, v40, v41\n"
"v_mfma_f32_16x16x32_bf16 v[176:179], a[112:115], a[144:147], v[176:179]\n"
"buffer_load_dwordx4 a[168:171], v17, %[rsW], s75 offen offset:2048 sc1\n"
"v_cvt_pk_bf16_f32 v41, v42, v43\n"
"v_mfma_f32_16x16x32_bf16 v[208:211], a[116:119], a[144:147], v[208:211]\n"
"v_cvt_pk_bf16_f32 v42, v72, v73\n"
"v_cvt_pk_bf16_f32 v43, v74, v75\n"
"v_mfma_f32_16x16x32_bf16 v[240:243], a[120:123], a[144:147], v[240:243]\n"
"buffer_load_dwordx4 a[172:175], v17, %[rsW], s75 offen offset:3072 sc1\n"
"v_accvgpr_write_b32 a176, v40\n"
"v_mfma_f32_16x16x32_bf16 a[16:19], a[124:127], a[144:147], a[16:19]\n"
"v_accvgpr_write_b32 a177, v41\n"
"v_accvgpr_write_b32 a178, v42\n"
"v_mfma_f32_16x16x32_bf16 v[52:55], a[96:99], a[148:151], v[52:55]\n"
"s_cmp_lg_u32 %[help], 0\n"
"s_cselect_b32 s72, 4, 8\n"
"s_lshl_b32 s72, s72, 14\n"
"s_add_u32 m0, s74, s72\n"
"s_add_u32 s75, %[p0], 0x70000\n"
"buffer_load_dwordx4 v18, %[rsP], s75 offen lds\n"
"v_accvgpr_write_b32 a179, v43\n"
"v_mfma_f32_16x16x32_bf16 v[84:87], a[100:103], a[148:151], v[84:87]\n"
"v_cvt_pk_bf16_f32 v104, v104, v105\n"
"v_cvt_pk_bf16_f32 v105, v106, v107\n"
"v_mfma_f32_16x16x32_bf16 v[116:119], a[104:107], a[148:151], v[116:119]\n"
"s_add_u32 s75, %[p0], 0x73400\n"
"buffer_load_dwordx4 v18, %[rsP], s75 offen offset:1024 lds\n"
"v_cvt_pk_bf16_f32 v106, v136, v137\n"
"v_mfma_f32_16x16x32_bf16 v[148:151], a[108:111], a[148:151], v[148:151]\n"
"v_cvt_pk_bf16_f32 v107, v138, v139\n"
"v_accvgpr_write_b32 a180, v104\n"
"v_mfma_f32_16x16x32_bf16 v[180:183], a[112:115], a[148:151], v[180:183]\n"
"s_add_u32 s75, %[p0], 0x76800\n"
"buffer_load_dwordx4 v18, %[rsP], s75 offen offset:2048 lds\n"
"v_accvgpr_write_b32 a181, v105\n"
"v_mfma_f32_16x16x32_bf16 v[212:215], a[116:119], a[148:151], v[212:215]\n"
"v_accvgpr_write_b32 a182, v106\n"
"v_accvgpr_write_b32 a183, v107\n"
"v_mfma_f32_16x16x32_bf16 v[244:247], a[120:123], a[148:151], v[244:247]\n"
"s_add_u32 s75, %[p0], 0x79c00\n"
"buffer_load_dwordx4 v18, %[rsP], s75 offen offset:3072 lds\n"
"v_cvt_pk_bf16_f32 v168, v168, v169\n"
"v_mfma_f32_16x16x32_bf16 a[20:23], a[124:127], a[148:151], a[20:23]\n"
"v_cvt_pk_bf16_f32 v169, v170, v171\n"
"v_cvt_pk_bf16_f32 v170, v200, v201\n"
"v_mfma_f32_16x16x32_bf16 v[56:59], a[96:99], a[152:155], v[56:59]\n"
"s_cmp_lg_u32 %[help], 0\n"
"s_cselect_b32 s72, 5, 9\n"
"s_lshl_b32 s72, s72, 14\n"
"s_add_u32 m0, s74, s72\n"
"s_add_u32 s75, %[p0], 0xa8000\n"
"buffer_load_dwordx4 v18, %[rsP], s75 offen lds\n"
"v_cvt_pk_bf16_f32 v171, v202, v203\n"
"v_mfma_f32_16x16x32_bf16 v[88:91], a[100:103], a[152:155], v[88:91]\n"
"v_accvgpr_write_b32 a184, v168\n"
"v_accvgpr_write_b32 a185, v169\n"
"v_mfma_f32_16x16x32_bf16 v[120:123], a[104:107], a[152:155], v[120:123]\n"
"s_add_u32 s75, %[p0], 0xab400\n"
"buffer_load_dwordx4 v18, %[rsP], s75 offen offset:1024 lds\n"
"v_accvgpr_write_b32 a186, v170\n"
"v_mfma_f32_16x16x32_bf16 v[152:155], a[108:111], a[152:155], v[152:155]\n"
"v_accvgpr_write_b32 a187, v171\n"
"v_accvgpr_read_b32 v192, a8\n"
"v_mfma_f32_16x16x32_bf16 v[184:187], a[112:115], a[152:155], v[184:187]\n"
"s_add_u32 s75, %[p0], 0xae800\n"
"buffer_load_dwordx4 v18, %[rsP], s75 offen offset:2048 lds\n"
"v_accvgpr_read_b32 v193, a9\n"
"v_mfma_f32_16x16x32_bf16 v[216:219], a[116:119], a[152:155], v[216:219]\n"
"v_accvgpr_read_b32 v194, a10\n"
"v_accvgpr_read_b32 v195, a11\n"
"v_mfma_f32_16x16x32_bf16 v[248:251], a[120:123], a[152:155], v[248:251]\n"
"s_add_u32 s75, %[p0], 0xb1c00\n"
"buffer_load_dwordx4 v18, %[rsP], s75 offen offset:3072 lds\n"
"v_cvt_pk_bf16_f32 v232, v232, v233\n"
"v_mfma_f32_16x16x32_bf16 a[24:27], a[124:127], a[152:155], a[24:27]\n"
"v_cvt_pk_bf16_f32 v233, v234, v235\n"
"v_cvt_pk_bf16_f32 v234, v192, v193\n"
"v_mfma_f32_16x16x32_bf16 v[60:63], a[96:99], a[156:159], v[60:63]\n"
"s_cmp_lg_u32 %[help], 0\n"
"s_cselect_b32 s72, 6, 0\n"
"s_lshl_b32 s72, s72, 14\n"
"s_add_u32 m0, s74, s72\n"
"s_add_u32 s75, %[p0], 0xe0000\n"
"buffer_load_dwordx4 v18, %[rsP], s75 offen lds\n"
"v_cvt_pk_bf16_f32 v235, v194, v195\n"
"v_mfma_f32_16x16x32_bf16 v[92:95], a[100:103], a[156:159], v[92:95]\n"
"v_accvgpr_write_b32 a188, v232\n"
"v_accvgpr_write_b32 a189, v233\n"
"v_mfma_f32_16x16x32_bf16 v[124:127], a[104:107], a[156:159], v[124:127]\n"
"s_add_u32 s75, %[p0], 0xe3400\n"
"buffer_load_dwordx4 v18, %[rsP], s75 offen offset:1024 lds\n"
"v_accvgpr_write_b32 a190, v234\n"
"v_mfma_f32_16x16x32_bf16 v[156:159], a[108:111], a[156:159], v[156:159]\n"
"v_accvgpr_write_b32 a191, v235\n"
"v_cvt_pk_bf16_f32 v44, v44, v45\n"
"v_mfma_f32_16x16x32_bf16 v[188:191], a[112:115], a[156:159], v[188:191]\n"
"s_add_u32 s75, %[p0], 0xe6800\n"
"buffer_load_dwordx4 v18, %[rsP], s75 offen offset:2048 lds\n"
"v_cvt_pk_bf16_f32 v45, v46, v47\n"
"v_mfma_f32_16x16x32_bf16 v[220:223], a[116:119], a[156:159], v[220:223]\n"
"v_cvt_pk_bf16_f32 v46, v76, v77\n"
"v_cvt_pk_bf16_f32 v47, v78, v79\n"
"v_mfma_f32_16x16x32_bf16 v[252:255], a[120:123], a[156:159], v[252:255]\n"
"s_add_u32 s75, %[p0], 0xe9c00\n"
"buffer_load_dwordx4 v18, %[rsP], s75 offen offset:3072 lds\n"
"v_accvgpr_write_b32 a192, v44\n"
"v_mfma_f32_16x16x32_bf16 a[28:31], a[124:127], a[156:159], a[28:31]\n"
"v_accvgpr_write_b32 a193, v45\n"
"v_accvgpr_write_b32 a194, v46\n"
"s_cmp_lg_u32 %[help], 0\n"
"s_cselect_b32 s72, 7, 1\n"
"s_lshl_b32 s72, s72, 14\n"
"s_add_u32 m0, s74, s72\n"
"s_add_u32 s75, %[p0], 0x118000\n"
"buffer_load_dwordx4 v18, %[rsP], s75 offen lds\n"
"v_accvgpr_write_b32 a195, v47\n"
"v_cvt_pk_bf16_f32 v108, v108, v109\n"
"v_cvt_pk_bf16_f32 v109, v110, v111\n"
"v_cvt_pk_bf16_f32 v110, v140, v141\n"
"v_cvt_pk_bf16_f32 v111, v142, v143\n"
"s_add_u32 s75, %[p0], 0x11b400\n"
"buffer_load_dwordx4 v18, %[rsP], s75 offen offset:1024 lds\n"
"v_accvgpr_write_b32 a196, v108\n"
"v_accvgpr_write_b32 a197, v109\n"
"v_accvgpr_write_b32 a198, v110\n"
"v_accvgpr_write_b32 a199, v111\n"
"v_cvt_pk_bf16_f32 v172, v172, v173\n"
"s_add_u32 s75, %[p0], 0x11e800\n"
"buffer_load_dwordx4 v18, %[rsP], s75 offen offset:2048 lds\n"
"v_cvt_pk_bf16_f32 v173, v174, v175\n"
"v_cvt_pk_bf16_f32 v174, v204, v205\n"
"v_cvt_pk_bf16_f32 v175, v206, v207\n"
"v_accvgpr_write_b32 a200, v172\n"
"v_accvgpr_write_b32 a201, v173\n"
"s_add_u32 s75, %[p0], 0x121c00\n"
"buffer_load_dwordx4 v18, %[rsP], s75 offen offset:3072 lds\n"
"v_accvgpr_write_b32 a202, v174\n"
"v_accvgpr_write_b32 a203, v175\n"
"v_accvgpr_read_b32 v64, a12\n"
"v_accvgpr_read_b32 v65, a13\n"
"v_accvgpr_read_b32 v66, a14\n"
"s_cmp_lg_u32 %[help], 0\n"
"s_cselect_b32 s72, 8, 2\n"
"s_lshl_b32 s72, s72, 14\n"
"s_add_u32 m0, s74, s72\n"
"s_add_u32 s75, %[p0], 0x150000\n"
"buffer_load_dwordx4 v18, %[rsP], s75 offen lds\n"
"v_accvgpr_read_b32 v67, a15\n"
"v_cvt_pk_bf16_f32 v236, v236, v237\n"
"v_cvt_pk_bf16_f32 v237, v238, v239\n"
"v_cvt_pk_bf16_f32 v238, v64, v65\n"
"v_cvt_pk_bf16_f32 v239, v66, v67\n"
"s_add_u32 s75, %[p0], 0x153400\n"
"buffer_load_dwordx4 v18, %[rsP], s75 offen offset:1024 lds\n"
"v_accvgpr_write_b32 a204, v236\n"
"v_accvgpr_write_b32 a205, v237\n"
"v_accvgpr_write_b32 a206, v238\n"
"v_accvgpr_write_b32 a207, v239\n"
"v_cvt_pk_bf16_f32 v48, v48, v49\n"
"s_add_u32 s75, %[p0], 0x156800\n"
"buffer_load_dwordx4 v18, %[rsP], s75 offen offset:2048 lds\n"
"v_cvt_pk_bf16_f32 v49, v50, v51\n"
"v_cvt_pk_bf16_f32 v50, v80, v81\n"
"v_cvt_pk_bf16_f32 v51, v82, v83\n"
"v_accvgpr_write_b32 a208, v48\n"
"v_accvgpr_write_b32 a209, v49\n"
"s_add_u32 s75, %[p0], 0x159c00\n"
"buffer_load_dwordx4 v18, %[rsP], s75 offen offset:3072 lds\n"
"v_accvgpr_write_b32 a210, v50\n"
"v_accvgpr_write_b32 a211, v51\n"
"v_cvt_pk_bf16_f32 v112, v112, v113\n"
"v_cvt_pk_bf16_f32 v113, v114, v115\n"
"v_cvt_pk_bf16_f32 v114, v144, v145\n"
"s_cmp_lg_u32 %[help], 0\n"
"s_cselect_b32 s72, 0, 4\n"
"s_lshl_b32 s72, s72, 14\n"
"s_add_u32 m0, %[wa], s72\n"
"s_add_u32 s75, %[q0], 0\n"
"buffer_load_dwordx4 v22, %[rsQ], s75 offen lds\n"
"v_cvt_pk_bf16_f32 v115, v146, v147\n"
"v_accvgpr_write_b32 a212, v112\n"
"v_accvgpr_write_b32 a213, v113\n"
"v_accvgpr_write_b32 a214, v114\n"
"v_accvgpr_write_b32 a215, v115\n"
"s_add_u32 s75, %[q0], 0x3c00\n"
"buffer_load_dwordx4 v23, %[rsQ], s75 offen offset:1024 lds\n"
"v_cvt_pk_bf16_f32 v176, v176, v177\n"
"v_cvt_pk_bf16_f32 v177, v178, v179\n"
"v_cvt_pk_bf16_f32 v178, v208, v209\n"
"v_cvt_pk_bf16_f32 v179, v210, v211\n"
"v_accvgpr_write_b32 a216, v176\n"
"s_add_u32 s75, %[q0], 0x7800\n"
"buffer_load_dwordx4 v22, %[rsQ], s75 offen offset:2048 lds\n"
"v_accvgpr_write_b32 a217, v177\n"
"v_accvgpr_write_b32 a218, v178\n"
"v_accvgpr_write_b32 a219, v179\n"
"v_accvgpr_read_b32 v68, a16\n"
"v_accvgpr_read_b32 v69, a17\n"
"s_add_u32 s75, %[q0], 0xb400\n"
"buffer_load_dwordx4 v23, %[rsQ], s75 offen offset:3072 lds\n"
"v_accvgpr_read_b32 v70, a18\n"
"v_accvgpr_read_b32 v71, a19\n"
"v_cvt_pk_bf16_f32 v240, v240, v241\n"
"v_cvt_pk_bf16_f32 v241, v242, v243\n"
"v_cvt_pk_bf16_f32 v242, v68, v69\n"
"s_add_u32 m0, m0, 0x1000\n"
"s_add_u32 s75, %[q0], 0x10000\n"
"buffer_load_dwordx4 v22, %[rsQ], s75 offen lds\n"
"v_cvt_pk_bf16_f32 v243, v70, v71\n"
"v_accvgpr_write_b32 a220, v240\n"
"v_accvgpr_write_b32 a221, v241\n"
"v_accvgpr_write_b32 a222, v242\n"
"v_accvgpr_write_b32 a223, v243\n"
"s_add_u32 s75, %[q0], 0x13c00\n"
"buffer_load_dwordx4 v23, %[rsQ], s75 offen offset:1024 lds\n"
"v_cvt_pk_bf16_f32 v52, v52, v53\n"
"v_cvt_pk_bf16_f32 v53, v54, v55\n"
"v_cvt_pk_bf16_f32 v54, v84, v85\n"
"v_cvt_pk_bf16_f32 v55, v86, v87\n"
"v_accvgpr_write_b32 a224, v52\n"
"s_add_u32 s75, %[q0], 0x17800\n"
"buffer_load_dwordx4 v22, %[rsQ], s75 offen offset:2048 lds\n"
"v_accvgpr_write_b32 a225, v53\n"
"v_accvgpr_write_b32 a226, v54\n"
"v_accvgpr_write_b32 a227, v55\n"
"v_cvt_pk_bf16_f32 v116, v116, v117\n"
"v_cvt_pk_bf16_f32 v117, v118, v119\n"
"s_add_u32 s75, %[q0], 0x1b400\n"
"buffer_load_dwordx4 v23, %[rsQ], s75 offen offset:3072 lds\n"
"v_cvt_pk_bf16_f32 v118, v148, v149\n"
"v_cvt_pk_bf16_f32 v119, v150, v151\n"
"v_accvgpr_write_b32 a228, v116\n"
"v_accvgpr_write_b32 a229, v117\n"
"v_accvgpr_write_b32 a230, v118\n"
"s_add_u32 s75, %[xoff], 0x2000\n"
"buffer_load_dwordx4 a[96:99], v17, %[rsW], s75 offen sc1\n"
"v_accvgpr_write_b32 a231, v119\n"
"v_cvt_pk_bf16_f32 v180, v180, v181\n"
"v_cvt_pk_bf16_f32 v181, v182, v183\n"
"v_cvt_pk_bf16_f32 v182, v212, v213\n"
"v_cvt_pk_bf16_f32 v183, v214, v215\n"
"buffer_load_dwordx4 a[100:103], v17, %[rsW], s75 offen offset:1024 sc1\n"
"v_accvgpr_write_b32 a232, v180\n"
"v_accvgpr_write_b32 a233, v181\n"
"v_accvgpr_write_b32 a234, v182\n"
"v_accvgpr_write_b32 a235, v183\n"
"v_accvgpr_read_b32 v132, a20\n"
"buffer_load_dwordx4 a[104:107], v17, %[rsW], s75 offen offset:2048 sc1\n"
"v_accvgpr_read_b32 v133, a21\n"
"v_accvgpr_read_b32 v134, a22\n"
"v_accvgpr_read_b32 v135, a23\n"
"v_cvt_pk_bf16_f32 v244, v244, v245\n"
"v_cvt_pk_bf16_f32 v245, v246, v247\n"
"buffer_load_dwordx4 a[108:111], v17, %[rsW], s75 offen offset:3072 sc1\n"
"v_cvt_pk_bf16_f32 v246, v132, v133\n"
"v_cvt_pk_bf16_f32 v247, v134, v135\n"
"v_accvgpr_write_b32 a236, v244\n"
"v_accvgpr_write_b32 a237, v245\n"
"v_accvgpr_write_b32 a238, v246\n"
"s_add_u32 s75, %[xoff], 0x3000\n"
"buffer_load_dwordx4 a[112:115], v17, %[rsW], s75 offen sc1\n"
"v_accvgpr_write_b32 a239, v247\n"
"v_cvt_pk_bf16_f32 v56, v56, v57\n"
"v_cvt_pk_bf16_f32 v57, v58, v59\n"
"v_cvt_pk_bf16_f32 v58, v88, v89\n"
"v_cvt_pk_bf16_f32 v59, v90, v91\n"
"buffer_load_dwordx4 a[116:119], v17, %[rsW], s75 offen offset:1024 sc1\n"
"v_accvgpr_write_b32 a32, v56\n"
"v_accvgpr_write_b32 a33, v57\n"
"v_accvgpr_write_b32 a34, v58\n"
"v_accvgpr_write_b32 a35, v59\n"
"v_cvt_pk_bf16_f32 v120, v120, v121\n"
"buffer_load_dwordx4 a[120:123], v17, %[rsW], s75 offen offset:2048 sc1\n"
"v_cvt_pk_bf16_f32 v121, v122, v123\n"
"v_cvt_pk_bf16_f32 v122, v152, v153\n"
"v_cvt_pk_bf16_f32 v123, v154, v155\n"
"v_accvgpr_write_b32 a36, v120\n"
"v_accvgpr_write_b32 a37, v121\n"
"buffer_load_dwordx4 a[124:127], v17, %[rsW], s75 offen offset:3072 sc1\n"
"v_accvgpr_write_b32 a38, v122\n"
"v_accvgpr_write_b32 a39, v123\n"
"v_cvt_pk_bf16_f32 v184, v184, v185\n"
"v_cvt_pk_bf16_f32 v185, v186, v187\n"
"v_cvt_pk_bf16_f32 v186, v216, v217\n"
"s_add_u32 s75, %[xoff], 0x4000\n"
"buffer_load_dwordx4 a[128:131], v17, %[rsW], s75 offen sc1\n"
"v_cvt_pk_bf16_f32 v187, v218, v219\n"
"v_accvgpr_write_b32 a40, v184\n"
"v_accvgpr_write_b32 a41, v185\n"
"v_accvgpr_write_b32 a42, v186\n"
"v_accvgpr_write_b32 a43, v187\n"
"buffer_load_dwordx4 a[132:135], v17, %[rsW], s75 offen offset:1024 sc1\n"
"v_accvgpr_read_b32 v196, a24\n"
"v_accvgpr_read_b32 v197, a25\n"
"v_accvgpr_read_b32 v198, a26\n"
"v_accvgpr_read_b32 v199, a27\n"
"v_cvt_pk_bf16_f32 v248, v248, v249\n"
"buffer_load_dwordx4 a[136:139], v17, %[rsW], s75 offen offset:2048 sc1\n"
"v_cvt_pk_bf16_f32 v249, v250, v251\n"
"v_cvt_pk_bf16_f32 v250, v196, v197\n"
"v_cvt_pk_bf16_f32 v251, v198, v199\n"
"v_accvgpr_write_b32 a44, v248\n"
"v_accvgpr_write_b32 a45, v249\n"
"buffer_load_dwordx4 a[140:143], v17, %[rsW], s75 offen offset:3072 sc1\n"
"v_accvgpr_write_b32 a46, v250\n"
"v_accvgpr_write_b32 a47, v251\n"
"v_cvt_pk_bf16_f32 v60, v60, v61\n"
"v_cvt_pk_bf16_f32 v61, v62, v63\n"
"v_cvt_pk_bf16_f32 v62, v92, v93\n"
"s_add_u32 s75, %[xoff], 0x5000\n"
"buffer_load_dwordx4 a[144:147], v17, %[rsW], s75 offen sc1\n"
"v_cvt_pk_bf16_f32 v63, v94, v95\n"
"v_accvgpr_write_b32 a48, v60\n"
"v_accvgpr_write_b32 a49, v61\n"
"v_accvgpr_write_b32 a50, v62\n"
"v_accvgpr_write_b32 a51, v63\n"
"buffer_load_dwordx4 a[148:151], v17, %[rsW], s75 offen offset:1024 sc1\n"
"v_cvt_pk_bf16_f32 v124, v124, v125\n"
"v_cvt_pk_bf16_f32 v125, v126, v127\n"
"v_cvt_pk_bf16_f32 v126, v156, v157\n"
"v_cvt_pk_bf16_f32 v127, v158, v159\n"
"v_accvgpr_write_b32 a52, v124\n"
"buffer_load_dwordx4 a[152:155], v17, %[rsW], s75 offen offset:2048 sc1\n"
"v_accvgpr_write_b32 a53, v125\n"
"v_accvgpr_write_b32 a54, v126\n"
"v_accvgpr_write_b32 a55, v127\n"
"v_cvt_pk_bf16_f32 v188, v188, v189\n"
"v_cvt_pk_bf16_f32 v189, v190, v191\n"
"buffer_load_dwordx4 a[156:159], v17, %[rsW], s75 offen offset:3072 sc1\n"
"v_cvt_pk_bf16_f32 v190, v220, v221\n"
"v_cvt_pk_bf16_f32 v191, v222, v223\n"
"v_accvgpr_write_b32 a56, v188\n"
"v_accvgpr_write_b32 a57, v189\n"
"v_accvgpr_write_b32 a58, v190\n"
"s_add_u32 s70, %[out], 0\n"
"buffer_store_dwordx4 v[32:35], %[vst], %[rsW], s70 offen sc1\n"
"v_accvgpr_write_b32 a59, v191\n"
"v_accvgpr_read_b32 v128, a28\n"
"v_accvgpr_read_b32 v129, a29\n"
"v_accvgpr_read_b32 v130, a30\n"
"v_accvgpr_read_b32 v131, a31\n"
"s_add_u32 s70, %[out], 0x4000\n"
"buffer_store_dwordx4 v[96:99], %[vst], %[rsW], s70 offen sc1\n"
"v_cvt_pk_bf16_f32 v252, v252, v253\n"
"v_cvt_pk_bf16_f32 v253, v254, v255\n"
"v_cvt_pk_bf16_f32 v254, v128, v129\n"
"v_cvt_pk_bf16_f32 v255, v130, v131\n"
"v_accvgpr_write_b32 a60, v252\n"
"s_add_u32 s70, %[out], 0x8000\n"
"buffer_store_dwordx4 v[160:163], %[vst], %[rsW], s70 offen sc1\n"
"v_accvgpr_write_b32 a61, v253\n"
"v_accvgpr_write_b32 a62, v254\n"
"v_accvgpr_write_b32 a63, v255\n"
"s_add_u32 s70, %[out], 0xc000\n"
"buffer_store_dwordx4 v[224:227], %[vst], %[rsW], s70 offen sc1\n"
"s_add_u32 s70, %[out], 0\n"
"buffer_store_dwordx4 v[36:39], %[vst], %[rsW], s70 offen offset:256 sc1\n"
"s_add_u32 s70, %[out], 0x4000\n"
"buffer_store_dwordx4 v[100:103], %[vst], %[rsW], s70 offen offset:256 sc1\n"
"s_add_u32 s70, %[out], 0x8000\n"
"buffer_store_dwordx4 v[164:167], %[vst], %[rsW], s70 offen offset:256 sc1\n"
"s_add_u32 s70, %[out], 0xc000\n"
"buffer_store_dwordx4 v[228:231], %[vst], %[rsW], s70 offen offset:256 sc1\n"
"v_mov_b32 v92, %[vst]\n"
"v_add_u32 v93, 0x100, %[vst]\n"
"s_waitcnt vmcnt(8)\n"
"s_branch K1E%=\n"
"LNFk%=:\n"
"buffer_store_dwordx2 v[28:29], off, %[rsW], %[lnf] sc1\n"
"s_branch LNBk%=\n"
"PFWk%=:\n"
"s_cmp_lt_u32 %[wa], 0x4000\n"
"s_cbranch_scc0 PF1k%=\n"
"buffer_load_dword v27, %[vpw], %[rsQ], 0 offen\n"
"buffer_load_dword v26, %[vpz], %[rsZ], 0 offen\n"
"s_branch PFBk%=\n"
"PF1k%=:\n"
"buffer_load_dword v27, %[vpw], %[rsP], 0 offen\n"
"s_cmp_eq_u32 %[wa], 0x6000\n"
"s_cbranch_scc1 PF2k%=\n"
"buffer_load_dword v26, %[vpz], %[rsZ], 0 offen\n"
"s_branch PFBk%=\n"
"PF2k%=:\n"
"s_getpc_b64 s[76:77]\n"
"kf_pck:\n"
"v_min_u32 v26, kf_end-kf_pck-4, %[vpz]\n"
"global_load_dword v26, v26, s[76:77]\n"
"s_branch PFBk%=\n"
"LNWk%=:\n"
"s_mov_b32 s71, 0\n"
"LNPk%=:\n"
"s_sleep 2\n"
"buffer_load_dwordx2 v[26:27], %[vpl], %[rsW], 0 offen sc1\n"
"s_waitcnt vmcnt(0)\n"
"v_cmp_ne_u64 vcc, %[tag], v[26:27]\n"
"s_add_u32 s71, s71, 1\n"
"s_cmp_lt_u32 s71, 0x100000\n"
"s_cbranch_vccz LNDk%=\n"
"s_cbranch_scc1 LNPk%=\n"
"LNDk%=:\n"
"s_branch LNGk%=\n"
"LNFv%=:\n"
"buffer_store_dwordx2 v[28:29], off, %[rsW], %[lnf] sc1\n"
"s_branch LNBv%=\n"
"PFWv%=:\n"
"s_cmp_lt_u32 %[wa], 0x4000\n"
"s_cbranch_scc0 PF1v%=\n"
"buffer_load_dword v27, %[vpw], %[rsQ], 0 offen\n"
"buffer_load_dword v26, %[vpz], %[rsZ], 0 offen\n"
"s_branch PFBv%=\n"
"PF1v%=:\n"
"buffer_load_dword v27, %[vpw], %[rsP], 0 offen\n"
"s_cmp_eq_u32 %[wa], 0x6000\n"
"s_cbranch_scc1 PF2v%=\n"
"buffer_load_dword v26, %[vpz], %[rsZ], 0 offen\n"
"s_branch PFBv%=\n"
"PF2v%=:\n"
"s_getpc_b64 s[76:77]\n"
"kf_pcv:\n"
"v_min_u32 v26, kf_end-kf_pcv-4, %[vpz]\n"
"global_load_dword v26, v26, s[76:77]\n"
"s_branch PFBv%=\n"
"LNWv%=:\n"
"s_mov_b32 s71, 0\n"
"LNPv%=:\n"
"s_sleep 2\n"
"buffer_load_dwordx2 v[26:27], %[vpl], %[rsW], 0 offen sc1\n"
"s_waitcnt vmcnt(0)\n"
"v_cmp_ne_u64 vcc, %[tag], v[26:27]\n"
"s_add_u32 s71, s71, 1\n"
"s_cmp_lt_u32 s71, 0x100000\n"
"s_cbranch_vccz LNDv%=\n"
"s_cbranch_scc1 LNPv%=\n"
"LNDv%=:\n"
"s_branch LNGv%=\n"
"K1E%=:\n"
    :
    : [rsC] "s"(rsC), [rsB] "s"(rsB), [rsW] "s"(rsW), [ca] "s"(ca), [cb] "s"(cb), [wa] "s"(wa), [wb] "s"(wb), [vt] "s"(vt), [out] "s"(out), [stoff] "s"(stoff),
      [vA0] "v"(vA[0]), [vA1] "v"(vA[1]), [vA2] "v"(vA[2]), [vA3] "v"(vA[3]), [vB0] "v"(vB[0]), [vB1] "v"(vB[1]), [vB2] "v"(vB[2]), [vB3] "v"(vB[3]),
      [lA] "v"(lA), [lB] "v"(lB), [vst] "v"(vst), [vpl] "v"(vpl), [vpz] "v"(vpz), [vpw] "v"(vpw), [rsZ] "s"(rsZ), [rsP] "s"(rsP), [rsQ] "s"(rsQ), [q0] "s"(q0), [p0] "s"(p0), [xoff] "s"(xoff), [help] "s"(help), [tag] "s"(tag), [lnf] "s"(lnf)
    : CL1);
}

  asm volatile("" : "+s"(ap), "+s"(b), "+s"(w));
  {
  const int xcd = b & 7, j = b >> 3, h = 2 * xcd + (j >> 4), pi = (j & 15) >> 1, role = j & 1;
  const int split = (int)((unsigned)(2 * pi + NA + 1 - 32) >> 31);
  const rsrc_t rsQ = mkrsrc(ap->wq, M * H * D * 2), rs1 = mkrsrc(ap->w1, M * F * 2), rsW = mkrsrc(ap->ws, (unsigned)WS_END);
  const int fb0 = (h >> 1) * 7 + (h & 1) * 3;
  const int nseg = 1 + ((role ^ 1) & split), owner = role & split, qo = pi + role * (15 - 2 * pi);
  const unsigned q0 = h * 512 + w * 16 * 8192, f0 = fb0 * 64 + w * 16 * (F * 2);
  const unsigned pair = h * 8 + pi, poff = (unsigned)WS_PO + pair * (unsigned)PO_STRIDE + w * 33 * 1024, flag = WS_PRF + pair * 128 + w * 8;
  const unsigned kvf = WS_KVF + (2 * xcd + ((j >> 3) & 1)) * 512 + (((j >> 4) * 8 + (j & 7)) * 4 + w) * 8, kvl = WS_KVF + h * 512;
  const unsigned vt = j >> 4, stp = 2048 >> vt, out = (unsigned)WS_KF + vt * (unsigned)(WS_VF - WS_KF) + (2 * xcd + ((j >> 3) & 1)) * (S * D * 2) + ((j & 7) * 8 + (w >> 1) * 4) * 16384 + (w & 1) * (8192 >> vt);
#pragma nounroll
  for (int seg = 0; seg < nseg; ++seg) {
    const int helper = (role ^ 1) & split & (seg ^ 1);
    const int qb = qo + helper * (15 - 2 * pi);
    const int t0 = helper * NA;
    const int t1 = 2 * qb + 2 + owner * (NA - 2 * qb - 2);
    const int n = t1 - t0, tm = (2 * qb - t0) & ~((2 * qb - t0) >> 31);
    const unsigned xoff = (unsigned)WS_XF + (qb * 4 + w) * 28 * 1024;
    const unsigned ks = (unsigned)WS_KF + h * (S * D * 2) + t0 * 32768 + w * 8192, vs = ks + (unsigned)(WS_VF - WS_KF);
    const int nk0 = qb * 128 + w * 32 - t0 * 64;
    const unsigned mode = helper + 2 * owner + 3 * seg;
    const unsigned arow = (unsigned)WS_AF + (qo * 4 + w) * (KS3 * 1024);
    const unsigned hoff = arow + (256 + 2 * fb0) * 1024, coff = arow + h * 16 * 1024;
    const unsigned stoff = (unsigned)WS_STAMP + 262144 + ((b * 4 + w) * 16 + seg * 8) * 8;
    asm volatile(
"v_mbcnt_lo_u32_b32 v0, -1, 0\n"
"v_mbcnt_hi_u32_b32 v0, -1, v0\n"
"s_lshl_b32 s74, %[w], 13\n"
"s_lshl_b32 s75, %[w], 12\n"
"v_lshlrev_b32 v1, 4, v0\n"
"v_lshrrev_b32 v10, 5, v0\n"
"v_and_b32 v14, 31, v0\n"
"v_lshlrev_b32 v15, 2, v10\n"
"v_xor_b32 v14, v14, v15\n"
"v_lshlrev_b32 v14, 4, v14\n"
"v_lshl_add_u32 v80, v10, 13, v14\n"
"v_xor_b32 v81, 0x80, v80\n"
"v_lshrrev_b32 v11, 4, v0\n"
"v_and_b32 v14, 15, v0\n"
"v_lshlrev_b32 v15, 2, v11\n"
"v_xor_b32 v14, v14, v15\n"
"v_lshlrev_b32 v14, 4, v14\n"
"v_mul_u32_u24 v15, 0xe00, v11\n"
"v_add_u32 v82, v14, v15\n"
"v_bfe_u32 v11, v0, 2, 2\n"
"v_and_b32 v12, 3, v0\n"
"v_bfe_u32 v13, v0, 4, 1\n"
"v_lshl_add_u32 v14, v10, 3, v11\n"
"v_and_b32 v15, 1, v12\n"
"v_lshlrev_b32 v15, 3, v15\n"
"v_lshl_add_u32 v15, v11, 6, v15\n"
"v_lshrrev_b32 v12, 1, v12\n"
"v_lshl_add_u32 v84, v14, 9, v15\n"
"v_lshl_add_u32 v84, v12, 5, v84\n"
"v_lshl_add_u32 v84, v13, 4, v84\n"
"v_lshl_add_u32 v88, v14, 8, v15\n"
"v_lshl_add_u32 v88, v13, 5, v88\n"
"v_lshl_add_u32 v88, v12, 4, v88\n"
"v_xor_b32 v85, 64, v84\n"
"v_xor_b32 v89, 64, v88\n"
"v_xor_b32 v86, 0x80, v84\n"
"v_xor_b32 v90, 0x80, v88\n"
"v_xor_b32 v87, 0xc0, v84\n"
"v_xor_b32 v91, 0xc0, v88\n"
"v_and_b32 v14, 31, v0\n"
"v_lshlrev_b32 v15, 2, v10\n"
"v_sub_u32 v14, v14, v15\n"
"v_add_u32 v7, %[nk0], v14\n"
"v_mov_b32 v8, 0xff800000\n"
"v_add_u32 v2, 0x20000, v1\n"
"v_add_u32 v3, 0x8000, v1\n"
"v_mov_b32 v5, 0\n"
"s_add_u32 s64, %[ks], 0x18000\n"
"s_add_u32 s65, %[vs], 0x8000\n"
"s_mov_b32 s66, 0x10000\n"
"s_mov_b32 s67, 0x20000\n"
"s_mov_b32 s68, 0\n"
"s_add_u32 s69, s74, 0x18000\n"
"s_mov_b32 s70, 1\n"
"s_mov_b32 s72, 0\n"
"s_mov_b32 s73, 1\n"
"s_min_u32 s76, %[tm], %[n]\n"
"s_mov_b32 s78, %[out]\n"
"s_add_u32 s79, s78, %[stp]\n"
"s_add_u32 s80, s79, %[stp]\n"
"s_add_u32 s81, s80, %[stp]\n"
"v_mov_b64 v[12:13], %[tag]\n"
"v_lshlrev_b32 v11, 3, v0\n"
"s_cmp_eq_u32 %[mode], 3\n"
"s_cbranch_scc1 RESUME%=\n"
"s_barrier\n"
"s_cmp_eq_u32 %[mode], 1\n"
"s_cbranch_scc1 HELP%=\n"
"v_add_u32 v88, 0x10000, v88\n"
"ds_read_b64_tr_b16 a[0:1], v88 offset:32768\n"
"ds_read_b64_tr_b16 a[2:3], v88 offset:33792\n"
"v_add_u32 v89, 0x10000, v89\n"
"ds_read_b64_tr_b16 a[4:5], v89 offset:32768\n"
"ds_read_b64_tr_b16 a[6:7], v89 offset:33792\n"
"v_add_u32 v90, 0x10000, v90\n"
"ds_read_b64_tr_b16 a[8:9], v90 offset:32768\n"
"ds_read_b64_tr_b16 a[10:11], v90 offset:33792\n"
"v_add_u32 v91, 0x10000, v91\n"
"ds_read_b64_tr_b16 a[12:13], v91 offset:32768\n"
"ds_read_b64_tr_b16 a[14:15], v91 offset:33792\n"
"ds_read_b64_tr_b16 a[16:17], v88 offset:36864\n"
"ds_read_b64_tr_b16 a[18:19], v88 offset:37888\n"
"ds_read_b64_tr_b16 a[20:21], v89 offset:36864\n"
"ds_read_b64_tr_b16 a[22:23], v89 offset:37888\n"
"s_waitcnt lgkmcnt(10)\n"
"v_mfma_f32_32x32x16_bf16 v[16:31], a[0:3], a[64:67], 0\n"
"ds_read_b64_tr_b16 a[24:25], v90 offset:36864\n"
"ds_read_b64_tr_b16 a[26:27], v90 offset:37888\n"
"s_waitcnt lgkmcnt(10)\n"
"v_mfma_f32_32x32x16_bf16 v[32:47], a[4:7], a[64:67], 0\n"
"ds_read_b64_tr_b16 a[28:29], v91 offset:36864\n"
"ds_read_b64_tr_b16 a[30:31], v91 offset:37888\n"
"buffer_store_dwordx4 a[176:179], v92, %[rsW], s79 offen sc1\n"
"s_waitcnt lgkmcnt(10)\n"
"v_mfma_f32_32x32x16_bf16 v[48:63], a[8:11], a[64:67], 0\n"
"ds_read_b64_tr_b16 a[0:1], v88 offset:40960\n"
"ds_read_b64_tr_b16 a[2:3], v88 offset:41984\n"
"s_waitcnt lgkmcnt(10)\n"
"v_mfma_f32_32x32x16_bf16 v[64:79], a[12:15], a[64:67], 0\n"
"ds_read_b64_tr_b16 a[4:5], v89 offset:40960\n"
"ds_read_b64_tr_b16 a[6:7], v89 offset:41984\n"
"s_waitcnt lgkmcnt(10)\n"
"v_mfma_f32_32x32x16_bf16 v[16:31], a[16:19], a[68:71], v[16:31]\n"
"ds_read_b64_tr_b16 a[8:9], v90 offset:40960\n"
"ds_read_b64_tr_b16 a[10:11], v90 offset:41984\n"
"s_waitcnt lgkmcnt(10)\n"
"v_mfma_f32_32x32x16_bf16 v[32:47], a[20:23], a[68:71], v[32:47]\n"
"ds_read_b64_tr_b16 a[12:13], v91 offset:40960\n"
"ds_read_b64_tr_b16 a[14:15], v91 offset:41984\n"
"s_add_u32 s82, s79, 0x4000\n"
"buffer_store_dwordx4 a[180:183], v92, %[rsW], s82 offen sc1\n"
"s_waitcnt lgkmcnt(10)\n"
"v_mfma_f32_32x32x16_bf16 v[48:63], a[24:27], a[68:71], v[48:63]\n"
"ds_read_b64_tr_b16 a[16:17], v88 offset:45056\n"
"ds_read_b64_tr_b16 a[18:19], v88 offset:46080\n"
"s_waitcnt lgkmcnt(10)\n"
"v_mfma_f32_32x32x16_bf16 v[64:79], a[28:31], a[68:71], v[64:79]\n"
"ds_read_b64_tr_b16 a[20:21], v89 offset:45056\n"
"ds_read_b64_tr_b16 a[22:23], v89 offset:46080\n"
"s_waitcnt lgkmcnt(10)\n"
"v_mfma_f32_32x32x16_bf16 v[16:31], a[0:3], a[72:75], v[16:31]\n"
"ds_read_b64_tr_b16 a[24:25], v90 offset:45056\n"
"ds_read_b64_tr_b16 a[26:27], v90 offset:46080\n"
"s_waitcnt lgkmcnt(10)\n"
"v_mfma_f32_32x32x16_bf16 v[32:47], a[4:7], a[72:75], v[32:47]\n"
"ds_read_b64_tr_b16 a[28:29], v91 offset:45056\n"
"ds_read_b64_tr_b16 a[30:31], v91 offset:46080\n"
"s_add_u32 s82, s79, 0x8000\n"
"buffer_store_dwordx4 a[184:187], v92, %[rsW], s82 offen sc1\n"
"s_waitcnt lgkmcnt(10)\n"
"v_mfma_f32_32x32x16_bf16 v[48:63], a[8:11], a[72:75], v[48:63]\n"
"ds_read_b64_tr_b16 a[0:1], v88 offset:49152\n"
"ds_read_b64_tr_b16 a[2:3], v88 offset:50176\n"
"s_waitcnt lgkmcnt(10)\n"
"v_mfma_f32_32x32x16_bf16 v[64:79], a[12:15], a[72:75], v[64:79]\n"
"ds_read_b64_tr_b16 a[4:5], v89 offset:49152\n"
"ds_read_b64_tr_b16 a[6:7], v89 offset:50176\n"
"s_waitcnt lgkmcnt(10)\n"
"v_mfma_f32_32x32x16_bf16 v[16:31], a[16:19], a[76:79], v[16:31]\n"
"ds_read_b64_tr_b16 a[8:9], v90 offset:49152\n"
"ds_read_b64_tr_b16 a[10:11], v90 offset:50176\n"
"s_waitcnt lgkmcnt(10)\n"
"v_mfma_f32_32x32x16_bf16 v[32:47], a[20:23], a[76:79], v[32:47]\n"
"ds_read_b64_tr_b16 a[12:13], v91 offset:49152\n"
"ds_read_b64_tr_b16 a[14:15], v91 offset:50176\n"
"s_waitcnt lgkmcnt(10)\n"
"v_mfma_f32_32x32x16_bf16 v[48:63], a[24:27], a[76:79], v[48:63]\n"
"ds_read_b64_tr_b16 a[16:17], v88 offset:53248\n"
"ds_read_b64_tr_b16 a[18:19], v88 offset:54272\n"
"s_waitcnt lgkmcnt(10)\n"
"v_mfma_f32_32x32x16_bf16 v[64:79], a[28:31], a[76:79], v[64:79]\n"
"ds_read_b64_tr_b16 a[20:21], v89 offset:53248\n"
"ds_read_b64_tr_b16 a[22:23], v89 offset:54272\n"
"s_add_u32 s82, s79, 0xc000\n"
"buffer_store_dwordx4 a[188:191], v92, %[rsW], s82 offen sc1\n"
"s_waitcnt lgkmcnt(10)\n"
"v_mfma_f32_32x32x16_bf16 v[16:31], a[0:3], a[80:83], v[16:31]\n"
"ds_read_b64_tr_b16 a[24:25], v90 offset:53248\n"
"ds_read_b64_tr_b16 a[26:27], v90 offset:54272\n"
"s_waitcnt lgkmcnt(10)\n"
"v_mfma_f32_32x32x16_bf16 v[32:47], a[4:7], a[80:83], v[32:47]\n"
"ds_read_b64_tr_b16 a[28:29], v91 offset:53248\n"
"ds_read_b64_tr_b16 a[30:31], v91 offset:54272\n"
"s_waitcnt lgkmcnt(10)\n"
"v_mfma_f32_32x32x16_bf16 v[48:63], a[8:11], a[80:83], v[48:63]\n"
"ds_read_b64_tr_b16 a[0:1], v88 offset:57344\n"
"ds_read_b64_tr_b16 a[2:3], v88 offset:58368\n"
"s_waitcnt lgkmcnt(10)\n"
"v_mfma_f32_32x32x16_bf16 v[64:79], a[12:15], a[80:83], v[64:79]\n"
"ds_read_b64_tr_b16 a[4:5], v89 offset:57344\n"
"ds_read_b64_tr_b16 a[6:7], v89 offset:58368\n"
"buffer_store_dwordx4 a[192:195], v93, %[rsW], s79 offen sc1\n"
"s_waitcnt lgkmcnt(10)\n"
"v_mfma_f32_32x32x16_bf16 v[16:31], a[16:19], a[84:87], v[16:31]\n"
"ds_read_b64_tr_b16 a[8:9], v90 offset:57344\n"
"ds_read_b64_tr_b16 a[10:11], v90 offset:58368\n"
"s_waitcnt lgkmcnt(10)\n"
"v_mfma_f32_32x32x16_bf16 v[32:47], a[20:23], a[84:87], v[32:47]\n"
"ds_read_b64_tr_b16 a[12:13], v91 offset:57344\n"
"ds_read_b64_tr_b16 a[14:15], v91 offset:58368\n"
"s_waitcnt lgkmcnt(10)\n"
"v_mfma_f32_32x32x16_bf16 v[48:63], a[24:27], a[84:87], v[48:63]\n"
"ds_read_b64_tr_b16 a[16:17], v88 offset:61440\n"
"ds_read_b64_tr_b16 a[18:19], v88 offset:62464\n"
"s_waitcnt lgkmcnt(10)\n"
"v_mfma_f32_32x32x16_bf16 v[64:79], a[28:31], a[84:87], v[64:79]\n"
"ds_read_b64_tr_b16 a[20:21], v89 offset:61440\n"
"ds_read_b64_tr_b16 a[22:23], v89 offset:62464\n"
"s_add_u32 s82, s79, 0x4000\n"
"buffer_store_dwordx4 a[196:199], v93, %[rsW], s82 offen sc1\n"
"s_waitcnt lgkmcnt(10)\n"
"v_mfma_f32_32x32x16_bf16 v[16:31], a[0:3], a[88:91], v[16:31]\n"
"ds_read_b64_tr_b16 a[24:25], v90 offset:61440\n"
"ds_read_b64_tr_b16 a[26:27], v90 offset:62464\n"
"s_waitcnt lgkmcnt(10)\n"
"v_mfma_f32_32x32x16_bf16 v[32:47], a[4:7], a[88:91], v[32:47]\n"
"ds_read_b64_tr_b16 a[28:29], v91 offset:61440\n"
"ds_read_b64_tr_b16 a[30:31], v91 offset:62464\n"
"s_waitcnt lgkmcnt(10)\n"
"v_mfma_f32_32x32x16_bf16 v[48:63], a[8:11], a[88:91], v[48:63]\n"
"v_add_u32 v88, 0x10000, v88\n"
"ds_read_b64_tr_b16 a[0:1], v88\n"
"ds_read_b64_tr_b16 a[2:3], v88 offset:1024\n"
"s_waitcnt lgkmcnt(10)\n"
"v_mfma_f32_32x32x16_bf16 v[64:79], a[12:15], a[88:91], v[64:79]\n"
"v_add_u32 v89, 0x10000, v89\n"
"ds_read_b64_tr_b16 a[4:5], v89\n"
"ds_read_b64_tr_b16 a[6:7], v89 offset:1024\n"
"s_waitcnt lgkmcnt(10)\n"
"v_mfma_f32_32x32x16_bf16 v[16:31], a[16:19], a[92:95], v[16:31]\n"
"v_add_u32 v90, 0x10000, v90\n"
"ds_read_b64_tr_b16 a[8:9], v90\n"
"ds_read_b64_tr_b16 a[10:11], v90 offset:1024\n"
"s_waitcnt lgkmcnt(10)\n"
"v_mfma_f32_32x32x16_bf16 v[32:47], a[20:23], a[92:95], v[32:47]\n"
"v_add_u32 v91, 0x10000, v91\n"
"ds_read_b64_tr_b16 a[12:13], v91\n"
"ds_read_b64_tr_b16 a[14:15], v91 offset:1024\n"
"s_add_u32 s82, s79, 0x8000\n"
"buffer_store_dwordx4 a[200:203], v93, %[rsW], s82 offen sc1\n"
"s_waitcnt lgkmcnt(10)\n"
"v_mfma_f32_32x32x16_bf16 v[48:63], a[24:27], a[92:95], v[48:63]\n"
"ds_read_b64_tr_b16 a[16:17], v88 offset:4096\n"
"ds_read_b64_tr_b16 a[18:19], v88 offset:5120\n"
"s_waitcnt lgkmcnt(10)\n"
"v_mfma_f32_32x32x16_bf16 v[64:79], a[28:31], a[92:95], v[64:79]\n"
"ds_read_b64_tr_b16 a[20:21], v89 offset:4096\n"
"ds_read_b64_tr_b16 a[22:23], v89 offset:5120\n"
"s_barrier\n"
"s_waitcnt lgkmcnt(10)\n"
"v_mfma_f32_32x32x16_bf16 v[16:31], a[0:3], a[96:99], v[16:31]\n"
"ds_read_b64_tr_b16 a[24:25], v90 offset:4096\n"
"ds_read_b64_tr_b16 a[26:27], v90 offset:5120\n"
"s_add_u32 m0, s74, 0x18000\n"
"s_add_u32 s71, %[q0], 0x80000\n"
"buffer_load_dwordx4 v80, %[rsQ], s71 offen lds\n"
"s_waitcnt lgkmcnt(10)\n"
"v_mfma_f32_32x32x16_bf16 v[32:47], a[4:7], a[96:99], v[32:47]\n"
"ds_read_b64_tr_b16 a[28:29], v91 offset:4096\n"
"ds_read_b64_tr_b16 a[30:31], v91 offset:5120\n"
"s_add_u32 s82, s79, 0xc000\n"
"buffer_store_dwordx4 a[204:207], v93, %[rsW], s82 offen sc1\n"
"s_waitcnt lgkmcnt(10)\n"
"v_mfma_f32_32x32x16_bf16 v[48:63], a[8:11], a[96:99], v[48:63]\n"
"ds_read_b64_tr_b16 a[0:1], v88 offset:8192\n"
"ds_read_b64_tr_b16 a[2:3], v88 offset:9216\n"
"s_add_u32 s71, %[q0], 0x83c00\n"
"buffer_load_dwordx4 v81, %[rsQ], s71 offen offset:1024 lds\n"
"s_waitcnt lgkmcnt(10)\n"
"v_mfma_f32_32x32x16_bf16 v[64:79], a[12:15], a[96:99], v[64:79]\n"
"ds_read_b64_tr_b16 a[4:5], v89 offset:8192\n"
"ds_read_b64_tr_b16 a[6:7], v89 offset:9216\n"
"s_waitcnt lgkmcnt(10)\n"
"v_mfma_f32_32x32x16_bf16 v[16:31], a[16:19], a[100:103], v[16:31]\n"
"ds_read_b64_tr_b16 a[8:9], v90 offset:8192\n"
"ds_read_b64_tr_b16 a[10:11], v90 offset:9216\n"
"s_add_u32 s71, %[q0], 0x87800\n"
"buffer_load_dwordx4 v80, %[rsQ], s71 offen offset:2048 lds\n"
"s_waitcnt lgkmcnt(10)\n"
"v_mfma_f32_32x32x16_bf16 v[32:47], a[20:23], a[100:103], v[32:47]\n"
"ds_read_b64_tr_b16 a[12:13], v91 offset:8192\n"
"ds_read_b64_tr_b16 a[14:15], v91 offset:9216\n"
"buffer_store_dwordx4 a[208:211], v92, %[rsW], s80 offen sc1\n"
"s_waitcnt lgkmcnt(10)\n"
"v_mfma_f32_32x32x16_bf16 v[48:63], a[24:27], a[100:103], v[48:63]\n"
"ds_read_b64_tr_b16 a[16:17], v88 offset:12288\n"
"ds_read_b64_tr_b16 a[18:19], v88 offset:13312\n"
"s_add_u32 s71, %[q0], 0x8b400\n"
"buffer_load_dwordx4 v81, %[rsQ], s71 offen offset:3072 lds\n"
"s_waitcnt lgkmcnt(10)\n"
"v_mfma_f32_32x32x16_bf16 v[64:79], a[28:31], a[100:103], v[64:79]\n"
"ds_read_b64_tr_b16 a[20:21], v89 offset:12288\n"
"ds_read_b64_tr_b16 a[22:23], v89 offset:13312\n"
"s_waitcnt lgkmcnt(10)\n"
"v_mfma_f32_32x32x16_bf16 v[16:31], a[0:3], a[104:107], v[16:31]\n"
"ds_read_b64_tr_b16 a[24:25], v90 offset:12288\n"
"ds_read_b64_tr_b16 a[26:27], v90 offset:13312\n"
"s_add_u32 m0, s74, 0x19000\n"
"s_add_u32 s71, %[q0], 0x90000\n"
"buffer_load_dwordx4 v80, %[rsQ], s71 offen lds\n"
"s_waitcnt lgkmcnt(10)\n"
"v_mfma_f32_32x32x16_bf16 v[32:47], a[4:7], a[104:107], v[32:47]\n"
"ds_read_b64_tr_b16 a[28:29], v91 offset:12288\n"
"ds_read_b64_tr_b16 a[30:31], v91 offset:13312\n"
"s_waitcnt lgkmcnt(10)\n"
"v_mfma_f32_32x32x16_bf16 v[48:63], a[8:11], a[104:107], v[48:63]\n"
"ds_read_b64_tr_b16 a[0:1], v88 offset:16384\n"
"ds_read_b64_tr_b16 a[2:3], v88 offset:17408\n"
"s_add_u32 s71, %[q0], 0x93c00\n"
"buffer_load_dwordx4 v81, %[rsQ], s71 offen offset:1024 lds\n"
"s_waitcnt lgkmcnt(10)\n"
"v_mfma_f32_32x32x16_bf16 v[64:79], a[12:15], a[104:107], v[64:79]\n"
"ds_read_b64_tr_b16 a[4:5], v89 offset:16384\n"
"ds_read_b64_tr_b16 a[6:7], v89 offset:17408\n"
"s_add_u32 s82, s80, 0x4000\n"
"buffer_store_dwordx4 a[212:215], v92, %[rsW], s82 offen sc1\n"
"s_waitcnt lgkmcnt(10)\n"
"v_mfma_f32_32x32x16_bf16 v[16:31], a[16:19], a[108:111], v[16:31]\n"
"ds_read_b64_tr_b16 a[8:9], v90 offset:16384\n"
"ds_read_b64_tr_b16 a[10:11], v90 offset:17408\n"
"s_add_u32 s71, %[q0], 0x97800\n"
"buffer_load_dwordx4 v80, %[rsQ], s71 offen offset:2048 lds\n"
"s_waitcnt lgkmcnt(10)\n"
"v_mfma_f32_32x32x16_bf16 v[32:47], a[20:23], a[108:111], v[32:47]\n"
"ds_read_b64_tr_b16 a[12:13], v91 offset:16384\n"
"ds_read_b64_tr_b16 a[14:15], v91 offset:17408\n"
"s_waitcnt lgkmcnt(10)\n"
"v_mfma_f32_32x32x16_bf16 v[48:63], a[24:27], a[108:111], v[48:63]\n"
"ds_read_b64_tr_b16 a[16:17], v88 offset:20480\n"
"ds_read_b64_tr_b16 a[18:19], v88 offset:21504\n"
"s_add_u32 s71, %[q0], 0x9b400\n"
"buffer_load_dwordx4 v81, %[rsQ], s71 offen offset:3072 lds\n"
"s_waitcnt lgkmcnt(10)\n"
"v_mfma_f32_32x32x16_bf16 v[64:79], a[28:31], a[108:111], v[64:79]\n"
"ds_read_b64_tr_b16 a[20:21], v89 offset:20480\n"
"ds_read_b64_tr_b16 a[22:23], v89 offset:21504\n"
"s_add_u32 s82, s80, 0x8000\n"
"buffer_store_dwordx4 a[216:219], v92, %[rsW], s82 offen sc1\n"
"s_waitcnt lgkmcnt(10)\n"
"v_mfma_f32_32x32x16_bf16 v[16:31], a[0:3], a[112:115], v[16:31]\n"
"ds_read_b64_tr_b16 a[24:25], v90 offset:20480\n"
"ds_read_b64_tr_b16 a[26:27], v90 offset:21504\n"
"s_waitcnt lgkmcnt(10)\n"
"v_mfma_f32_32x32x16_bf16 v[32:47], a[4:7], a[112:115], v[32:47]\n"
"ds_read_b64_tr_b16 a[28:29], v91 offset:20480\n"
"ds_read_b64_tr_b16 a[30:31], v91 offset:21504\n"
"s_waitcnt lgkmcnt(10)\n"
"v_mfma_f32_32x32x16_bf16 v[48:63], a[8:11], a[112:115], v[48:63]\n"
"ds_read_b64_tr_b16 a[0:1], v88 offset:24576\n"
"ds_read_b64_tr_b16 a[2:3], v88 offset:25600\n"
"s_waitcnt lgkmcnt(10)\n"
"v_mfma_f32_32x32x16_bf16 v[64:79], a[12:15], a[112:115], v[64:79]\n"
"ds_read_b64_tr_b16 a[4:5], v89 offset:24576\n"
"ds_read_b64_tr_b16 a[6:7], v89 offset:25600\n"
"s_add_u32 s82, s80, 0xc000\n"
"buffer_store_dwordx4 a[220:223], v92, %[rsW], s82 offen sc1\n"
"s_waitcnt lgkmcnt(10)\n"
"v_mfma_f32_32x32x16_bf16 v[16:31], a[16:19], a[116:119], v[16:31]\n"
"ds_read_b64_tr_b16 a[8:9], v90 offset:24576\n"
"ds_read_b64_tr_b16 a[10:11], v90 offset:25600\n"
"s_waitcnt lgkmcnt(10)\n"
"v_mfma_f32_32x32x16_bf16 v[32:47], a[20:23], a[116:119], v[32:47]\n"
"ds_read_b64_tr_b16 a[12:13], v91 offset:24576\n"
"ds_read_b64_tr_b16 a[14:15], v91 offset:25600\n"
"s_waitcnt lgkmcnt(10)\n"
"v_mfma_f32_32x32x16_bf16 v[48:63], a[24:27], a[116:119], v[48:63]\n"
"ds_read_b64_tr_b16 a[16:17], v88 offset:28672\n"
"ds_read_b64_tr_b16 a[18:19], v88 offset:29696\n"
"s_waitcnt lgkmcnt(10)\n"
"v_mfma_f32_32x32x16_bf16 v[64:79], a[28:31], a[116:119], v[64:79]\n"
"ds_read_b64_tr_b16 a[20:21], v89 offset:28672\n"
"ds_read_b64_tr_b16 a[22:23], v89 offset:29696\n"
"s_waitcnt lgkmcnt(10)\n"
"v_mfma_f32_32x32x16_bf16 v[16:31], a[0:3], a[120:123], v[16:31]\n"
"ds_read_b64_tr_b16 a[24:25], v90 offset:28672\n"
"ds_read_b64_tr_b16 a[26:27], v90 offset:29696\n"
"s_waitcnt lgkmcnt(10)\n"
"v_mfma_f32_32x32x16_bf16 v[32:47], a[4:7], a[120:123], v[32:47]\n"
"ds_read_b64_tr_b16 a[28:29], v91 offset:28672\n"
"ds_read_b64_tr_b16 a[30:31], v91 offset:29696\n"
"buffer_store_dwordx4 a[224:227], v93, %[rsW], s80 offen sc1\n"
"s_waitcnt lgkmcnt(10)\n"
"v_mfma_f32_32x32x16_bf16 v[48:63], a[8:11], a[120:123], v[48:63]\n"
"v_add_u32 v88, 0xfffe0000, v88\n"
"ds_read_b64_tr_b16 a[0:1], v88\n"
"ds_read_b64_tr_b16 a[2:3], v88 offset:1024\n"
"s_waitcnt lgkmcnt(10)\n"
"v_mfma_f32_32x32x16_bf16 v[64:79], a[12:15], a[120:123], v[64:79]\n"
"v_add_u32 v89, 0xfffe0000, v89\n"
"ds_read_b64_tr_b16 a[4:5], v89\n"
"ds_read_b64_tr_b16 a[6:7], v89 offset:1024\n"
"s_waitcnt lgkmcnt(10)\n"
"v_mfma_f32_32x32x16_bf16 v[16:31], a[16:19], a[124:127], v[16:31]\n"
"v_add_u32 v90, 0xfffe0000, v90\n"
"ds_read_b64_tr_b16 a[8:9], v90\n"
"ds_read_b64_tr_b16 a[10:11], v90 offset:1024\n"
"s_waitcnt lgkmcnt(10)\n"
"v_mfma_f32_32x32x16_bf16 v[32:47], a[20:23], a[124:127], v[32:47]\n"
"v_add_u32 v91, 0xfffe0000, v91\n"
"ds_read_b64_tr_b16 a[12:13], v91\n"
"ds_read_b64_tr_b16 a[14:15], v91 offset:1024\n"
"s_add_u32 s82, s80, 0x4000\n"
"buffer_store_dwordx4 a[228:231], v93, %[rsW], s82 offen sc1\n"
"s_waitcnt lgkmcnt(10)\n"
"v_mfma_f32_32x32x16_bf16 v[48:63], a[24:27], a[124:127], v[48:63]\n"
"ds_read_b64_tr_b16 a[16:17], v88 offset:4096\n"
"ds_read_b64_tr_b16 a[18:19], v88 offset:5120\n"
"s_waitcnt lgkmcnt(10)\n"
"v_mfma_f32_32x32x16_bf16 v[64:79], a[28:31], a[124:127], v[64:79]\n"
"ds_read_b64_tr_b16 a[20:21], v89 offset:4096\n"
"ds_read_b64_tr_b16 a[22:23], v89 offset:5120\n"
"s_barrier\n"
"s_waitcnt lgkmcnt(10)\n"
"v_mfma_f32_32x32x16_bf16 v[16:31], a[0:3], a[128:131], v[16:31]\n"
"ds_read_b64_tr_b16 a[24:25], v90 offset:4096\n"
"ds_read_b64_tr_b16 a[26:27], v90 offset:5120\n"
"s_add_u32 m0, s74, 0x20000\n"
"s_add_u32 s71, %[q0], 0x100000\n"
"buffer_load_dwordx4 v80, %[rsQ], s71 offen lds\n"
"s_waitcnt lgkmcnt(10)\n"
"v_mfma_f32_32x32x16_bf16 v[32:47], a[4:7], a[128:131], v[32:47]\n"
"ds_read_b64_tr_b16 a[28:29], v91 offset:4096\n"
"ds_read_b64_tr_b16 a[30:31], v91 offset:5120\n"
"s_add_u32 s82, s80, 0x8000\n"
"buffer_store_dwordx4 a[232:235], v93, %[rsW], s82 offen sc1\n"
"s_waitcnt lgkmcnt(10)\n"
"v_mfma_f32_32x32x16_bf16 v[48:63], a[8:11], a[128:131], v[48:63]\n"
"ds_read_b64_tr_b16 a[0:1], v88 offset:8192\n"
"ds_read_b64_tr_b16 a[2:3], v88 offset:9216\n"
"s_add_u32 s71, %[q0], 0x103c00\n"
"buffer_load_dwordx4 v81, %[rsQ], s71 offen offset:1024 lds\n"
"s_waitcnt lgkmcnt(10)\n"
"v_mfma_f32_32x32x16_bf16 v[64:79], a[12:15], a[128:131], v[64:79]\n"
"ds_read_b64_tr_b16 a[4:5], v89 offset:8192\n"
"ds_read_b64_tr_b16 a[6:7], v89 offset:9216\n"
"s_waitcnt lgkmcnt(10)\n"
"v_mfma_f32_32x32x16_bf16 v[16:31], a[16:19], a[132:135], v[16:31]\n"
"ds_read_b64_tr_b16 a[8:9], v90 offset:8192\n"
"ds_read_b64_tr_b16 a[10:11], v90 offset:9216\n"
"s_add_u32 s71, %[q0], 0x107800\n"
"buffer_load_dwordx4 v80, %[rsQ], s71 offen offset:2048 lds\n"
"s_waitcnt lgkmcnt(10)\n"
"v_mfma_f32_32x32x16_bf16 v[32:47], a[20:23], a[132:135], v[32:47]\n"
"ds_read_b64_tr_b16 a[12:13], v91 offset:8192\n"
"ds_read_b64_tr_b16 a[14:15], v91 offset:9216\n"
"s_waitcnt lgkmcnt(10)\n"
"v_mfma_f32_32x32x16_bf16 v[48:63], a[24:27], a[132:135], v[48:63]\n"
"ds_read_b64_tr_b16 a[16:17], v88 offset:12288\n"
"ds_read_b64_tr_b16 a[18:19], v88 offset:13312\n"
"s_add_u32 s71, %[q0], 0x10b400\n"
"buffer_load_dwordx4 v81, %[rsQ], s71 offen offset:3072 lds\n"
"s_waitcnt lgkmcnt(10)\n"
"v_mfma_f32_32x32x16_bf16 v[64:79], a[28:31], a[132:135], v[64:79]\n"
"ds_read_b64_tr_b16 a[20:21], v89 offset:12288\n"
"ds_read_b64_tr_b16 a[22:23], v89 offset:13312\n"
"s_add_u32 s82, s80, 0xc000\n"
"buffer_store_dwordx4 a[236:239], v93, %[rsW], s82 offen sc1\n"
"s_waitcnt lgkmcnt(10)\n"
"v_mfma_f32_32x32x16_bf16 v[16:31], a[0:3], a[136:139], v[16:31]\n"
"ds_read_b64_tr_b16 a[24:25], v90 offset:12288\n"
"ds_read_b64_tr_b16 a[26:27], v90 offset:13312\n"
"s_add_u32 m0, s74, 0x21000\n"
"s_add_u32 s71, %[q0], 0x110000\n"
"buffer_load_dwordx4 v80, %[rsQ], s71 offen lds\n"
"s_waitcnt lgkmcnt(10)\n"
"v_mfma_f32_32x32x16_bf16 v[32:47], a[4:7], a[136:139], v[32:47]\n"
"ds_read_b64_tr_b16 a[28:29], v91 offset:12288\n"
"ds_read_b64_tr_b16 a[30:31], v91 offset:13312\n"
"s_waitcnt lgkmcnt(10)\n"
"v_mfma_f32_32x32x16_bf16 v[48:63], a[8:11], a[136:139], v[48:63]\n"
"ds_read_b64_tr_b16 a[0:1], v88 offset:16384\n"
"ds_read_b64_tr_b16 a[2:3], v88 offset:17408\n"
"s_add_u32 s71, %[q0], 0x113c00\n"
"buffer_load_dwordx4 v81, %[rsQ], s71 offen offset:1024 lds\n"
"s_waitcnt lgkmcnt(10)\n"
"v_mfma_f32_32x32x16_bf16 v[64:79], a[12:15], a[136:139], v[64:79]\n"
"ds_read_b64_tr_b16 a[4:5], v89 offset:16384\n"
"ds_read_b64_tr_b16 a[6:7], v89 offset:17408\n"
"buffer_store_dwordx4 a[32:35], v92, %[rsW], s81 offen sc1\n"
"s_waitcnt lgkmcnt(10)\n"
"v_mfma_f32_32x32x16_bf16 v[16:31], a[16:19], a[140:143], v[16:31]\n"
"ds_read_b64_tr_b16 a[8:9], v90 offset:16384\n"
"ds_read_b64_tr_b16 a[10:11], v90 offset:17408\n"
"s_add_u32 s71, %[q0], 0x117800\n"
"buffer_load_dwordx4 v80, %[rsQ], s71 offen offset:2048 lds\n"
"s_waitcnt lgkmcnt(10)\n"
"v_mfma_f32_32x32x16_bf16 v[32:47], a[20:23], a[140:143], v[32:47]\n"
"ds_read_b64_tr_b16 a[12:13], v91 offset:16384\n"
"ds_read_b64_tr_b16 a[14:15], v91 offset:17408\n"
"s_waitcnt lgkmcnt(10)\n"
"v_mfma_f32_32x32x16_bf16 v[48:63], a[24:27], a[140:143], v[48:63]\n"
"ds_read_b64_tr_b16 a[16:17], v88 offset:20480\n"
"ds_read_b64_tr_b16 a[18:19], v88 offset:21504\n"
"s_add_u32 s71, %[q0], 0x11b400\n"
"buffer_load_dwordx4 v81, %[rsQ], s71 offen offset:3072 lds\n"
"s_waitcnt lgkmcnt(10)\n"
"v_mfma_f32_32x32x16_bf16 v[64:79], a[28:31], a[140:143], v[64:79]\n"
"ds_read_b64_tr_b16 a[20:21], v89 offset:20480\n"
"ds_read_b64_tr_b16 a[22:23], v89 offset:21504\n"
"s_add_u32 s82, s81, 0x4000\n"
"buffer_store_dwordx4 a[36:39], v92, %[rsW], s82 offen sc1\n"
"s_waitcnt lgkmcnt(10)\n"
"v_mfma_f32_32x32x16_bf16 v[16:31], a[0:3], a[144:147], v[16:31]\n"
"ds_read_b64_tr_b16 a[24:25], v90 offset:20480\n"
"ds_read_b64_tr_b16 a[26:27], v90 offset:21504\n"
"s_waitcnt lgkmcnt(10)\n"
"v_mfma_f32_32x32x16_bf16 v[32:47], a[4:7], a[144:147], v[32:47]\n"
"ds_read_b64_tr_b16 a[28:29], v91 offset:20480\n"
"ds_read_b64_tr_b16 a[30:31], v91 offset:21504\n"
"s_waitcnt lgkmcnt(10)\n"
"v_mfma_f32_32x32x16_bf16 v[48:63], a[8:11], a[144:147], v[48:63]\n"
"ds_read_b64_tr_b16 a[0:1], v88 offset:24576\n"
"ds_read_b64_tr_b16 a[2:3], v88 offset:25600\n"
"s_waitcnt lgkmcnt(10)\n"
"v_mfma_f32_32x32x16_bf16 v[64:79], a[12:15], a[144:147], v[64:79]\n"
"ds_read_b64_tr_b16 a[4:5], v89 offset:24576\n"
"ds_read_b64_tr_b16 a[6:7], v89 offset:25600\n"
"s_waitcnt lgkmcnt(10)\n"
"v_mfma_f32_32x32x16_bf16 v[16:31], a[16:19], a[148:151], v[16:31]\n"
"ds_read_b64_tr_b16 a[8:9], v90 offset:24576\n"
"ds_read_b64_tr_b16 a[10:11], v90 offset:25600\n"
"s_waitcnt lgkmcnt(10)\n"
"v_mfma_f32_32x32x16_bf16 v[32:47], a[20:23], a[148:151], v[32:47]\n"
"ds_read_b64_tr_b16 a[12:13], v91 offset:24576\n"
"ds_read_b64_tr_b16 a[14:15], v91 offset:25600\n"
"s_add_u32 s82, s81, 0x8000\n"
"buffer_store_dwordx4 a[40:43], v92, %[rsW], s82 offen sc1\n"
"s_waitcnt lgkmcnt(10)\n"
"v_mfma_f32_32x32x16_bf16 v[48:63], a[24:27], a[148:151], v[48:63]\n"
"ds_read_b64_tr_b16 a[16:17], v88 offset:28672\n"
"ds_read_b64_tr_b16 a[18:19], v88 offset:29696\n"
"s_waitcnt lgkmcnt(10)\n"
"v_mfma_f32_32x32x16_bf16 v[64:79], a[28:31], a[148:151], v[64:79]\n"
"ds_read_b64_tr_b16 a[20:21], v89 offset:28672\n"
"ds_read_b64_tr_b16 a[22:23], v89 offset:29696\n"
"s_waitcnt lgkmcnt(10)\n"
"v_mfma_f32_32x32x16_bf16 v[16:31], a[0:3], a[152:155], v[16:31]\n"
"ds_read_b64_tr_b16 a[24:25], v90 offset:28672\n"
"ds_read_b64_tr_b16 a[26:27], v90 offset:29696\n"
"s_waitcnt lgkmcnt(10)\n"
"v_mfma_f32_32x32x16_bf16 v[32:47], a[4:7], a[152:155], v[32:47]\n"
"ds_read_b64_tr_b16 a[28:29], v91 offset:28672\n"
"ds_read_b64_tr_b16 a[30:31], v91 offset:29696\n"
"s_add_u32 s82, s81, 0xc000\n"
"buffer_store_dwordx4 a[44:47], v92, %[rsW], s82 offen sc1\n"
"s_waitcnt lgkmcnt(10)\n"
"v_mfma_f32_32x32x16_bf16 v[48:63], a[8:11], a[152:155], v[48:63]\n"
"ds_read_b64_tr_b16 a[0:1], v88 offset:32768\n"
"ds_read_b64_tr_b16 a[2:3], v88 offset:33792\n"
"s_waitcnt lgkmcnt(10)\n"
"v_mfma_f32_32x32x16_bf16 v[64:79], a[12:15], a[152:155], v[64:79]\n"
"ds_read_b64_tr_b16 a[4:5], v89 offset:32768\n"
"ds_read_b64_tr_b16 a[6:7], v89 offset:33792\n"
"s_waitcnt lgkmcnt(10)\n"
"v_mfma_f32_32x32x16_bf16 v[16:31], a[16:19], a[156:159], v[16:31]\n"
"ds_read_b64_tr_b16 a[8:9], v90 offset:32768\n"
"ds_read_b64_tr_b16 a[10:11], v90 offset:33792\n"
"s_waitcnt lgkmcnt(10)\n"
"v_mfma_f32_32x32x16_bf16 v[32:47], a[20:23], a[156:159], v[32:47]\n"
"ds_read_b64_tr_b16 a[12:13], v91 offset:32768\n"
"ds_read_b64_tr_b16 a[14:15], v91 offset:33792\n"
"buffer_store_dwordx4 a[48:51], v93, %[rsW], s81 offen sc1\n"
"s_waitcnt lgkmcnt(10)\n"
"v_mfma_f32_32x32x16_bf16 v[48:63], a[24:27], a[156:159], v[48:63]\n"
"ds_read_b64_tr_b16 a[16:17], v88 offset:36864\n"
"ds_read_b64_tr_b16 a[18:19], v88 offset:37888\n"
"s_waitcnt lgkmcnt(10)\n"
"v_mfma_f32_32x32x16_bf16 v[64:79], a[28:31], a[156:159], v[64:79]\n"
"ds_read_b64_tr_b16 a[20:21], v89 offset:36864\n"
"ds_read_b64_tr_b16 a[22:23], v89 offset:37888\n"
"s_barrier\n"
"s_waitcnt lgkmcnt(10)\n"
"v_mfma_f32_32x32x16_bf16 v[16:31], a[0:3], a[160:163], v[16:31]\n"
"ds_read_b64_tr_b16 a[24:25], v90 offset:36864\n"
"ds_read_b64_tr_b16 a[26:27], v90 offset:37888\n"
"s_add_u32 m0, s74, 0\n"
"s_add_u32 s71, %[q0], 0x180000\n"
"buffer_load_dwordx4 v80, %[rsQ], s71 offen lds\n"
"s_waitcnt lgkmcnt(10)\n"
"v_mfma_f32_32x32x16_bf16 v[32:47], a[4:7], a[160:163], v[32:47]\n"
"ds_read_b64_tr_b16 a[28:29], v91 offset:36864\n"
"ds_read_b64_tr_b16 a[30:31], v91 offset:37888\n"
"s_waitcnt lgkmcnt(10)\n"
"v_mfma_f32_32x32x16_bf16 v[48:63], a[8:11], a[160:163], v[48:63]\n"
"ds_read_b64_tr_b16 a[0:1], v88 offset:40960\n"
"ds_read_b64_tr_b16 a[2:3], v88 offset:41984\n"
"s_add_u32 s71, %[q0], 0x183c00\n"
"buffer_load_dwordx4 v81, %[rsQ], s71 offen offset:1024 lds\n"
"s_waitcnt lgkmcnt(10)\n"
"v_mfma_f32_32x32x16_bf16 v[64:79], a[12:15], a[160:163], v[64:79]\n"
"ds_read_b64_tr_b16 a[4:5], v89 offset:40960\n"
"ds_read_b64_tr_b16 a[6:7], v89 offset:41984\n"
"s_add_u32 s82, s81, 0x4000\n"
"buffer_store_dwordx4 a[52:55], v93, %[rsW], s82 offen sc1\n"
"s_waitcnt lgkmcnt(10)\n"
"v_mfma_f32_32x32x16_bf16 v[16:31], a[16:19], a[164:167], v[16:31]\n"
"ds_read_b64_tr_b16 a[8:9], v90 offset:40960\n"
"ds_read_b64_tr_b16 a[10:11], v90 offset:41984\n"
"s_add_u32 s71, %[q0], 0x187800\n"
"buffer_load_dwordx4 v80, %[rsQ], s71 offen offset:2048 lds\n"
"s_waitcnt lgkmcnt(10)\n"
"v_mfma_f32_32x32x16_bf16 v[32:47], a[20:23], a[164:167], v[32:47]\n"
"ds_read_b64_tr_b16 a[12:13], v91 offset:40960\n"
"ds_read_b64_tr_b16 a[14:15], v91 offset:41984\n"
"s_waitcnt lgkmcnt(10)\n"
"v_mfma_f32_32x32x16_bf16 v[48:63], a[24:27], a[164:167], v[48:63]\n"
"ds_read_b64_tr_b16 a[16:17], v88 offset:45056\n"
"ds_read_b64_tr_b16 a[18:19], v88 offset:46080\n"
"s_add_u32 s71, %[q0], 0x18b400\n"
"buffer_load_dwordx4 v81, %[rsQ], s71 offen offset:3072 lds\n"
"s_waitcnt lgkmcnt(10)\n"
"v_mfma_f32_32x32x16_bf16 v[64:79], a[28:31], a[164:167], v[64:79]\n"
"ds_read_b64_tr_b16 a[20:21], v89 offset:45056\n"
"ds_read_b64_tr_b16 a[22:23], v89 offset:46080\n"
"s_add_u32 s82, s81, 0x8000\n"
"buffer_store_dwordx4 a[56:59], v93, %[rsW], s82 offen sc1\n"
"s_waitcnt lgkmcnt(10)\n"
"v_mfma_f32_32x32x16_bf16 v[16:31], a[0:3], a[168:171], v[16:31]\n"
"ds_read_b64_tr_b16 a[24:25], v90 offset:45056\n"
"ds_read_b64_tr_b16 a[26:27], v90 offset:46080\n"
"s_add_u32 m0, s74, 0x1000\n"
"s_add_u32 s71, %[q0], 0x190000\n"
"buffer_load_dwordx4 v80, %[rsQ], s71 offen lds\n"
"s_waitcnt lgkmcnt(10)\n"
"v_mfma_f32_32x32x16_bf16 v[32:47], a[4:7], a[168:171], v[32:47]\n"
"ds_read_b64_tr_b16 a[28:29], v91 offset:45056\n"
"ds_read_b64_tr_b16 a[30:31], v91 offset:46080\n"
"s_waitcnt lgkmcnt(10)\n"
"v_mfma_f32_32x32x16_bf16 v[48:63], a[8:11], a[168:171], v[48:63]\n"
"v_add_u32 v84, 0x10000, v84\n"
"ds_read_b64_tr_b16 a[0:1], v84\n"
"ds_read_b64_tr_b16 a[2:3], v84 offset:2048\n"
"s_add_u32 s71, %[q0], 0x193c00\n"
"buffer_load_dwordx4 v81, %[rsQ], s71 offen offset:1024 lds\n"
"s_waitcnt lgkmcnt(10)\n"
"v_mfma_f32_32x32x16_bf16 v[64:79], a[12:15], a[168:171], v[64:79]\n"
"v_add_u32 v85, 0x10000, v85\n"
"ds_read_b64_tr_b16 a[4:5], v85\n"
"ds_read_b64_tr_b16 a[6:7], v85 offset:2048\n"
"s_add_u32 s82, s81, 0xc000\n"
"buffer_store_dwordx4 a[60:63], v93, %[rsW], s82 offen sc1\n"
"s_waitcnt lgkmcnt(10)\n"
"v_mfma_f32_32x32x16_bf16 v[16:31], a[16:19], a[172:175], v[16:31]\n"
"v_add_u32 v86, 0x10000, v86\n"
"ds_read_b64_tr_b16 a[8:9], v86\n"
"ds_read_b64_tr_b16 a[10:11], v86 offset:2048\n"
"s_add_u32 s71, %[q0], 0x197800\n"
"buffer_load_dwordx4 v80, %[rsQ], s71 offen offset:2048 lds\n"
"s_waitcnt lgkmcnt(10)\n"
"v_mfma_f32_32x32x16_bf16 v[32:47], a[20:23], a[172:175], v[32:47]\n"
"v_add_u32 v87, 0x10000, v87\n"
"ds_read_b64_tr_b16 a[12:13], v87\n"
"ds_read_b64_tr_b16 a[14:15], v87 offset:2048\n"
"s_waitcnt lgkmcnt(10)\n"
"v_mfma_f32_32x32x16_bf16 v[48:63], a[24:27], a[172:175], v[48:63]\n"
"ds_read_b64_tr_b16 a[16:17], v84 offset:256\n"
"ds_read_b64_tr_b16 a[18:19], v84 offset:2304\n"
"s_add_u32 s71, %[q0], 0x19b400\n"
"buffer_load_dwordx4 v81, %[rsQ], s71 offen offset:3072 lds\n"
"s_waitcnt lgkmcnt(10)\n"
"v_mfma_f32_32x32x16_bf16 v[64:79], a[28:31], a[172:175], v[64:79]\n"
"ds_read_b64_tr_b16 a[20:21], v85 offset:256\n"
"ds_read_b64_tr_b16 a[22:23], v85 offset:2304\n"
"s_waitcnt vmcnt(30)\n"
"s_barrier\n"
"s_mov_b32 s77, 0xbfb8aa3b\n"
"s_waitcnt lgkmcnt(10)\n"
"v_mfma_f32_32x32x16_bf16 v[128:143], a[0:3], a[64:67], 0\n"
"ds_read_b64_tr_b16 a[24:25], v86 offset:256\n"
"ds_read_b64_tr_b16 a[26:27], v86 offset:2304\n"
"s_add_u32 m0, s74, 0x8000\n"
"s_add_u32 s71, %[q0], 0x200000\n"
"buffer_load_dwordx4 v80, %[rsQ], s71 offen lds\n"
"s_waitcnt lgkmcnt(10)\n"
"v_mfma_f32_32x32x16_bf16 v[144:159], a[4:7], a[64:67], 0\n"
"ds_read_b64_tr_b16 a[28:29], v87 offset:256\n"
"ds_read_b64_tr_b16 a[30:31], v87 offset:2304\n"
"s_waitcnt lgkmcnt(10)\n"
"v_mfma_f32_32x32x16_bf16 v[160:175], a[8:11], a[64:67], 0\n"
"ds_read_b64_tr_b16 a[0:1], v84 offset:8192\n"
"ds_read_b64_tr_b16 a[2:3], v84 offset:10240\n"
"s_waitcnt lgkmcnt(10)\n"
"v_mfma_f32_32x32x16_bf16 v[176:191], a[12:15], a[64:67], 0\n"
"ds_read_b64_tr_b16 a[4:5], v85 offset:8192\n"
"ds_read_b64_tr_b16 a[6:7], v85 offset:10240\n"
"v_mul_f32 v96, s77, v16\n"
"v_mul_f32 v97, s77, v17\n"
"s_waitcnt lgkmcnt(10)\n"
"v_mfma_f32_32x32x16_bf16 v[192:207], a[16:19], a[64:67], 0\n"
"ds_read_b64_tr_b16 a[8:9], v86 offset:8192\n"
"ds_read_b64_tr_b16 a[10:11], v86 offset:10240\n"
"s_add_u32 s71, %[q0], 0x203c00\n"
"buffer_load_dwordx4 v81, %[rsQ], s71 offen offset:1024 lds\n"
"s_waitcnt lgkmcnt(10)\n"
"v_mfma_f32_32x32x16_bf16 v[208:223], a[20:23], a[64:67], 0\n"
"ds_read_b64_tr_b16 a[12:13], v87 offset:8192\n"
"ds_read_b64_tr_b16 a[14:15], v87 offset:10240\n"
"v_exp_f32 v96, v96\n"
"v_mul_f32 v98, s77, v18\n"
"s_waitcnt lgkmcnt(10)\n"
"v_mfma_f32_32x32x16_bf16 v[224:239], a[24:27], a[64:67], 0\n"
"ds_read_b64_tr_b16 a[16:17], v84 offset:8448\n"
"ds_read_b64_tr_b16 a[18:19], v84 offset:10496\n"
"v_exp_f32 v97, v97\n"
"v_add_f32 v96, 1.0, v96\n"
"s_waitcnt lgkmcnt(10)\n"
"v_mfma_f32_32x32x16_bf16 v[240:255], a[28:31], a[64:67], 0\n"
"ds_read_b64_tr_b16 a[20:21], v85 offset:8448\n"
"ds_read_b64_tr_b16 a[22:23], v85 offset:10496\n"
"v_mul_f32 v99, s77, v19\n"
"v_exp_f32 v98, v98\n"
"s_waitcnt lgkmcnt(10)\n"
"v_mfma_f32_32x32x16_bf16 v[128:143], a[0:3], a[68:71], v[128:143]\n"
"ds_read_b64_tr_b16 a[24:25], v86 offset:8448\n"
"ds_read_b64_tr_b16 a[26:27], v86 offset:10496\n"
"s_add_u32 s71, %[q0], 0x207800\n"
"buffer_load_dwordx4 v80, %[rsQ], s71 offen offset:2048 lds\n"
"v_add_f32 v97, 1.0, v97\n"
"s_waitcnt lgkmcnt(10)\n"
"v_mfma_f32_32x32x16_bf16 v[144:159], a[4:7], a[68:71], v[144:159]\n"
"ds_read_b64_tr_b16 a[28:29], v87 offset:8448\n"
"ds_read_b64_tr_b16 a[30:31], v87 offset:10496\n"
"v_rcp_f32 v96, v96\n"
"v_mul_f32 v100, s77, v20\n"
"s_waitcnt lgkmcnt(10)\n"
"v_mfma_f32_32x32x16_bf16 v[160:175], a[8:11], a[68:71], v[160:175]\n"
"ds_read_b64_tr_b16 a[0:1], v84 offset:16384\n"
"ds_read_b64_tr_b16 a[2:3], v84 offset:18432\n"
"v_exp_f32 v99, v99\n"
"v_add_f32 v98, 1.0, v98\n"
"s_waitcnt lgkmcnt(10)\n"
"v_mfma_f32_32x32x16_bf16 v[176:191], a[12:15], a[68:71], v[176:191]\n"
"ds_read_b64_tr_b16 a[4:5], v85 offset:16384\n"
"ds_read_b64_tr_b16 a[6:7], v85 offset:18432\n"
"v_rcp_f32 v97, v97\n"
"v_mul_f32 v96, v96, v16\n"
"s_waitcnt lgkmcnt(10)\n"
"v_mfma_f32_32x32x16_bf16 v[192:207], a[16:19], a[68:71], v[192:207]\n"
"ds_read_b64_tr_b16 a[8:9], v86 offset:16384\n"
"ds_read_b64_tr_b16 a[10:11], v86 offset:18432\n"
"s_add_u32 s71, %[q0], 0x20b400\n"
"buffer_load_dwordx4 v81, %[rsQ], s71 offen offset:3072 lds\n"
"v_mul_f32 v101, s77, v21\n"
"s_waitcnt lgkmcnt(10)\n"
"v_mfma_f32_32x32x16_bf16 v[208:223], a[20:23], a[68:71], v[208:223]\n"
"ds_read_b64_tr_b16 a[12:13], v87 offset:16384\n"
"ds_read_b64_tr_b16 a[14:15], v87 offset:18432\n"
"v_exp_f32 v100, v100\n"
"v_add_f32 v99, 1.0, v99\n"
"s_waitcnt lgkmcnt(10)\n"
"v_mfma_f32_32x32x16_bf16 v[224:239], a[24:27], a[68:71], v[224:239]\n"
"ds_read_b64_tr_b16 a[16:17], v84 offset:16640\n"
"ds_read_b64_tr_b16 a[18:19], v84 offset:18688\n"
"v_rcp_f32 v98, v98\n"
"v_mul_f32 v97, v97, v17\n"
"s_waitcnt lgkmcnt(10)\n"
"v_mfma_f32_32x32x16_bf16 v[240:255], a[28:31], a[68:71], v[240:255]\n"
"ds_read_b64_tr_b16 a[20:21], v85 offset:16640\n"
"ds_read_b64_tr_b16 a[22:23], v85 offset:18688\n"
"v_mul_f32 v102, s77, v22\n"
"v_exp_f32 v101, v101\n"
"s_waitcnt lgkmcnt(10)\n"
"v_mfma_f32_32x32x16_bf16 v[128:143], a[0:3], a[72:75], v[128:143]\n"
"ds_read_b64_tr_b16 a[24:25], v86 offset:16640\n"
"ds_read_b64_tr_b16 a[26:27], v86 offset:18688\n"
"s_add_u32 m0, s74, 0x9000\n"
"s_add_u32 s71, %[q0], 0x210000\n"
"buffer_load_dwordx4 v80, %[rsQ], s71 offen lds\n"
"v_add_f32 v100, 1.0, v100\n"
"s_waitcnt lgkmcnt(10)\n"
"v_mfma_f32_32x32x16_bf16 v[144:159], a[4:7], a[72:75], v[144:159]\n"
"ds_read_b64_tr_b16 a[28:29], v87 offset:16640\n"
"ds_read_b64_tr_b16 a[30:31], v87 offset:18688\n"
"v_rcp_f32 v99, v99\n"
"v_mul_f32 v98, v98, v18\n"
"s_waitcnt lgkmcnt(10)\n"
"v_mfma_f32_32x32x16_bf16 v[160:175], a[8:11], a[72:75], v[160:175]\n"
"ds_read_b64_tr_b16 a[0:1], v84 offset:24576\n"
"ds_read_b64_tr_b16 a[2:3], v84 offset:26624\n"
"v_cvt_pk_bf16_f32 v104, v96, v97\n"
"v_mul_f32 v103, s77, v23\n"
"s_waitcnt lgkmcnt(10)\n"
"v_mfma_f32_32x32x16_bf16 v[176:191], a[12:15], a[72:75], v[176:191]\n"
"ds_read_b64_tr_b16 a[4:5], v85 offset:24576\n"
"ds_read_b64_tr_b16 a[6:7], v85 offset:26624\n"
"v_exp_f32 v102, v102\n"
"v_add_f32 v101, 1.0, v101\n"
"s_waitcnt lgkmcnt(10)\n"
"v_mfma_f32_32x32x16_bf16 v[192:207], a[16:19], a[72:75], v[192:207]\n"
"ds_read_b64_tr_b16 a[8:9], v86 offset:24576\n"
"ds_read_b64_tr_b16 a[10:11], v86 offset:26624\n"
"s_add_u32 s71, %[q0], 0x213c00\n"
"buffer_load_dwordx4 v81, %[rsQ], s71 offen offset:1024 lds\n"
"s_waitcnt lgkmcnt(10)\n"
"v_mfma_f32_32x32x16_bf16 v[208:223], a[20:23], a[72:75], v[208:223]\n"
"ds_read_b64_tr_b16 a[12:13], v87 offset:24576\n"
"ds_read_b64_tr_b16 a[14:15], v87 offset:26624\n"
"v_rcp_f32 v100, v100\n"
"v_mul_f32 v99, v99, v19\n"
"s_waitcnt lgkmcnt(10)\n"
"v_mfma_f32_32x32x16_bf16 v[224:239], a[24:27], a[72:75], v[224:239]\n"
"ds_read_b64_tr_b16 a[16:17], v84 offset:24832\n"
"ds_read_b64_tr_b16 a[18:19], v84 offset:26880\n"
"v_mul_f32 v96, s77, v24\n"
"v_exp_f32 v103, v103\n"
"s_waitcnt lgkmcnt(10)\n"
"v_mfma_f32_32x32x16_bf16 v[240:255], a[28:31], a[72:75], v[240:255]\n"
"ds_read_b64_tr_b16 a[20:21], v85 offset:24832\n"
"ds_read_b64_tr_b16 a[22:23], v85 offset:26880\n"
"v_add_f32 v102, 1.0, v102\n"
"v_rcp_f32 v101, v101\n"
"s_waitcnt lgkmcnt(10)\n"
"v_mfma_f32_32x32x16_bf16 v[128:143], a[0:3], a[76:79], v[128:143]\n"
"ds_read_b64_tr_b16 a[24:25], v86 offset:24832\n"
"ds_read_b64_tr_b16 a[26:27], v86 offset:26880\n"
"s_add_u32 s71, %[q0], 0x217800\n"
"buffer_load_dwordx4 v80, %[rsQ], s71 offen offset:2048 lds\n"
"v_mul_f32 v100, v100, v20\n"
"s_waitcnt lgkmcnt(10)\n"
"v_mfma_f32_32x32x16_bf16 v[144:159], a[4:7], a[76:79], v[144:159]\n"
"ds_read_b64_tr_b16 a[28:29], v87 offset:24832\n"
"ds_read_b64_tr_b16 a[30:31], v87 offset:26880\n"
"v_cvt_pk_bf16_f32 v105, v98, v99\n"
"v_mul_f32 v97, s77, v25\n"
"s_waitcnt lgkmcnt(10)\n"
"v_mfma_f32_32x32x16_bf16 v[160:175], a[8:11], a[76:79], v[160:175]\n"
"ds_read_b64_tr_b16 a[0:1], v84 offset:32768\n"
"ds_read_b64_tr_b16 a[2:3], v84 offset:34816\n"
"v_exp_f32 v96, v96\n"
"v_add_f32 v103, 1.0, v103\n"
"s_waitcnt lgkmcnt(10)\n"
"v_mfma_f32_32x32x16_bf16 v[176:191], a[12:15], a[76:79], v[176:191]\n"
"ds_read_b64_tr_b16 a[4:5], v85 offset:32768\n"
"ds_read_b64_tr_b16 a[6:7], v85 offset:34816\n"
"v_rcp_f32 v102, v102\n"
"v_mul_f32 v101, v101, v21\n"
"s_waitcnt lgkmcnt(10)\n"
"v_mfma_f32_32x32x16_bf16 v[192:207], a[16:19], a[76:79], v[192:207]\n"
"ds_read_b64_tr_b16 a[8:9], v86 offset:32768\n"
"ds_read_b64_tr_b16 a[10:11], v86 offset:34816\n"
"s_add_u32 s71, %[q0], 0x21b400\n"
"buffer_load_dwordx4 v81, %[rsQ], s71 offen offset:3072 lds\n"
"v_mul_f32 v98, s77, v26\n"
"s_waitcnt lgkmcnt(10)\n"
"v_mfma_f32_32x32x16_bf16 v[208:223], a[20:23], a[76:79], v[208:223]\n"
"ds_read_b64_tr_b16 a[12:13], v87 offset:32768\n"
"ds_read_b64_tr_b16 a[14:15], v87 offset:34816\n"
"v_exp_f32 v97, v97\n"
"v_add_f32 v96, 1.0, v96\n"
"s_waitcnt lgkmcnt(10)\n"
"v_mfma_f32_32x32x16_bf16 v[224:239], a[24:27], a[76:79], v[224:239]\n"
"ds_read_b64_tr_b16 a[16:17], v84 offset:33024\n"
"ds_read_b64_tr_b16 a[18:19], v84 offset:35072\n"
"v_rcp_f32 v103, v103\n"
"v_mul_f32 v102, v102, v22\n"
"s_waitcnt lgkmcnt(10)\n"
"v_mfma_f32_32x32x16_bf16 v[240:255], a[28:31], a[76:79], v[240:255]\n"
"ds_read_b64_tr_b16 a[20:21], v85 offset:33024\n"
"ds_read_b64_tr_b16 a[22:23], v85 offset:35072\n"
"v_cvt_pk_bf16_f32 v106, v100, v101\n"
"v_mul_f32 v99, s77, v27\n"
"s_waitcnt vmcnt(23)\n"
"s_waitcnt vmcnt(10)\n"
"buffer_store_dwordx2 v[12:13], off, %[rsW], %[kvf] sc1\n"
"s_barrier\n"
"s_waitcnt lgkmcnt(10)\n"
"v_mfma_f32_32x32x16_bf16 v[128:143], a[0:3], a[80:83], v[128:143]\n"
"ds_read_b64_tr_b16 a[24:25], v86 offset:33024\n"
"ds_read_b64_tr_b16 a[26:27], v86 offset:35072\n"
"s_add_u32 m0, s74, 0x10000\n"
"s_add_u32 s71, %[q0], 0x280000\n"
"buffer_load_dwordx4 v80, %[rsQ], s71 offen lds\n"
"s_waitcnt lgkmcnt(10)\n"
"v_mfma_f32_32x32x16_bf16 v[144:159], a[4:7], a[80:83], v[144:159]\n"
"ds_read_b64_tr_b16 a[28:29], v87 offset:33024\n"
"ds_read_b64_tr_b16 a[30:31], v87 offset:35072\n"
"v_exp_f32 v98, v98\n"
"v_add_f32 v97, 1.0, v97\n"
"s_waitcnt lgkmcnt(10)\n"
"v_mfma_f32_32x32x16_bf16 v[160:175], a[8:11], a[80:83], v[160:175]\n"
"ds_read_b64_tr_b16 a[0:1], v84 offset:40960\n"
"ds_read_b64_tr_b16 a[2:3], v84 offset:43008\n"
"v_rcp_f32 v96, v96\n"
"v_mul_f32 v103, v103, v23\n"
"s_waitcnt lgkmcnt(10)\n"
"v_mfma_f32_32x32x16_bf16 v[176:191], a[12:15], a[80:83], v[176:191]\n"
"ds_read_b64_tr_b16 a[4:5], v85 offset:40960\n"
"ds_read_b64_tr_b16 a[6:7], v85 offset:43008\n"
"v_mul_f32 v100, s77, v28\n"
"v_exp_f32 v99, v99\n"
"s_waitcnt lgkmcnt(10)\n"
"v_mfma_f32_32x32x16_bf16 v[192:207], a[16:19], a[80:83], v[192:207]\n"
"ds_read_b64_tr_b16 a[8:9], v86 offset:40960\n"
"ds_read_b64_tr_b16 a[10:11], v86 offset:43008\n"
"s_add_u32 s71, %[q0], 0x283c00\n"
"buffer_load_dwordx4 v81, %[rsQ], s71 offen offset:1024 lds\n"
"v_add_f32 v98, 1.0, v98\n"
"s_waitcnt lgkmcnt(10)\n"
"v_mfma_f32_32x32x16_bf16 v[208:223], a[20:23], a[80:83], v[208:223]\n"
"ds_read_b64_tr_b16 a[12:13], v87 offset:40960\n"
"ds_read_b64_tr_b16 a[14:15], v87 offset:43008\n"
"v_rcp_f32 v97, v97\n"
"v_mul_f32 v96, v96, v24\n"
"s_waitcnt lgkmcnt(10)\n"
"v_mfma_f32_32x32x16_bf16 v[224:239], a[24:27], a[80:83], v[224:239]\n"
"ds_read_b64_tr_b16 a[16:17], v84 offset:41216\n"
"ds_read_b64_tr_b16 a[18:19], v84 offset:43264\n"
"v_cvt_pk_bf16_f32 v107, v102, v103\n"
"s_add_u32 s68, %[hoff], 0\n"
"buffer_store_dwordx4 v[104:107], v1, %[rsW], s68 offen sc1\n"
"s_waitcnt lgkmcnt(10)\n"
"v_mfma_f32_32x32x16_bf16 v[240:255], a[28:31], a[80:83], v[240:255]\n"
"ds_read_b64_tr_b16 a[20:21], v85 offset:41216\n"
"ds_read_b64_tr_b16 a[22:23], v85 offset:43264\n"
"v_mul_f32 v101, s77, v29\n"
"v_exp_f32 v100, v100\n"
"s_waitcnt lgkmcnt(10)\n"
"v_mfma_f32_32x32x16_bf16 v[128:143], a[0:3], a[84:87], v[128:143]\n"
"ds_read_b64_tr_b16 a[24:25], v86 offset:41216\n"
"ds_read_b64_tr_b16 a[26:27], v86 offset:43264\n"
"s_add_u32 s71, %[q0], 0x287800\n"
"buffer_load_dwordx4 v80, %[rsQ], s71 offen offset:2048 lds\n"
"v_add_f32 v99, 1.0, v99\n"
"s_waitcnt lgkmcnt(10)\n"
"v_mfma_f32_32x32x16_bf16 v[144:159], a[4:7], a[84:87], v[144:159]\n"
"ds_read_b64_tr_b16 a[28:29], v87 offset:41216\n"
"ds_read_b64_tr_b16 a[30:31], v87 offset:43264\n"
"v_rcp_f32 v98, v98\n"
"v_mul_f32 v97, v97, v25\n"
"s_waitcnt lgkmcnt(10)\n"
"v_mfma_f32_32x32x16_bf16 v[160:175], a[8:11], a[84:87], v[160:175]\n"
"ds_read_b64_tr_b16 a[0:1], v84 offset:49152\n"
"ds_read_b64_tr_b16 a[2:3], v84 offset:51200\n"
"v_mul_f32 v102, s77, v30\n"
"v_exp_f32 v101, v101\n"
"s_waitcnt lgkmcnt(10)\n"
"v_mfma_f32_32x32x16_bf16 v[176:191], a[12:15], a[84:87], v[176:191]\n"
"ds_read_b64_tr_b16 a[4:5], v85 offset:49152\n"
"ds_read_b64_tr_b16 a[6:7], v85 offset:51200\n"
"v_add_f32 v100, 1.0, v100\n"
"v_rcp_f32 v99, v99\n"
"s_waitcnt lgkmcnt(10)\n"
"v_mfma_f32_32x32x16_bf16 v[192:207], a[16:19], a[84:87], v[192:207]\n"
"ds_read_b64_tr_b16 a[8:9], v86 offset:49152\n"
"ds_read_b64_tr_b16 a[10:11], v86 offset:51200\n"
"s_add_u32 s71, %[q0], 0x28b400\n"
"buffer_load_dwordx4 v81, %[rsQ], s71 offen offset:3072 lds\n"
"v_mul_f32 v98, v98, v26\n"
"s_waitcnt lgkmcnt(10)\n"
"v_mfma_f32_32x32x16_bf16 v[208:223], a[20:23], a[84:87], v[208:223]\n"
"ds_read_b64_tr_b16 a[12:13], v87 offset:49152\n"
"ds_read_b64_tr_b16 a[14:15], v87 offset:51200\n"
"v_cvt_pk_bf16_f32 v108, v96, v97\n"
"v_mul_f32 v103, s77, v31\n"
"s_waitcnt lgkmcnt(10)\n"
"v_mfma_f32_32x32x16_bf16 v[224:239], a[24:27], a[84:87], v[224:239]\n"
"ds_read_b64_tr_b16 a[16:17], v84 offset:49408\n"
"ds_read_b64_tr_b16 a[18:19], v84 offset:51456\n"
"v_exp_f32 v102, v102\n"
"v_add_f32 v101, 1.0, v101\n"
"s_waitcnt lgkmcnt(10)\n"
"v_mfma_f32_32x32x16_bf16 v[240:255], a[28:31], a[84:87], v[240:255]\n"
"ds_read_b64_tr_b16 a[20:21], v85 offset:49408\n"
"ds_read_b64_tr_b16 a[22:23], v85 offset:51456\n"
"v_rcp_f32 v100, v100\n"
"v_mul_f32 v99, v99, v27\n"
"s_waitcnt lgkmcnt(10)\n"
"v_mfma_f32_32x32x16_bf16 v[128:143], a[0:3], a[88:91], v[128:143]\n"
"ds_read_b64_tr_b16 a[24:25], v86 offset:49408\n"
"ds_read_b64_tr_b16 a[26:27], v86 offset:51456\n"
"s_add_u32 m0, s74, 0x11000\n"
"s_add_u32 s71, %[q0], 0x290000\n"
"buffer_load_dwordx4 v80, %[rsQ], s71 offen lds\n"
"v_mul_f32 v96, s77, v32\n"
"s_waitcnt lgkmcnt(10)\n"
"v_mfma_f32_32x32x16_bf16 v[144:159], a[4:7], a[88:91], v[144:159]\n"
"ds_read_b64_tr_b16 a[28:29], v87 offset:49408\n"
"ds_read_b64_tr_b16 a[30:31], v87 offset:51456\n"
"v_exp_f32 v103, v103\n"
"v_add_f32 v102, 1.0, v102\n"
"s_waitcnt lgkmcnt(10)\n"
"v_mfma_f32_32x32x16_bf16 v[160:175], a[8:11], a[88:91], v[160:175]\n"
"ds_read_b64_tr_b16 a[0:1], v84 offset:57344\n"
"ds_read_b64_tr_b16 a[2:3], v84 offset:59392\n"
"v_rcp_f32 v101, v101\n"
"v_mul_f32 v100, v100, v28\n"
"s_waitcnt lgkmcnt(10)\n"
"v_mfma_f32_32x32x16_bf16 v[176:191], a[12:15], a[88:91], v[176:191]\n"
"ds_read_b64_tr_b16 a[4:5], v85 offset:57344\n"
"ds_read_b64_tr_b16 a[6:7], v85 offset:59392\n"
"v_cvt_pk_bf16_f32 v109, v98, v99\n"
"v_mul_f32 v97, s77, v33\n"
"s_waitcnt lgkmcnt(10)\n"
"v_mfma_f32_32x32x16_bf16 v[192:207], a[16:19], a[88:91], v[192:207]\n"
"ds_read_b64_tr_b16 a[8:9], v86 offset:57344\n"
"ds_read_b64_tr_b16 a[10:11], v86 offset:59392\n"
"s_add_u32 s71, %[q0], 0x293c00\n"
"buffer_load_dwordx4 v81, %[rsQ], s71 offen offset:1024 lds\n"
"s_waitcnt lgkmcnt(10)\n"
"v_mfma_f32_32x32x16_bf16 v[208:223], a[20:23], a[88:91], v[208:223]\n"
"ds_read_b64_tr_b16 a[12:13], v87 offset:57344\n"
"ds_read_b64_tr_b16 a[14:15], v87 offset:59392\n"
"v_exp_f32 v96, v96\n"
"v_add_f32 v103, 1.0, v103\n"
"s_waitcnt lgkmcnt(10)\n"
"v_mfma_f32_32x32x16_bf16 v[224:239], a[24:27], a[88:91], v[224:239]\n"
"ds_read_b64_tr_b16 a[16:17], v84 offset:57600\n"
"ds_read_b64_tr_b16 a[18:19], v84 offset:59648\n"
"v_rcp_f32 v102, v102\n"
"v_mul_f32 v101, v101, v29\n"
"s_waitcnt lgkmcnt(10)\n"
"v_mfma_f32_32x32x16_bf16 v[240:255], a[28:31], a[88:91], v[240:255]\n"
"ds_read_b64_tr_b16 a[20:21], v85 offset:57600\n"
"ds_read_b64_tr_b16 a[22:23], v85 offset:59648\n"
"v_mul_f32 v98, s77, v34\n"
"v_exp_f32 v97, v97\n"
"s_waitcnt lgkmcnt(10)\n"
"v_mfma_f32_32x32x16_bf16 v[128:143], a[0:3], a[92:95], v[128:143]\n"
"ds_read_b64_tr_b16 a[24:25], v86 offset:57600\n"
"ds_read_b64_tr_b16 a[26:27], v86 offset:59648\n"
"s_add_u32 s71, %[q0], 0x297800\n"
"buffer_load_dwordx4 v80, %[rsQ], s71 offen offset:2048 lds\n"
"v_add_f32 v96, 1.0, v96\n"
"s_waitcnt lgkmcnt(10)\n"
"v_mfma_f32_32x32x16_bf16 v[144:159], a[4:7], a[92:95], v[144:159]\n"
"ds_read_b64_tr_b16 a[28:29], v87 offset:57600\n"
"ds_read_b64_tr_b16 a[30:31], v87 offset:59648\n"
"v_rcp_f32 v103, v103\n"
"v_mul_f32 v102, v102, v30\n"
"s_waitcnt lgkmcnt(10)\n"
"v_mfma_f32_32x32x16_bf16 v[160:175], a[8:11], a[92:95], v[160:175]\n"
"v_add_u32 v84, 0x10000, v84\n"
"ds_read_b64_tr_b16 a[0:1], v84\n"
"ds_read_b64_tr_b16 a[2:3], v84 offset:2048\n"
"v_cvt_pk_bf16_f32 v110, v100, v101\n"
"v_mul_f32 v99, s77, v35\n"
"s_waitcnt lgkmcnt(10)\n"
"v_mfma_f32_32x32x16_bf16 v[176:191], a[12:15], a[92:95], v[176:191]\n"
"v_add_u32 v85, 0x10000, v85\n"
"ds_read_b64_tr_b16 a[4:5], v85\n"
"ds_read_b64_tr_b16 a[6:7], v85 offset:2048\n"
"v_exp_f32 v98, v98\n"
"v_add_f32 v97, 1.0, v97\n"
"s_waitcnt lgkmcnt(10)\n"
"v_mfma_f32_32x32x16_bf16 v[192:207], a[16:19], a[92:95], v[192:207]\n"
"v_add_u32 v86, 0x10000, v86\n"
"ds_read_b64_tr_b16 a[8:9], v86\n"
"ds_read_b64_tr_b16 a[10:11], v86 offset:2048\n"
"s_add_u32 s71, %[q0], 0x29b400\n"
"buffer_load_dwordx4 v81, %[rsQ], s71 offen offset:3072 lds\n"
"s_waitcnt lgkmcnt(10)\n"
"v_mfma_f32_32x32x16_bf16 v[208:223], a[20:23], a[92:95], v[208:223]\n"
"v_add_u32 v87, 0x10000, v87\n"
"ds_read_b64_tr_b16 a[12:13], v87\n"
"ds_read_b64_tr_b16 a[14:15], v87 offset:2048\n"
"v_rcp_f32 v96, v96\n"
"v_mul_f32 v103, v103, v31\n"
"s_waitcnt lgkmcnt(10)\n"
"v_mfma_f32_32x32x16_bf16 v[224:239], a[24:27], a[92:95], v[224:239]\n"
"ds_read_b64_tr_b16 a[16:17], v84 offset:256\n"
"ds_read_b64_tr_b16 a[18:19], v84 offset:2304\n"
"v_mul_f32 v100, s77, v36\n"
"v_exp_f32 v99, v99\n"
"s_waitcnt lgkmcnt(10)\n"
"v_mfma_f32_32x32x16_bf16 v[240:255], a[28:31], a[92:95], v[240:255]\n"
"ds_read_b64_tr_b16 a[20:21], v85 offset:256\n"
"ds_read_b64_tr_b16 a[22:23], v85 offset:2304\n"
"v_add_f32 v98, 1.0, v98\n"
"v_rcp_f32 v97, v97\n"
"s_waitcnt vmcnt(18)\n"
"s_barrier\n"
"s_waitcnt lgkmcnt(10)\n"
"v_mfma_f32_32x32x16_bf16 v[128:143], a[0:3], a[96:99], v[128:143]\n"
"ds_read_b64_tr_b16 a[24:25], v86 offset:256\n"
"ds_read_b64_tr_b16 a[26:27], v86 offset:2304\n"
"s_add_u32 m0, s74, 0x18000\n"
"s_add_u32 s71, %[q0], 0x300000\n"
"buffer_load_dwordx4 v80, %[rsQ], s71 offen lds\n"
"v_mul_f32 v96, v96, v32\n"
"s_waitcnt lgkmcnt(10)\n"
"v_mfma_f32_32x32x16_bf16 v[144:159], a[4:7], a[96:99], v[144:159]\n"
"ds_read_b64_tr_b16 a[28:29], v87 offset:256\n"
"ds_read_b64_tr_b16 a[30:31], v87 offset:2304\n"
"v_cvt_pk_bf16_f32 v111, v102, v103\n"
"s_add_u32 s68, %[hoff], 0\n"
"buffer_store_dwordx4 v[108:111], v1, %[rsW], s68 offen offset:1024 sc1\n"
"s_waitcnt lgkmcnt(10)\n"
"v_mfma_f32_32x32x16_bf16 v[160:175], a[8:11], a[96:99], v[160:175]\n"
"ds_read_b64_tr_b16 a[0:1], v84 offset:8192\n"
"ds_read_b64_tr_b16 a[2:3], v84 offset:10240\n"
"v_mul_f32 v101, s77, v37\n"
"v_exp_f32 v100, v100\n"
"s_waitcnt lgkmcnt(10)\n"
"v_mfma_f32_32x32x16_bf16 v[176:191], a[12:15], a[96:99], v[176:191]\n"
"ds_read_b64_tr_b16 a[4:5], v85 offset:8192\n"
"ds_read_b64_tr_b16 a[6:7], v85 offset:10240\n"
"v_add_f32 v99, 1.0, v99\n"
"v_rcp_f32 v98, v98\n"
"s_waitcnt lgkmcnt(10)\n"
"v_mfma_f32_32x32x16_bf16 v[192:207], a[16:19], a[96:99], v[192:207]\n"
"ds_read_b64_tr_b16 a[8:9], v86 offset:8192\n"
"ds_read_b64_tr_b16 a[10:11], v86 offset:10240\n"
"s_add_u32 s71, %[q0], 0x303c00\n"
"buffer_load_dwordx4 v81, %[rsQ], s71 offen offset:1024 lds\n"
"v_mul_f32 v97, v97, v33\n"
"s_waitcnt lgkmcnt(10)\n"
"v_mfma_f32_32x32x16_bf16 v[208:223], a[20:23], a[96:99], v[208:223]\n"
"ds_read_b64_tr_b16 a[12:13], v87 offset:8192\n"
"ds_read_b64_tr_b16 a[14:15], v87 offset:10240\n"
"v_mul_f32 v102, s77, v38\n"
"v_exp_f32 v101, v101\n"
"s_waitcnt lgkmcnt(10)\n"
"v_mfma_f32_32x32x16_bf16 v[224:239], a[24:27], a[96:99], v[224:239]\n"
"ds_read_b64_tr_b16 a[16:17], v84 offset:8448\n"
"ds_read_b64_tr_b16 a[18:19], v84 offset:10496\n"
"v_add_f32 v100, 1.0, v100\n"
"v_rcp_f32 v99, v99\n"
"s_waitcnt lgkmcnt(10)\n"
"v_mfma_f32_32x32x16_bf16 v[240:255], a[28:31], a[96:99], v[240:255]\n"
"ds_read_b64_tr_b16 a[20:21], v85 offset:8448\n"
"ds_read_b64_tr_b16 a[22:23], v85 offset:10496\n"
"v_mul_f32 v98, v98, v34\n"
"v_cvt_pk_bf16_f32 v104, v96, v97\n"
"v_mul_f32 v103, s77, v39\n"
"s_waitcnt lgkmcnt(10)\n"
"v_mfma_f32_32x32x16_bf16 v[128:143], a[0:3], a[100:103], v[128:143]\n"
"ds_read_b64_tr_b16 a[24:25], v86 offset:8448\n"
"ds_read_b64_tr_b16 a[26:27], v86 offset:10496\n"
"s_add_u32 s71, %[q0], 0x307800\n"
"buffer_load_dwordx4 v80, %[rsQ], s71 offen offset:2048 lds\n"
"s_waitcnt lgkmcnt(10)\n"
"v_mfma_f32_32x32x16_bf16 v[144:159], a[4:7], a[100:103], v[144:159]\n"
"ds_read_b64_tr_b16 a[28:29], v87 offset:8448\n"
"ds_read_b64_tr_b16 a[30:31], v87 offset:10496\n"
"v_exp_f32 v102, v102\n"
"v_add_f32 v101, 1.0, v101\n"
"s_waitcnt lgkmcnt(10)\n"
"v_mfma_f32_32x32x16_bf16 v[160:175], a[8:11], a[100:103], v[160:175]\n"
"ds_read_b64_tr_b16 a[0:1], v84 offset:16384\n"
"ds_read_b64_tr_b16 a[2:3], v84 offset:18432\n"
"v_rcp_f32 v100, v100\n"
"v_mul_f32 v99, v99, v35\n"
"s_waitcnt lgkmcnt(10)\n"
"v_mfma_f32_32x32x16_bf16 v[176:191], a[12:15], a[100:103], v[176:191]\n"
"ds_read_b64_tr_b16 a[4:5], v85 offset:16384\n"
"ds_read_b64_tr_b16 a[6:7], v85 offset:18432\n"
"v_mul_f32 v96, s77, v40\n"
"v_exp_f32 v103, v103\n"
"s_waitcnt lgkmcnt(10)\n"
"v_mfma_f32_32x32x16_bf16 v[192:207], a[16:19], a[100:103], v[192:207]\n"
"ds_read_b64_tr_b16 a[8:9], v86 offset:16384\n"
"ds_read_b64_tr_b16 a[10:11], v86 offset:18432\n"
"s_add_u32 s71, %[q0], 0x30b400\n"
"buffer_load_dwordx4 v81, %[rsQ], s71 offen offset:3072 lds\n"
"v_add_f32 v102, 1.0, v102\n"
"s_waitcnt lgkmcnt(10)\n"
"v_mfma_f32_32x32x16_bf16 v[208:223], a[20:23], a[100:103], v[208:223]\n"
"ds_read_b64_tr_b16 a[12:13], v87 offset:16384\n"
"ds_read_b64_tr_b16 a[14:15], v87 offset:18432\n"
"v_rcp_f32 v101, v101\n"
"v_mul_f32 v100, v100, v36\n"
"s_waitcnt lgkmcnt(10)\n"
"v_mfma_f32_32x32x16_bf16 v[224:239], a[24:27], a[100:103], v[224:239]\n"
"ds_read_b64_tr_b16 a[16:17], v84 offset:16640\n"
"ds_read_b64_tr_b16 a[18:19], v84 offset:18688\n"
"v_cvt_pk_bf16_f32 v105, v98, v99\n"
"v_mul_f32 v97, s77, v41\n"
"s_waitcnt lgkmcnt(10)\n"
"v_mfma_f32_32x32x16_bf16 v[240:255], a[28:31], a[100:103], v[240:255]\n"
"ds_read_b64_tr_b16 a[20:21], v85 offset:16640\n"
"ds_read_b64_tr_b16 a[22:23], v85 offset:18688\n"
"v_exp_f32 v96, v96\n"
"v_add_f32 v103, 1.0, v103\n"
"s_waitcnt lgkmcnt(10)\n"
"v_mfma_f32_32x32x16_bf16 v[128:143], a[0:3], a[104:107], v[128:143]\n"
"ds_read_b64_tr_b16 a[24:25], v86 offset:16640\n"
"ds_read_b64_tr_b16 a[26:27], v86 offset:18688\n"
"s_add_u32 m0, s74, 0x19000\n"
"s_add_u32 s71, %[q0], 0x310000\n"
"buffer_load_dwordx4 v80, %[rsQ], s71 offen lds\n"
"s_waitcnt lgkmcnt(10)\n"
"v_mfma_f32_32x32x16_bf16 v[144:159], a[4:7], a[104:107], v[144:159]\n"
"ds_read_b64_tr_b16 a[28:29], v87 offset:16640\n"
"ds_read_b64_tr_b16 a[30:31], v87 offset:18688\n"
"v_rcp_f32 v102, v102\n"
"v_mul_f32 v101, v101, v37\n"
"s_waitcnt lgkmcnt(10)\n"
"v_mfma_f32_32x32x16_bf16 v[160:175], a[8:11], a[104:107], v[160:175]\n"
"ds_read_b64_tr_b16 a[0:1], v84 offset:24576\n"
"ds_read_b64_tr_b16 a[2:3], v84 offset:26624\n"
"v_mul_f32 v98, s77, v42\n"
"v_exp_f32 v97, v97\n"
"s_waitcnt lgkmcnt(10)\n"
"v_mfma_f32_32x32x16_bf16 v[176:191], a[12:15], a[104:107], v[176:191]\n"
"ds_read_b64_tr_b16 a[4:5], v85 offset:24576\n"
"ds_read_b64_tr_b16 a[6:7], v85 offset:26624\n"
"v_add_f32 v96, 1.0, v96\n"
"v_rcp_f32 v103, v103\n"
"s_waitcnt lgkmcnt(10)\n"
"v_mfma_f32_32x32x16_bf16 v[192:207], a[16:19], a[104:107], v[192:207]\n"
"ds_read_b64_tr_b16 a[8:9], v86 offset:24576\n"
"ds_read_b64_tr_b16 a[10:11], v86 offset:26624\n"
"s_add_u32 s71, %[q0], 0x313c00\n"
"buffer_load_dwordx4 v81, %[rsQ], s71 offen offset:1024 lds\n"
"v_mul_f32 v102, v102, v38\n"
"s_waitcnt lgkmcnt(10)\n"
"v_mfma_f32_32x32x16_bf16 v[208:223], a[20:23], a[104:107], v[208:223]\n"
"ds_read_b64_tr_b16 a[12:13], v87 offset:24576\n"
"ds_read_b64_tr_b16 a[14:15], v87 offset:26624\n"
"v_cvt_pk_bf16_f32 v106, v100, v101\n"
"v_mul_f32 v99, s77, v43\n"
"s_waitcnt lgkmcnt(10)\n"
"v_mfma_f32_32x32x16_bf16 v[224:239], a[24:27], a[104:107], v[224:239]\n"
"ds_read_b64_tr_b16 a[16:17], v84 offset:24832\n"
"ds_read_b64_tr_b16 a[18:19], v84 offset:26880\n"
"v_exp_f32 v98, v98\n"
"v_add_f32 v97, 1.0, v97\n"
"s_waitcnt lgkmcnt(10)\n"
"v_mfma_f32_32x32x16_bf16 v[240:255], a[28:31], a[104:107], v[240:255]\n"
"ds_read_b64_tr_b16 a[20:21], v85 offset:24832\n"
"ds_read_b64_tr_b16 a[22:23], v85 offset:26880\n"
"v_rcp_f32 v96, v96\n"
"v_mul_f32 v103, v103, v39\n"
"s_waitcnt lgkmcnt(10)\n"
"v_mfma_f32_32x32x16_bf16 v[128:143], a[0:3], a[108:111], v[128:143]\n"
"ds_read_b64_tr_b16 a[24:25], v86 offset:24832\n"
"ds_read_b64_tr_b16 a[26:27], v86 offset:26880\n"
"s_add_u32 s71, %[q0], 0x317800\n"
"buffer_load_dwordx4 v80, %[rsQ], s71 offen offset:2048 lds\n"
"v_mul_f32 v100, s77, v44\n"
"s_waitcnt lgkmcnt(10)\n"
"v_mfma_f32_32x32x16_bf16 v[144:159], a[4:7], a[108:111], v[144:159]\n"
"ds_read_b64_tr_b16 a[28:29], v87 offset:24832\n"
"ds_read_b64_tr_b16 a[30:31], v87 offset:26880\n"
"v_exp_f32 v99, v99\n"
"v_add_f32 v98, 1.0, v98\n"
"s_waitcnt lgkmcnt(10)\n"
"v_mfma_f32_32x32x16_bf16 v[160:175], a[8:11], a[108:111], v[160:175]\n"
"v_add_u32 v84, 0xfffe0000, v84\n"
"ds_read_b64_tr_b16 a[0:1], v84\n"
"ds_read_b64_tr_b16 a[2:3], v84 offset:2048\n"
"v_rcp_f32 v97, v97\n"
"v_mul_f32 v96, v96, v40\n"
"s_waitcnt lgkmcnt(10)\n"
"v_mfma_f32_32x32x16_bf16 v[176:191], a[12:15], a[108:111], v[176:191]\n"
"v_add_u32 v85, 0xfffe0000, v85\n"
"ds_read_b64_tr_b16 a[4:5], v85\n"
"ds_read_b64_tr_b16 a[6:7], v85 offset:2048\n"
"v_cvt_pk_bf16_f32 v107, v102, v103\n"
"s_add_u32 s68, %[hoff], 0\n"
"buffer_store_dwordx4 v[104:107], v1, %[rsW], s68 offen offset:2048 sc1\n"
"s_waitcnt lgkmcnt(10)\n"
"v_mfma_f32_32x32x16_bf16 v[192:207], a[16:19], a[108:111], v[192:207]\n"
"v_add_u32 v86, 0xfffe0000, v86\n"
"ds_read_b64_tr_b16 a[8:9], v86\n"
"ds_read_b64_tr_b16 a[10:11], v86 offset:2048\n"
"s_add_u32 s71, %[q0], 0x31b400\n"
"buffer_load_dwordx4 v81, %[rsQ], s71 offen offset:3072 lds\n"
"v_mul_f32 v101, s77, v45\n"
"s_waitcnt lgkmcnt(10)\n"
"v_mfma_f32_32x32x16_bf16 v[208:223], a[20:23], a[108:111], v[208:223]\n"
"v_add_u32 v87, 0xfffe0000, v87\n"
"ds_read_b64_tr_b16 a[12:13], v87\n"
"ds_read_b64_tr_b16 a[14:15], v87 offset:2048\n"
"v_exp_f32 v100, v100\n"
"v_add_f32 v99, 1.0, v99\n"
"s_waitcnt lgkmcnt(10)\n"
"v_mfma_f32_32x32x16_bf16 v[224:239], a[24:27], a[108:111], v[224:239]\n"
"ds_read_b64_tr_b16 a[16:17], v84 offset:256\n"
"ds_read_b64_tr_b16 a[18:19], v84 offset:2304\n"
"v_rcp_f32 v98, v98\n"
"v_mul_f32 v97, v97, v41\n"
"s_waitcnt lgkmcnt(10)\n"
"v_mfma_f32_32x32x16_bf16 v[240:255], a[28:31], a[108:111], v[240:255]\n"
"ds_read_b64_tr_b16 a[20:21], v85 offset:256\n"
"ds_read_b64_tr_b16 a[22:23], v85 offset:2304\n"
"v_mul_f32 v102, s77, v46\n"
"v_exp_f32 v101, v101\n"
"s_waitcnt vmcnt(20)\n"
"s_barrier\n"
"s_waitcnt lgkmcnt(10)\n"
"v_mfma_f32_32x32x16_bf16 v[128:143], a[0:3], a[112:115], v[128:143]\n"
"ds_read_b64_tr_b16 a[24:25], v86 offset:256\n"
"ds_read_b64_tr_b16 a[26:27], v86 offset:2304\n"
"v_add_f32 v100, 1.0, v100\n"
"v_rcp_f32 v99, v99\n"
"v_mul_f32 v98, v98, v42\n"
"s_waitcnt lgkmcnt(10)\n"
"v_mfma_f32_32x32x16_bf16 v[144:159], a[4:7], a[112:115], v[144:159]\n"
"ds_read_b64_tr_b16 a[28:29], v87 offset:256\n"
"ds_read_b64_tr_b16 a[30:31], v87 offset:2304\n"
"v_cvt_pk_bf16_f32 v108, v96, v97\n"
"v_mul_f32 v103, s77, v47\n"
"v_exp_f32 v102, v102\n"
"s_waitcnt lgkmcnt(10)\n"
"v_mfma_f32_32x32x16_bf16 v[160:175], a[8:11], a[112:115], v[160:175]\n"
"ds_read_b64_tr_b16 a[0:1], v84 offset:8192\n"
"ds_read_b64_tr_b16 a[2:3], v84 offset:10240\n"
"v_add_f32 v101, 1.0, v101\n"
"v_rcp_f32 v100, v100\n"
"v_mul_f32 v99, v99, v43\n"
"s_waitcnt lgkmcnt(10)\n"
"v_mfma_f32_32x32x16_bf16 v[176:191], a[12:15], a[112:115], v[176:191]\n"
"ds_read_b64_tr_b16 a[4:5], v85 offset:8192\n"
"ds_read_b64_tr_b16 a[6:7], v85 offset:10240\n"
"v_mul_f32 v96, s77, v48\n"
"v_exp_f32 v103, v103\n"
"v_add_f32 v102, 1.0, v102\n"
"s_waitcnt lgkmcnt(10)\n"
"v_mfma_f32_32x32x16_bf16 v[192:207], a[16:19], a[112:115], v[192:207]\n"
"ds_read_b64_tr_b16 a[8:9], v86 offset:8192\n"
"ds_read_b64_tr_b16 a[10:11], v86 offset:10240\n"
"v_rcp_f32 v101, v101\n"
"v_mul_f32 v100, v100, v44\n"
"v_cvt_pk_bf16_f32 v109, v98, v99\n"
"s_waitcnt lgkmcnt(10)\n"
"v_mfma_f32_32x32x16_bf16 v[208:223], a[20:23], a[112:115], v[208:223]\n"
"ds_read_b64_tr_b16 a[12:13], v87 offset:8192\n"
"ds_read_b64_tr_b16 a[14:15], v87 offset:10240\n"
"v_mul_f32 v97, s77, v49\n"
"v_exp_f32 v96, v96\n"
"v_add_f32 v103, 1.0, v103\n"
"s_waitcnt lgkmcnt(10)\n"
"v_mfma_f32_32x32x16_bf16 v[224:239], a[24:27], a[112:115], v[224:239]\n"
"ds_read_b64_tr_b16 a[16:17], v84 offset:8448\n"
"ds_read_b64_tr_b16 a[18:19], v84 offset:10496\n"
"v_rcp_f32 v102, v102\n"
"v_mul_f32 v101, v101, v45\n"
"v_mul_f32 v98, s77, v50\n"
"s_waitcnt lgkmcnt(10)\n"
"v_mfma_f32_32x32x16_bf16 v[240:255], a[28:31], a[112:115], v[240:255]\n"
"ds_read_b64_tr_b16 a[20:21], v85 offset:8448\n"
"ds_read_b64_tr_b16 a[22:23], v85 offset:10496\n"
"v_exp_f32 v97, v97\n"
"v_add_f32 v96, 1.0, v96\n"
"s_waitcnt lgkmcnt(10)\n"
"v_mfma_f32_32x32x16_bf16 v[128:143], a[0:3], a[116:119], v[128:143]\n"
"ds_read_b64_tr_b16 a[24:25], v86 offset:8448\n"
"ds_read_b64_tr_b16 a[26:27], v86 offset:10496\n"
"v_rcp_f32 v103, v103\n"
"v_mul_f32 v102, v102, v46\n"
"v_cvt_pk_bf16_f32 v110, v100, v101\n"
"s_waitcnt lgkmcnt(10)\n"
"v_mfma_f32_32x32x16_bf16 v[144:159], a[4:7], a[116:119], v[144:159]\n"
"ds_read_b64_tr_b16 a[28:29], v87 offset:8448\n"
"ds_read_b64_tr_b16 a[30:31], v87 offset:10496\n"
"v_mul_f32 v99, s77, v51\n"
"v_exp_f32 v98, v98\n"
"v_add_f32 v97, 1.0, v97\n"
"s_waitcnt lgkmcnt(10)\n"
"v_mfma_f32_32x32x16_bf16 v[160:175], a[8:11], a[116:119], v[160:175]\n"
"ds_read_b64_tr_b16 a[0:1], v84 offset:16384\n"
"ds_read_b64_tr_b16 a[2:3], v84 offset:18432\n"
"v_rcp_f32 v96, v96\n"
"v_mul_f32 v103, v103, v47\n"
"v_mul_f32 v100, s77, v52\n"
"s_waitcnt lgkmcnt(10)\n"
"v_mfma_f32_32x32x16_bf16 v[176:191], a[12:15], a[116:119], v[176:191]\n"
"ds_read_b64_tr_b16 a[4:5], v85 offset:16384\n"
"ds_read_b64_tr_b16 a[6:7], v85 offset:18432\n"
"v_exp_f32 v99, v99\n"
"v_add_f32 v98, 1.0, v98\n"
"s_waitcnt lgkmcnt(10)\n"
"v_mfma_f32_32x32x16_bf16 v[192:207], a[16:19], a[116:119], v[192:207]\n"
"ds_read_b64_tr_b16 a[8:9], v86 offset:16384\n"
"ds_read_b64_tr_b16 a[10:11], v86 offset:18432\n"
"v_rcp_f32 v97, v97\n"
"v_mul_f32 v96, v96, v48\n"
"v_cvt_pk_bf16_f32 v111, v102, v103\n"
"s_waitcnt lgkmcnt(10)\n"
"v_mfma_f32_32x32x16_bf16 v[208:223], a[20:23], a[116:119], v[208:223]\n"
"ds_read_b64_tr_b16 a[12:13], v87 offset:16384\n"
"ds_read_b64_tr_b16 a[14:15], v87 offset:18432\n"
"s_add_u32 s68, %[hoff], 0\n"
"buffer_store_dwordx4 v[108:111], v1, %[rsW], s68 offen offset:3072 sc1\n"
"v_mul_f32 v101, s77, v53\n"
"s_waitcnt lgkmcnt(10)\n"
"v_mfma_f32_32x32x16_bf16 v[224:239], a[24:27], a[116:119], v[224:239]\n"
"ds_read_b64_tr_b16 a[16:17], v84 offset:16640\n"
"ds_read_b64_tr_b16 a[18:19], v84 offset:18688\n"
"v_exp_f32 v100, v100\n"
"v_add_f32 v99, 1.0, v99\n"
"s_waitcnt lgkmcnt(10)\n"
"v_mfma_f32_32x32x16_bf16 v[240:255], a[28:31], a[116:119], v[240:255]\n"
"ds_read_b64_tr_b16 a[20:21], v85 offset:16640\n"
"ds_read_b64_tr_b16 a[22:23], v85 offset:18688\n"
"v_rcp_f32 v98, v98\n"
"v_mul_f32 v97, v97, v49\n"
"v_mul_f32 v102, s77, v54\n"
"s_waitcnt lgkmcnt(10)\n"
"v_mfma_f32_32x32x16_bf16 v[128:143], a[0:3], a[120:123], v[128:143]\n"
"ds_read_b64_tr_b16 a[24:25], v86 offset:16640\n"
"ds_read_b64_tr_b16 a[26:27], v86 offset:18688\n"
"v_exp_f32 v101, v101\n"
"v_add_f32 v100, 1.0, v100\n"
"s_waitcnt lgkmcnt(10)\n"
"v_mfma_f32_32x32x16_bf16 v[144:159], a[4:7], a[120:123], v[144:159]\n"
"ds_read_b64_tr_b16 a[28:29], v87 offset:16640\n"
"ds_read_b64_tr_b16 a[30:31], v87 offset:18688\n"
"v_rcp_f32 v99, v99\n"
"v_mul_f32 v98, v98, v50\n"
"v_cvt_pk_bf16_f32 v104, v96, v97\n"
"s_waitcnt lgkmcnt(10)\n"
"v_mfma_f32_32x32x16_bf16 v[160:175], a[8:11], a[120:123], v[160:175]\n"
"ds_read_b64_tr_b16 a[0:1], v84 offset:24576\n"
"ds_read_b64_tr_b16 a[2:3], v84 offset:26624\n"
"v_mul_f32 v103, s77, v55\n"
"v_exp_f32 v102, v102\n"
"v_add_f32 v101, 1.0, v101\n"
"s_waitcnt lgkmcnt(10)\n"
"v_mfma_f32_32x32x16_bf16 v[176:191], a[12:15], a[120:123], v[176:191]\n"
"ds_read_b64_tr_b16 a[4:5], v85 offset:24576\n"
"ds_read_b64_tr_b16 a[6:7], v85 offset:26624\n"
"v_rcp_f32 v100, v100\n"
"v_mul_f32 v99, v99, v51\n"
"v_mul_f32 v96, s77, v56\n"
"s_waitcnt lgkmcnt(10)\n"
"v_mfma_f32_32x32x16_bf16 v[192:207], a[16:19], a[120:123], v[192:207]\n"
"ds_read_b64_tr_b16 a[8:9], v86 offset:24576\n"
"ds_read_b64_tr_b16 a[10:11], v86 offset:26624\n"
"v_exp_f32 v103, v103\n"
"v_add_f32 v102, 1.0, v102\n"
"s_waitcnt lgkmcnt(10)\n"
"v_mfma_f32_32x32x16_bf16 v[208:223], a[20:23], a[120:123], v[208:223]\n"
"ds_read_b64_tr_b16 a[12:13], v87 offset:24576\n"
"ds_read_b64_tr_b16 a[14:15], v87 offset:26624\n"
"v_rcp_f32 v101, v101\n"
"v_mul_f32 v100, v100, v52\n"
"v_cvt_pk_bf16_f32 v105, v98, v99\n"
"s_waitcnt lgkmcnt(10)\n"
"v_mfma_f32_32x32x16_bf16 v[224:239], a[24:27], a[120:123], v[224:239]\n"
"ds_read_b64_tr_b16 a[16:17], v84 offset:24832\n"
"ds_read_b64_tr_b16 a[18:19], v84 offset:26880\n"
"v_mul_f32 v97, s77, v57\n"
"v_exp_f32 v96, v96\n"
"v_add_f32 v103, 1.0, v103\n"
"s_waitcnt lgkmcnt(10)\n"
"v_mfma_f32_32x32x16_bf16 v[240:255], a[28:31], a[120:123], v[240:255]\n"
"ds_read_b64_tr_b16 a[20:21], v85 offset:24832\n"
"ds_read_b64_tr_b16 a[22:23], v85 offset:26880\n"
"v_rcp_f32 v102, v102\n"
"v_mul_f32 v101, v101, v53\n"
"v_mul_f32 v98, s77, v58\n"
"s_waitcnt lgkmcnt(10)\n"
"v_mfma_f32_32x32x16_bf16 v[128:143], a[0:3], a[124:127], v[128:143]\n"
"ds_read_b64_tr_b16 a[24:25], v86 offset:24832\n"
"ds_read_b64_tr_b16 a[26:27], v86 offset:26880\n"
"v_exp_f32 v97, v97\n"
"v_add_f32 v96, 1.0, v96\n"
"s_waitcnt lgkmcnt(10)\n"
"v_mfma_f32_32x32x16_bf16 v[144:159], a[4:7], a[124:127], v[144:159]\n"
"ds_read_b64_tr_b16 a[28:29], v87 offset:24832\n"
"ds_read_b64_tr_b16 a[30:31], v87 offset:26880\n"
"v_rcp_f32 v103, v103\n"
"v_mul_f32 v102, v102, v54\n"
"v_cvt_pk_bf16_f32 v106, v100, v101\n"
"s_waitcnt lgkmcnt(10)\n"
"v_mfma_f32_32x32x16_bf16 v[160:175], a[8:11], a[124:127], v[160:175]\n"
"ds_read_b64_tr_b16 a[0:1], v84 offset:32768\n"
"ds_read_b64_tr_b16 a[2:3], v84 offset:34816\n"
"v_mul_f32 v99, s77, v59\n"
"v_exp_f32 v98, v98\n"
"v_add_f32 v97, 1.0, v97\n"
"s_waitcnt lgkmcnt(10)\n"
"v_mfma_f32_32x32x16_bf16 v[176:191], a[12:15], a[124:127], v[176:191]\n"
"ds_read_b64_tr_b16 a[4:5], v85 offset:32768\n"
"ds_read_b64_tr_b16 a[6:7], v85 offset:34816\n"
"v_rcp_f32 v96, v96\n"
"v_mul_f32 v103, v103, v55\n"
"v_mul_f32 v100, s77, v60\n"
"s_waitcnt lgkmcnt(10)\n"
"v_mfma_f32_32x32x16_bf16 v[192:207], a[16:19], a[124:127], v[192:207]\n"
"ds_read_b64_tr_b16 a[8:9], v86 offset:32768\n"
"ds_read_b64_tr_b16 a[10:11], v86 offset:34816\n"
"v_exp_f32 v99, v99\n"
"v_add_f32 v98, 1.0, v98\n"
"s_waitcnt lgkmcnt(10)\n"
"v_mfma_f32_32x32x16_bf16 v[208:223], a[20:23], a[124:127], v[208:223]\n"
"ds_read_b64_tr_b16 a[12:13], v87 offset:32768\n"
"ds_read_b64_tr_b16 a[14:15], v87 offset:34816\n"
"v_rcp_f32 v97, v97\n"
"v_mul_f32 v96, v96, v56\n"
"v_cvt_pk_bf16_f32 v107, v102, v103\n"
"s_waitcnt lgkmcnt(10)\n"
"v_mfma_f32_32x32x16_bf16 v[224:239], a[24:27], a[124:127], v[224:239]\n"
"ds_read_b64_tr_b16 a[16:17], v84 offset:33024\n"
"ds_read_b64_tr_b16 a[18:19], v84 offset:35072\n"
"s_add_u32 s68, %[hoff], 0x1000\n"
"buffer_store_dwordx4 v[104:107], v1, %[rsW], s68 offen sc1\n"
"v_mul_f32 v101, s77, v61\n"
"s_waitcnt lgkmcnt(10)\n"
"v_mfma_f32_32x32x16_bf16 v[240:255], a[28:31], a[124:127], v[240:255]\n"
"ds_read_b64_tr_b16 a[20:21], v85 offset:33024\n"
"ds_read_b64_tr_b16 a[22:23], v85 offset:35072\n"
"v_exp_f32 v100, v100\n"
"v_add_f32 v99, 1.0, v99\n"
"s_waitcnt vmcnt(12)\n"
"s_barrier\n"
"s_waitcnt lgkmcnt(10)\n"
"v_mfma_f32_32x32x16_bf16 v[128:143], a[0:3], a[128:131], v[128:143]\n"
"ds_read_b64_tr_b16 a[24:25], v86 offset:33024\n"
"ds_read_b64_tr_b16 a[26:27], v86 offset:35072\n"
"v_rcp_f32 v98, v98\n"
"v_mul_f32 v97, v97, v57\n"
"v_mul_f32 v102, s77, v62\n"
"s_waitcnt lgkmcnt(10)\n"
"v_mfma_f32_32x32x16_bf16 v[144:159], a[4:7], a[128:131], v[144:159]\n"
"ds_read_b64_tr_b16 a[28:29], v87 offset:33024\n"
"ds_read_b64_tr_b16 a[30:31], v87 offset:35072\n"
"v_exp_f32 v101, v101\n"
"v_add_f32 v100, 1.0, v100\n"
"s_waitcnt lgkmcnt(10)\n"
"v_mfma_f32_32x32x16_bf16 v[160:175], a[8:11], a[128:131], v[160:175]\n"
"ds_read_b64_tr_b16 a[0:1], v84 offset:40960\n"
"ds_read_b64_tr_b16 a[2:3], v84 offset:43008\n"
"buffer_load_dwordx2 v[14:15], v11, %[rsW], %[kvl] offen sc1\n"
"v_rcp_f32 v99, v99\n"
"v_mul_f32 v98, v98, v58\n"
"v_cvt_pk_bf16_f32 v108, v96, v97\n"
"s_waitcnt lgkmcnt(10)\n"
"v_mfma_f32_32x32x16_bf16 v[176:191], a[12:15], a[128:131], v[176:191]\n"
"ds_read_b64_tr_b16 a[4:5], v85 offset:40960\n"
"ds_read_b64_tr_b16 a[6:7], v85 offset:43008\n"
"v_mul_f32 v103, s77, v63\n"
"v_exp_f32 v102, v102\n"
"v_add_f32 v101, 1.0, v101\n"
"s_waitcnt lgkmcnt(10)\n"
"v_mfma_f32_32x32x16_bf16 v[192:207], a[16:19], a[128:131], v[192:207]\n"
"ds_read_b64_tr_b16 a[8:9], v86 offset:40960\n"
"ds_read_b64_tr_b16 a[10:11], v86 offset:43008\n"
"v_rcp_f32 v100, v100\n"
"v_mul_f32 v99, v99, v59\n"
"v_mul_f32 v96, s77, v64\n"
"s_waitcnt lgkmcnt(10)\n"
"v_mfma_f32_32x32x16_bf16 v[208:223], a[20:23], a[128:131], v[208:223]\n"
"ds_read_b64_tr_b16 a[12:13], v87 offset:40960\n"
"ds_read_b64_tr_b16 a[14:15], v87 offset:43008\n"
"v_exp_f32 v103, v103\n"
"v_add_f32 v102, 1.0, v102\n"
"s_waitcnt lgkmcnt(10)\n"
"v_mfma_f32_32x32x16_bf16 v[224:239], a[24:27], a[128:131], v[224:239]\n"
"ds_read_b64_tr_b16 a[16:17], v84 offset:41216\n"
"ds_read_b64_tr_b16 a[18:19], v84 offset:43264\n"
"v_rcp_f32 v101, v101\n"
"v_mul_f32 v100, v100, v60\n"
"v_cvt_pk_bf16_f32 v109, v98, v99\n"
"s_waitcnt lgkmcnt(10)\n"
"v_mfma_f32_32x32x16_bf16 v[240:255], a[28:31], a[128:131], v[240:255]\n"
"ds_read_b64_tr_b16 a[20:21], v85 offset:41216\n"
"ds_read_b64_tr_b16 a[22:23], v85 offset:43264\n"
"v_mul_f32 v97, s77, v65\n"
"v_exp_f32 v96, v96\n"
"v_add_f32 v103, 1.0, v103\n"
"s_waitcnt lgkmcnt(10)\n"
"v_mfma_f32_32x32x16_bf16 v[128:143], a[0:3], a[132:135], v[128:143]\n"
"ds_read_b64_tr_b16 a[24:25], v86 offset:41216\n"
"ds_read_b64_tr_b16 a[26:27], v86 offset:43264\n"
"v_rcp_f32 v102, v102\n"
"v_mul_f32 v101, v101, v61\n"
"v_mul_f32 v98, s77, v66\n"
"s_waitcnt lgkmcnt(10)\n"
"v_mfma_f32_32x32x16_bf16 v[144:159], a[4:7], a[132:135], v[144:159]\n"
"ds_read_b64_tr_b16 a[28:29], v87 offset:41216\n"
"ds_read_b64_tr_b16 a[30:31], v87 offset:43264\n"
"v_exp_f32 v97, v97\n"
"v_add_f32 v96, 1.0, v96\n"
"s_waitcnt lgkmcnt(10)\n"
"v_mfma_f32_32x32x16_bf16 v[160:175], a[8:11], a[132:135], v[160:175]\n"
"ds_read_b64_tr_b16 a[0:1], v84 offset:49152\n"
"ds_read_b64_tr_b16 a[2:3], v84 offset:51200\n"
"v_rcp_f32 v103, v103\n"
"v_mul_f32 v102, v102, v62\n"
"v_cvt_pk_bf16_f32 v110, v100, v101\n"
"s_waitcnt lgkmcnt(10)\n"
"v_mfma_f32_32x32x16_bf16 v[176:191], a[12:15], a[132:135], v[176:191]\n"
"ds_read_b64_tr_b16 a[4:5], v85 offset:49152\n"
"ds_read_b64_tr_b16 a[6:7], v85 offset:51200\n"
"v_mul_f32 v99, s77, v67\n"
"v_exp_f32 v98, v98\n"
"v_add_f32 v97, 1.0, v97\n"
"s_waitcnt lgkmcnt(10)\n"
"v_mfma_f32_32x32x16_bf16 v[192:207], a[16:19], a[132:135], v[192:207]\n"
"ds_read_b64_tr_b16 a[8:9], v86 offset:49152\n"
"ds_read_b64_tr_b16 a[10:11], v86 offset:51200\n"
"v_rcp_f32 v96, v96\n"
"v_mul_f32 v103, v103, v63\n"
"v_mul_f32 v100, s77, v68\n"
"s_waitcnt lgkmcnt(10)\n"
"v_mfma_f32_32x32x16_bf16 v[208:223], a[20:23], a[132:135], v[208:223]\n"
"ds_read_b64_tr_b16 a[12:13], v87 offset:49152\n"
"ds_read_b64_tr_b16 a[14:15], v87 offset:51200\n"
"v_exp_f32 v99, v99\n"
"v_add_f32 v98, 1.0, v98\n"
"s_waitcnt lgkmcnt(10)\n"
"v_mfma_f32_32x32x16_bf16 v[224:239], a[24:27], a[132:135], v[224:239]\n"
"ds_read_b64_tr_b16 a[16:17], v84 offset:49408\n"
"ds_read_b64_tr_b16 a[18:19], v84 offset:51456\n"
"v_rcp_f32 v97, v97\n"
"v_mul_f32 v96, v96, v64\n"
"v_cvt_pk_bf16_f32 v111, v102, v103\n"
"s_waitcnt lgkmcnt(10)\n"
"v_mfma_f32_32x32x16_bf16 v[240:255], a[28:31], a[132:135], v[240:255]\n"
"ds_read_b64_tr_b16 a[20:21], v85 offset:49408\n"
"ds_read_b64_tr_b16 a[22:23], v85 offset:51456\n"
"s_add_u32 s68, %[hoff], 0x1000\n"
"buffer_store_dwordx4 v[108:111], v1, %[rsW], s68 offen offset:1024 sc1\n"
"v_mul_f32 v101, s77, v69\n"
"s_waitcnt lgkmcnt(10)\n"
"v_mfma_f32_32x32x16_bf16 v[128:143], a[0:3], a[136:139], v[128:143]\n"
"ds_read_b64_tr_b16 a[24:25], v86 offset:49408\n"
"ds_read_b64_tr_b16 a[26:27], v86 offset:51456\n"
"v_exp_f32 v100, v100\n"
"v_add_f32 v99, 1.0, v99\n"
"s_waitcnt lgkmcnt(10)\n"
"v_mfma_f32_32x32x16_bf16 v[144:159], a[4:7], a[136:139], v[144:159]\n"
"ds_read_b64_tr_b16 a[28:29], v87 offset:49408\n"
"ds_read_b64_tr_b16 a[30:31], v87 offset:51456\n"
"v_rcp_f32 v98, v98\n"
"v_mul_f32 v97, v97, v65\n"
"v_mul_f32 v102, s77, v70\n"
"s_waitcnt lgkmcnt(10)\n"
"v_mfma_f32_32x32x16_bf16 v[160:175], a[8:11], a[136:139], v[160:175]\n"
"ds_read_b64_tr_b16 a[0:1], v84 offset:57344\n"
"ds_read_b64_tr_b16 a[2:3], v84 offset:59392\n"
"v_exp_f32 v101, v101\n"
"v_add_f32 v100, 1.0, v100\n"
"s_waitcnt lgkmcnt(10)\n"
"v_mfma_f32_32x32x16_bf16 v[176:191], a[12:15], a[136:139], v[176:191]\n"
"ds_read_b64_tr_b16 a[4:5], v85 offset:57344\n"
"ds_read_b64_tr_b16 a[6:7], v85 offset:59392\n"
"v_rcp_f32 v99, v99\n"
"v_mul_f32 v98, v98, v66\n"
"v_cvt_pk_bf16_f32 v104, v96, v97\n"
"s_waitcnt lgkmcnt(10)\n"
"v_mfma_f32_32x32x16_bf16 v[192:207], a[16:19], a[136:139], v[192:207]\n"
"ds_read_b64_tr_b16 a[8:9], v86 offset:57344\n"
"ds_read_b64_tr_b16 a[10:11], v86 offset:59392\n"
"v_mul_f32 v103, s77, v71\n"
"v_exp_f32 v102, v102\n"
"v_add_f32 v101, 1.0, v101\n"
"s_waitcnt lgkmcnt(10)\n"
"v_mfma_f32_32x32x16_bf16 v[208:223], a[20:23], a[136:139], v[208:223]\n"
"ds_read_b64_tr_b16 a[12:13], v87 offset:57344\n"
"ds_read_b64_tr_b16 a[14:15], v87 offset:59392\n"
"v_rcp_f32 v100, v100\n"
"v_mul_f32 v99, v99, v67\n"
"v_mul_f32 v96, s77, v72\n"
"s_waitcnt lgkmcnt(10)\n"
"v_mfma_f32_32x32x16_bf16 v[224:239], a[24:27], a[136:139], v[224:239]\n"
"ds_read_b64_tr_b16 a[16:17], v84 offset:57600\n"
"ds_read_b64_tr_b16 a[18:19], v84 offset:59648\n"
"v_exp_f32 v103, v103\n"
"v_add_f32 v102, 1.0, v102\n"
"s_waitcnt lgkmcnt(10)\n"
"v_mfma_f32_32x32x16_bf16 v[240:255], a[28:31], a[136:139], v[240:255]\n"
"ds_read_b64_tr_b16 a[20:21], v85 offset:57600\n"
"ds_read_b64_tr_b16 a[22:23], v85 offset:59648\n"
"v_rcp_f32 v101, v101\n"
"v_mul_f32 v100, v100, v68\n"
"v_cvt_pk_bf16_f32 v105, v98, v99\n"
"s_waitcnt lgkmcnt(10)\n"
"v_mfma_f32_32x32x16_bf16 v[128:143], a[0:3], a[140:143], v[128:143]\n"
"ds_read_b64_tr_b16 a[24:25], v86 offset:57600\n"
"ds_read_b64_tr_b16 a[26:27], v86 offset:59648\n"
"v_mul_f32 v97, s77, v73\n"
"v_exp_f32 v96, v96\n"
"v_add_f32 v103, 1.0, v103\n"
"s_waitcnt lgkmcnt(10)\n"
"v_mfma_f32_32x32x16_bf16 v[144:159], a[4:7], a[140:143], v[144:159]\n"
"ds_read_b64_tr_b16 a[28:29], v87 offset:57600\n"
"ds_read_b64_tr_b16 a[30:31], v87 offset:59648\n"
"v_rcp_f32 v102, v102\n"
"v_mul_f32 v101, v101, v69\n"
"v_mul_f32 v98, s77, v74\n"
"s_waitcnt lgkmcnt(10)\n"
"v_mfma_f32_32x32x16_bf16 v[160:175], a[8:11], a[140:143], v[160:175]\n"
"v_add_u32 v84, 0x10000, v84\n"
"ds_read_b64_tr_b16 a[0:1], v84\n"
"ds_read_b64_tr_b16 a[2:3], v84 offset:2048\n"
"v_exp_f32 v97, v97\n"
"v_add_f32 v96, 1.0, v96\n"
"s_waitcnt lgkmcnt(10)\n"
"v_mfma_f32_32x32x16_bf16 v[176:191], a[12:15], a[140:143], v[176:191]\n"
"v_add_u32 v85, 0x10000, v85\n"
"ds_read_b64_tr_b16 a[4:5], v85\n"
"ds_read_b64_tr_b16 a[6:7], v85 offset:2048\n"
"v_rcp_f32 v103, v103\n"
"v_mul_f32 v102, v102, v70\n"
"v_cvt_pk_bf16_f32 v106, v100, v101\n"
"s_waitcnt lgkmcnt(10)\n"
"v_mfma_f32_32x32x16_bf16 v[192:207], a[16:19], a[140:143], v[192:207]\n"
"v_add_u32 v86, 0x10000, v86\n"
"ds_read_b64_tr_b16 a[8:9], v86\n"
"ds_read_b64_tr_b16 a[10:11], v86 offset:2048\n"
"v_mul_f32 v99, s77, v75\n"
"v_exp_f32 v98, v98\n"
"v_add_f32 v97, 1.0, v97\n"
"s_waitcnt lgkmcnt(10)\n"
"v_mfma_f32_32x32x16_bf16 v[208:223], a[20:23], a[140:143], v[208:223]\n"
"v_add_u32 v87, 0x10000, v87\n"
"ds_read_b64_tr_b16 a[12:13], v87\n"
"ds_read_b64_tr_b16 a[14:15], v87 offset:2048\n"
"v_rcp_f32 v96, v96\n"
"v_mul_f32 v103, v103, v71\n"
"v_mul_f32 v100, s77, v76\n"
"s_waitcnt lgkmcnt(10)\n"
"v_mfma_f32_32x32x16_bf16 v[224:239], a[24:27], a[140:143], v[224:239]\n"
"ds_read_b64_tr_b16 a[16:17], v84 offset:256\n"
"ds_read_b64_tr_b16 a[18:19], v84 offset:2304\n"
"v_exp_f32 v99, v99\n"
"v_add_f32 v98, 1.0, v98\n"
"s_waitcnt lgkmcnt(10)\n"
"v_mfma_f32_32x32x16_bf16 v[240:255], a[28:31], a[140:143], v[240:255]\n"
"ds_read_b64_tr_b16 a[20:21], v85 offset:256\n"
"ds_read_b64_tr_b16 a[22:23], v85 offset:2304\n"
"v_rcp_f32 v97, v97\n"
"v_mul_f32 v96, v96, v72\n"
"v_cvt_pk_bf16_f32 v107, v102, v103\n"
"s_waitcnt vmcnt(1)\n"
"v_cmp_ne_u64 vcc, %[tag], v[14:15]\n"
"s_nop 0\n"
"s_cbranch_vccnz KVWF%=\n"
"KVGF%=:\n"
"s_barrier\n"
"s_waitcnt lgkmcnt(10)\n"
"v_mfma_f32_32x32x16_bf16 v[128:143], a[0:3], a[144:147], v[128:143]\n"
"ds_read_b64_tr_b16 a[24:25], v86 offset:256\n"
"ds_read_b64_tr_b16 a[26:27], v86 offset:2304\n"
"s_add_u32 m0, s74, 0x20000\n"
"s_add_u32 s71, %[ks], 0\n"
"buffer_load_dwordx4 v1, %[rsW], s71 offen sc1 lds\n"
"s_waitcnt lgkmcnt(10)\n"
"v_mfma_f32_32x32x16_bf16 v[144:159], a[4:7], a[144:147], v[144:159]\n"
"ds_read_b64_tr_b16 a[28:29], v87 offset:256\n"
"ds_read_b64_tr_b16 a[30:31], v87 offset:2304\n"
"buffer_load_dwordx4 v1, %[rsW], s71 offen offset:1024 sc1 lds\n"
"s_waitcnt lgkmcnt(10)\n"
"v_mfma_f32_32x32x16_bf16 v[160:175], a[8:11], a[144:147], v[160:175]\n"
"ds_read_b64_tr_b16 a[0:1], v84 offset:8192\n"
"ds_read_b64_tr_b16 a[2:3], v84 offset:10240\n"
"buffer_load_dwordx4 v1, %[rsW], s71 offen offset:2048 sc1 lds\n"
"s_waitcnt lgkmcnt(10)\n"
"v_mfma_f32_32x32x16_bf16 v[176:191], a[12:15], a[144:147], v[176:191]\n"
"ds_read_b64_tr_b16 a[4:5], v85 offset:8192\n"
"ds_read_b64_tr_b16 a[6:7], v85 offset:10240\n"
"buffer_load_dwordx4 v1, %[rsW], s71 offen offset:3072 sc1 lds\n"
"s_waitcnt lgkmcnt(10)\n"
"v_mfma_f32_32x32x16_bf16 v[192:207], a[16:19], a[144:147], v[192:207]\n"
"ds_read_b64_tr_b16 a[8:9], v86 offset:8192\n"
"ds_read_b64_tr_b16 a[10:11], v86 offset:10240\n"
"s_add_u32 m0, s74, 0x21000\n"
"s_add_u32 s71, %[ks], 0x1000\n"
"buffer_load_dwordx4 v1, %[rsW], s71 offen sc1 lds\n"
"s_waitcnt lgkmcnt(10)\n"
"v_mfma_f32_32x32x16_bf16 v[208:223], a[20:23], a[144:147], v[208:223]\n"
"ds_read_b64_tr_b16 a[12:13], v87 offset:8192\n"
"ds_read_b64_tr_b16 a[14:15], v87 offset:10240\n"
"buffer_load_dwordx4 v1, %[rsW], s71 offen offset:1024 sc1 lds\n"
"s_waitcnt lgkmcnt(10)\n"
"v_mfma_f32_32x32x16_bf16 v[224:239], a[24:27], a[144:147], v[224:239]\n"
"ds_read_b64_tr_b16 a[16:17], v84 offset:8448\n"
"ds_read_b64_tr_b16 a[18:19], v84 offset:10496\n"
"buffer_load_dwordx4 v1, %[rsW], s71 offen offset:2048 sc1 lds\n"
"s_waitcnt lgkmcnt(10)\n"
"v_mfma_f32_32x32x16_bf16 v[240:255], a[28:31], a[144:147], v[240:255]\n"
"ds_read_b64_tr_b16 a[20:21], v85 offset:8448\n"
"ds_read_b64_tr_b16 a[22:23], v85 offset:10496\n"
"buffer_load_dwordx4 v1, %[rsW], s71 offen offset:3072 sc1 lds\n"
"s_waitcnt lgkmcnt(10)\n"
"v_mfma_f32_32x32x16_bf16 v[128:143], a[0:3], a[148:151], v[128:143]\n"
"ds_read_b64_tr_b16 a[24:25], v86 offset:8448\n"
"ds_read_b64_tr_b16 a[26:27], v86 offset:10496\n"
"s_add_u32 s68, %[hoff], 0x1000\n"
"buffer_store_dwordx4 v[104:107], v1, %[rsW], s68 offen offset:2048 sc1\n"
"v_mul_f32 v101, s77, v77\n"
"s_waitcnt lgkmcnt(10)\n"
"v_mfma_f32_32x32x16_bf16 v[144:159], a[4:7], a[148:151], v[144:159]\n"
"ds_read_b64_tr_b16 a[28:29], v87 offset:8448\n"
"ds_read_b64_tr_b16 a[30:31], v87 offset:10496\n"
"v_exp_f32 v100, v100\n"
"v_add_f32 v99, 1.0, v99\n"
"s_waitcnt lgkmcnt(10)\n"
"v_mfma_f32_32x32x16_bf16 v[160:175], a[8:11], a[148:151], v[160:175]\n"
"ds_read_b64_tr_b16 a[0:1], v84 offset:16384\n"
"ds_read_b64_tr_b16 a[2:3], v84 offset:18432\n"
"v_rcp_f32 v98, v98\n"
"v_mul_f32 v97, v97, v73\n"
"s_waitcnt lgkmcnt(10)\n"
"v_mfma_f32_32x32x16_bf16 v[176:191], a[12:15], a[148:151], v[176:191]\n"
"ds_read_b64_tr_b16 a[4:5], v85 offset:16384\n"
"ds_read_b64_tr_b16 a[6:7], v85 offset:18432\n"
"v_mul_f32 v102, s77, v78\n"
"v_exp_f32 v101, v101\n"
"s_waitcnt lgkmcnt(10)\n"
"v_mfma_f32_32x32x16_bf16 v[192:207], a[16:19], a[148:151], v[192:207]\n"
"ds_read_b64_tr_b16 a[8:9], v86 offset:16384\n"
"ds_read_b64_tr_b16 a[10:11], v86 offset:18432\n"
"v_add_f32 v100, 1.0, v100\n"
"v_rcp_f32 v99, v99\n"
"s_waitcnt lgkmcnt(10)\n"
"v_mfma_f32_32x32x16_bf16 v[208:223], a[20:23], a[148:151], v[208:223]\n"
"ds_read_b64_tr_b16 a[12:13], v87 offset:16384\n"
"ds_read_b64_tr_b16 a[14:15], v87 offset:18432\n"
"v_mul_f32 v98, v98, v74\n"
"v_cvt_pk_bf16_f32 v108, v96, v97\n"
"v_mul_f32 v103, s77, v79\n"
"s_waitcnt lgkmcnt(10)\n"
"v_mfma_f32_32x32x16_bf16 v[224:239], a[24:27], a[148:151], v[224:239]\n"
"ds_read_b64_tr_b16 a[16:17], v84 offset:16640\n"
"ds_read_b64_tr_b16 a[18:19], v84 offset:18688\n"
"v_exp_f32 v102, v102\n"
"v_add_f32 v101, 1.0, v101\n"
"s_waitcnt lgkmcnt(10)\n"
"v_mfma_f32_32x32x16_bf16 v[240:255], a[28:31], a[148:151], v[240:255]\n"
"ds_read_b64_tr_b16 a[20:21], v85 offset:16640\n"
"ds_read_b64_tr_b16 a[22:23], v85 offset:18688\n"
"v_rcp_f32 v100, v100\n"
"v_mul_f32 v99, v99, v75\n"
"s_waitcnt lgkmcnt(10)\n"
"v_mfma_f32_32x32x16_bf16 v[128:143], a[0:3], a[152:155], v[128:143]\n"
"ds_read_b64_tr_b16 a[24:25], v86 offset:16640\n"
"ds_read_b64_tr_b16 a[26:27], v86 offset:18688\n"
"s_add_u32 m0, s74, 0\n"
"s_add_u32 s71, %[ks], 0x8000\n"
"buffer_load_dwordx4 v1, %[rsW], s71 offen sc1 lds\n"
"s_waitcnt lgkmcnt(10)\n"
"v_mfma_f32_32x32x16_bf16 v[144:159], a[4:7], a[152:155], v[144:159]\n"
"ds_read_b64_tr_b16 a[28:29], v87 offset:16640\n"
"ds_read_b64_tr_b16 a[30:31], v87 offset:18688\n"
"v_exp_f32 v103, v103\n"
"v_add_f32 v102, 1.0, v102\n"
"s_waitcnt lgkmcnt(10)\n"
"v_mfma_f32_32x32x16_bf16 v[160:175], a[8:11], a[152:155], v[160:175]\n"
"ds_read_b64_tr_b16 a[0:1], v84 offset:24576\n"
"ds_read_b64_tr_b16 a[2:3], v84 offset:26624\n"
"buffer_load_dwordx4 v1, %[rsW], s71 offen offset:1024 sc1 lds\n"
"s_waitcnt lgkmcnt(10)\n"
"v_mfma_f32_32x32x16_bf16 v[176:191], a[12:15], a[152:155], v[176:191]\n"
"ds_read_b64_tr_b16 a[4:5], v85 offset:24576\n"
"ds_read_b64_tr_b16 a[6:7], v85 offset:26624\n"
"v_rcp_f32 v101, v101\n"
"v_mul_f32 v100, v100, v76\n"
"s_waitcnt lgkmcnt(10)\n"
"v_mfma_f32_32x32x16_bf16 v[192:207], a[16:19], a[152:155], v[192:207]\n"
"ds_read_b64_tr_b16 a[8:9], v86 offset:24576\n"
"ds_read_b64_tr_b16 a[10:11], v86 offset:26624\n"
"buffer_load_dwordx4 v1, %[rsW], s71 offen offset:2048 sc1 lds\n"
"v_cvt_pk_bf16_f32 v109, v98, v99\n"
"s_waitcnt lgkmcnt(10)\n"
"v_mfma_f32_32x32x16_bf16 v[208:223], a[20:23], a[152:155], v[208:223]\n"
"ds_read_b64_tr_b16 a[12:13], v87 offset:24576\n"
"ds_read_b64_tr_b16 a[14:15], v87 offset:26624\n"
"v_add_f32 v103, 1.0, v103\n"
"v_rcp_f32 v102, v102\n"
"s_waitcnt lgkmcnt(10)\n"
"v_mfma_f32_32x32x16_bf16 v[224:239], a[24:27], a[152:155], v[224:239]\n"
"ds_read_b64_tr_b16 a[16:17], v84 offset:24832\n"
"ds_read_b64_tr_b16 a[18:19], v84 offset:26880\n"
"buffer_load_dwordx4 v1, %[rsW], s71 offen offset:3072 sc1 lds\n"
"v_mul_f32 v101, v101, v77\n"
"s_waitcnt lgkmcnt(10)\n"
"v_mfma_f32_32x32x16_bf16 v[240:255], a[28:31], a[152:155], v[240:255]\n"
"ds_read_b64_tr_b16 a[20:21], v85 offset:24832\n"
"ds_read_b64_tr_b16 a[22:23], v85 offset:26880\n"
"v_rcp_f32 v103, v103\n"
"v_mul_f32 v102, v102, v78\n"
"s_waitcnt lgkmcnt(10)\n"
"v_mfma_f32_32x32x16_bf16 v[128:143], a[0:3], a[156:159], v[128:143]\n"
"ds_read_b64_tr_b16 a[24:25], v86 offset:24832\n"
"ds_read_b64_tr_b16 a[26:27], v86 offset:26880\n"
"s_add_u32 m0, s74, 0x1000\n"
"s_add_u32 s71, %[ks], 0x9000\n"
"buffer_load_dwordx4 v1, %[rsW], s71 offen sc1 lds\n"
"v_cvt_pk_bf16_f32 v110, v100, v101\n"
"s_waitcnt lgkmcnt(10)\n"
"v_mfma_f32_32x32x16_bf16 v[144:159], a[4:7], a[156:159], v[144:159]\n"
"ds_read_b64_tr_b16 a[28:29], v87 offset:24832\n"
"ds_read_b64_tr_b16 a[30:31], v87 offset:26880\n"
"v_mul_f32 v103, v103, v79\n"
"v_cvt_pk_bf16_f32 v111, v102, v103\n"
"s_waitcnt lgkmcnt(10)\n"
"v_mfma_f32_32x32x16_bf16 v[160:175], a[8:11], a[156:159], v[160:175]\n"
"ds_read_b64_tr_b16 a[0:1], v84 offset:32768\n"
"ds_read_b64_tr_b16 a[2:3], v84 offset:34816\n"
"buffer_load_dwordx4 v1, %[rsW], s71 offen offset:1024 sc1 lds\n"
"s_waitcnt lgkmcnt(10)\n"
"v_mfma_f32_32x32x16_bf16 v[176:191], a[12:15], a[156:159], v[176:191]\n"
"ds_read_b64_tr_b16 a[4:5], v85 offset:32768\n"
"ds_read_b64_tr_b16 a[6:7], v85 offset:34816\n"
"s_add_u32 s68, %[hoff], 0x1000\n"
"buffer_store_dwordx4 v[108:111], v1, %[rsW], s68 offen offset:3072 sc1\n"
"s_mov_b32 s68, 0\n"
"s_waitcnt lgkmcnt(10)\n"
"v_mfma_f32_32x32x16_bf16 v[192:207], a[16:19], a[156:159], v[192:207]\n"
"ds_read_b64_tr_b16 a[8:9], v86 offset:32768\n"
"ds_read_b64_tr_b16 a[10:11], v86 offset:34816\n"
"buffer_load_dwordx4 v1, %[rsW], s71 offen offset:2048 sc1 lds\n"
"s_waitcnt lgkmcnt(10)\n"
"v_mfma_f32_32x32x16_bf16 v[208:223], a[20:23], a[156:159], v[208:223]\n"
"ds_read_b64_tr_b16 a[12:13], v87 offset:32768\n"
"ds_read_b64_tr_b16 a[14:15], v87 offset:34816\n"
"s_waitcnt lgkmcnt(10)\n"
"v_mfma_f32_32x32x16_bf16 v[224:239], a[24:27], a[156:159], v[224:239]\n"
"ds_read_b64_tr_b16 a[16:17], v84 offset:33024\n"
"ds_read_b64_tr_b16 a[18:19], v84 offset:35072\n"
"buffer_load_dwordx4 v1, %[rsW], s71 offen offset:3072 sc1 lds\n"
"s_waitcnt lgkmcnt(10)\n"
"v_mfma_f32_32x32x16_bf16 v[240:255], a[28:31], a[156:159], v[240:255]\n"
"ds_read_b64_tr_b16 a[20:21], v85 offset:33024\n"
"ds_read_b64_tr_b16 a[22:23], v85 offset:35072\n"
"s_waitcnt vmcnt(10)\n"
"s_barrier\n"
"s_waitcnt lgkmcnt(10)\n"
"v_mfma_f32_32x32x16_bf16 v[128:143], a[0:3], a[160:163], v[128:143]\n"
"ds_read_b64_tr_b16 a[24:25], v86 offset:33024\n"
"ds_read_b64_tr_b16 a[26:27], v86 offset:35072\n"
"s_add_u32 m0, s74, 0x8000\n"
"s_add_u32 s71, %[vs], 0\n"
"buffer_load_dwordx4 v1, %[rsW], s71 offen sc1 lds\n"
"s_waitcnt lgkmcnt(10)\n"
"v_mfma_f32_32x32x16_bf16 v[144:159], a[4:7], a[160:163], v[144:159]\n"
"ds_read_b64_tr_b16 a[28:29], v87 offset:33024\n"
"ds_read_b64_tr_b16 a[30:31], v87 offset:35072\n"
"s_waitcnt lgkmcnt(10)\n"
"v_mfma_f32_32x32x16_bf16 v[160:175], a[8:11], a[160:163], v[160:175]\n"
"ds_read_b64_tr_b16 a[0:1], v84 offset:40960\n"
"ds_read_b64_tr_b16 a[2:3], v84 offset:43008\n"
"buffer_load_dwordx4 v1, %[rsW], s71 offen offset:1024 sc1 lds\n"
"s_waitcnt lgkmcnt(10)\n"
"v_mfma_f32_32x32x16_bf16 v[176:191], a[12:15], a[160:163], v[176:191]\n"
"ds_read_b64_tr_b16 a[4:5], v85 offset:40960\n"
"ds_read_b64_tr_b16 a[6:7], v85 offset:43008\n"
"s_waitcnt lgkmcnt(10)\n"
"v_mfma_f32_32x32x16_bf16 v[192:207], a[16:19], a[160:163], v[192:207]\n"
"ds_read_b64_tr_b16 a[8:9], v86 offset:40960\n"
"ds_read_b64_tr_b16 a[10:11], v86 offset:43008\n"
"buffer_load_dwordx4 v1, %[rsW], s71 offen offset:2048 sc1 lds\n"
"s_waitcnt lgkmcnt(10)\n"
"v_mfma_f32_32x32x16_bf16 v[208:223], a[20:23], a[160:163], v[208:223]\n"
"ds_read_b64_tr_b16 a[12:13], v87 offset:40960\n"
"ds_read_b64_tr_b16 a[14:15], v87 offset:43008\n"
"s_waitcnt lgkmcnt(10)\n"
"v_mfma_f32_32x32x16_bf16 v[224:239], a[24:27], a[160:163], v[224:239]\n"
"ds_read_b64_tr_b16 a[16:17], v84 offset:41216\n"
"ds_read_b64_tr_b16 a[18:19], v84 offset:43264\n"
"buffer_load_dwordx4 v1, %[rsW], s71 offen offset:3072 sc1 lds\n"
"s_waitcnt lgkmcnt(10)\n"
"v_mfma_f32_32x32x16_bf16 v[240:255], a[28:31], a[160:163], v[240:255]\n"
"ds_read_b64_tr_b16 a[20:21], v85 offset:41216\n"
"ds_read_b64_tr_b16 a[22:23], v85 offset:43264\n"
"s_waitcnt lgkmcnt(10)\n"
"v_mfma_f32_32x32x16_bf16 v[128:143], a[0:3], a[164:167], v[128:143]\n"
"ds_read_b64_tr_b16 a[24:25], v86 offset:41216\n"
"ds_read_b64_tr_b16 a[26:27], v86 offset:43264\n"
"s_add_u32 m0, s74, 0x9000\n"
"s_add_u32 s71, %[vs], 0x1000\n"
"buffer_load_dwordx4 v1, %[rsW], s71 offen sc1 lds\n"
"s_waitcnt lgkmcnt(10)\n"
"v_mfma_f32_32x32x16_bf16 v[144:159], a[4:7], a[164:167], v[144:159]\n"
"ds_read_b64_tr_b16 a[28:29], v87 offset:41216\n"
"ds_read_b64_tr_b16 a[30:31], v87 offset:43264\n"
"s_waitcnt lgkmcnt(10)\n"
"v_mfma_f32_32x32x16_bf16 v[160:175], a[8:11], a[164:167], v[160:175]\n"
"ds_read_b64_tr_b16 a[0:1], v84 offset:49152\n"
"ds_read_b64_tr_b16 a[2:3], v84 offset:51200\n"
"buffer_load_dwordx4 v1, %[rsW], s71 offen offset:1024 sc1 lds\n"
"s_waitcnt lgkmcnt(10)\n"
"v_mfma_f32_32x32x16_bf16 v[176:191], a[12:15], a[164:167], v[176:191]\n"
"ds_read_b64_tr_b16 a[4:5], v85 offset:49152\n"
"ds_read_b64_tr_b16 a[6:7], v85 offset:51200\n"
"s_waitcnt lgkmcnt(10)\n"
"v_mfma_f32_32x32x16_bf16 v[192:207], a[16:19], a[164:167], v[192:207]\n"
"ds_read_b64_tr_b16 a[8:9], v86 offset:49152\n"
"ds_read_b64_tr_b16 a[10:11], v86 offset:51200\n"
"buffer_load_dwordx4 v1, %[rsW], s71 offen offset:2048 sc1 lds\n"
"s_waitcnt lgkmcnt(10)\n"
"v_mfma_f32_32x32x16_bf16 v[208:223], a[20:23], a[164:167], v[208:223]\n"
"ds_read_b64_tr_b16 a[12:13], v87 offset:49152\n"
"ds_read_b64_tr_b16 a[14:15], v87 offset:51200\n"
"s_waitcnt lgkmcnt(10)\n"
"v_mfma_f32_32x32x16_bf16 v[224:239], a[24:27], a[164:167], v[224:239]\n"
"ds_read_b64_tr_b16 a[16:17], v84 offset:49408\n"
"ds_read_b64_tr_b16 a[18:19], v84 offset:51456\n"
"buffer_load_dwordx4 v1, %[rsW], s71 offen offset:3072 sc1 lds\n"
"s_waitcnt lgkmcnt(10)\n"
"v_mfma_f32_32x32x16_bf16 v[240:255], a[28:31], a[164:167], v[240:255]\n"
"ds_read_b64_tr_b16 a[20:21], v85 offset:49408\n"
"ds_read_b64_tr_b16 a[22:23], v85 offset:51456\n"
"s_waitcnt lgkmcnt(10)\n"
"v_mfma_f32_32x32x16_bf16 v[128:143], a[0:3], a[168:171], v[128:143]\n"
"ds_read_b64_tr_b16 a[24:25], v86 offset:49408\n"
"ds_read_b64_tr_b16 a[26:27], v86 offset:51456\n"
"s_add_u32 m0, s74, 0x10000\n"
"s_add_u32 s71, %[ks], 0x10000\n"
"buffer_load_dwordx4 v1, %[rsW], s71 offen sc1 lds\n"
"s_waitcnt lgkmcnt(10)\n"
"v_mfma_f32_32x32x16_bf16 v[144:159], a[4:7], a[168:171], v[144:159]\n"
"ds_read_b64_tr_b16 a[28:29], v87 offset:49408\n"
"ds_read_b64_tr_b16 a[30:31], v87 offset:51456\n"
"s_waitcnt lgkmcnt(10)\n"
"v_mfma_f32_32x32x16_bf16 v[160:175], a[8:11], a[168:171], v[160:175]\n"
"ds_read_b64_tr_b16 a[0:1], v84 offset:57344\n"
"ds_read_b64_tr_b16 a[2:3], v84 offset:59392\n"
"buffer_load_dwordx4 v1, %[rsW], s71 offen offset:1024 sc1 lds\n"
"s_waitcnt lgkmcnt(10)\n"
"v_mfma_f32_32x32x16_bf16 v[176:191], a[12:15], a[168:171], v[176:191]\n"
"ds_read_b64_tr_b16 a[4:5], v85 offset:57344\n"
"ds_read_b64_tr_b16 a[6:7], v85 offset:59392\n"
"s_waitcnt lgkmcnt(10)\n"
"v_mfma_f32_32x32x16_bf16 v[192:207], a[16:19], a[168:171], v[192:207]\n"
"ds_read_b64_tr_b16 a[8:9], v86 offset:57344\n"
"ds_read_b64_tr_b16 a[10:11], v86 offset:59392\n"
"buffer_load_dwordx4 v1, %[rsW], s71 offen offset:2048 sc1 lds\n"
"s_waitcnt lgkmcnt(10)\n"
"v_mfma_f32_32x32x16_bf16 v[208:223], a[20:23], a[168:171], v[208:223]\n"
"ds_read_b64_tr_b16 a[12:13], v87 offset:57344\n"
"ds_read_b64_tr_b16 a[14:15], v87 offset:59392\n"
"s_waitcnt lgkmcnt(10)\n"
"v_mfma_f32_32x32x16_bf16 v[224:239], a[24:27], a[168:171], v[224:239]\n"
"ds_read_b64_tr_b16 a[16:17], v84 offset:57600\n"
"ds_read_b64_tr_b16 a[18:19], v84 offset:59648\n"
"buffer_load_dwordx4 v1, %[rsW], s71 offen offset:3072 sc1 lds\n"
"s_waitcnt lgkmcnt(10)\n"
"v_mfma_f32_32x32x16_bf16 v[240:255], a[28:31], a[168:171], v[240:255]\n"
"ds_read_b64_tr_b16 a[20:21], v85 offset:57600\n"
"ds_read_b64_tr_b16 a[22:23], v85 offset:59648\n"
"s_waitcnt lgkmcnt(10)\n"
"v_mfma_f32_32x32x16_bf16 v[128:143], a[0:3], a[172:175], v[128:143]\n"
"ds_read_b64_tr_b16 a[24:25], v86 offset:57600\n"
"ds_read_b64_tr_b16 a[26:27], v86 offset:59648\n"
"s_add_u32 m0, s74, 0x11000\n"
"s_add_u32 s71, %[ks], 0x11000\n"
"buffer_load_dwordx4 v1, %[rsW], s71 offen sc1 lds\n"
"s_waitcnt lgkmcnt(10)\n"
"v_mfma_f32_32x32x16_bf16 v[144:159], a[4:7], a[172:175], v[144:159]\n"
"ds_read_b64_tr_b16 a[28:29], v87 offset:57600\n"
"ds_read_b64_tr_b16 a[30:31], v87 offset:59648\n"
"s_waitcnt lgkmcnt(10)\n"
"v_mfma_f32_32x32x16_bf16 v[160:175], a[8:11], a[172:175], v[160:175]\n"
"ds_read_b128 a[0:3], v2\n"
"buffer_load_dwordx4 v1, %[rsW], s71 offen offset:1024 sc1 lds\n"
"s_waitcnt lgkmcnt(9)\n"
"v_mfma_f32_32x32x16_bf16 v[176:191], a[12:15], a[172:175], v[176:191]\n"
"ds_read_b128 a[4:7], v2 offset:1024\n"
"s_waitcnt lgkmcnt(8)\n"
"v_mfma_f32_32x32x16_bf16 v[192:207], a[16:19], a[172:175], v[192:207]\n"
"ds_read_b128 a[8:11], v2 offset:2048\n"
"buffer_load_dwordx4 v1, %[rsW], s71 offen offset:2048 sc1 lds\n"
"v_cvt_pk_bf16_f32 v80, v128, v129\n"
"v_cvt_pk_bf16_f32 v81, v130, v131\n"
"v_cvt_pk_bf16_f32 v82, v132, v133\n"
"v_cvt_pk_bf16_f32 v83, v134, v135\n"
"s_waitcnt lgkmcnt(7)\n"
"v_mfma_f32_32x32x16_bf16 v[208:223], a[20:23], a[172:175], v[208:223]\n"
"ds_read_b128 a[12:15], v2 offset:3072\n"
"v_cvt_pk_bf16_f32 v84, v136, v137\n"
"v_cvt_pk_bf16_f32 v85, v138, v139\n"
"v_cvt_pk_bf16_f32 v86, v140, v141\n"
"v_cvt_pk_bf16_f32 v87, v142, v143\n"
"s_waitcnt lgkmcnt(6)\n"
"v_mfma_f32_32x32x16_bf16 v[224:239], a[24:27], a[172:175], v[224:239]\n"
"ds_read_b128 a[16:19], v2 offset:4096\n"
"buffer_load_dwordx4 v1, %[rsW], s71 offen offset:3072 sc1 lds\n"
"v_cvt_pk_bf16_f32 v88, v144, v145\n"
"v_cvt_pk_bf16_f32 v89, v146, v147\n"
"v_cvt_pk_bf16_f32 v90, v148, v149\n"
"v_cvt_pk_bf16_f32 v91, v150, v151\n"
"s_waitcnt lgkmcnt(5)\n"
"v_mfma_f32_32x32x16_bf16 v[240:255], a[28:31], a[172:175], v[240:255]\n"
"ds_read_b128 a[20:23], v2 offset:5120\n"
"v_cvt_pk_bf16_f32 v92, v152, v153\n"
"v_cvt_pk_bf16_f32 v93, v154, v155\n"
"v_cvt_pk_bf16_f32 v94, v156, v157\n"
"v_cvt_pk_bf16_f32 v95, v158, v159\n"
"s_waitcnt vmcnt(16)\n"
"s_barrier\n"
"T0%=:\n"
"s_waitcnt lgkmcnt(5)\n"
"v_mfma_f32_32x32x16_bf16 v[32:47], a[0:3], v[80:83], 0\n"
"ds_read_b128 a[24:27], v2 offset:6144\n"
"v_cvt_pk_bf16_f32 v96, v160, v161\n"
"v_cvt_pk_bf16_f32 v97, v162, v163\n"
"v_cvt_pk_bf16_f32 v98, v164, v165\n"
"v_cvt_pk_bf16_f32 v99, v166, v167\n"
"v_cvt_pk_bf16_f32 v100, v168, v169\n"
"s_waitcnt lgkmcnt(5)\n"
"v_mfma_f32_32x32x16_bf16 v[32:47], a[4:7], v[84:87], v[32:47]\n"
"ds_read_b128 a[28:31], v2 offset:7168\n"
"v_cvt_pk_bf16_f32 v101, v170, v171\n"
"v_cvt_pk_bf16_f32 v102, v172, v173\n"
"v_cvt_pk_bf16_f32 v103, v174, v175\n"
"v_cvt_pk_bf16_f32 v104, v176, v177\n"
"v_cvt_pk_bf16_f32 v105, v178, v179\n"
"s_waitcnt lgkmcnt(5)\n"
"v_mfma_f32_32x32x16_bf16 v[32:47], a[8:11], v[88:91], v[32:47]\n"
"ds_read_b128 a[0:3], v2 offset:8192\n"
"v_cvt_pk_bf16_f32 v106, v180, v181\n"
"v_cvt_pk_bf16_f32 v107, v182, v183\n"
"v_cvt_pk_bf16_f32 v108, v184, v185\n"
"v_cvt_pk_bf16_f32 v109, v186, v187\n"
"v_cvt_pk_bf16_f32 v110, v188, v189\n"
"s_waitcnt lgkmcnt(5)\n"
"v_mfma_f32_32x32x16_bf16 v[32:47], a[12:15], v[92:95], v[32:47]\n"
"ds_read_b128 a[4:7], v2 offset:9216\n"
"v_cvt_pk_bf16_f32 v111, v190, v191\n"
"v_cvt_pk_bf16_f32 v112, v192, v193\n"
"v_cvt_pk_bf16_f32 v113, v194, v195\n"
"v_cvt_pk_bf16_f32 v114, v196, v197\n"
"v_cvt_pk_bf16_f32 v115, v198, v199\n"
"s_waitcnt lgkmcnt(5)\n"
"v_mfma_f32_32x32x16_bf16 v[32:47], a[16:19], v[96:99], v[32:47]\n"
"ds_read_b128 a[8:11], v2 offset:10240\n"
"v_cvt_pk_bf16_f32 v116, v200, v201\n"
"v_cvt_pk_bf16_f32 v117, v202, v203\n"
"v_cvt_pk_bf16_f32 v118, v204, v205\n"
"v_cvt_pk_bf16_f32 v119, v206, v207\n"
"v_cvt_pk_bf16_f32 v120, v208, v209\n"
"s_waitcnt lgkmcnt(5)\n"
"v_mfma_f32_32x32x16_bf16 v[32:47], a[20:23], v[100:103], v[32:47]\n"
"ds_read_b128 a[12:15], v2 offset:11264\n"
"v_cvt_pk_bf16_f32 v121, v210, v211\n"
"v_cvt_pk_bf16_f32 v122, v212, v213\n"
"v_cvt_pk_bf16_f32 v123, v214, v215\n"
"v_cvt_pk_bf16_f32 v124, v216, v217\n"
"v_cvt_pk_bf16_f32 v125, v218, v219\n"
"s_waitcnt lgkmcnt(5)\n"
"v_mfma_f32_32x32x16_bf16 v[32:47], a[24:27], v[104:107], v[32:47]\n"
"ds_read_b128 a[16:19], v2 offset:12288\n"
"v_cvt_pk_bf16_f32 v126, v220, v221\n"
"v_cvt_pk_bf16_f32 v127, v222, v223\n"
"v_cvt_pk_bf16_f32 v64, v224, v225\n"
"v_cvt_pk_bf16_f32 v65, v226, v227\n"
"v_cvt_pk_bf16_f32 v66, v228, v229\n"
"s_waitcnt lgkmcnt(5)\n"
"v_mfma_f32_32x32x16_bf16 v[32:47], a[28:31], v[108:111], v[32:47]\n"
"ds_read_b128 a[20:23], v2 offset:13312\n"
"v_cvt_pk_bf16_f32 v67, v230, v231\n"
"v_accvgpr_write_b32 a32, v64\n"
"v_accvgpr_write_b32 a33, v65\n"
"v_accvgpr_write_b32 a34, v66\n"
"v_accvgpr_write_b32 a35, v67\n"
"s_waitcnt lgkmcnt(5)\n"
"v_mfma_f32_32x32x16_bf16 v[32:47], a[0:3], v[112:115], v[32:47]\n"
"ds_read_b128 a[24:27], v2 offset:14336\n"
"v_cvt_pk_bf16_f32 v68, v232, v233\n"
"v_cvt_pk_bf16_f32 v69, v234, v235\n"
"v_cvt_pk_bf16_f32 v70, v236, v237\n"
"v_cvt_pk_bf16_f32 v71, v238, v239\n"
"v_accvgpr_write_b32 a36, v68\n"
"s_waitcnt lgkmcnt(5)\n"
"v_mfma_f32_32x32x16_bf16 v[32:47], a[4:7], v[116:119], v[32:47]\n"
"ds_read_b128 a[28:31], v2 offset:15360\n"
"v_accvgpr_write_b32 a37, v69\n"
"v_accvgpr_write_b32 a38, v70\n"
"v_accvgpr_write_b32 a39, v71\n"
"v_cvt_pk_bf16_f32 v72, v240, v241\n"
"v_cvt_pk_bf16_f32 v73, v242, v243\n"
"s_waitcnt lgkmcnt(5)\n"
"v_mfma_f32_32x32x16_bf16 v[32:47], a[8:11], v[120:123], v[32:47]\n"
"ds_read_b128 a[0:3], v2 offset:16384\n"
"v_cvt_pk_bf16_f32 v74, v244, v245\n"
"v_cvt_pk_bf16_f32 v75, v246, v247\n"
"v_accvgpr_write_b32 a40, v72\n"
"v_accvgpr_write_b32 a41, v73\n"
"v_accvgpr_write_b32 a42, v74\n"
"s_waitcnt lgkmcnt(5)\n"
"v_mfma_f32_32x32x16_bf16 v[32:47], a[12:15], v[124:127], v[32:47]\n"
"ds_read_b128 a[4:7], v2 offset:17408\n"
"v_accvgpr_write_b32 a43, v75\n"
"v_cvt_pk_bf16_f32 v76, v248, v249\n"
"v_cvt_pk_bf16_f32 v77, v250, v251\n"
"v_cvt_pk_bf16_f32 v78, v252, v253\n"
"v_cvt_pk_bf16_f32 v79, v254, v255\n"
"s_waitcnt lgkmcnt(5)\n"
"v_mfma_f32_32x32x16_bf16 v[32:47], a[16:19], a[32:35], v[32:47]\n"
"ds_read_b128 a[8:11], v2 offset:18432\n"
"v_accvgpr_write_b32 a44, v76\n"
"v_accvgpr_write_b32 a45, v77\n"
"v_accvgpr_write_b32 a46, v78\n"
"v_accvgpr_write_b32 a47, v79\n"
"s_waitcnt lgkmcnt(5)\n"
"v_mfma_f32_32x32x16_bf16 v[32:47], a[20:23], a[36:39], v[32:47]\n"
"ds_read_b128 a[12:15], v2 offset:19456\n"
"s_waitcnt lgkmcnt(5)\n"
"v_mfma_f32_32x32x16_bf16 v[32:47], a[24:27], a[40:43], v[32:47]\n"
"ds_read_b128 a[16:19], v2 offset:20480\n"
"s_waitcnt lgkmcnt(5)\n"
"v_mfma_f32_32x32x16_bf16 v[32:47], a[28:31], a[44:47], v[32:47]\n"
"ds_read_b128 a[20:23], v2 offset:21504\n"
"s_waitcnt lgkmcnt(5)\n"
"v_mfma_f32_32x32x16_bf16 v[48:63], a[0:3], v[80:83], 0\n"
"ds_read_b128 a[24:27], v2 offset:22528\n"
"s_waitcnt lgkmcnt(5)\n"
"v_mfma_f32_32x32x16_bf16 v[48:63], a[4:7], v[84:87], v[48:63]\n"
"ds_read_b128 a[28:31], v2 offset:23552\n"
"s_waitcnt lgkmcnt(5)\n"
"v_mfma_f32_32x32x16_bf16 v[48:63], a[8:11], v[88:91], v[48:63]\n"
"ds_read_b128 a[0:3], v2 offset:24576\n"
"s_waitcnt lgkmcnt(5)\n"
"v_mfma_f32_32x32x16_bf16 v[48:63], a[12:15], v[92:95], v[48:63]\n"
"ds_read_b128 a[4:7], v2 offset:25600\n"
"s_waitcnt lgkmcnt(5)\n"
"v_mfma_f32_32x32x16_bf16 v[48:63], a[16:19], v[96:99], v[48:63]\n"
"ds_read_b128 a[8:11], v2 offset:26624\n"
"s_waitcnt lgkmcnt(5)\n"
"v_mfma_f32_32x32x16_bf16 v[48:63], a[20:23], v[100:103], v[48:63]\n"
"ds_read_b128 a[12:15], v2 offset:27648\n"
"s_waitcnt lgkmcnt(5)\n"
"v_mfma_f32_32x32x16_bf16 v[48:63], a[24:27], v[104:107], v[48:63]\n"
"ds_read_b128 a[16:19], v2 offset:28672\n"
"v_max_f32 v6, v32, v33\n"
"s_waitcnt lgkmcnt(5)\n"
"v_mfma_f32_32x32x16_bf16 v[48:63], a[28:31], v[108:111], v[48:63]\n"
"ds_read_b128 a[20:23], v2 offset:29696\n"
"v_max3_f32 v6, v6, v34, v35\n"
"s_waitcnt lgkmcnt(5)\n"
"v_mfma_f32_32x32x16_bf16 v[48:63], a[0:3], v[112:115], v[48:63]\n"
"ds_read_b128 a[24:27], v2 offset:30720\n"
"v_max3_f32 v6, v6, v36, v37\n"
"s_waitcnt lgkmcnt(5)\n"
"v_mfma_f32_32x32x16_bf16 v[48:63], a[4:7], v[116:119], v[48:63]\n"
"ds_read_b128 a[28:31], v2 offset:31744\n"
"v_max3_f32 v6, v6, v38, v39\n"
"s_waitcnt lgkmcnt(5)\n"
"v_mfma_f32_32x32x16_bf16 v[48:63], a[8:11], v[120:123], v[48:63]\n"
"v_mov_b32 v2, v1\n"
"ds_read_b128 a[0:3], v2\n"
"v_max3_f32 v6, v6, v40, v41\n"
"s_waitcnt lgkmcnt(5)\n"
"v_mfma_f32_32x32x16_bf16 v[48:63], a[12:15], v[124:127], v[48:63]\n"
"ds_read_b128 a[4:7], v2 offset:1024\n"
"v_max3_f32 v6, v6, v42, v43\n"
"s_waitcnt lgkmcnt(5)\n"
"v_mfma_f32_32x32x16_bf16 v[48:63], a[16:19], a[32:35], v[48:63]\n"
"ds_read_b128 a[8:11], v2 offset:2048\n"
"v_max3_f32 v6, v6, v44, v45\n"
"s_waitcnt lgkmcnt(5)\n"
"v_mfma_f32_32x32x16_bf16 v[48:63], a[20:23], a[36:39], v[48:63]\n"
"ds_read_b128 a[12:15], v2 offset:3072\n"
"v_max3_f32 v6, v6, v46, v47\n"
"s_waitcnt lgkmcnt(5)\n"
"v_mfma_f32_32x32x16_bf16 v[48:63], a[24:27], a[40:43], v[48:63]\n"
"ds_read_b128 a[16:19], v2 offset:4096\n"
"s_waitcnt lgkmcnt(5)\n"
"v_mfma_f32_32x32x16_bf16 v[48:63], a[28:31], a[44:47], v[48:63]\n"
"ds_read_b128 a[20:23], v2 offset:5120\n"
"s_cmp_eq_u32 %[tm], 0\n"
"s_cbranch_scc1 T0MASK%=\n"
"s_nop 8\n"
"T0SM%=:\n"
"v_max3_f32 v6, v6, v48, v49\n"
"v_max3_f32 v6, v6, v50, v51\n"
"v_max3_f32 v6, v6, v52, v53\n"
"v_max3_f32 v6, v6, v54, v55\n"
"v_max3_f32 v6, v6, v56, v57\n"
"v_max3_f32 v6, v6, v58, v59\n"
"v_max3_f32 v6, v6, v60, v61\n"
"v_max3_f32 v6, v6, v62, v63\n"
"v_mov_b32 v10, v6\n"
"s_nop 1\n"
"v_permlane32_swap_b32 v10, v6\n"
"s_nop 0\n"
"v_max3_f32 v4, v10, v6, v8\n"
"v_max_f32 v4, 0xf149f2ca, v4\n"
"v_sub_f32 v32, v32, v4\n"
"v_sub_f32 v33, v33, v4\n"
"v_sub_f32 v34, v34, v4\n"
"v_sub_f32 v35, v35, v4\n"
"v_sub_f32 v36, v36, v4\n"
"v_sub_f32 v37, v37, v4\n"
"v_sub_f32 v38, v38, v4\n"
"v_sub_f32 v39, v39, v4\n"
"v_sub_f32 v40, v40, v4\n"
"v_sub_f32 v41, v41, v4\n"
"v_sub_f32 v42, v42, v4\n"
"v_sub_f32 v43, v43, v4\n"
"v_sub_f32 v44, v44, v4\n"
"v_sub_f32 v45, v45, v4\n"
"v_sub_f32 v46, v46, v4\n"
"v_sub_f32 v47, v47, v4\n"
"v_exp_f32 v32, v32\n"
"v_sub_f32 v48, v48, v4\n"
"v_exp_f32 v33, v33\n"
"v_sub_f32 v49, v49, v4\n"
"v_exp_f32 v34, v34\n"
"v_sub_f32 v50, v50, v4\n"
"v_exp_f32 v35, v35\n"
"v_sub_f32 v51, v51, v4\n"
"v_exp_f32 v36, v36\n"
"v_sub_f32 v52, v52, v4\n"
"v_exp_f32 v37, v37\n"
"v_sub_f32 v53, v53, v4\n"
"v_exp_f32 v38, v38\n"
"v_sub_f32 v54, v54, v4\n"
"v_exp_f32 v39, v39\n"
"v_sub_f32 v55, v55, v4\n"
"v_exp_f32 v40, v40\n"
"v_sub_f32 v56, v56, v4\n"
"v_exp_f32 v41, v41\n"
"v_sub_f32 v57, v57, v4\n"
"v_exp_f32 v42, v42\n"
"v_sub_f32 v58, v58, v4\n"
"v_exp_f32 v43, v43\n"
"v_sub_f32 v59, v59, v4\n"
"v_exp_f32 v44, v44\n"
"v_sub_f32 v60, v60, v4\n"
"v_exp_f32 v45, v45\n"
"v_sub_f32 v61, v61, v4\n"
"v_exp_f32 v46, v46\n"
"v_sub_f32 v62, v62, v4\n"
"v_exp_f32 v47, v47\n"
"v_sub_f32 v63, v63, v4\n"
"v_exp_f32 v48, v48\n"
"v_add_f32 v5, v5, v32\n"
"v_add_f32 v5, v5, v33\n"
"v_cvt_pk_bf16_f32 v64, v32, v33\n"
"v_exp_f32 v49, v49\n"
"v_add_f32 v5, v5, v34\n"
"v_add_f32 v5, v5, v35\n"
"v_cvt_pk_bf16_f32 v65, v34, v35\n"
"v_exp_f32 v50, v50\n"
"v_add_f32 v5, v5, v36\n"
"v_add_f32 v5, v5, v37\n"
"v_cvt_pk_bf16_f32 v66, v36, v37\n"
"v_exp_f32 v51, v51\n"
"v_add_f32 v5, v5, v38\n"
"v_add_f32 v5, v5, v39\n"
"v_cvt_pk_bf16_f32 v67, v38, v39\n"
"v_exp_f32 v52, v52\n"
"v_add_f32 v5, v5, v40\n"
"v_add_f32 v5, v5, v41\n"
"v_cvt_pk_bf16_f32 v68, v40, v41\n"
"v_exp_f32 v53, v53\n"
"v_add_f32 v5, v5, v42\n"
"v_add_f32 v5, v5, v43\n"
"v_cvt_pk_bf16_f32 v69, v42, v43\n"
"v_exp_f32 v54, v54\n"
"v_add_f32 v5, v5, v44\n"
"v_add_f32 v5, v5, v45\n"
"v_cvt_pk_bf16_f32 v70, v44, v45\n"
"v_exp_f32 v55, v55\n"
"v_add_f32 v5, v5, v46\n"
"v_add_f32 v5, v5, v47\n"
"v_cvt_pk_bf16_f32 v71, v46, v47\n"
"v_mul_f32 v16, -1.0, v4\n"
"v_mul_f32 v17, -1.0, v4\n"
"v_mul_f32 v18, -1.0, v4\n"
"v_mul_f32 v19, -1.0, v4\n"
"v_mul_f32 v20, -1.0, v4\n"
"v_mul_f32 v21, -1.0, v4\n"
"v_mul_f32 v22, -1.0, v4\n"
"v_mul_f32 v23, -1.0, v4\n"
"v_mul_f32 v24, -1.0, v4\n"
"v_mul_f32 v25, -1.0, v4\n"
"v_mul_f32 v26, -1.0, v4\n"
"v_mul_f32 v27, -1.0, v4\n"
"v_mul_f32 v28, -1.0, v4\n"
"v_mul_f32 v29, -1.0, v4\n"
"v_mul_f32 v30, -1.0, v4\n"
"v_mul_f32 v31, -1.0, v4\n"
"v_add_f32 v5, v5, v48\n"
"v_add_f32 v5, v5, v49\n"
"v_add_f32 v5, v5, v50\n"
"v_add_f32 v5, v5, v51\n"
"v_add_f32 v5, v5, v52\n"
"v_add_f32 v5, v5, v53\n"
"v_add_f32 v5, v5, v54\n"
"v_cvt_pk_bf16_f32 v72, v48, v49\n"
"v_cvt_pk_bf16_f32 v73, v50, v51\n"
"v_cvt_pk_bf16_f32 v74, v52, v53\n"
"v_subrev_u32 v7, 64, v7\n"
"s_waitcnt vmcnt(0)\n"
"s_barrier\n"
"s_cmp_lt_u32 s70, s76\n"
"s_cbranch_scc0 MSTEP%=\n"
"USTEP%=:\n"
"s_waitcnt lgkmcnt(5)\n"
"v_mfma_f32_32x32x16_bf16 v[32:47], a[0:3], v[80:83], v[16:31]\n"
"ds_read_b128 a[24:27], v2 offset:6144\n"
"v_exp_f32 v56, v56\n"
"v_add_f32 v5, v5, v55\n"
"v_cvt_pk_bf16_f32 v75, v54, v55\n"
"s_waitcnt lgkmcnt(5)\n"
"v_mfma_f32_32x32x16_bf16 v[32:47], a[4:7], v[84:87], v[32:47]\n"
"ds_read_b128 a[28:31], v2 offset:7168\n"
"v_exp_f32 v57, v57\n"
"v_add_f32 v5, v5, v56\n"
"s_waitcnt lgkmcnt(5)\n"
"v_mfma_f32_32x32x16_bf16 v[32:47], a[8:11], v[88:91], v[32:47]\n"
"ds_read_b128 a[0:3], v2 offset:8192\n"
"v_exp_f32 v58, v58\n"
"v_add_f32 v5, v5, v57\n"
"v_cvt_pk_bf16_f32 v76, v56, v57\n"
"s_waitcnt lgkmcnt(5)\n"
"v_mfma_f32_32x32x16_bf16 v[32:47], a[12:15], v[92:95], v[32:47]\n"
"ds_read_b128 a[4:7], v2 offset:9216\n"
"v_exp_f32 v59, v59\n"
"v_add_f32 v5, v5, v58\n"
"s_waitcnt lgkmcnt(5)\n"
"v_mfma_f32_32x32x16_bf16 v[32:47], a[16:19], v[96:99], v[32:47]\n"
"ds_read_b128 a[8:11], v2 offset:10240\n"
"v_exp_f32 v60, v60\n"
"v_add_f32 v5, v5, v59\n"
"v_cvt_pk_bf16_f32 v77, v58, v59\n"
"s_waitcnt lgkmcnt(5)\n"
"v_mfma_f32_32x32x16_bf16 v[32:47], a[20:23], v[100:103], v[32:47]\n"
"ds_read_b128 a[12:15], v2 offset:11264\n"
"v_exp_f32 v61, v61\n"
"v_add_f32 v5, v5, v60\n"
"s_waitcnt lgkmcnt(5)\n"
"v_mfma_f32_32x32x16_bf16 v[32:47], a[24:27], v[104:107], v[32:47]\n"
"ds_read_b128 a[16:19], v2 offset:12288\n"
"v_exp_f32 v62, v62\n"
"v_add_f32 v5, v5, v61\n"
"v_cvt_pk_bf16_f32 v78, v60, v61\n"
"s_waitcnt lgkmcnt(5)\n"
"v_mfma_f32_32x32x16_bf16 v[32:47], a[28:31], v[108:111], v[32:47]\n"
"ds_read_b128 a[20:23], v2 offset:13312\n"
"v_exp_f32 v63, v63\n"
"v_add_f32 v5, v5, v62\n"
"s_waitcnt lgkmcnt(5)\n"
"v_mfma_f32_32x32x16_bf16 v[32:47], a[0:3], v[112:115], v[32:47]\n"
"ds_read_b128 a[24:27], v2 offset:14336\n"
"v_add_f32 v5, v5, v63\n"
"v_cvt_pk_bf16_f32 v79, v62, v63\n"
"s_mov_b32 m0, s69\n"
"s_mov_b32 s71, s65\n"
"s_waitcnt lgkmcnt(5)\n"
"v_mfma_f32_32x32x16_bf16 v[32:47], a[4:7], v[116:119], v[32:47]\n"
"ds_read_b128 a[28:31], v2 offset:15360\n"
"buffer_load_dwordx4 v1, %[rsW], s71 offen sc1 lds\n"
"s_waitcnt lgkmcnt(5)\n"
"v_mfma_f32_32x32x16_bf16 v[32:47], a[8:11], v[120:123], v[32:47]\n"
"ds_read_b128 a[0:3], v2 offset:16384\n"
"buffer_load_dwordx4 v1, %[rsW], s71 offen offset:1024 sc1 lds\n"
"s_waitcnt lgkmcnt(5)\n"
"v_mfma_f32_32x32x16_bf16 v[32:47], a[12:15], v[124:127], v[32:47]\n"
"ds_read_b128 a[4:7], v2 offset:17408\n"
"buffer_load_dwordx4 v1, %[rsW], s71 offen offset:2048 sc1 lds\n"
"s_waitcnt lgkmcnt(5)\n"
"v_mfma_f32_32x32x16_bf16 v[32:47], a[16:19], a[32:35], v[32:47]\n"
"ds_read_b128 a[8:11], v2 offset:18432\n"
"buffer_load_dwordx4 v1, %[rsW], s71 offen offset:3072 sc1 lds\n"
"s_add_u32 m0, m0, 0x1000\n"
"s_add_u32 s71, s71, 0x1000\n"
"s_waitcnt lgkmcnt(5)\n"
"v_mfma_f32_32x32x16_bf16 v[32:47], a[20:23], a[36:39], v[32:47]\n"
"ds_read_b128 a[12:15], v2 offset:19456\n"
"buffer_load_dwordx4 v1, %[rsW], s71 offen sc1 lds\n"
"s_waitcnt lgkmcnt(5)\n"
"v_mfma_f32_32x32x16_bf16 v[32:47], a[24:27], a[40:43], v[32:47]\n"
"ds_read_b128 a[16:19], v2 offset:20480\n"
"buffer_load_dwordx4 v1, %[rsW], s71 offen offset:1024 sc1 lds\n"
"s_waitcnt lgkmcnt(5)\n"
"v_mfma_f32_32x32x16_bf16 v[32:47], a[28:31], a[44:47], v[32:47]\n"
"ds_read_b128 a[20:23], v2 offset:21504\n"
"buffer_load_dwordx4 v1, %[rsW], s71 offen offset:2048 sc1 lds\n"
"s_waitcnt lgkmcnt(5)\n"
"v_mfma_f32_32x32x16_bf16 v[48:63], a[0:3], v[80:83], v[16:31]\n"
"ds_read_b128 a[24:27], v2 offset:22528\n"
"buffer_load_dwordx4 v1, %[rsW], s71 offen offset:3072 sc1 lds\n"
"s_add_u32 m0, s67, s74\n"
"s_mov_b32 s71, s64\n"
"s_waitcnt lgkmcnt(5)\n"
"v_mfma_f32_32x32x16_bf16 v[48:63], a[4:7], v[84:87], v[48:63]\n"
"ds_read_b128 a[28:31], v2 offset:23552\n"
"buffer_load_dwordx4 v1, %[rsW], s71 offen sc1 lds\n"
"s_waitcnt lgkmcnt(5)\n"
"v_mfma_f32_32x32x16_bf16 v[48:63], a[8:11], v[88:91], v[48:63]\n"
"ds_read_b128 a[0:3], v2 offset:24576\n"
"buffer_load_dwordx4 v1, %[rsW], s71 offen offset:1024 sc1 lds\n"
"s_waitcnt lgkmcnt(5)\n"
"v_mfma_f32_32x32x16_bf16 v[48:63], a[12:15], v[92:95], v[48:63]\n"
"ds_read_b128 a[4:7], v2 offset:25600\n"
"buffer_load_dwordx4 v1, %[rsW], s71 offen offset:2048 sc1 lds\n"
"s_waitcnt lgkmcnt(5)\n"
"v_mfma_f32_32x32x16_bf16 v[48:63], a[16:19], v[96:99], v[48:63]\n"
"ds_read_b128 a[8:11], v2 offset:26624\n"
"buffer_load_dwordx4 v1, %[rsW], s71 offen offset:3072 sc1 lds\n"
"s_add_u32 m0, m0, 0x1000\n"
"s_add_u32 s71, s71, 0x1000\n"
"s_waitcnt lgkmcnt(5)\n"
"v_mfma_f32_32x32x16_bf16 v[48:63], a[20:23], v[100:103], v[48:63]\n"
"ds_read_b128 a[12:15], v2 offset:27648\n"
"buffer_load_dwordx4 v1, %[rsW], s71 offen sc1 lds\n"
"s_waitcnt lgkmcnt(5)\n"
"v_mfma_f32_32x32x16_bf16 v[48:63], a[24:27], v[104:107], v[48:63]\n"
"ds_read_b128 a[16:19], v2 offset:28672\n"
"buffer_load_dwordx4 v1, %[rsW], s71 offen offset:1024 sc1 lds\n"
"s_waitcnt lgkmcnt(5)\n"
"v_mfma_f32_32x32x16_bf16 v[48:63], a[28:31], v[108:111], v[48:63]\n"
"ds_read_b128 a[20:23], v2 offset:29696\n"
"buffer_load_dwordx4 v1, %[rsW], s71 offen offset:2048 sc1 lds\n"
"s_waitcnt lgkmcnt(5)\n"
"v_mfma_f32_32x32x16_bf16 v[48:63], a[0:3], v[112:115], v[48:63]\n"
"ds_read_b128 a[24:27], v2 offset:30720\n"
"buffer_load_dwordx4 v1, %[rsW], s71 offen offset:3072 sc1 lds\n"
"v_max_f32 v6, v32, v33\n"
"s_waitcnt lgkmcnt(5)\n"
"v_mfma_f32_32x32x16_bf16 v[48:63], a[4:7], v[116:119], v[48:63]\n"
"ds_read_b128 a[28:31], v2 offset:31744\n"
"v_max3_f32 v6, v6, v34, v35\n"
"s_waitcnt lgkmcnt(5)\n"
"v_mfma_f32_32x32x16_bf16 v[48:63], a[8:11], v[120:123], v[48:63]\n"
"ds_read_b128 a[0:3], v3\n"
"v_max3_f32 v6, v6, v36, v37\n"
"s_waitcnt lgkmcnt(5)\n"
"v_mfma_f32_32x32x16_bf16 v[48:63], a[12:15], v[124:127], v[48:63]\n"
"ds_read_b128 a[4:7], v3 offset:1024\n"
"v_max3_f32 v6, v6, v38, v39\n"
"s_waitcnt lgkmcnt(5)\n"
"v_mfma_f32_32x32x16_bf16 v[48:63], a[16:19], a[32:35], v[48:63]\n"
"ds_read_b128 a[8:11], v3 offset:2048\n"
"v_max3_f32 v6, v6, v40, v41\n"
"s_xor_b32 s69, s69, 0x10000\n"
"s_add_u32 s65, s65, 0x8000\n"
"s_waitcnt lgkmcnt(5)\n"
"v_mfma_f32_32x32x16_bf16 v[48:63], a[20:23], a[36:39], v[48:63]\n"
"ds_read_b128 a[12:15], v3 offset:3072\n"
"v_max3_f32 v6, v6, v42, v43\n"
"s_add_u32 s64, s64, 0x8000\n"
"s_waitcnt lgkmcnt(5)\n"
"v_mfma_f32_32x32x16_bf16 v[48:63], a[24:27], a[40:43], v[48:63]\n"
"ds_read_b128 a[16:19], v3 offset:4096\n"
"v_max3_f32 v6, v6, v44, v45\n"
"s_waitcnt lgkmcnt(5)\n"
"v_mfma_f32_32x32x16_bf16 v[48:63], a[28:31], a[44:47], v[48:63]\n"
"ds_read_b128 a[20:23], v3 offset:5120\n"
"v_max3_f32 v6, v6, v46, v47\n"
"s_cmp_lg_u32 s73, 0\n"
"s_cbranch_scc1 USTEPF%=\n"
"s_waitcnt lgkmcnt(5)\n"
"v_mfma_f32_32x32x16_bf16 v[128:143], a[0:3], v[64:67], v[128:143]\n"
"ds_read_b128 a[24:27], v3 offset:6144\n"
"v_add_u32 v2, s66, v1\n"
"s_mov_b32 s77, s66\n"
"s_mov_b32 s66, s67\n"
"s_waitcnt lgkmcnt(5)\n"
"v_mfma_f32_32x32x16_bf16 v[144:159], a[4:7], v[64:67], v[144:159]\n"
"ds_read_b128 a[28:31], v3 offset:7168\n"
"s_mov_b32 s67, s68\n"
"s_mov_b32 s68, s77\n"
"s_add_u32 s70, s70, 1\n"
"s_waitcnt lgkmcnt(5)\n"
"v_mfma_f32_32x32x16_bf16 v[160:175], a[8:11], v[64:67], v[160:175]\n"
"ds_read_b128 a[0:3], v3 offset:8192\n"
"s_waitcnt lgkmcnt(5)\n"
"v_mfma_f32_32x32x16_bf16 v[176:191], a[12:15], v[64:67], v[176:191]\n"
"ds_read_b128 a[4:7], v3 offset:9216\n"
"v_max3_f32 v6, v6, v48, v49\n"
"v_max3_f32 v6, v6, v50, v51\n"
"v_max3_f32 v6, v6, v52, v53\n"
"s_waitcnt lgkmcnt(5)\n"
"v_mfma_f32_32x32x16_bf16 v[192:207], a[16:19], v[64:67], v[192:207]\n"
"ds_read_b128 a[8:11], v3 offset:10240\n"
"v_max3_f32 v6, v6, v54, v55\n"
"v_max3_f32 v6, v6, v56, v57\n"
"v_max3_f32 v6, v6, v58, v59\n"
"s_waitcnt lgkmcnt(5)\n"
"v_mfma_f32_32x32x16_bf16 v[208:223], a[20:23], v[64:67], v[208:223]\n"
"ds_read_b128 a[12:15], v3 offset:11264\n"
"v_max3_f32 v6, v6, v60, v61\n"
"v_max3_f32 v6, v6, v62, v63\n"
"s_waitcnt lgkmcnt(5)\n"
"v_mfma_f32_32x32x16_bf16 v[224:239], a[24:27], v[64:67], v[224:239]\n"
"ds_read_b128 a[16:19], v3 offset:12288\n"
"v_mov_b32 v10, v6\n"
"v_subrev_u32 v7, 64, v7\n"
"s_nop 0\n"
"v_permlane32_swap_b32 v10, v6\n"
"s_waitcnt lgkmcnt(5)\n"
"v_mfma_f32_32x32x16_bf16 v[240:255], a[28:31], v[64:67], v[240:255]\n"
"ds_read_b128 a[20:23], v3 offset:13312\n"
"v_max_f32 v6, v10, v6\n"
"v_cmp_lt_f32 vcc, 0x41c00000, v6\n"
"USTEPJ%=:\n"
"s_waitcnt lgkmcnt(5)\n"
"v_mfma_f32_32x32x16_bf16 v[128:143], a[0:3], v[68:71], v[128:143]\n"
"s_cbranch_vccnz USTEPR%=\n"
"USTEPRB%=:\n"
"ds_read_b128 a[24:27], v3 offset:14336\n"
"v_exp_f32 v32, v32\n"
"s_waitcnt lgkmcnt(5)\n"
"v_mfma_f32_32x32x16_bf16 v[144:159], a[4:7], v[68:71], v[144:159]\n"
"ds_read_b128 a[28:31], v3 offset:15360\n"
"v_exp_f32 v33, v33\n"
"v_add_f32 v5, v5, v32\n"
"s_waitcnt lgkmcnt(5)\n"
"v_mfma_f32_32x32x16_bf16 v[160:175], a[8:11], v[68:71], v[160:175]\n"
"ds_read_b128 a[0:3], v3 offset:16384\n"
"v_exp_f32 v34, v34\n"
"v_add_f32 v5, v5, v33\n"
"v_cvt_pk_bf16_f32 v64, v32, v33\n"
"s_waitcnt lgkmcnt(5)\n"
"v_mfma_f32_32x32x16_bf16 v[176:191], a[12:15], v[68:71], v[176:191]\n"
"ds_read_b128 a[4:7], v3 offset:17408\n"
"v_exp_f32 v35, v35\n"
"v_add_f32 v5, v5, v34\n"
"s_waitcnt lgkmcnt(5)\n"
"v_mfma_f32_32x32x16_bf16 v[192:207], a[16:19], v[68:71], v[192:207]\n"
"ds_read_b128 a[8:11], v3 offset:18432\n"
"v_exp_f32 v36, v36\n"
"v_add_f32 v5, v5, v35\n"
"v_cvt_pk_bf16_f32 v65, v34, v35\n"
"s_waitcnt lgkmcnt(5)\n"
"v_mfma_f32_32x32x16_bf16 v[208:223], a[20:23], v[68:71], v[208:223]\n"
"ds_read_b128 a[12:15], v3 offset:19456\n"
"v_exp_f32 v37, v37\n"
"v_add_f32 v5, v5, v36\n"
"s_waitcnt lgkmcnt(5)\n"
"v_mfma_f32_32x32x16_bf16 v[224:239], a[24:27], v[68:71], v[224:239]\n"
"ds_read_b128 a[16:19], v3 offset:20480\n"
"v_exp_f32 v38, v38\n"
"v_add_f32 v5, v5, v37\n"
"v_cvt_pk_bf16_f32 v66, v36, v37\n"
"s_waitcnt lgkmcnt(5)\n"
"v_mfma_f32_32x32x16_bf16 v[240:255], a[28:31], v[68:71], v[240:255]\n"
"ds_read_b128 a[20:23], v3 offset:21504\n"
"v_exp_f32 v39, v39\n"
"v_add_f32 v5, v5, v38\n"
"s_waitcnt lgkmcnt(5)\n"
"v_mfma_f32_32x32x16_bf16 v[128:143], a[0:3], v[72:75], v[128:143]\n"
"ds_read_b128 a[24:27], v3 offset:22528\n"
"v_exp_f32 v40, v40\n"
"v_add_f32 v5, v5, v39\n"
"v_cvt_pk_bf16_f32 v67, v38, v39\n"
"s_waitcnt lgkmcnt(5)\n"
"v_mfma_f32_32x32x16_bf16 v[144:159], a[4:7], v[72:75], v[144:159]\n"
"ds_read_b128 a[28:31], v3 offset:23552\n"
"v_exp_f32 v41, v41\n"
"v_add_f32 v5, v5, v40\n"
"s_waitcnt lgkmcnt(5)\n"
"v_mfma_f32_32x32x16_bf16 v[160:175], a[8:11], v[72:75], v[160:175]\n"
"ds_read_b128 a[0:3], v3 offset:24576\n"
"v_exp_f32 v42, v42\n"
"v_add_f32 v5, v5, v41\n"
"v_cvt_pk_bf16_f32 v68, v40, v41\n"
"s_waitcnt lgkmcnt(5)\n"
"v_mfma_f32_32x32x16_bf16 v[176:191], a[12:15], v[72:75], v[176:191]\n"
"ds_read_b128 a[4:7], v3 offset:25600\n"
"v_exp_f32 v43, v43\n"
"v_add_f32 v5, v5, v42\n"
"s_waitcnt lgkmcnt(5)\n"
"v_mfma_f32_32x32x16_bf16 v[192:207], a[16:19], v[72:75], v[192:207]\n"
"ds_read_b128 a[8:11], v3 offset:26624\n"
"v_exp_f32 v44, v44\n"
"v_add_f32 v5, v5, v43\n"
"v_cvt_pk_bf16_f32 v69, v42, v43\n"
"s_waitcnt lgkmcnt(5)\n"
"v_mfma_f32_32x32x16_bf16 v[208:223], a[20:23], v[72:75], v[208:223]\n"
"ds_read_b128 a[12:15], v3 offset:27648\n"
"v_exp_f32 v45, v45\n"
"v_add_f32 v5, v5, v44\n"
"s_waitcnt lgkmcnt(5)\n"
"v_mfma_f32_32x32x16_bf16 v[224:239], a[24:27], v[72:75], v[224:239]\n"
"ds_read_b128 a[16:19], v3 offset:28672\n"
"v_exp_f32 v46, v46\n"
"v_add_f32 v5, v5, v45\n"
"v_cvt_pk_bf16_f32 v70, v44, v45\n"
"s_waitcnt lgkmcnt(5)\n"
"v_mfma_f32_32x32x16_bf16 v[240:255], a[28:31], v[72:75], v[240:255]\n"
"ds_read_b128 a[20:23], v3 offset:29696\n"
"v_exp_f32 v47, v47\n"
"v_add_f32 v5, v5, v46\n"
"s_waitcnt lgkmcnt(5)\n"
"v_mfma_f32_32x32x16_bf16 v[128:143], a[0:3], v[76:79], v[128:143]\n"
"ds_read_b128 a[24:27], v3 offset:30720\n"
"v_exp_f32 v48, v48\n"
"v_add_f32 v5, v5, v47\n"
"v_cvt_pk_bf16_f32 v71, v46, v47\n"
"s_waitcnt lgkmcnt(5)\n"
"v_mfma_f32_32x32x16_bf16 v[144:159], a[4:7], v[76:79], v[144:159]\n"
"ds_read_b128 a[28:31], v3 offset:31744\n"
"v_exp_f32 v49, v49\n"
"v_add_f32 v5, v5, v48\n"
"s_waitcnt lgkmcnt(5)\n"
"v_mfma_f32_32x32x16_bf16 v[160:175], a[8:11], v[76:79], v[160:175]\n"
"ds_read_b128 a[0:3], v2\n"
"v_exp_f32 v50, v50\n"
"v_add_f32 v5, v5, v49\n"
"v_cvt_pk_bf16_f32 v72, v48, v49\n"
"s_waitcnt lgkmcnt(5)\n"
"v_mfma_f32_32x32x16_bf16 v[176:191], a[12:15], v[76:79], v[176:191]\n"
"ds_read_b128 a[4:7], v2 offset:1024\n"
"v_exp_f32 v51, v51\n"
"v_add_f32 v5, v5, v50\n"
"v_xor_b32 v3, 0x10000, v3\n"
"s_waitcnt lgkmcnt(5)\n"
"v_mfma_f32_32x32x16_bf16 v[192:207], a[16:19], v[76:79], v[192:207]\n"
"ds_read_b128 a[8:11], v2 offset:2048\n"
"v_exp_f32 v52, v52\n"
"v_add_f32 v5, v5, v51\n"
"v_cvt_pk_bf16_f32 v73, v50, v51\n"
"s_waitcnt lgkmcnt(5)\n"
"v_mfma_f32_32x32x16_bf16 v[208:223], a[20:23], v[76:79], v[208:223]\n"
"ds_read_b128 a[12:15], v2 offset:3072\n"
"v_exp_f32 v53, v53\n"
"v_add_f32 v5, v5, v52\n"
"s_waitcnt lgkmcnt(5)\n"
"v_mfma_f32_32x32x16_bf16 v[224:239], a[24:27], v[76:79], v[224:239]\n"
"ds_read_b128 a[16:19], v2 offset:4096\n"
"v_exp_f32 v54, v54\n"
"v_add_f32 v5, v5, v53\n"
"v_cvt_pk_bf16_f32 v74, v52, v53\n"
"s_waitcnt lgkmcnt(5)\n"
"v_mfma_f32_32x32x16_bf16 v[240:255], a[28:31], v[76:79], v[240:255]\n"
"ds_read_b128 a[20:23], v2 offset:5120\n"
"v_exp_f32 v55, v55\n"
"v_add_f32 v5, v5, v54\n"
"s_cmp_lg_u32 s72, 0\n"
"s_cbranch_scc1 USTEPS%=\n"
"USTEPSB%=:\n"
"s_cmp_lt_u32 s70, s76\n"
"s_waitcnt vmcnt(0)\n"
"s_barrier\n"
"s_cbranch_scc1 USTEP%=\n"
"s_cmp_ge_u32 s70, %[n]\n"
"s_cbranch_scc1 DRAIN%=\n"
"MSTEP%=:\n"
"s_waitcnt lgkmcnt(5)\n"
"v_mfma_f32_32x32x16_bf16 v[32:47], a[0:3], v[80:83], v[16:31]\n"
"ds_read_b128 a[24:27], v2 offset:6144\n"
"v_exp_f32 v56, v56\n"
"v_add_f32 v5, v5, v55\n"
"v_cvt_pk_bf16_f32 v75, v54, v55\n"
"s_waitcnt lgkmcnt(5)\n"
"v_mfma_f32_32x32x16_bf16 v[32:47], a[4:7], v[84:87], v[32:47]\n"
"ds_read_b128 a[28:31], v2 offset:7168\n"
"v_exp_f32 v57, v57\n"
"v_add_f32 v5, v5, v56\n"
"s_waitcnt lgkmcnt(5)\n"
"v_mfma_f32_32x32x16_bf16 v[32:47], a[8:11], v[88:91], v[32:47]\n"
"ds_read_b128 a[0:3], v2 offset:8192\n"
"v_exp_f32 v58, v58\n"
"v_add_f32 v5, v5, v57\n"
"v_cvt_pk_bf16_f32 v76, v56, v57\n"
"s_waitcnt lgkmcnt(5)\n"
"v_mfma_f32_32x32x16_bf16 v[32:47], a[12:15], v[92:95], v[32:47]\n"
"ds_read_b128 a[4:7], v2 offset:9216\n"
"v_exp_f32 v59, v59\n"
"v_add_f32 v5, v5, v58\n"
"s_waitcnt lgkmcnt(5)\n"
"v_mfma_f32_32x32x16_bf16 v[32:47], a[16:19], v[96:99], v[32:47]\n"
"ds_read_b128 a[8:11], v2 offset:10240\n"
"v_exp_f32 v60, v60\n"
"v_add_f32 v5, v5, v59\n"
"v_cvt_pk_bf16_f32 v77, v58, v59\n"
"s_waitcnt lgkmcnt(5)\n"
"v_mfma_f32_32x32x16_bf16 v[32:47], a[20:23], v[100:103], v[32:47]\n"
"ds_read_b128 a[12:15], v2 offset:11264\n"
"v_exp_f32 v61, v61\n"
"v_add_f32 v5, v5, v60\n"
"s_waitcnt lgkmcnt(5)\n"
"v_mfma_f32_32x32x16_bf16 v[32:47], a[24:27], v[104:107], v[32:47]\n"
"ds_read_b128 a[16:19], v2 offset:12288\n"
"v_exp_f32 v62, v62\n"
"v_add_f32 v5, v5, v61\n"
"v_cvt_pk_bf16_f32 v78, v60, v61\n"
"s_waitcnt lgkmcnt(5)\n"
"v_mfma_f32_32x32x16_bf16 v[32:47], a[28:31], v[108:111], v[32:47]\n"
"ds_read_b128 a[20:23], v2 offset:13312\n"
"v_exp_f32 v63, v63\n"
"v_add_f32 v5, v5, v62\n"
"s_waitcnt lgkmcnt(5)\n"
"v_mfma_f32_32x32x16_bf16 v[32:47], a[0:3], v[112:115], v[32:47]\n"
"ds_read_b128 a[24:27], v2 offset:14336\n"
"v_add_f32 v5, v5, v63\n"
"v_cvt_pk_bf16_f32 v79, v62, v63\n"
"s_mov_b32 m0, s69\n"
"s_mov_b32 s71, s65\n"
"s_waitcnt lgkmcnt(5)\n"
"v_mfma_f32_32x32x16_bf16 v[32:47], a[4:7], v[116:119], v[32:47]\n"
"ds_read_b128 a[28:31], v2 offset:15360\n"
"buffer_load_dwordx4 v1, %[rsW], s71 offen sc1 lds\n"
"s_waitcnt lgkmcnt(5)\n"
"v_mfma_f32_32x32x16_bf16 v[32:47], a[8:11], v[120:123], v[32:47]\n"
"ds_read_b128 a[0:3], v2 offset:16384\n"
"buffer_load_dwordx4 v1, %[rsW], s71 offen offset:1024 sc1 lds\n"
"s_waitcnt lgkmcnt(5)\n"
"v_mfma_f32_32x32x16_bf16 v[32:47], a[12:15], v[124:127], v[32:47]\n"
"ds_read_b128 a[4:7], v2 offset:17408\n"
"buffer_load_dwordx4 v1, %[rsW], s71 offen offset:2048 sc1 lds\n"
"s_waitcnt lgkmcnt(5)\n"
"v_mfma_f32_32x32x16_bf16 v[32:47], a[16:19], a[32:35], v[32:47]\n"
"ds_read_b128 a[8:11], v2 offset:18432\n"
"buffer_load_dwordx4 v1, %[rsW], s71 offen offset:3072 sc1 lds\n"
"s_add_u32 m0, m0, 0x1000\n"
"s_add_u32 s71, s71, 0x1000\n"
"s_waitcnt lgkmcnt(5)\n"
"v_mfma_f32_32x32x16_bf16 v[32:47], a[20:23], a[36:39], v[32:47]\n"
"ds_read_b128 a[12:15], v2 offset:19456\n"
"buffer_load_dwordx4 v1, %[rsW], s71 offen sc1 lds\n"
"s_waitcnt lgkmcnt(5)\n"
"v_mfma_f32_32x32x16_bf16 v[32:47], a[24:27], a[40:43], v[32:47]\n"
"ds_read_b128 a[16:19], v2 offset:20480\n"
"buffer_load_dwordx4 v1, %[rsW], s71 offen offset:1024 sc1 lds\n"
"s_waitcnt lgkmcnt(5)\n"
"v_mfma_f32_32x32x16_bf16 v[32:47], a[28:31], a[44:47], v[32:47]\n"
"ds_read_b128 a[20:23], v2 offset:21504\n"
"buffer_load_dwordx4 v1, %[rsW], s71 offen offset:2048 sc1 lds\n"
"s_waitcnt lgkmcnt(5)\n"
"v_mfma_f32_32x32x16_bf16 v[48:63], a[0:3], v[80:83], v[16:31]\n"
"ds_read_b128 a[24:27], v2 offset:22528\n"
"buffer_load_dwordx4 v1, %[rsW], s71 offen offset:3072 sc1 lds\n"
"s_add_u32 m0, s67, s74\n"
"s_mov_b32 s71, s64\n"
"s_waitcnt lgkmcnt(5)\n"
"v_mfma_f32_32x32x16_bf16 v[48:63], a[4:7], v[84:87], v[48:63]\n"
"ds_read_b128 a[28:31], v2 offset:23552\n"
"buffer_load_dwordx4 v1, %[rsW], s71 offen sc1 lds\n"
"s_waitcnt lgkmcnt(5)\n"
"v_mfma_f32_32x32x16_bf16 v[48:63], a[8:11], v[88:91], v[48:63]\n"
"ds_read_b128 a[0:3], v2 offset:24576\n"
"buffer_load_dwordx4 v1, %[rsW], s71 offen offset:1024 sc1 lds\n"
"s_waitcnt lgkmcnt(5)\n"
"v_mfma_f32_32x32x16_bf16 v[48:63], a[12:15], v[92:95], v[48:63]\n"
"ds_read_b128 a[4:7], v2 offset:25600\n"
"buffer_load_dwordx4 v1, %[rsW], s71 offen offset:2048 sc1 lds\n"
"s_waitcnt lgkmcnt(5)\n"
"v_mfma_f32_32x32x16_bf16 v[48:63], a[16:19], v[96:99], v[48:63]\n"
"ds_read_b128 a[8:11], v2 offset:26624\n"
"buffer_load_dwordx4 v1, %[rsW], s71 offen offset:3072 sc1 lds\n"
"s_add_u32 m0, m0, 0x1000\n"
"s_add_u32 s71, s71, 0x1000\n"
"v_cmp_lt_i32_e64 s[78:79], v7, 0\n"
"v_cmp_lt_i32_e64 s[80:81], v7, 1\n"
"v_cmp_lt_i32_e64 s[82:83], v7, 2\n"
"v_cndmask_b32_e64 v32, v32, v8, s[78:79]\n"
"s_waitcnt lgkmcnt(5)\n"
"v_mfma_f32_32x32x16_bf16 v[48:63], a[20:23], v[100:103], v[48:63]\n"
"ds_read_b128 a[12:15], v2 offset:27648\n"
"buffer_load_dwordx4 v1, %[rsW], s71 offen sc1 lds\n"
"v_cmp_lt_i32_e64 s[78:79], v7, 3\n"
"v_cndmask_b32_e64 v33, v33, v8, s[80:81]\n"
"v_cmp_lt_i32_e64 s[80:81], v7, 16\n"
"v_cndmask_b32_e64 v34, v34, v8, s[82:83]\n"
"s_waitcnt lgkmcnt(5)\n"
"v_mfma_f32_32x32x16_bf16 v[48:63], a[24:27], v[104:107], v[48:63]\n"
"ds_read_b128 a[16:19], v2 offset:28672\n"
"buffer_load_dwordx4 v1, %[rsW], s71 offen offset:1024 sc1 lds\n"
"v_cmp_lt_i32_e64 s[82:83], v7, 17\n"
"v_cndmask_b32_e64 v35, v35, v8, s[78:79]\n"
"v_cmp_lt_i32_e64 s[78:79], v7, 18\n"
"v_cndmask_b32_e64 v36, v36, v8, s[80:81]\n"
"s_waitcnt lgkmcnt(5)\n"
"v_mfma_f32_32x32x16_bf16 v[48:63], a[28:31], v[108:111], v[48:63]\n"
"ds_read_b128 a[20:23], v2 offset:29696\n"
"buffer_load_dwordx4 v1, %[rsW], s71 offen offset:2048 sc1 lds\n"
"v_cmp_lt_i32_e64 s[80:81], v7, 19\n"
"v_cndmask_b32_e64 v37, v37, v8, s[82:83]\n"
"v_cmp_lt_i32_e64 s[82:83], v7, 8\n"
"v_cndmask_b32_e64 v38, v38, v8, s[78:79]\n"
"s_waitcnt lgkmcnt(5)\n"
"v_mfma_f32_32x32x16_bf16 v[48:63], a[0:3], v[112:115], v[48:63]\n"
"ds_read_b128 a[24:27], v2 offset:30720\n"
"buffer_load_dwordx4 v1, %[rsW], s71 offen offset:3072 sc1 lds\n"
"v_cmp_lt_i32_e64 s[78:79], v7, 9\n"
"v_cndmask_b32_e64 v39, v39, v8, s[80:81]\n"
"v_cmp_lt_i32_e64 s[80:81], v7, 10\n"
"s_waitcnt lgkmcnt(5)\n"
"v_mfma_f32_32x32x16_bf16 v[48:63], a[4:7], v[116:119], v[48:63]\n"
"ds_read_b128 a[28:31], v2 offset:31744\n"
"v_cndmask_b32_e64 v40, v40, v8, s[82:83]\n"
"v_cmp_lt_i32_e64 s[82:83], v7, 11\n"
"v_cndmask_b32_e64 v41, v41, v8, s[78:79]\n"
"s_waitcnt lgkmcnt(5)\n"
"v_mfma_f32_32x32x16_bf16 v[48:63], a[8:11], v[120:123], v[48:63]\n"
"ds_read_b128 a[0:3], v3\n"
"v_cmp_lt_i32_e64 s[78:79], v7, 24\n"
"v_cndmask_b32_e64 v42, v42, v8, s[80:81]\n"
"v_cmp_lt_i32_e64 s[80:81], v7, 25\n"
"s_waitcnt lgkmcnt(5)\n"
"v_mfma_f32_32x32x16_bf16 v[48:63], a[12:15], v[124:127], v[48:63]\n"
"ds_read_b128 a[4:7], v3 offset:1024\n"
"v_cndmask_b32_e64 v43, v43, v8, s[82:83]\n"
"v_cmp_lt_i32_e64 s[82:83], v7, 26\n"
"v_cndmask_b32_e64 v44, v44, v8, s[78:79]\n"
"s_waitcnt lgkmcnt(5)\n"
"v_mfma_f32_32x32x16_bf16 v[48:63], a[16:19], a[32:35], v[48:63]\n"
"ds_read_b128 a[8:11], v3 offset:2048\n"
"v_cmp_lt_i32_e64 s[78:79], v7, 27\n"
"v_cndmask_b32_e64 v45, v45, v8, s[80:81]\n"
"v_cndmask_b32_e64 v46, v46, v8, s[82:83]\n"
"s_xor_b32 s69, s69, 0x10000\n"
"s_add_u32 s65, s65, 0x8000\n"
"s_waitcnt lgkmcnt(5)\n"
"v_mfma_f32_32x32x16_bf16 v[48:63], a[20:23], a[36:39], v[48:63]\n"
"ds_read_b128 a[12:15], v3 offset:3072\n"
"v_cndmask_b32_e64 v47, v47, v8, s[78:79]\n"
"v_max_f32 v6, v32, v33\n"
"v_max3_f32 v6, v6, v34, v35\n"
"s_add_u32 s64, s64, 0x8000\n"
"s_waitcnt lgkmcnt(5)\n"
"v_mfma_f32_32x32x16_bf16 v[48:63], a[24:27], a[40:43], v[48:63]\n"
"ds_read_b128 a[16:19], v3 offset:4096\n"
"v_max3_f32 v6, v6, v36, v37\n"
"v_max3_f32 v6, v6, v38, v39\n"
"v_max3_f32 v6, v6, v40, v41\n"
"s_waitcnt lgkmcnt(5)\n"
"v_mfma_f32_32x32x16_bf16 v[48:63], a[28:31], a[44:47], v[48:63]\n"
"ds_read_b128 a[20:23], v3 offset:5120\n"
"v_max3_f32 v6, v6, v42, v43\n"
"v_max3_f32 v6, v6, v44, v45\n"
"v_max3_f32 v6, v6, v46, v47\n"
"s_cmp_lg_u32 s73, 0\n"
"s_cbranch_scc1 MSTEPF%=\n"
"s_waitcnt lgkmcnt(5)\n"
"v_mfma_f32_32x32x16_bf16 v[128:143], a[0:3], v[64:67], v[128:143]\n"
"ds_read_b128 a[24:27], v3 offset:6144\n"
"v_add_u32 v2, s66, v1\n"
"s_mov_b32 s77, s66\n"
"s_mov_b32 s66, s67\n"
"s_waitcnt lgkmcnt(5)\n"
"v_mfma_f32_32x32x16_bf16 v[144:159], a[4:7], v[64:67], v[144:159]\n"
"ds_read_b128 a[28:31], v3 offset:7168\n"
"s_mov_b32 s67, s68\n"
"s_mov_b32 s68, s77\n"
"s_add_u32 s70, s70, 1\n"
"s_waitcnt lgkmcnt(5)\n"
"v_mfma_f32_32x32x16_bf16 v[160:175], a[8:11], v[64:67], v[160:175]\n"
"ds_read_b128 a[0:3], v3 offset:8192\n"
"s_waitcnt lgkmcnt(5)\n"
"v_mfma_f32_32x32x16_bf16 v[176:191], a[12:15], v[64:67], v[176:191]\n"
"ds_read_b128 a[4:7], v3 offset:9216\n"
"v_cmp_lt_i32_e64 s[78:79], v7, 32\n"
"v_cmp_lt_i32_e64 s[80:81], v7, 33\n"
"v_cmp_lt_i32_e64 s[82:83], v7, 34\n"
"v_cndmask_b32_e64 v48, v48, v8, s[78:79]\n"
"v_cmp_lt_i32_e64 s[78:79], v7, 35\n"
"v_cndmask_b32_e64 v49, v49, v8, s[80:81]\n"
"v_cmp_lt_i32_e64 s[80:81], v7, 48\n"
"v_cndmask_b32_e64 v50, v50, v8, s[82:83]\n"
"v_cmp_lt_i32_e64 s[82:83], v7, 49\n"
"v_cndmask_b32_e64 v51, v51, v8, s[78:79]\n"
"v_cmp_lt_i32_e64 s[78:79], v7, 50\n"
"v_cndmask_b32_e64 v52, v52, v8, s[80:81]\n"
"v_cmp_lt_i32_e64 s[80:81], v7, 51\n"
"v_cndmask_b32_e64 v53, v53, v8, s[82:83]\n"
"s_waitcnt lgkmcnt(5)\n"
"v_mfma_f32_32x32x16_bf16 v[192:207], a[16:19], v[64:67], v[192:207]\n"
"ds_read_b128 a[8:11], v3 offset:10240\n"
"v_cmp_lt_i32_e64 s[82:83], v7, 40\n"
"v_cndmask_b32_e64 v54, v54, v8, s[78:79]\n"
"v_cmp_lt_i32_e64 s[78:79], v7, 41\n"
"v_cndmask_b32_e64 v55, v55, v8, s[80:81]\n"
"v_cmp_lt_i32_e64 s[80:81], v7, 42\n"
"v_cndmask_b32_e64 v56, v56, v8, s[82:83]\n"
"v_cmp_lt_i32_e64 s[82:83], v7, 43\n"
"v_cndmask_b32_e64 v57, v57, v8, s[78:79]\n"
"v_cmp_lt_i32_e64 s[78:79], v7, 56\n"
"v_cndmask_b32_e64 v58, v58, v8, s[80:81]\n"
"v_cmp_lt_i32_e64 s[80:81], v7, 57\n"
"v_cndmask_b32_e64 v59, v59, v8, s[82:83]\n"
"v_cmp_lt_i32_e64 s[82:83], v7, 58\n"
"s_waitcnt lgkmcnt(5)\n"
"v_mfma_f32_32x32x16_bf16 v[208:223], a[20:23], v[64:67], v[208:223]\n"
"ds_read_b128 a[12:15], v3 offset:11264\n"
"v_cndmask_b32_e64 v60, v60, v8, s[78:79]\n"
"v_cmp_lt_i32_e64 s[78:79], v7, 59\n"
"v_cndmask_b32_e64 v61, v61, v8, s[80:81]\n"
"v_cndmask_b32_e64 v62, v62, v8, s[82:83]\n"
"v_cndmask_b32_e64 v63, v63, v8, s[78:79]\n"
"v_max3_f32 v6, v6, v48, v49\n"
"v_max3_f32 v6, v6, v50, v51\n"
"v_max3_f32 v6, v6, v52, v53\n"
"v_max3_f32 v6, v6, v54, v55\n"
"v_max3_f32 v6, v6, v56, v57\n"
"v_max3_f32 v6, v6, v58, v59\n"
"v_max3_f32 v6, v6, v60, v61\n"
"v_max3_f32 v6, v6, v62, v63\n"
"s_waitcnt lgkmcnt(5)\n"
"v_mfma_f32_32x32x16_bf16 v[224:239], a[24:27], v[64:67], v[224:239]\n"
"ds_read_b128 a[16:19], v3 offset:12288\n"
"v_mov_b32 v10, v6\n"
"v_subrev_u32 v7, 64, v7\n"
"s_nop 0\n"
"v_permlane32_swap_b32 v10, v6\n"
"s_waitcnt lgkmcnt(5)\n"
"v_mfma_f32_32x32x16_bf16 v[240:255], a[28:31], v[64:67], v[240:255]\n"
"ds_read_b128 a[20:23], v3 offset:13312\n"
"v_max_f32 v6, v10, v6\n"
"v_cmp_lt_f32 vcc, 0x41c00000, v6\n"
"MSTEPJ%=:\n"
"s_waitcnt lgkmcnt(5)\n"
"v_mfma_f32_32x32x16_bf16 v[128:143], a[0:3], v[68:71], v[128:143]\n"
"s_cbranch_vccnz MSTEPR%=\n"
"MSTEPRB%=:\n"
"ds_read_b128 a[24:27], v3 offset:14336\n"
"v_exp_f32 v32, v32\n"
"s_waitcnt lgkmcnt(5)\n"
"v_mfma_f32_32x32x16_bf16 v[144:159], a[4:7], v[68:71], v[144:159]\n"
"ds_read_b128 a[28:31], v3 offset:15360\n"
"v_exp_f32 v33, v33\n"
"v_add_f32 v5, v5, v32\n"
"s_waitcnt lgkmcnt(5)\n"
"v_mfma_f32_32x32x16_bf16 v[160:175], a[8:11], v[68:71], v[160:175]\n"
"ds_read_b128 a[0:3], v3 offset:16384\n"
"v_exp_f32 v34, v34\n"
"v_add_f32 v5, v5, v33\n"
"v_cvt_pk_bf16_f32 v64, v32, v33\n"
"s_waitcnt lgkmcnt(5)\n"
"v_mfma_f32_32x32x16_bf16 v[176:191], a[12:15], v[68:71], v[176:191]\n"
"ds_read_b128 a[4:7], v3 offset:17408\n"
"v_exp_f32 v35, v35\n"
"v_add_f32 v5, v5, v34\n"
"s_waitcnt lgkmcnt(5)\n"
"v_mfma_f32_32x32x16_bf16 v[192:207], a[16:19], v[68:71], v[192:207]\n"
"ds_read_b128 a[8:11], v3 offset:18432\n"
"v_exp_f32 v36, v36\n"
"v_add_f32 v5, v5, v35\n"
"v_cvt_pk_bf16_f32 v65, v34, v35\n"
"s_waitcnt lgkmcnt(5)\n"
"v_mfma_f32_32x32x16_bf16 v[208:223], a[20:23], v[68:71], v[208:223]\n"
"ds_read_b128 a[12:15], v3 offset:19456\n"
"v_exp_f32 v37, v37\n"
"v_add_f32 v5, v5, v36\n"
"s_waitcnt lgkmcnt(5)\n"
"v_mfma_f32_32x32x16_bf16 v[224:239], a[24:27], v[68:71], v[224:239]\n"
"ds_read_b128 a[16:19], v3 offset:20480\n"
"v_exp_f32 v38, v38\n"
"v_add_f32 v5, v5, v37\n"
"v_cvt_pk_bf16_f32 v66, v36, v37\n"
"s_waitcnt lgkmcnt(5)\n"
"v_mfma_f32_32x32x16_bf16 v[240:255], a[28:31], v[68:71], v[240:255]\n"
"ds_read_b128 a[20:23], v3 offset:21504\n"
"v_exp_f32 v39, v39\n"
"v_add_f32 v5, v5, v38\n"
"s_waitcnt lgkmcnt(5)\n"
"v_mfma_f32_32x32x16_bf16 v[128:143], a[0:3], v[72:75], v[128:143]\n"
"ds_read_b128 a[24:27], v3 offset:22528\n"
"v_exp_f32 v40, v40\n"
"v_add_f32 v5, v5, v39\n"
"v_cvt_pk_bf16_f32 v67, v38, v39\n"
"s_waitcnt lgkmcnt(5)\n"
"v_mfma_f32_32x32x16_bf16 v[144:159], a[4:7], v[72:75], v[144:159]\n"
"ds_read_b128 a[28:31], v3 offset:23552\n"
"v_exp_f32 v41, v41\n"
"v_add_f32 v5, v5, v40\n"
"s_waitcnt lgkmcnt(5)\n"
"v_mfma_f32_32x32x16_bf16 v[160:175], a[8:11], v[72:75], v[160:175]\n"
"ds_read_b128 a[0:3], v3 offset:24576\n"
"v_exp_f32 v42, v42\n"
"v_add_f32 v5, v5, v41\n"
"v_cvt_pk_bf16_f32 v68, v40, v41\n"
"s_waitcnt lgkmcnt(5)\n"
"v_mfma_f32_32x32x16_bf16 v[176:191], a[12:15], v[72:75], v[176:191]\n"
"ds_read_b128 a[4:7], v3 offset:25600\n"
"v_exp_f32 v43, v43\n"
"v_add_f32 v5, v5, v42\n"
"s_waitcnt lgkmcnt(5)\n"
"v_mfma_f32_32x32x16_bf16 v[192:207], a[16:19], v[72:75], v[192:207]\n"
"ds_read_b128 a[8:11], v3 offset:26624\n"
"v_exp_f32 v44, v44\n"
"v_add_f32 v5, v5, v43\n"
"v_cvt_pk_bf16_f32 v69, v42, v43\n"
"s_waitcnt lgkmcnt(5)\n"
"v_mfma_f32_32x32x16_bf16 v[208:223], a[20:23], v[72:75], v[208:223]\n"
"ds_read_b128 a[12:15], v3 offset:27648\n"
"v_exp_f32 v45, v45\n"
"v_add_f32 v5, v5, v44\n"
"s_waitcnt lgkmcnt(5)\n"
"v_mfma_f32_32x32x16_bf16 v[224:239], a[24:27], v[72:75], v[224:239]\n"
"ds_read_b128 a[16:19], v3 offset:28672\n"
"v_exp_f32 v46, v46\n"
"v_add_f32 v5, v5, v45\n"
"v_cvt_pk_bf16_f32 v70, v44, v45\n"
"s_waitcnt lgkmcnt(5)\n"
"v_mfma_f32_32x32x16_bf16 v[240:255], a[28:31], v[72:75], v[240:255]\n"
"ds_read_b128 a[20:23], v3 offset:29696\n"
"v_exp_f32 v47, v47\n"
"v_add_f32 v5, v5, v46\n"
"s_waitcnt lgkmcnt(5)\n"
"v_mfma_f32_32x32x16_bf16 v[128:143], a[0:3], v[76:79], v[128:143]\n"
"ds_read_b128 a[24:27], v3 offset:30720\n"
"v_exp_f32 v48, v48\n"
"v_add_f32 v5, v5, v47\n"
"v_cvt_pk_bf16_f32 v71, v46, v47\n"
"s_waitcnt lgkmcnt(5)\n"
"v_mfma_f32_32x32x16_bf16 v[144:159], a[4:7], v[76:79], v[144:159]\n"
"ds_read_b128 a[28:31], v3 offset:31744\n"
"v_exp_f32 v49, v49\n"
"v_add_f32 v5, v5, v48\n"
"s_waitcnt lgkmcnt(5)\n"
"v_mfma_f32_32x32x16_bf16 v[160:175], a[8:11], v[76:79], v[160:175]\n"
"ds_read_b128 a[0:3], v2\n"
"v_exp_f32 v50, v50\n"
"v_add_f32 v5, v5, v49\n"
"v_cvt_pk_bf16_f32 v72, v48, v49\n"
"s_waitcnt lgkmcnt(5)\n"
"v_mfma_f32_32x32x16_bf16 v[176:191], a[12:15], v[76:79], v[176:191]\n"
"ds_read_b128 a[4:7], v2 offset:1024\n"
"v_exp_f32 v51, v51\n"
"v_add_f32 v5, v5, v50\n"
"v_xor_b32 v3, 0x10000, v3\n"
"s_waitcnt lgkmcnt(5)\n"
"v_mfma_f32_32x32x16_bf16 v[192:207], a[16:19], v[76:79], v[192:207]\n"
"ds_read_b128 a[8:11], v2 offset:2048\n"
"v_exp_f32 v52, v52\n"
"v_add_f32 v5, v5, v51\n"
"v_cvt_pk_bf16_f32 v73, v50, v51\n"
"s_waitcnt lgkmcnt(5)\n"
"v_mfma_f32_32x32x16_bf16 v[208:223], a[20:23], v[76:79], v[208:223]\n"
"ds_read_b128 a[12:15], v2 offset:3072\n"
"v_exp_f32 v53, v53\n"
"v_add_f32 v5, v5, v52\n"
"s_waitcnt lgkmcnt(5)\n"
"v_mfma_f32_32x32x16_bf16 v[224:239], a[24:27], v[76:79], v[224:239]\n"
"ds_read_b128 a[16:19], v2 offset:4096\n"
"v_exp_f32 v54, v54\n"
"v_add_f32 v5, v5, v53\n"
"v_cvt_pk_bf16_f32 v74, v52, v53\n"
"s_waitcnt lgkmcnt(5)\n"
"v_mfma_f32_32x32x16_bf16 v[240:255], a[28:31], v[76:79], v[240:255]\n"
"ds_read_b128 a[20:23], v2 offset:5120\n"
"v_exp_f32 v55, v55\n"
"v_add_f32 v5, v5, v54\n"
"s_cmp_lg_u32 s72, 0\n"
"s_cbranch_scc1 MSTEPS%=\n"
"MSTEPSB%=:\n"
"s_cmp_lt_u32 s70, %[n]\n"
"s_waitcnt vmcnt(0)\n"
"s_barrier\n"
"s_cbranch_scc1 MSTEP%=\n"
"DRAIN%=:\n"
"s_cmp_eq_u32 %[mode], 2\n"
"s_cbranch_scc1 COMB%=\n"
"s_waitcnt lgkmcnt(5)\n"
"ds_read_b128 a[0:3], v3\n"
"s_waitcnt lgkmcnt(5)\n"
"ds_read_b128 a[4:7], v3 offset:1024\n"
"s_waitcnt lgkmcnt(5)\n"
"ds_read_b128 a[8:11], v3 offset:2048\n"
"s_waitcnt lgkmcnt(5)\n"
"ds_read_b128 a[12:15], v3 offset:3072\n"
"s_waitcnt lgkmcnt(5)\n"
"ds_read_b128 a[16:19], v3 offset:4096\n"
"s_waitcnt lgkmcnt(5)\n"
"ds_read_b128 a[20:23], v3 offset:5120\n"
"v_exp_f32 v56, v56\n"
"v_add_f32 v5, v5, v55\n"
"v_cvt_pk_bf16_f32 v75, v54, v55\n"
"v_exp_f32 v57, v57\n"
"v_add_f32 v5, v5, v56\n"
"v_exp_f32 v58, v58\n"
"v_add_f32 v5, v5, v57\n"
"v_cvt_pk_bf16_f32 v76, v56, v57\n"
"v_exp_f32 v59, v59\n"
"v_add_f32 v5, v5, v58\n"
"v_exp_f32 v60, v60\n"
"v_add_f32 v5, v5, v59\n"
"v_cvt_pk_bf16_f32 v77, v58, v59\n"
"v_exp_f32 v61, v61\n"
"v_add_f32 v5, v5, v60\n"
"v_exp_f32 v62, v62\n"
"v_add_f32 v5, v5, v61\n"
"v_cvt_pk_bf16_f32 v78, v60, v61\n"
"v_exp_f32 v63, v63\n"
"v_add_f32 v5, v5, v62\n"
"s_waitcnt lgkmcnt(5)\n"
"v_mfma_f32_32x32x16_bf16 v[128:143], a[0:3], v[64:67], v[128:143]\n"
"ds_read_b128 a[24:27], v3 offset:6144\n"
"v_add_f32 v5, v5, v63\n"
"v_cvt_pk_bf16_f32 v79, v62, v63\n"
"s_waitcnt lgkmcnt(5)\n"
"v_mfma_f32_32x32x16_bf16 v[144:159], a[4:7], v[64:67], v[144:159]\n"
"ds_read_b128 a[28:31], v3 offset:7168\n"
"s_waitcnt lgkmcnt(5)\n"
"v_mfma_f32_32x32x16_bf16 v[160:175], a[8:11], v[64:67], v[160:175]\n"
"ds_read_b128 a[0:3], v3 offset:8192\n"
"v_mov_b32 v10, v5\n"
"s_waitcnt lgkmcnt(5)\n"
"v_mfma_f32_32x32x16_bf16 v[176:191], a[12:15], v[64:67], v[176:191]\n"
"ds_read_b128 a[4:7], v3 offset:9216\n"
"v_permlane32_swap_b32 v10, v5\n"
"s_waitcnt lgkmcnt(5)\n"
"v_mfma_f32_32x32x16_bf16 v[192:207], a[16:19], v[64:67], v[192:207]\n"
"ds_read_b128 a[8:11], v3 offset:10240\n"
"v_add_f32 v5, v10, v5\n"
"s_waitcnt lgkmcnt(5)\n"
"v_mfma_f32_32x32x16_bf16 v[208:223], a[20:23], v[64:67], v[208:223]\n"
"ds_read_b128 a[12:15], v3 offset:11264\n"
"v_rcp_f32 v9, v5\n"
"s_waitcnt lgkmcnt(5)\n"
"v_mfma_f32_32x32x16_bf16 v[224:239], a[24:27], v[64:67], v[224:239]\n"
"ds_read_b128 a[16:19], v3 offset:12288\n"
"s_waitcnt lgkmcnt(5)\n"
"v_mfma_f32_32x32x16_bf16 v[240:255], a[28:31], v[64:67], v[240:255]\n"
"ds_read_b128 a[20:23], v3 offset:13312\n"
"s_waitcnt lgkmcnt(5)\n"
"v_mfma_f32_32x32x16_bf16 v[128:143], a[0:3], v[68:71], v[128:143]\n"
"ds_read_b128 a[24:27], v3 offset:14336\n"
"s_waitcnt lgkmcnt(5)\n"
"v_mfma_f32_32x32x16_bf16 v[144:159], a[4:7], v[68:71], v[144:159]\n"
"ds_read_b128 a[28:31], v3 offset:15360\n"
"s_waitcnt lgkmcnt(5)\n"
"v_mfma_f32_32x32x16_bf16 v[160:175], a[8:11], v[68:71], v[160:175]\n"
"ds_read_b128 a[0:3], v3 offset:16384\n"
"s_waitcnt lgkmcnt(5)\n"
"v_mfma_f32_32x32x16_bf16 v[176:191], a[12:15], v[68:71], v[176:191]\n"
"ds_read_b128 a[4:7], v3 offset:17408\n"
"s_waitcnt lgkmcnt(5)\n"
"v_mfma_f32_32x32x16_bf16 v[192:207], a[16:19], v[68:71], v[192:207]\n"
"ds_read_b128 a[8:11], v3 offset:18432\n"
"s_waitcnt lgkmcnt(5)\n"
"v_mfma_f32_32x32x16_bf16 v[208:223], a[20:23], v[68:71], v[208:223]\n"
"ds_read_b128 a[12:15], v3 offset:19456\n"
"s_waitcnt lgkmcnt(5)\n"
"v_mfma_f32_32x32x16_bf16 v[224:239], a[24:27], v[68:71], v[224:239]\n"
"ds_read_b128 a[16:19], v3 offset:20480\n"
"s_waitcnt lgkmcnt(5)\n"
"v_mfma_f32_32x32x16_bf16 v[240:255], a[28:31], v[68:71], v[240:255]\n"
"ds_read_b128 a[20:23], v3 offset:21504\n"
"s_waitcnt lgkmcnt(5)\n"
"v_mfma_f32_32x32x16_bf16 v[128:143], a[0:3], v[72:75], v[128:143]\n"
"ds_read_b128 a[24:27], v3 offset:22528\n"
"s_waitcnt lgkmcnt(5)\n"
"v_mfma_f32_32x32x16_bf16 v[144:159], a[4:7], v[72:75], v[144:159]\n"
"ds_read_b128 a[28:31], v3 offset:23552\n"
"s_waitcnt lgkmcnt(5)\n"
"v_mfma_f32_32x32x16_bf16 v[160:175], a[8:11], v[72:75], v[160:175]\n"
"ds_read_b128 a[0:3], v3 offset:24576\n"
"s_waitcnt lgkmcnt(5)\n"
"v_mfma_f32_32x32x16_bf16 v[176:191], a[12:15], v[72:75], v[176:191]\n"
"ds_read_b128 a[4:7], v3 offset:25600\n"
"s_waitcnt lgkmcnt(5)\n"
"v_mfma_f32_32x32x16_bf16 v[192:207], a[16:19], v[72:75], v[192:207]\n"
"ds_read_b128 a[8:11], v3 offset:26624\n"
"s_waitcnt lgkmcnt(5)\n"
"v_mfma_f32_32x32x16_bf16 v[208:223], a[20:23], v[72:75], v[208:223]\n"
"ds_read_b128 a[12:15], v3 offset:27648\n"
"s_waitcnt lgkmcnt(5)\n"
"v_mfma_f32_32x32x16_bf16 v[224:239], a[24:27], v[72:75], v[224:239]\n"
"ds_read_b128 a[16:19], v3 offset:28672\n"
"s_waitcnt lgkmcnt(5)\n"
"v_mfma_f32_32x32x16_bf16 v[240:255], a[28:31], v[72:75], v[240:255]\n"
"ds_read_b128 a[20:23], v3 offset:29696\n"
"s_waitcnt lgkmcnt(5)\n"
"v_mfma_f32_32x32x16_bf16 v[128:143], a[0:3], v[76:79], v[128:143]\n"
"ds_read_b128 a[24:27], v3 offset:30720\n"
"s_waitcnt lgkmcnt(5)\n"
"v_mfma_f32_32x32x16_bf16 v[144:159], a[4:7], v[76:79], v[144:159]\n"
"ds_read_b128 a[28:31], v3 offset:31744\n"
"s_waitcnt lgkmcnt(5)\n"
"v_mfma_f32_32x32x16_bf16 v[160:175], a[8:11], v[76:79], v[160:175]\n"
"s_waitcnt lgkmcnt(4)\n"
"v_mfma_f32_32x32x16_bf16 v[176:191], a[12:15], v[76:79], v[176:191]\n"
"s_waitcnt lgkmcnt(3)\n"
"v_mfma_f32_32x32x16_bf16 v[192:207], a[16:19], v[76:79], v[192:207]\n"
"s_waitcnt lgkmcnt(2)\n"
"v_mfma_f32_32x32x16_bf16 v[208:223], a[20:23], v[76:79], v[208:223]\n"
"s_waitcnt lgkmcnt(1)\n"
"v_mfma_f32_32x32x16_bf16 v[224:239], a[24:27], v[76:79], v[224:239]\n"
"s_waitcnt lgkmcnt(0)\n"
"v_mfma_f32_32x32x16_bf16 v[240:255], a[28:31], v[76:79], v[240:255]\n"
"s_cmp_eq_u32 %[mode], 1\n"
"s_cbranch_scc1 PUB%=\n"
"v_mov_b32 v8, v9\n"
"s_nop 1\n"
"v_pk_mul_f32 v[128:129], v[128:129], v[8:9] op_sel_hi:[1,0]\n"
"v_pk_mul_f32 v[130:131], v[130:131], v[8:9] op_sel_hi:[1,0]\n"
"v_pk_mul_f32 v[132:133], v[132:133], v[8:9] op_sel_hi:[1,0]\n"
"v_pk_mul_f32 v[134:135], v[134:135], v[8:9] op_sel_hi:[1,0]\n"
"v_cvt_pk_bf16_f32 v16, v128, v129\n"
"v_cvt_pk_bf16_f32 v17, v130, v131\n"
"v_cvt_pk_bf16_f32 v18, v132, v133\n"
"v_cvt_pk_bf16_f32 v19, v134, v135\n"
"s_add_u32 s71, %[coff], 0\n"
"buffer_store_dwordx4 v[16:19], v1, %[rsW], s71 offen sc1\n"
"v_pk_mul_f32 v[136:137], v[136:137], v[8:9] op_sel_hi:[1,0]\n"
"v_pk_mul_f32 v[138:139], v[138:139], v[8:9] op_sel_hi:[1,0]\n"
"v_pk_mul_f32 v[140:141], v[140:141], v[8:9] op_sel_hi:[1,0]\n"
"v_pk_mul_f32 v[142:143], v[142:143], v[8:9] op_sel_hi:[1,0]\n"
"v_cvt_pk_bf16_f32 v20, v136, v137\n"
"v_cvt_pk_bf16_f32 v21, v138, v139\n"
"v_cvt_pk_bf16_f32 v22, v140, v141\n"
"v_cvt_pk_bf16_f32 v23, v142, v143\n"
"buffer_store_dwordx4 v[20:23], v1, %[rsW], s71 offen offset:1024 sc1\n"
"v_pk_mul_f32 v[144:145], v[144:145], v[8:9] op_sel_hi:[1,0]\n"
"v_pk_mul_f32 v[146:147], v[146:147], v[8:9] op_sel_hi:[1,0]\n"
"v_pk_mul_f32 v[148:149], v[148:149], v[8:9] op_sel_hi:[1,0]\n"
"v_pk_mul_f32 v[150:151], v[150:151], v[8:9] op_sel_hi:[1,0]\n"
"v_cvt_pk_bf16_f32 v24, v144, v145\n"
"v_cvt_pk_bf16_f32 v25, v146, v147\n"
"v_cvt_pk_bf16_f32 v26, v148, v149\n"
"v_cvt_pk_bf16_f32 v27, v150, v151\n"
"buffer_store_dwordx4 v[24:27], v1, %[rsW], s71 offen offset:2048 sc1\n"
"v_pk_mul_f32 v[152:153], v[152:153], v[8:9] op_sel_hi:[1,0]\n"
"v_pk_mul_f32 v[154:155], v[154:155], v[8:9] op_sel_hi:[1,0]\n"
"v_pk_mul_f32 v[156:157], v[156:157], v[8:9] op_sel_hi:[1,0]\n"
"v_pk_mul_f32 v[158:159], v[158:159], v[8:9] op_sel_hi:[1,0]\n"
"v_cvt_pk_bf16_f32 v28, v152, v153\n"
"v_cvt_pk_bf16_f32 v29, v154, v155\n"
"v_cvt_pk_bf16_f32 v30, v156, v157\n"
"v_cvt_pk_bf16_f32 v31, v158, v159\n"
"buffer_store_dwordx4 v[28:31], v1, %[rsW], s71 offen offset:3072 sc1\n"
"v_pk_mul_f32 v[160:161], v[160:161], v[8:9] op_sel_hi:[1,0]\n"
"v_pk_mul_f32 v[162:163], v[162:163], v[8:9] op_sel_hi:[1,0]\n"
"v_pk_mul_f32 v[164:165], v[164:165], v[8:9] op_sel_hi:[1,0]\n"
"v_pk_mul_f32 v[166:167], v[166:167], v[8:9] op_sel_hi:[1,0]\n"
"v_cvt_pk_bf16_f32 v32, v160, v161\n"
"v_cvt_pk_bf16_f32 v33, v162, v163\n"
"v_cvt_pk_bf16_f32 v34, v164, v165\n"
"v_cvt_pk_bf16_f32 v35, v166, v167\n"
"s_add_u32 s71, %[coff], 0x1000\n"
"buffer_store_dwordx4 v[32:35], v1, %[rsW], s71 offen sc1\n"
"v_pk_mul_f32 v[168:169], v[168:169], v[8:9] op_sel_hi:[1,0]\n"
"v_pk_mul_f32 v[170:171], v[170:171], v[8:9] op_sel_hi:[1,0]\n"
"v_pk_mul_f32 v[172:173], v[172:173], v[8:9] op_sel_hi:[1,0]\n"
"v_pk_mul_f32 v[174:175], v[174:175], v[8:9] op_sel_hi:[1,0]\n"
"v_cvt_pk_bf16_f32 v36, v168, v169\n"
"v_cvt_pk_bf16_f32 v37, v170, v171\n"
"v_cvt_pk_bf16_f32 v38, v172, v173\n"
"v_cvt_pk_bf16_f32 v39, v174, v175\n"
"buffer_store_dwordx4 v[36:39], v1, %[rsW], s71 offen offset:1024 sc1\n"
"v_pk_mul_f32 v[176:177], v[176:177], v[8:9] op_sel_hi:[1,0]\n"
"v_pk_mul_f32 v[178:179], v[178:179], v[8:9] op_sel_hi:[1,0]\n"
"v_pk_mul_f32 v[180:181], v[180:181], v[8:9] op_sel_hi:[1,0]\n"
"v_pk_mul_f32 v[182:183], v[182:183], v[8:9] op_sel_hi:[1,0]\n"
"v_cvt_pk_bf16_f32 v40, v176, v177\n"
"v_cvt_pk_bf16_f32 v41, v178, v179\n"
"v_cvt_pk_bf16_f32 v42, v180, v181\n"
"v_cvt_pk_bf16_f32 v43, v182, v183\n"
"buffer_store_dwordx4 v[40:43], v1, %[rsW], s71 offen offset:2048 sc1\n"
"v_pk_mul_f32 v[184:185], v[184:185], v[8:9] op_sel_hi:[1,0]\n"
"v_pk_mul_f32 v[186:187], v[186:187], v[8:9] op_sel_hi:[1,0]\n"
"v_pk_mul_f32 v[188:189], v[188:189], v[8:9] op_sel_hi:[1,0]\n"
"v_pk_mul_f32 v[190:191], v[190:191], v[8:9] op_sel_hi:[1,0]\n"
"v_cvt_pk_bf16_f32 v44, v184, v185\n"
"v_cvt_pk_bf16_f32 v45, v186, v187\n"
"v_cvt_pk_bf16_f32 v46, v188, v189\n"
"v_cvt_pk_bf16_f32 v47, v190, v191\n"
"buffer_store_dwordx4 v[44:47], v1, %[rsW], s71 offen offset:3072 sc1\n"
"v_pk_mul_f32 v[192:193], v[192:193], v[8:9] op_sel_hi:[1,0]\n"
"v_pk_mul_f32 v[194:195], v[194:195], v[8:9] op_sel_hi:[1,0]\n"
"v_pk_mul_f32 v[196:197], v[196:197], v[8:9] op_sel_hi:[1,0]\n"
"v_pk_mul_f32 v[198:199], v[198:199], v[8:9] op_sel_hi:[1,0]\n"
"v_cvt_pk_bf16_f32 v48, v192, v193\n"
"v_cvt_pk_bf16_f32 v49, v194, v195\n"
"v_cvt_pk_bf16_f32 v50, v196, v197\n"
"v_cvt_pk_bf16_f32 v51, v198, v199\n"
"s_add_u32 s71, %[coff], 0x2000\n"
"buffer_store_dwordx4 v[48:51], v1, %[rsW], s71 offen sc1\n"
"v_pk_mul_f32 v[200:201], v[200:201], v[8:9] op_sel_hi:[1,0]\n"
"v_pk_mul_f32 v[202:203], v[202:203], v[8:9] op_sel_hi:[1,0]\n"
"v_pk_mul_f32 v[204:205], v[204:205], v[8:9] op_sel_hi:[1,0]\n"
"v_pk_mul_f32 v[206:207], v[206:207], v[8:9] op_sel_hi:[1,0]\n"
"v_cvt_pk_bf16_f32 v52, v200, v201\n"
"v_cvt_pk_bf16_f32 v53, v202, v203\n"
"v_cvt_pk_bf16_f32 v54, v204, v205\n"
"v_cvt_pk_bf16_f32 v55, v206, v207\n"
"buffer_store_dwordx4 v[52:55], v1, %[rsW], s71 offen offset:1024 sc1\n"
"v_pk_mul_f32 v[208:209], v[208:209], v[8:9] op_sel_hi:[1,0]\n"
"v_pk_mul_f32 v[210:211], v[210:211], v[8:9] op_sel_hi:[1,0]\n"
"v_pk_mul_f32 v[212:213], v[212:213], v[8:9] op_sel_hi:[1,0]\n"
"v_pk_mul_f32 v[214:215], v[214:215], v[8:9] op_sel_hi:[1,0]\n"
"v_cvt_pk_bf16_f32 v56, v208, v209\n"
"v_cvt_pk_bf16_f32 v57, v210, v211\n"
"v_cvt_pk_bf16_f32 v58, v212, v213\n"
"v_cvt_pk_bf16_f32 v59, v214, v215\n"
"buffer_store_dwordx4 v[56:59], v1, %[rsW], s71 offen offset:2048 sc1\n"
"v_pk_mul_f32 v[216:217], v[216:217], v[8:9] op_sel_hi:[1,0]\n"
"v_pk_mul_f32 v[218:219], v[218:219], v[8:9] op_sel_hi:[1,0]\n"
"v_pk_mul_f32 v[220:221], v[220:221], v[8:9] op_sel_hi:[1,0]\n"
"v_pk_mul_f32 v[222:223], v[222:223], v[8:9] op_sel_hi:[1,0]\n"
"v_cvt_pk_bf16_f32 v60, v216, v217\n"
"v_cvt_pk_bf16_f32 v61, v218, v219\n"
"v_cvt_pk_bf16_f32 v62, v220, v221\n"
"v_cvt_pk_bf16_f32 v63, v222, v223\n"
"buffer_store_dwordx4 v[60:63], v1, %[rsW], s71 offen offset:3072 sc1\n"
"v_pk_mul_f32 v[224:225], v[224:225], v[8:9] op_sel_hi:[1,0]\n"
"v_pk_mul_f32 v[226:227], v[226:227], v[8:9] op_sel_hi:[1,0]\n"
"v_pk_mul_f32 v[228:229], v[228:229], v[8:9] op_sel_hi:[1,0]\n"
"v_pk_mul_f32 v[230:231], v[230:231], v[8:9] op_sel_hi:[1,0]\n"
"v_cvt_pk_bf16_f32 v64, v224, v225\n"
"v_cvt_pk_bf16_f32 v65, v226, v227\n"
"v_cvt_pk_bf16_f32 v66, v228, v229\n"
"v_cvt_pk_bf16_f32 v67, v230, v231\n"
"s_add_u32 s71, %[coff], 0x3000\n"
"buffer_store_dwordx4 v[64:67], v1, %[rsW], s71 offen sc1\n"
"v_pk_mul_f32 v[232:233], v[232:233], v[8:9] op_sel_hi:[1,0]\n"
"v_pk_mul_f32 v[234:235], v[234:235], v[8:9] op_sel_hi:[1,0]\n"
"v_pk_mul_f32 v[236:237], v[236:237], v[8:9] op_sel_hi:[1,0]\n"
"v_pk_mul_f32 v[238:239], v[238:239], v[8:9] op_sel_hi:[1,0]\n"
"v_cvt_pk_bf16_f32 v68, v232, v233\n"
"v_cvt_pk_bf16_f32 v69, v234, v235\n"
"v_cvt_pk_bf16_f32 v70, v236, v237\n"
"v_cvt_pk_bf16_f32 v71, v238, v239\n"
"buffer_store_dwordx4 v[68:71], v1, %[rsW], s71 offen offset:1024 sc1\n"
"v_pk_mul_f32 v[240:241], v[240:241], v[8:9] op_sel_hi:[1,0]\n"
"v_pk_mul_f32 v[242:243], v[242:243], v[8:9] op_sel_hi:[1,0]\n"
"v_pk_mul_f32 v[244:245], v[244:245], v[8:9] op_sel_hi:[1,0]\n"
"v_pk_mul_f32 v[246:247], v[246:247], v[8:9] op_sel_hi:[1,0]\n"
"v_cvt_pk_bf16_f32 v72, v240, v241\n"
"v_cvt_pk_bf16_f32 v73, v242, v243\n"
"v_cvt_pk_bf16_f32 v74, v244, v245\n"
"v_cvt_pk_bf16_f32 v75, v246, v247\n"
"buffer_store_dwordx4 v[72:75], v1, %[rsW], s71 offen offset:2048 sc1\n"
"v_pk_mul_f32 v[248:249], v[248:249], v[8:9] op_sel_hi:[1,0]\n"
"v_pk_mul_f32 v[250:251], v[250:251], v[8:9] op_sel_hi:[1,0]\n"
"v_pk_mul_f32 v[252:253], v[252:253], v[8:9] op_sel_hi:[1,0]\n"
"v_pk_mul_f32 v[254:255], v[254:255], v[8:9] op_sel_hi:[1,0]\n"
"v_cvt_pk_bf16_f32 v76, v248, v249\n"
"v_cvt_pk_bf16_f32 v77, v250, v251\n"
"v_cvt_pk_bf16_f32 v78, v252, v253\n"
"v_cvt_pk_bf16_f32 v79, v254, v255\n"
"buffer_store_dwordx4 v[76:79], v1, %[rsW], s71 offen offset:3072 sc1\n"
"s_branch END%=\n"
"PUB%=:\n"
"s_waitcnt lgkmcnt(0)\n"
"s_barrier\n"
"s_sub_u32 s64, %[ks], 0x98000\n"
"s_sub_u32 s65, %[vs], 0x98000\n"
"v_mov_b32 v10, v4\n"
"v_mov_b32 v11, v5\n"
"s_add_u32 s71, %[poff], 0x8000\n"
"buffer_store_dwordx2 v[10:11], v1, %[rsW], s71 offen sc1\n"
"v_cvt_pk_bf16_f32 v16, v128, v129\n"
"v_cvt_pk_bf16_f32 v17, v130, v131\n"
"v_cvt_pk_bf16_f32 v18, v132, v133\n"
"v_cvt_pk_bf16_f32 v19, v134, v135\n"
"s_add_u32 s71, %[poff], 0\n"
"buffer_store_dwordx4 v[16:19], v1, %[rsW], s71 offen sc1\n"
"v_cvt_pk_bf16_f32 v20, v136, v137\n"
"v_cvt_pk_bf16_f32 v21, v138, v139\n"
"v_cvt_pk_bf16_f32 v22, v140, v141\n"
"v_cvt_pk_bf16_f32 v23, v142, v143\n"
"buffer_store_dwordx4 v[20:23], v1, %[rsW], s71 offen offset:1024 sc1\n"
"v_cvt_pk_bf16_f32 v24, v144, v145\n"
"v_cvt_pk_bf16_f32 v25, v146, v147\n"
"v_cvt_pk_bf16_f32 v26, v148, v149\n"
"v_cvt_pk_bf16_f32 v27, v150, v151\n"
"buffer_store_dwordx4 v[24:27], v1, %[rsW], s71 offen offset:2048 sc1\n"
"v_cvt_pk_bf16_f32 v28, v152, v153\n"
"v_cvt_pk_bf16_f32 v29, v154, v155\n"
"v_cvt_pk_bf16_f32 v30, v156, v157\n"
"v_cvt_pk_bf16_f32 v31, v158, v159\n"
"buffer_store_dwordx4 v[28:31], v1, %[rsW], s71 offen offset:3072 sc1\n"
"v_cvt_pk_bf16_f32 v32, v160, v161\n"
"v_cvt_pk_bf16_f32 v33, v162, v163\n"
"v_cvt_pk_bf16_f32 v34, v164, v165\n"
"v_cvt_pk_bf16_f32 v35, v166, v167\n"
"s_add_u32 s71, %[poff], 0x1000\n"
"buffer_store_dwordx4 v[32:35], v1, %[rsW], s71 offen sc1\n"
"v_cvt_pk_bf16_f32 v36, v168, v169\n"
"v_cvt_pk_bf16_f32 v37, v170, v171\n"
"v_cvt_pk_bf16_f32 v38, v172, v173\n"
"v_cvt_pk_bf16_f32 v39, v174, v175\n"
"buffer_store_dwordx4 v[36:39], v1, %[rsW], s71 offen offset:1024 sc1\n"
"v_cvt_pk_bf16_f32 v40, v176, v177\n"
"v_cvt_pk_bf16_f32 v41, v178, v179\n"
"v_cvt_pk_bf16_f32 v42, v180, v181\n"
"v_cvt_pk_bf16_f32 v43, v182, v183\n"
"buffer_store_dwordx4 v[40:43], v1, %[rsW], s71 offen offset:2048 sc1\n"
"v_cvt_pk_bf16_f32 v44, v184, v185\n"
"v_cvt_pk_bf16_f32 v45, v186, v187\n"
"v_cvt_pk_bf16_f32 v46, v188, v189\n"
"v_cvt_pk_bf16_f32 v47, v190, v191\n"
"buffer_store_dwordx4 v[44:47], v1, %[rsW], s71 offen offset:3072 sc1\n"
"v_cvt_pk_bf16_f32 v48, v192, v193\n"
"v_cvt_pk_bf16_f32 v49, v194, v195\n"
"v_cvt_pk_bf16_f32 v50, v196, v197\n"
"v_cvt_pk_bf16_f32 v51, v198, v199\n"
"s_add_u32 s71, %[poff], 0x2000\n"
"buffer_store_dwordx4 v[48:51], v1, %[rsW], s71 offen sc1\n"
"v_cvt_pk_bf16_f32 v52, v200, v201\n"
"v_cvt_pk_bf16_f32 v53, v202, v203\n"
"v_cvt_pk_bf16_f32 v54, v204, v205\n"
"v_cvt_pk_bf16_f32 v55, v206, v207\n"
"buffer_store_dwordx4 v[52:55], v1, %[rsW], s71 offen offset:1024 sc1\n"
"v_cvt_pk_bf16_f32 v56, v208, v209\n"
"v_cvt_pk_bf16_f32 v57, v210, v211\n"
"v_cvt_pk_bf16_f32 v58, v212, v213\n"
"v_cvt_pk_bf16_f32 v59, v214, v215\n"
"buffer_store_dwordx4 v[56:59], v1, %[rsW], s71 offen offset:2048 sc1\n"
"v_cvt_pk_bf16_f32 v60, v216, v217\n"
"v_cvt_pk_bf16_f32 v61, v218, v219\n"
"v_cvt_pk_bf16_f32 v62, v220, v221\n"
"v_cvt_pk_bf16_f32 v63, v222, v223\n"
"buffer_store_dwordx4 v[60:63], v1, %[rsW], s71 offen offset:3072 sc1\n"
"v_cvt_pk_bf16_f32 v64, v224, v225\n"
"v_cvt_pk_bf16_f32 v65, v226, v227\n"
"v_cvt_pk_bf16_f32 v66, v228, v229\n"
"v_cvt_pk_bf16_f32 v67, v230, v231\n"
"s_add_u32 s71, %[poff], 0x3000\n"
"buffer_store_dwordx4 v[64:67], v1, %[rsW], s71 offen sc1\n"
"v_cvt_pk_bf16_f32 v68, v232, v233\n"
"v_cvt_pk_bf16_f32 v69, v234, v235\n"
"v_cvt_pk_bf16_f32 v70, v236, v237\n"
"v_cvt_pk_bf16_f32 v71, v238, v239\n"
"buffer_store_dwordx4 v[68:71], v1, %[rsW], s71 offen offset:1024 sc1\n"
"v_cvt_pk_bf16_f32 v72, v240, v241\n"
"v_cvt_pk_bf16_f32 v73, v242, v243\n"
"v_cvt_pk_bf16_f32 v74, v244, v245\n"
"v_cvt_pk_bf16_f32 v75, v246, v247\n"
"buffer_store_dwordx4 v[72:75], v1, %[rsW], s71 offen offset:2048 sc1\n"
"v_cvt_pk_bf16_f32 v76, v248, v249\n"
"v_cvt_pk_bf16_f32 v77, v250, v251\n"
"v_cvt_pk_bf16_f32 v78, v252, v253\n"
"v_cvt_pk_bf16_f32 v79, v254, v255\n"
"buffer_store_dwordx4 v[76:79], v1, %[rsW], s71 offen offset:3072 sc1\n"
"s_add_u32 m0, s74, 0x20000\n"
"s_add_u32 s71, s64, 0\n"
"buffer_load_dwordx4 v1, %[rsW], s71 offen sc1 lds\n"
"buffer_load_dwordx4 v1, %[rsW], s71 offen offset:1024 sc1 lds\n"
"buffer_load_dwordx4 v1, %[rsW], s71 offen offset:2048 sc1 lds\n"
"buffer_load_dwordx4 v1, %[rsW], s71 offen offset:3072 sc1 lds\n"
"s_add_u32 m0, s74, 0x21000\n"
"s_add_u32 s71, s64, 0x1000\n"
"buffer_load_dwordx4 v1, %[rsW], s71 offen sc1 lds\n"
"buffer_load_dwordx4 v1, %[rsW], s71 offen offset:1024 sc1 lds\n"
"buffer_load_dwordx4 v1, %[rsW], s71 offen offset:2048 sc1 lds\n"
"buffer_load_dwordx4 v1, %[rsW], s71 offen offset:3072 sc1 lds\n"
"s_add_u32 m0, s74, 0\n"
"s_add_u32 s71, s64, 0x8000\n"
"buffer_load_dwordx4 v1, %[rsW], s71 offen sc1 lds\n"
"buffer_load_dwordx4 v1, %[rsW], s71 offen offset:1024 sc1 lds\n"
"buffer_load_dwordx4 v1, %[rsW], s71 offen offset:2048 sc1 lds\n"
"buffer_load_dwordx4 v1, %[rsW], s71 offen offset:3072 sc1 lds\n"
"s_add_u32 m0, s74, 0x1000\n"
"s_add_u32 s71, s64, 0x9000\n"
"buffer_load_dwordx4 v1, %[rsW], s71 offen sc1 lds\n"
"buffer_load_dwordx4 v1, %[rsW], s71 offen offset:1024 sc1 lds\n"
"buffer_load_dwordx4 v1, %[rsW], s71 offen offset:2048 sc1 lds\n"
"buffer_load_dwordx4 v1, %[rsW], s71 offen offset:3072 sc1 lds\n"
"s_add_u32 m0, s74, 0x8000\n"
"s_add_u32 s71, s65, 0\n"
"buffer_load_dwordx4 v1, %[rsW], s71 offen sc1 lds\n"
"buffer_load_dwordx4 v1, %[rsW], s71 offen offset:1024 sc1 lds\n"
"buffer_load_dwordx4 v1, %[rsW], s71 offen offset:2048 sc1 lds\n"
"buffer_load_dwordx4 v1, %[rsW], s71 offen offset:3072 sc1 lds\n"
"s_add_u32 m0, s74, 0x9000\n"
"s_add_u32 s71, s65, 0x1000\n"
"buffer_load_dwordx4 v1, %[rsW], s71 offen sc1 lds\n"
"buffer_load_dwordx4 v1, %[rsW], s71 offen offset:1024 sc1 lds\n"
"buffer_load_dwordx4 v1, %[rsW], s71 offen offset:2048 sc1 lds\n"
"buffer_load_dwordx4 v1, %[rsW], s71 offen offset:3072 sc1 lds\n"
"s_add_u32 m0, s74, 0x10000\n"
"s_add_u32 s71, s64, 0x10000\n"
"buffer_load_dwordx4 v1, %[rsW], s71 offen sc1 lds\n"
"buffer_load_dwordx4 v1, %[rsW], s71 offen offset:1024 sc1 lds\n"
"buffer_load_dwordx4 v1, %[rsW], s71 offen offset:2048 sc1 lds\n"
"buffer_load_dwordx4 v1, %[rsW], s71 offen offset:3072 sc1 lds\n"
"s_add_u32 m0, s74, 0x11000\n"
"s_add_u32 s71, s64, 0x11000\n"
"buffer_load_dwordx4 v1, %[rsW], s71 offen sc1 lds\n"
"buffer_load_dwordx4 v1, %[rsW], s71 offen offset:1024 sc1 lds\n"
"buffer_load_dwordx4 v1, %[rsW], s71 offen offset:2048 sc1 lds\n"
"buffer_load_dwordx4 v1, %[rsW], s71 offen offset:3072 sc1 lds\n"
"v_mov_b64 v[12:13], %[tag]\n"
"s_waitcnt vmcnt(32)\n"
"buffer_store_dwordx2 v[12:13], off, %[rsW], %[flag] sc1\n"
"s_branch FIN%=\n"
"COMB%=:\n"
"buffer_load_dwordx2 v[14:15], off, %[rsW], %[flag] sc1\n"
"s_waitcnt lgkmcnt(5)\n"
"ds_read_b128 a[0:3], v3\n"
"s_waitcnt lgkmcnt(5)\n"
"ds_read_b128 a[4:7], v3 offset:1024\n"
"s_waitcnt lgkmcnt(5)\n"
"ds_read_b128 a[8:11], v3 offset:2048\n"
"s_waitcnt lgkmcnt(5)\n"
"ds_read_b128 a[12:15], v3 offset:3072\n"
"s_waitcnt lgkmcnt(5)\n"
"ds_read_b128 a[16:19], v3 offset:4096\n"
"s_waitcnt lgkmcnt(5)\n"
"ds_read_b128 a[20:23], v3 offset:5120\n"
"v_exp_f32 v56, v56\n"
"v_add_f32 v5, v5, v55\n"
"v_cvt_pk_bf16_f32 v75, v54, v55\n"
"v_exp_f32 v57, v57\n"
"v_add_f32 v5, v5, v56\n"
"v_exp_f32 v58, v58\n"
"v_add_f32 v5, v5, v57\n"
"v_cvt_pk_bf16_f32 v76, v56, v57\n"
"v_exp_f32 v59, v59\n"
"v_add_f32 v5, v5, v58\n"
"v_exp_f32 v60, v60\n"
"v_add_f32 v5, v5, v59\n"
"v_cvt_pk_bf16_f32 v77, v58, v59\n"
"v_exp_f32 v61, v61\n"
"v_add_f32 v5, v5, v60\n"
"v_exp_f32 v62, v62\n"
"v_add_f32 v5, v5, v61\n"
"v_cvt_pk_bf16_f32 v78, v60, v61\n"
"v_exp_f32 v63, v63\n"
"v_add_f32 v5, v5, v62\n"
"s_waitcnt lgkmcnt(5)\n"
"v_mfma_f32_32x32x16_bf16 v[128:143], a[0:3], v[64:67], v[128:143]\n"
"ds_read_b128 a[24:27], v3 offset:6144\n"
"v_add_f32 v5, v5, v63\n"
"v_cvt_pk_bf16_f32 v79, v62, v63\n"
"s_waitcnt lgkmcnt(5)\n"
"v_mfma_f32_32x32x16_bf16 v[144:159], a[4:7], v[64:67], v[144:159]\n"
"ds_read_b128 a[28:31], v3 offset:7168\n"
"s_waitcnt lgkmcnt(5)\n"
"v_mfma_f32_32x32x16_bf16 v[160:175], a[8:11], v[64:67], v[160:175]\n"
"ds_read_b128 a[0:3], v3 offset:8192\n"
"v_mov_b32 v10, v5\n"
"s_waitcnt lgkmcnt(5)\n"
"v_mfma_f32_32x32x16_bf16 v[176:191], a[12:15], v[64:67], v[176:191]\n"
"ds_read_b128 a[4:7], v3 offset:9216\n"
"v_permlane32_swap_b32 v10, v5\n"
"s_waitcnt lgkmcnt(5)\n"
"v_mfma_f32_32x32x16_bf16 v[192:207], a[16:19], v[64:67], v[192:207]\n"
"ds_read_b128 a[8:11], v3 offset:10240\n"
"v_add_f32 v5, v10, v5\n"
"s_waitcnt lgkmcnt(5)\n"
"v_mfma_f32_32x32x16_bf16 v[208:223], a[20:23], v[64:67], v[208:223]\n"
"ds_read_b128 a[12:15], v3 offset:11264\n"
"v_rcp_f32 v9, v5\n"
"s_waitcnt lgkmcnt(5)\n"
"v_mfma_f32_32x32x16_bf16 v[224:239], a[24:27], v[64:67], v[224:239]\n"
"ds_read_b128 a[16:19], v3 offset:12288\n"
"s_waitcnt lgkmcnt(5)\n"
"v_mfma_f32_32x32x16_bf16 v[240:255], a[28:31], v[64:67], v[240:255]\n"
"ds_read_b128 a[20:23], v3 offset:13312\n"
"s_waitcnt lgkmcnt(5)\n"
"v_mfma_f32_32x32x16_bf16 v[128:143], a[0:3], v[68:71], v[128:143]\n"
"ds_read_b128 a[24:27], v3 offset:14336\n"
"s_waitcnt lgkmcnt(5)\n"
"v_mfma_f32_32x32x16_bf16 v[144:159], a[4:7], v[68:71], v[144:159]\n"
"ds_read_b128 a[28:31], v3 offset:15360\n"
"s_waitcnt lgkmcnt(5)\n"
"v_mfma_f32_32x32x16_bf16 v[160:175], a[8:11], v[68:71], v[160:175]\n"
"ds_read_b128 a[0:3], v3 offset:16384\n"
"s_waitcnt lgkmcnt(5)\n"
"v_mfma_f32_32x32x16_bf16 v[176:191], a[12:15], v[68:71], v[176:191]\n"
"ds_read_b128 a[4:7], v3 offset:17408\n"
"s_waitcnt lgkmcnt(5)\n"
"v_mfma_f32_32x32x16_bf16 v[192:207], a[16:19], v[68:71], v[192:207]\n"
"ds_read_b128 a[8:11], v3 offset:18432\n"
"s_waitcnt vmcnt(0)\n"
"v_cmp_ne_u64 vcc, %[tag], v[14:15]\n"
"s_add_u32 s71, %[poff], 0x8000\n"
"s_cbranch_vccnz PRW%=\n"
"PRG%=:\n"
"buffer_load_dwordx2 v[10:11], v1, %[rsW], s71 offen sc1\n"
"s_waitcnt lgkmcnt(5)\n"
"v_mfma_f32_32x32x16_bf16 v[208:223], a[20:23], v[68:71], v[208:223]\n"
"ds_read_b128 a[12:15], v3 offset:19456\n"
"s_add_u32 s71, %[poff], 0\n"
"buffer_load_dwordx4 v[80:83], v1, %[rsW], s71 offen sc1\n"
"s_waitcnt lgkmcnt(5)\n"
"v_mfma_f32_32x32x16_bf16 v[224:239], a[24:27], v[68:71], v[224:239]\n"
"ds_read_b128 a[16:19], v3 offset:20480\n"
"buffer_load_dwordx4 v[84:87], v1, %[rsW], s71 offen offset:1024 sc1\n"
"s_waitcnt lgkmcnt(5)\n"
"v_mfma_f32_32x32x16_bf16 v[240:255], a[28:31], v[68:71], v[240:255]\n"
"ds_read_b128 a[20:23], v3 offset:21504\n"
"buffer_load_dwordx4 v[88:91], v1, %[rsW], s71 offen offset:2048 sc1\n"
"s_waitcnt lgkmcnt(5)\n"
"v_mfma_f32_32x32x16_bf16 v[128:143], a[0:3], v[72:75], v[128:143]\n"
"ds_read_b128 a[24:27], v3 offset:22528\n"
"buffer_load_dwordx4 v[92:95], v1, %[rsW], s71 offen offset:3072 sc1\n"
"s_waitcnt lgkmcnt(5)\n"
"v_mfma_f32_32x32x16_bf16 v[144:159], a[4:7], v[72:75], v[144:159]\n"
"ds_read_b128 a[28:31], v3 offset:23552\n"
"s_add_u32 s71, %[poff], 0x1000\n"
"buffer_load_dwordx4 v[96:99], v1, %[rsW], s71 offen sc1\n"
"s_waitcnt lgkmcnt(5)\n"
"v_mfma_f32_32x32x16_bf16 v[160:175], a[8:11], v[72:75], v[160:175]\n"
"ds_read_b128 a[0:3], v3 offset:24576\n"
"buffer_load_dwordx4 v[100:103], v1, %[rsW], s71 offen offset:1024 sc1\n"
"s_waitcnt lgkmcnt(5)\n"
"v_mfma_f32_32x32x16_bf16 v[176:191], a[12:15], v[72:75], v[176:191]\n"
"ds_read_b128 a[4:7], v3 offset:25600\n"
"buffer_load_dwordx4 v[104:107], v1, %[rsW], s71 offen offset:2048 sc1\n"
"s_waitcnt lgkmcnt(5)\n"
"v_mfma_f32_32x32x16_bf16 v[192:207], a[16:19], v[72:75], v[192:207]\n"
"ds_read_b128 a[8:11], v3 offset:26624\n"
"buffer_load_dwordx4 v[108:111], v1, %[rsW], s71 offen offset:3072 sc1\n"
"s_waitcnt lgkmcnt(5)\n"
"v_mfma_f32_32x32x16_bf16 v[208:223], a[20:23], v[72:75], v[208:223]\n"
"ds_read_b128 a[12:15], v3 offset:27648\n"
"s_add_u32 s71, %[poff], 0x2000\n"
"buffer_load_dwordx4 v[112:115], v1, %[rsW], s71 offen sc1\n"
"s_waitcnt lgkmcnt(5)\n"
"v_mfma_f32_32x32x16_bf16 v[224:239], a[24:27], v[72:75], v[224:239]\n"
"ds_read_b128 a[16:19], v3 offset:28672\n"
"buffer_load_dwordx4 v[116:119], v1, %[rsW], s71 offen offset:1024 sc1\n"
"s_waitcnt lgkmcnt(5)\n"
"v_mfma_f32_32x32x16_bf16 v[240:255], a[28:31], v[72:75], v[240:255]\n"
"ds_read_b128 a[20:23], v3 offset:29696\n"
"buffer_load_dwordx4 v[120:123], v1, %[rsW], s71 offen offset:2048 sc1\n"
"s_waitcnt lgkmcnt(5)\n"
"v_mfma_f32_32x32x16_bf16 v[128:143], a[0:3], v[76:79], v[128:143]\n"
"ds_read_b128 a[24:27], v3 offset:30720\n"
"buffer_load_dwordx4 v[124:127], v1, %[rsW], s71 offen offset:3072 sc1\n"
"s_waitcnt lgkmcnt(5)\n"
"v_mfma_f32_32x32x16_bf16 v[144:159], a[4:7], v[76:79], v[144:159]\n"
"ds_read_b128 a[28:31], v3 offset:31744\n"
"s_add_u32 s71, %[poff], 0x3000\n"
"buffer_load_dwordx4 v[16:19], v1, %[rsW], s71 offen sc1\n"
"s_waitcnt lgkmcnt(5)\n"
"v_mfma_f32_32x32x16_bf16 v[160:175], a[8:11], v[76:79], v[160:175]\n"
"buffer_load_dwordx4 v[20:23], v1, %[rsW], s71 offen offset:1024 sc1\n"
"s_waitcnt lgkmcnt(4)\n"
"v_mfma_f32_32x32x16_bf16 v[176:191], a[12:15], v[76:79], v[176:191]\n"
"buffer_load_dwordx4 v[24:27], v1, %[rsW], s71 offen offset:2048 sc1\n"
"s_waitcnt lgkmcnt(3)\n"
"v_mfma_f32_32x32x16_bf16 v[192:207], a[16:19], v[76:79], v[192:207]\n"
"buffer_load_dwordx4 v[28:31], v1, %[rsW], s71 offen offset:3072 sc1\n"
"s_waitcnt lgkmcnt(2)\n"
"v_mfma_f32_32x32x16_bf16 v[208:223], a[20:23], v[76:79], v[208:223]\n"
"s_waitcnt lgkmcnt(1)\n"
"v_mfma_f32_32x32x16_bf16 v[224:239], a[24:27], v[76:79], v[224:239]\n"
"s_waitcnt lgkmcnt(0)\n"
"v_mfma_f32_32x32x16_bf16 v[240:255], a[28:31], v[76:79], v[240:255]\n"
"s_waitcnt vmcnt(16)\n"
"v_max_f32 v12, v4, v10\n"
"v_sub_f32 v8, v4, v12\n"
"v_sub_f32 v14, v10, v12\n"
"v_exp_f32 v8, v8\n"
"v_exp_f32 v14, v14\n"
"s_nop 0\n"
"v_mul_f32 v12, v5, v8\n"
"v_fma_f32 v12, v11, v14, v12\n"
"v_rcp_f32 v12, v12\n"
"s_nop 0\n"
"v_mul_f32 v8, v8, v12\n"
"v_mul_f32 v14, v14, v12\n"
"v_pk_mul_f32 v[128:129], v[128:129], v[8:9] op_sel_hi:[1,0]\n"
"v_pk_mul_f32 v[130:131], v[130:131], v[8:9] op_sel_hi:[1,0]\n"
"v_pk_mul_f32 v[132:133], v[132:133], v[8:9] op_sel_hi:[1,0]\n"
"v_pk_mul_f32 v[134:135], v[134:135], v[8:9] op_sel_hi:[1,0]\n"
"s_waitcnt vmcnt(15)\n"
"v_lshlrev_b32 v32, 16, v80\n"
"v_and_b32 v33, 0xffff0000, v80\n"
"v_lshlrev_b32 v34, 16, v81\n"
"v_and_b32 v35, 0xffff0000, v81\n"
"v_lshlrev_b32 v36, 16, v82\n"
"v_and_b32 v37, 0xffff0000, v82\n"
"v_lshlrev_b32 v38, 16, v83\n"
"v_and_b32 v39, 0xffff0000, v83\n"
"v_pk_fma_f32 v[128:129], v[32:33], v[14:15], v[128:129] op_sel_hi:[1,0,1]\n"
"v_pk_fma_f32 v[130:131], v[34:35], v[14:15], v[130:131] op_sel_hi:[1,0,1]\n"
"v_pk_fma_f32 v[132:133], v[36:37], v[14:15], v[132:133] op_sel_hi:[1,0,1]\n"
"v_pk_fma_f32 v[134:135], v[38:39], v[14:15], v[134:135] op_sel_hi:[1,0,1]\n"
"v_cvt_pk_bf16_f32 v80, v128, v129\n"
"v_cvt_pk_bf16_f32 v81, v130, v131\n"
"v_cvt_pk_bf16_f32 v82, v132, v133\n"
"v_cvt_pk_bf16_f32 v83, v134, v135\n"
"s_add_u32 s71, %[coff], 0\n"
"buffer_store_dwordx4 v[80:83], v1, %[rsW], s71 offen sc1\n"
"v_pk_mul_f32 v[136:137], v[136:137], v[8:9] op_sel_hi:[1,0]\n"
"v_pk_mul_f32 v[138:139], v[138:139], v[8:9] op_sel_hi:[1,0]\n"
"v_pk_mul_f32 v[140:141], v[140:141], v[8:9] op_sel_hi:[1,0]\n"
"v_pk_mul_f32 v[142:143], v[142:143], v[8:9] op_sel_hi:[1,0]\n"
"s_waitcnt vmcnt(15)\n"
"v_lshlrev_b32 v40, 16, v84\n"
"v_and_b32 v41, 0xffff0000, v84\n"
"v_lshlrev_b32 v42, 16, v85\n"
"v_and_b32 v43, 0xffff0000, v85\n"
"v_lshlrev_b32 v44, 16, v86\n"
"v_and_b32 v45, 0xffff0000, v86\n"
"v_lshlrev_b32 v46, 16, v87\n"
"v_and_b32 v47, 0xffff0000, v87\n"
"v_pk_fma_f32 v[136:137], v[40:41], v[14:15], v[136:137] op_sel_hi:[1,0,1]\n"
"v_pk_fma_f32 v[138:139], v[42:43], v[14:15], v[138:139] op_sel_hi:[1,0,1]\n"
"v_pk_fma_f32 v[140:141], v[44:45], v[14:15], v[140:141] op_sel_hi:[1,0,1]\n"
"v_pk_fma_f32 v[142:143], v[46:47], v[14:15], v[142:143] op_sel_hi:[1,0,1]\n"
"v_cvt_pk_bf16_f32 v84, v136, v137\n"
"v_cvt_pk_bf16_f32 v85, v138, v139\n"
"v_cvt_pk_bf16_f32 v86, v140, v141\n"
"v_cvt_pk_bf16_f32 v87, v142, v143\n"
"buffer_store_dwordx4 v[84:87], v1, %[rsW], s71 offen offset:1024 sc1\n"
"v_pk_mul_f32 v[144:145], v[144:145], v[8:9] op_sel_hi:[1,0]\n"
"v_pk_mul_f32 v[146:147], v[146:147], v[8:9] op_sel_hi:[1,0]\n"
"v_pk_mul_f32 v[148:149], v[148:149], v[8:9] op_sel_hi:[1,0]\n"
"v_pk_mul_f32 v[150:151], v[150:151], v[8:9] op_sel_hi:[1,0]\n"
"s_waitcnt vmcnt(15)\n"
"v_lshlrev_b32 v32, 16, v88\n"
"v_and_b32 v33, 0xffff0000, v88\n"
"v_lshlrev_b32 v34, 16, v89\n"
"v_and_b32 v35, 0xffff0000, v89\n"
"v_lshlrev_b32 v36, 16, v90\n"
"v_and_b32 v37, 0xffff0000, v90\n"
"v_lshlrev_b32 v38, 16, v91\n"
"v_and_b32 v39, 0xffff0000, v91\n"
"v_pk_fma_f32 v[144:145], v[32:33], v[14:15], v[144:145] op_sel_hi:[1,0,1]\n"
"v_pk_fma_f32 v[146:147], v[34:35], v[14:15], v[146:147] op_sel_hi:[1,0,1]\n"
"v_pk_fma_f32 v[148:149], v[36:37], v[14:15], v[148:149] op_sel_hi:[1,0,1]\n"
"v_pk_fma_f32 v[150:151], v[38:39], v[14:15], v[150:151] op_sel_hi:[1,0,1]\n"
"v_cvt_pk_bf16_f32 v88, v144, v145\n"
"v_cvt_pk_bf16_f32 v89, v146, v147\n"
"v_cvt_pk_bf16_f32 v90, v148, v149\n"
"v_cvt_pk_bf16_f32 v91, v150, v151\n"
"buffer_store_dwordx4 v[88:91], v1, %[rsW], s71 offen offset:2048 sc1\n"
"v_pk_mul_f32 v[152:153], v[152:153], v[8:9] op_sel_hi:[1,0]\n"
"v_pk_mul_f32 v[154:155], v[154:155], v[8:9] op_sel_hi:[1,0]\n"
"v_pk_mul_f32 v[156:157], v[156:157], v[8:9] op_sel_hi:[1,0]\n"
"v_pk_mul_f32 v[158:159], v[158:159], v[8:9] op_sel_hi:[1,0]\n"
"s_waitcnt vmcnt(15)\n"
"v_lshlrev_b32 v40, 16, v92\n"
"v_and_b32 v41, 0xffff0000, v92\n"
"v_lshlrev_b32 v42, 16, v93\n"
"v_and_b32 v43, 0xffff0000, v93\n"
"v_lshlrev_b32 v44, 16, v94\n"
"v_and_b32 v45, 0xffff0000, v94\n"
"v_lshlrev_b32 v46, 16, v95\n"
"v_and_b32 v47, 0xffff0000, v95\n"
"v_pk_fma_f32 v[152:153], v[40:41], v[14:15], v[152:153] op_sel_hi:[1,0,1]\n"
"v_pk_fma_f32 v[154:155], v[42:43], v[14:15], v[154:155] op_sel_hi:[1,0,1]\n"
"v_pk_fma_f32 v[156:157], v[44:45], v[14:15], v[156:157] op_sel_hi:[1,0,1]\n"
"v_pk_fma_f32 v[158:159], v[46:47], v[14:15], v[158:159] op_sel_hi:[1,0,1]\n"
"v_cvt_pk_bf16_f32 v92, v152, v153\n"
"v_cvt_pk_bf16_f32 v93, v154, v155\n"
"v_cvt_pk_bf16_f32 v94, v156, v157\n"
"v_cvt_pk_bf16_f32 v95, v158, v159\n"
"buffer_store_dwordx4 v[92:95], v1, %[rsW], s71 offen offset:3072 sc1\n"
"v_pk_mul_f32 v[160:161], v[160:161], v[8:9] op_sel_hi:[1,0]\n"
"v_pk_mul_f32 v[162:163], v[162:163], v[8:9] op_sel_hi:[1,0]\n"
"v_pk_mul_f32 v[164:165], v[164:165], v[8:9] op_sel_hi:[1,0]\n"
"v_pk_mul_f32 v[166:167], v[166:167], v[8:9] op_sel_hi:[1,0]\n"
"s_waitcnt vmcnt(15)\n"
"v_lshlrev_b32 v32, 16, v96\n"
"v_and_b32 v33, 0xffff0000, v96\n"
"v_lshlrev_b32 v34, 16, v97\n"
"v_and_b32 v35, 0xffff0000, v97\n"
"v_lshlrev_b32 v36, 16, v98\n"
"v_and_b32 v37, 0xffff0000, v98\n"
"v_lshlrev_b32 v38, 16, v99\n"
"v_and_b32 v39, 0xffff0000, v99\n"
"v_pk_fma_f32 v[160:161], v[32:33], v[14:15], v[160:161] op_sel_hi:[1,0,1]\n"
"v_pk_fma_f32 v[162:163], v[34:35], v[14:15], v[162:163] op_sel_hi:[1,0,1]\n"
"v_pk_fma_f32 v[164:165], v[36:37], v[14:15], v[164:165] op_sel_hi:[1,0,1]\n"
"v_pk_fma_f32 v[166:167], v[38:39], v[14:15], v[166:167] op_sel_hi:[1,0,1]\n"
"v_cvt_pk_bf16_f32 v96, v160, v161\n"
"v_cvt_pk_bf16_f32 v97, v162, v163\n"
"v_cvt_pk_bf16_f32 v98, v164, v165\n"
"v_cvt_pk_bf16_f32 v99, v166, v167\n"
"s_add_u32 s71, %[coff], 0x1000\n"
"buffer_store_dwordx4 v[96:99], v1, %[rsW], s71 offen sc1\n"
"v_pk_mul_f32 v[168:169], v[168:169], v[8:9] op_sel_hi:[1,0]\n"
"v_pk_mul_f32 v[170:171], v[170:171], v[8:9] op_sel_hi:[1,0]\n"
"v_pk_mul_f32 v[172:173], v[172:173], v[8:9] op_sel_hi:[1,0]\n"
"v_pk_mul_f32 v[174:175], v[174:175], v[8:9] op_sel_hi:[1,0]\n"
"s_waitcnt vmcnt(15)\n"
"v_lshlrev_b32 v40, 16, v100\n"
"v_and_b32 v41, 0xffff0000, v100\n"
"v_lshlrev_b32 v42, 16, v101\n"
"v_and_b32 v43, 0xffff0000, v101\n"
"v_lshlrev_b32 v44, 16, v102\n"
"v_and_b32 v45, 0xffff0000, v102\n"
"v_lshlrev_b32 v46, 16, v103\n"
"v_and_b32 v47, 0xffff0000, v103\n"
"v_pk_fma_f32 v[168:169], v[40:41], v[14:15], v[168:169] op_sel_hi:[1,0,1]\n"
"v_pk_fma_f32 v[170:171], v[42:43], v[14:15], v[170:171] op_sel_hi:[1,0,1]\n"
"v_pk_fma_f32 v[172:173], v[44:45], v[14:15], v[172:173] op_sel_hi:[1,0,1]\n"
"v_pk_fma_f32 v[174:175], v[46:47], v[14:15], v[174:175] op_sel_hi:[1,0,1]\n"
"v_cvt_pk_bf16_f32 v100, v168, v169\n"
"v_cvt_pk_bf16_f32 v101, v170, v171\n"
"v_cvt_pk_bf16_f32 v102, v172, v173\n"
"v_cvt_pk_bf16_f32 v103, v174, v175\n"
"buffer_store_dwordx4 v[100:103], v1, %[rsW], s71 offen offset:1024 sc1\n"
"v_pk_mul_f32 v[176:177], v[176:177], v[8:9] op_sel_hi:[1,0]\n"
"v_pk_mul_f32 v[178:179], v[178:179], v[8:9] op_sel_hi:[1,0]\n"
"v_pk_mul_f32 v[180:181], v[180:181], v[8:9] op_sel_hi:[1,0]\n"
"v_pk_mul_f32 v[182:183], v[182:183], v[8:9] op_sel_hi:[1,0]\n"
"s_waitcnt vmcnt(15)\n"
"v_lshlrev_b32 v32, 16, v104\n"
"v_and_b32 v33, 0xffff0000, v104\n"
"v_lshlrev_b32 v34, 16, v105\n"
"v_and_b32 v35, 0xffff0000, v105\n"
"v_lshlrev_b32 v36, 16, v106\n"
"v_and_b32 v37, 0xffff0000, v106\n"
"v_lshlrev_b32 v38, 16, v107\n"
"v_and_b32 v39, 0xffff0000, v107\n"
"v_pk_fma_f32 v[176:177], v[32:33], v[14:15], v[176:177] op_sel_hi:[1,0,1]\n"
"v_pk_fma_f32 v[178:179], v[34:35], v[14:15], v[178:179] op_sel_hi:[1,0,1]\n"
"v_pk_fma_f32 v[180:181], v[36:37], v[14:15], v[180:181] op_sel_hi:[1,0,1]\n"
"v_pk_fma_f32 v[182:183], v[38:39], v[14:15], v[182:183] op_sel_hi:[1,0,1]\n"
"v_cvt_pk_bf16_f32 v104, v176, v177\n"
"v_cvt_pk_bf16_f32 v105, v178, v179\n"
"v_cvt_pk_bf16_f32 v106, v180, v181\n"
"v_cvt_pk_bf16_f32 v107, v182, v183\n"
"buffer_store_dwordx4 v[104:107], v1, %[rsW], s71 offen offset:2048 sc1\n"
"v_pk_mul_f32 v[184:185], v[184:185], v[8:9] op_sel_hi:[1,0]\n"
"v_pk_mul_f32 v[186:187], v[186:187], v[8:9] op_sel_hi:[1,0]\n"
"v_pk_mul_f32 v[188:189], v[188:189], v[8:9] op_sel_hi:[1,0]\n"
"v_pk_mul_f32 v[190:191], v[190:191], v[8:9] op_sel_hi:[1,0]\n"
"s_waitcnt vmcnt(15)\n"
"v_lshlrev_b32 v40, 16, v108\n"
"v_and_b32 v41, 0xffff0000, v108\n"
"v_lshlrev_b32 v42, 16, v109\n"
"v_and_b32 v43, 0xffff0000, v109\n"
"v_lshlrev_b32 v44, 16, v110\n"
"v_and_b32 v45, 0xffff0000, v110\n"
"v_lshlrev_b32 v46, 16, v111\n"
"v_and_b32 v47, 0xffff0000, v111\n"
"v_pk_fma_f32 v[184:185], v[40:41], v[14:15], v[184:185] op_sel_hi:[1,0,1]\n"
"v_pk_fma_f32 v[186:187], v[42:43], v[14:15], v[186:187] op_sel_hi:[1,0,1]\n"
"v_pk_fma_f32 v[188:189], v[44:45], v[14:15], v[188:189] op_sel_hi:[1,0,1]\n"
"v_pk_fma_f32 v[190:191], v[46:47], v[14:15], v[190:191] op_sel_hi:[1,0,1]\n"
"v_cvt_pk_bf16_f32 v108, v184, v185\n"
"v_cvt_pk_bf16_f32 v109, v186, v187\n"
"v_cvt_pk_bf16_f32 v110, v188, v189\n"
"v_cvt_pk_bf16_f32 v111, v190, v191\n"
"buffer_store_dwordx4 v[108:111], v1, %[rsW], s71 offen offset:3072 sc1\n"
"v_pk_mul_f32 v[192:193], v[192:193], v[8:9] op_sel_hi:[1,0]\n"
"v_pk_mul_f32 v[194:195], v[194:195], v[8:9] op_sel_hi:[1,0]\n"
"v_pk_mul_f32 v[196:197], v[196:197], v[8:9] op_sel_hi:[1,0]\n"
"v_pk_mul_f32 v[198:199], v[198:199], v[8:9] op_sel_hi:[1,0]\n"
"s_waitcnt vmcnt(15)\n"
"v_lshlrev_b32 v32, 16, v112\n"
"v_and_b32 v33, 0xffff0000, v112\n"
"v_lshlrev_b32 v34, 16, v113\n"
"v_and_b32 v35, 0xffff0000, v113\n"
"v_lshlrev_b32 v36, 16, v114\n"
"v_and_b32 v37, 0xffff0000, v114\n"
"v_lshlrev_b32 v38, 16, v115\n"
"v_and_b32 v39, 0xffff0000, v115\n"
"v_pk_fma_f32 v[192:193], v[32:33], v[14:15], v[192:193] op_sel_hi:[1,0,1]\n"
"v_pk_fma_f32 v[194:195], v[34:35], v[14:15], v[194:195] op_sel_hi:[1,0,1]\n"
"v_pk_fma_f32 v[196:197], v[36:37], v[14:15], v[196:197] op_sel_hi:[1,0,1]\n"
"v_pk_fma_f32 v[198:199], v[38:39], v[14:15], v[198:199] op_sel_hi:[1,0,1]\n"
"v_cvt_pk_bf16_f32 v112, v192, v193\n"
"v_cvt_pk_bf16_f32 v113, v194, v195\n"
"v_cvt_pk_bf16_f32 v114, v196, v197\n"
"v_cvt_pk_bf16_f32 v115, v198, v199\n"
"s_add_u32 s71, %[coff], 0x2000\n"
"buffer_store_dwordx4 v[112:115], v1, %[rsW], s71 offen sc1\n"
"v_pk_mul_f32 v[200:201], v[200:201], v[8:9] op_sel_hi:[1,0]\n"
"v_pk_mul_f32 v[202:203], v[202:203], v[8:9] op_sel_hi:[1,0]\n"
"v_pk_mul_f32 v[204:205], v[204:205], v[8:9] op_sel_hi:[1,0]\n"
"v_pk_mul_f32 v[206:207], v[206:207], v[8:9] op_sel_hi:[1,0]\n"
"s_waitcnt vmcnt(15)\n"
"v_lshlrev_b32 v40, 16, v116\n"
"v_and_b32 v41, 0xffff0000, v116\n"
"v_lshlrev_b32 v42, 16, v117\n"
"v_and_b32 v43, 0xffff0000, v117\n"
"v_lshlrev_b32 v44, 16, v118\n"
"v_and_b32 v45, 0xffff0000, v118\n"
"v_lshlrev_b32 v46, 16, v119\n"
"v_and_b32 v47, 0xffff0000, v119\n"
"v_pk_fma_f32 v[200:201], v[40:41], v[14:15], v[200:201] op_sel_hi:[1,0,1]\n"
"v_pk_fma_f32 v[202:203], v[42:43], v[14:15], v[202:203] op_sel_hi:[1,0,1]\n"
"v_pk_fma_f32 v[204:205], v[44:45], v[14:15], v[204:205] op_sel_hi:[1,0,1]\n"
"v_pk_fma_f32 v[206:207], v[46:47], v[14:15], v[206:207] op_sel_hi:[1,0,1]\n"
"v_cvt_pk_bf16_f32 v116, v200, v201\n"
"v_cvt_pk_bf16_f32 v117, v202, v203\n"
"v_cvt_pk_bf16_f32 v118, v204, v205\n"
"v_cvt_pk_bf16_f32 v119, v206, v207\n"
"buffer_store_dwordx4 v[116:119], v1, %[rsW], s71 offen offset:1024 sc1\n"
"v_pk_mul_f32 v[208:209], v[208:209], v[8:9] op_sel_hi:[1,0]\n"
"v_pk_mul_f32 v[210:211], v[210:211], v[8:9] op_sel_hi:[1,0]\n"
"v_pk_mul_f32 v[212:213], v[212:213], v[8:9] op_sel_hi:[1,0]\n"
"v_pk_mul_f32 v[214:215], v[214:215], v[8:9] op_sel_hi:[1,0]\n"
"s_waitcnt vmcnt(15)\n"
"v_lshlrev_b32 v32, 16, v120\n"
"v_and_b32 v33, 0xffff0000, v120\n"
"v_lshlrev_b32 v34, 16, v121\n"
"v_and_b32 v35, 0xffff0000, v121\n"
"v_lshlrev_b32 v36, 16, v122\n"
"v_and_b32 v37, 0xffff0000, v122\n"
"v_lshlrev_b32 v38, 16, v123\n"
"v_and_b32 v39, 0xffff0000, v123\n"
"v_pk_fma_f32 v[208:209], v[32:33], v[14:15], v[208:209] op_sel_hi:[1,0,1]\n"
"v_pk_fma_f32 v[210:211], v[34:35], v[14:15], v[210:211] op_sel_hi:[1,0,1]\n"
"v_pk_fma_f32 v[212:213], v[36:37], v[14:15], v[212:213] op_sel_hi:[1,0,1]\n"
"v_pk_fma_f32 v[214:215], v[38:39], v[14:15], v[214:215] op_sel_hi:[1,0,1]\n"
"v_cvt_pk_bf16_f32 v120, v208, v209\n"
"v_cvt_pk_bf16_f32 v121, v210, v211\n"
"v_cvt_pk_bf16_f32 v122, v212, v213\n"
"v_cvt_pk_bf16_f32 v123, v214, v215\n"
"buffer_store_dwordx4 v[120:123], v1, %[rsW], s71 offen offset:2048 sc1\n"
"v_pk_mul_f32 v[216:217], v[216:217], v[8:9] op_sel_hi:[1,0]\n"
"v_pk_mul_f32 v[218:219], v[218:219], v[8:9] op_sel_hi:[1,0]\n"
"v_pk_mul_f32 v[220:221], v[220:221], v[8:9] op_sel_hi:[1,0]\n"
"v_pk_mul_f32 v[222:223], v[222:223], v[8:9] op_sel_hi:[1,0]\n"
"s_waitcnt vmcnt(15)\n"
"v_lshlrev_b32 v40, 16, v124\n"
"v_and_b32 v41, 0xffff0000, v124\n"
"v_lshlrev_b32 v42, 16, v125\n"
"v_and_b32 v43, 0xffff0000, v125\n"
"v_lshlrev_b32 v44, 16, v126\n"
"v_and_b32 v45, 0xffff0000, v126\n"
"v_lshlrev_b32 v46, 16, v127\n"
"v_and_b32 v47, 0xffff0000, v127\n"
"v_pk_fma_f32 v[216:217], v[40:41], v[14:15], v[216:217] op_sel_hi:[1,0,1]\n"
"v_pk_fma_f32 v[218:219], v[42:43], v[14:15], v[218:219] op_sel_hi:[1,0,1]\n"
"v_pk_fma_f32 v[220:221], v[44:45], v[14:15], v[220:221] op_sel_hi:[1,0,1]\n"
"v_pk_fma_f32 v[222:223], v[46:47], v[14:15], v[222:223] op_sel_hi:[1,0,1]\n"
"v_cvt_pk_bf16_f32 v124, v216, v217\n"
"v_cvt_pk_bf16_f32 v125, v218, v219\n"
"v_cvt_pk_bf16_f32 v126, v220, v221\n"
"v_cvt_pk_bf16_f32 v127, v222, v223\n"
"buffer_store_dwordx4 v[124:127], v1, %[rsW], s71 offen offset:3072 sc1\n"
"v_pk_mul_f32 v[224:225], v[224:225], v[8:9] op_sel_hi:[1,0]\n"
"v_pk_mul_f32 v[226:227], v[226:227], v[8:9] op_sel_hi:[1,0]\n"
"v_pk_mul_f32 v[228:229], v[228:229], v[8:9] op_sel_hi:[1,0]\n"
"v_pk_mul_f32 v[230:231], v[230:231], v[8:9] op_sel_hi:[1,0]\n"
"s_waitcnt vmcnt(15)\n"
"v_lshlrev_b32 v32, 16, v16\n"
"v_and_b32 v33, 0xffff0000, v16\n"
"v_lshlrev_b32 v34, 16, v17\n"
"v_and_b32 v35, 0xffff0000, v17\n"
"v_lshlrev_b32 v36, 16, v18\n"
"v_and_b32 v37, 0xffff0000, v18\n"
"v_lshlrev_b32 v38, 16, v19\n"
"v_and_b32 v39, 0xffff0000, v19\n"
"v_pk_fma_f32 v[224:225], v[32:33], v[14:15], v[224:225] op_sel_hi:[1,0,1]\n"
"v_pk_fma_f32 v[226:227], v[34:35], v[14:15], v[226:227] op_sel_hi:[1,0,1]\n"
"v_pk_fma_f32 v[228:229], v[36:37], v[14:15], v[228:229] op_sel_hi:[1,0,1]\n"
"v_pk_fma_f32 v[230:231], v[38:39], v[14:15], v[230:231] op_sel_hi:[1,0,1]\n"
"v_cvt_pk_bf16_f32 v16, v224, v225\n"
"v_cvt_pk_bf16_f32 v17, v226, v227\n"
"v_cvt_pk_bf16_f32 v18, v228, v229\n"
"v_cvt_pk_bf16_f32 v19, v230, v231\n"
"s_add_u32 s71, %[coff], 0x3000\n"
"buffer_store_dwordx4 v[16:19], v1, %[rsW], s71 offen sc1\n"
"v_pk_mul_f32 v[232:233], v[232:233], v[8:9] op_sel_hi:[1,0]\n"
"v_pk_mul_f32 v[234:235], v[234:235], v[8:9] op_sel_hi:[1,0]\n"
"v_pk_mul_f32 v[236:237], v[236:237], v[8:9] op_sel_hi:[1,0]\n"
"v_pk_mul_f32 v[238:239], v[238:239], v[8:9] op_sel_hi:[1,0]\n"
"s_waitcnt vmcnt(15)\n"
"v_lshlrev_b32 v40, 16, v20\n"
"v_and_b32 v41, 0xffff0000, v20\n"
"v_lshlrev_b32 v42, 16, v21\n"
"v_and_b32 v43, 0xffff0000, v21\n"
"v_lshlrev_b32 v44, 16, v22\n"
"v_and_b32 v45, 0xffff0000, v22\n"
"v_lshlrev_b32 v46, 16, v23\n"
"v_and_b32 v47, 0xffff0000, v23\n"
"v_pk_fma_f32 v[232:233], v[40:41], v[14:15], v[232:233] op_sel_hi:[1,0,1]\n"
"v_pk_fma_f32 v[234:235], v[42:43], v[14:15], v[234:235] op_sel_hi:[1,0,1]\n"
"v_pk_fma_f32 v[236:237], v[44:45], v[14:15], v[236:237] op_sel_hi:[1,0,1]\n"
"v_pk_fma_f32 v[238:239], v[46:47], v[14:15], v[238:239] op_sel_hi:[1,0,1]\n"
"v_cvt_pk_bf16_f32 v20, v232, v233\n"
"v_cvt_pk_bf16_f32 v21, v234, v235\n"
"v_cvt_pk_bf16_f32 v22, v236, v237\n"
"v_cvt_pk_bf16_f32 v23, v238, v239\n"
"buffer_store_dwordx4 v[20:23], v1, %[rsW], s71 offen offset:1024 sc1\n"
"v_pk_mul_f32 v[240:241], v[240:241], v[8:9] op_sel_hi:[1,0]\n"
"v_pk_mul_f32 v[242:243], v[242:243], v[8:9] op_sel_hi:[1,0]\n"
"v_pk_mul_f32 v[244:245], v[244:245], v[8:9] op_sel_hi:[1,0]\n"
"v_pk_mul_f32 v[246:247], v[246:247], v[8:9] op_sel_hi:[1,0]\n"
"s_waitcnt vmcnt(15)\n"
"v_lshlrev_b32 v32, 16, v24\n"
"v_and_b32 v33, 0xffff0000, v24\n"
"v_lshlrev_b32 v34, 16, v25\n"
"v_and_b32 v35, 0xffff0000, v25\n"
"v_lshlrev_b32 v36, 16, v26\n"
"v_and_b32 v37, 0xffff0000, v26\n"
"v_lshlrev_b32 v38, 16, v27\n"
"v_and_b32 v39, 0xffff0000, v27\n"
"v_pk_fma_f32 v[240:241], v[32:33], v[14:15], v[240:241] op_sel_hi:[1,0,1]\n"
"v_pk_fma_f32 v[242:243], v[34:35], v[14:15], v[242:243] op_sel_hi:[1,0,1]\n"
"v_pk_fma_f32 v[244:245], v[36:37], v[14:15], v[244:245] op_sel_hi:[1,0,1]\n"
"v_pk_fma_f32 v[246:247], v[38:39], v[14:15], v[246:247] op_sel_hi:[1,0,1]\n"
"v_cvt_pk_bf16_f32 v24, v240, v241\n"
"v_cvt_pk_bf16_f32 v25, v242, v243\n"
"v_cvt_pk_bf16_f32 v26, v244, v245\n"
"v_cvt_pk_bf16_f32 v27, v246, v247\n"
"buffer_store_dwordx4 v[24:27], v1, %[rsW], s71 offen offset:2048 sc1\n"
"v_pk_mul_f32 v[248:249], v[248:249], v[8:9] op_sel_hi:[1,0]\n"
"v_pk_mul_f32 v[250:251], v[250:251], v[8:9] op_sel_hi:[1,0]\n"
"v_pk_mul_f32 v[252:253], v[252:253], v[8:9] op_sel_hi:[1,0]\n"
"v_pk_mul_f32 v[254:255], v[254:255], v[8:9] op_sel_hi:[1,0]\n"
"s_waitcnt vmcnt(15)\n"
"v_lshlrev_b32 v40, 16, v28\n"
"v_and_b32 v41, 0xffff0000, v28\n"
"v_lshlrev_b32 v42, 16, v29\n"
"v_and_b32 v43, 0xffff0000, v29\n"
"v_lshlrev_b32 v44, 16, v30\n"
"v_and_b32 v45, 0xffff0000, v30\n"
"v_lshlrev_b32 v46, 16, v31\n"
"v_and_b32 v47, 0xffff0000, v31\n"
"v_pk_fma_f32 v[248:249], v[40:41], v[14:15], v[248:249] op_sel_hi:[1,0,1]\n"
"v_pk_fma_f32 v[250:251], v[42:43], v[14:15], v[250:251] op_sel_hi:[1,0,1]\n"
"v_pk_fma_f32 v[252:253], v[44:45], v[14:15], v[252:253] op_sel_hi:[1,0,1]\n"
"v_pk_fma_f32 v[254:255], v[46:47], v[14:15], v[254:255] op_sel_hi:[1,0,1]\n"
"v_cvt_pk_bf16_f32 v28, v248, v249\n"
"v_cvt_pk_bf16_f32 v29, v250, v251\n"
"v_cvt_pk_bf16_f32 v30, v252, v253\n"
"v_cvt_pk_bf16_f32 v31, v254, v255\n"
"buffer_store_dwordx4 v[28:31], v1, %[rsW], s71 offen offset:3072 sc1\n"
"END%=:\n"
"s_waitcnt lgkmcnt(0)\n"
"s_barrier\n"
"s_branch FIN%=\n"
"PRW%=:\n"
"s_mov_b32 s70, 0\n"
"PRP%=:\n"
"s_sleep 2\n"
"buffer_load_dwordx2 v[14:15], off, %[rsW], %[flag] sc1\n"
"s_waitcnt vmcnt(0)\n"
"v_cmp_ne_u64 vcc, %[tag], v[14:15]\n"
"s_add_u32 s70, s70, 1\n"
"s_cmp_lt_u32 s70, 0x100000\n"
"s_cbranch_vccz PRG%=\n"
"s_cbranch_scc1 PRP%=\n"
"s_branch PRG%=\n"
"KVWF%=:\n"
"s_mov_b32 s82, 0\n"
"KVPF%=:\n"
"s_sleep 2\n"
"buffer_load_dwordx2 v[14:15], v11, %[rsW], %[kvl] offen sc1\n"
"s_waitcnt vmcnt(0)\n"
"v_cmp_ne_u64 vcc, %[tag], v[14:15]\n"
"s_add_u32 s82, s82, 1\n"
"s_cmp_lt_u32 s82, 0x100000\n"
"s_cbranch_vccz KVGF%=\n"
"s_cbranch_scc1 KVPF%=\n"
"s_branch KVGF%=\n"
"KVWH%=:\n"
"s_mov_b32 s82, 0\n"
"KVPH%=:\n"
"s_sleep 2\n"
"buffer_load_dwordx2 v[14:15], v11, %[rsW], %[kvl] offen sc1\n"
"s_waitcnt vmcnt(0)\n"
"v_cmp_ne_u64 vcc, %[tag], v[14:15]\n"
"s_add_u32 s82, s82, 1\n"
"s_cmp_lt_u32 s82, 0x100000\n"
"s_cbranch_vccz KVGH%=\n"
"s_cbranch_scc1 KVPH%=\n"
"s_branch KVGH%=\n"
"HELP%=:\n"
"ds_read_b64_tr_b16 a[0:1], v88 offset:32768\n"
"ds_read_b64_tr_b16 a[2:3], v88 offset:33792\n"
"ds_read_b64_tr_b16 a[4:5], v89 offset:32768\n"
"ds_read_b64_tr_b16 a[6:7], v89 offset:33792\n"
"ds_read_b64_tr_b16 a[8:9], v90 offset:32768\n"
"ds_read_b64_tr_b16 a[10:11], v90 offset:33792\n"
"ds_read_b64_tr_b16 a[12:13], v91 offset:32768\n"
"ds_read_b64_tr_b16 a[14:15], v91 offset:33792\n"
"ds_read_b64_tr_b16 a[16:17], v88 offset:36864\n"
"ds_read_b64_tr_b16 a[18:19], v88 offset:37888\n"
"ds_read_b64_tr_b16 a[20:21], v89 offset:36864\n"
"ds_read_b64_tr_b16 a[22:23], v89 offset:37888\n"
"s_waitcnt lgkmcnt(10)\n"
"v_mfma_f32_32x32x16_bf16 v[16:31], a[0:3], a[64:67], 0\n"
"ds_read_b64_tr_b16 a[24:25], v90 offset:36864\n"
"ds_read_b64_tr_b16 a[26:27], v90 offset:37888\n"
"s_waitcnt lgkmcnt(10)\n"
"v_mfma_f32_32x32x16_bf16 v[32:47], a[4:7], a[64:67], 0\n"
"ds_read_b64_tr_b16 a[28:29], v91 offset:36864\n"
"ds_read_b64_tr_b16 a[30:31], v91 offset:37888\n"
"buffer_store_dwordx4 a[176:179], v92, %[rsW], s79 offen sc1\n"
"s_waitcnt lgkmcnt(10)\n"
"v_mfma_f32_32x32x16_bf16 v[48:63], a[8:11], a[64:67], 0\n"
"ds_read_b64_tr_b16 a[0:1], v88 offset:40960\n"
"ds_read_b64_tr_b16 a[2:3], v88 offset:41984\n"
"s_waitcnt lgkmcnt(10)\n"
"v_mfma_f32_32x32x16_bf16 v[64:79], a[12:15], a[64:67], 0\n"
"ds_read_b64_tr_b16 a[4:5], v89 offset:40960\n"
"ds_read_b64_tr_b16 a[6:7], v89 offset:41984\n"
"s_waitcnt lgkmcnt(10)\n"
"v_mfma_f32_32x32x16_bf16 v[16:31], a[16:19], a[68:71], v[16:31]\n"
"ds_read_b64_tr_b16 a[8:9], v90 offset:40960\n"
"ds_read_b64_tr_b16 a[10:11], v90 offset:41984\n"
"s_waitcnt lgkmcnt(10)\n"
"v_mfma_f32_32x32x16_bf16 v[32:47], a[20:23], a[68:71], v[32:47]\n"
"ds_read_b64_tr_b16 a[12:13], v91 offset:40960\n"
"ds_read_b64_tr_b16 a[14:15], v91 offset:41984\n"
"s_add_u32 s82, s79, 0x4000\n"
"buffer_store_dwordx4 a[180:183], v92, %[rsW], s82 offen sc1\n"
"s_waitcnt lgkmcnt(10)\n"
"v_mfma_f32_32x32x16_bf16 v[48:63], a[24:27], a[68:71], v[48:63]\n"
"ds_read_b64_tr_b16 a[16:17], v88 offset:45056\n"
"ds_read_b64_tr_b16 a[18:19], v88 offset:46080\n"
"s_waitcnt lgkmcnt(10)\n"
"v_mfma_f32_32x32x16_bf16 v[64:79], a[28:31], a[68:71], v[64:79]\n"
"ds_read_b64_tr_b16 a[20:21], v89 offset:45056\n"
"ds_read_b64_tr_b16 a[22:23], v89 offset:46080\n"
"s_waitcnt lgkmcnt(10)\n"
"v_mfma_f32_32x32x16_bf16 v[16:31], a[0:3], a[72:75], v[16:31]\n"
"ds_read_b64_tr_b16 a[24:25], v90 offset:45056\n"
"ds_read_b64_tr_b16 a[26:27], v90 offset:46080\n"
"s_waitcnt lgkmcnt(10)\n"
"v_mfma_f32_32x32x16_bf16 v[32:47], a[4:7], a[72:75], v[32:47]\n"
"ds_read_b64_tr_b16 a[28:29], v91 offset:45056\n"
"ds_read_b64_tr_b16 a[30:31], v91 offset:46080\n"
"s_add_u32 s82, s79, 0x8000\n"
"buffer_store_dwordx4 a[184:187], v92, %[rsW], s82 offen sc1\n"
"s_waitcnt lgkmcnt(10)\n"
"v_mfma_f32_32x32x16_bf16 v[48:63], a[8:11], a[72:75], v[48:63]\n"
"ds_read_b64_tr_b16 a[0:1], v88 offset:49152\n"
"ds_read_b64_tr_b16 a[2:3], v88 offset:50176\n"
"s_waitcnt lgkmcnt(10)\n"
"v_mfma_f32_32x32x16_bf16 v[64:79], a[12:15], a[72:75], v[64:79]\n"
"ds_read_b64_tr_b16 a[4:5], v89 offset:49152\n"
"ds_read_b64_tr_b16 a[6:7], v89 offset:50176\n"
"s_waitcnt lgkmcnt(10)\n"
"v_mfma_f32_32x32x16_bf16 v[16:31], a[16:19], a[76:79], v[16:31]\n"
"ds_read_b64_tr_b16 a[8:9], v90 offset:49152\n"
"ds_read_b64_tr_b16 a[10:11], v90 offset:50176\n"
"s_waitcnt lgkmcnt(10)\n"
"v_mfma_f32_32x32x16_bf16 v[32:47], a[20:23], a[76:79], v[32:47]\n"
"ds_read_b64_tr_b16 a[12:13], v91 offset:49152\n"
"ds_read_b64_tr_b16 a[14:15], v91 offset:50176\n"
"s_waitcnt lgkmcnt(10)\n"
"v_mfma_f32_32x32x16_bf16 v[48:63], a[24:27], a[76:79], v[48:63]\n"
"ds_read_b64_tr_b16 a[16:17], v88 offset:53248\n"
"ds_read_b64_tr_b16 a[18:19], v88 offset:54272\n"
"s_waitcnt lgkmcnt(10)\n"
"v_mfma_f32_32x32x16_bf16 v[64:79], a[28:31], a[76:79], v[64:79]\n"
"ds_read_b64_tr_b16 a[20:21], v89 offset:53248\n"
"ds_read_b64_tr_b16 a[22:23], v89 offset:54272\n"
"s_add_u32 s82, s79, 0xc000\n"
"buffer_store_dwordx4 a[188:191], v92, %[rsW], s82 offen sc1\n"
"s_waitcnt lgkmcnt(10)\n"
"v_mfma_f32_32x32x16_bf16 v[16:31], a[0:3], a[80:83], v[16:31]\n"
"ds_read_b64_tr_b16 a[24:25], v90 offset:53248\n"
"ds_read_b64_tr_b16 a[26:27], v90 offset:54272\n"
"s_waitcnt lgkmcnt(10)\n"
"v_mfma_f32_32x32x16_bf16 v[32:47], a[4:7], a[80:83], v[32:47]\n"
"ds_read_b64_tr_b16 a[28:29], v91 offset:53248\n"
"ds_read_b64_tr_b16 a[30:31], v91 offset:54272\n"
"s_waitcnt lgkmcnt(10)\n"
"v_mfma_f32_32x32x16_bf16 v[48:63], a[8:11], a[80:83], v[48:63]\n"
"ds_read_b64_tr_b16 a[0:1], v88 offset:57344\n"
"ds_read_b64_tr_b16 a[2:3], v88 offset:58368\n"
"s_waitcnt lgkmcnt(10)\n"
"v_mfma_f32_32x32x16_bf16 v[64:79], a[12:15], a[80:83], v[64:79]\n"
"ds_read_b64_tr_b16 a[4:5], v89 offset:57344\n"
"ds_read_b64_tr_b16 a[6:7], v89 offset:58368\n"
"buffer_store_dwordx4 a[192:195], v93, %[rsW], s79 offen sc1\n"
"s_waitcnt lgkmcnt(10)\n"
"v_mfma_f32_32x32x16_bf16 v[16:31], a[16:19], a[84:87], v[16:31]\n"
"ds_read_b64_tr_b16 a[8:9], v90 offset:57344\n"
"ds_read_b64_tr_b16 a[10:11], v90 offset:58368\n"
"s_waitcnt lgkmcnt(10)\n"
"v_mfma_f32_32x32x16_bf16 v[32:47], a[20:23], a[84:87], v[32:47]\n"
"ds_read_b64_tr_b16 a[12:13], v91 offset:57344\n"
"ds_read_b64_tr_b16 a[14:15], v91 offset:58368\n"
"s_waitcnt lgkmcnt(10)\n"
"v_mfma_f32_32x32x16_bf16 v[48:63], a[24:27], a[84:87], v[48:63]\n"
"ds_read_b64_tr_b16 a[16:17], v88 offset:61440\n"
"ds_read_b64_tr_b16 a[18:19], v88 offset:62464\n"
"s_waitcnt lgkmcnt(10)\n"
"v_mfma_f32_32x32x16_bf16 v[64:79], a[28:31], a[84:87], v[64:79]\n"
"ds_read_b64_tr_b16 a[20:21], v89 offset:61440\n"
"ds_read_b64_tr_b16 a[22:23], v89 offset:62464\n"
"s_add_u32 s82, s79, 0x4000\n"
"buffer_store_dwordx4 a[196:199], v93, %[rsW], s82 offen sc1\n"
"s_waitcnt lgkmcnt(10)\n"
"v_mfma_f32_32x32x16_bf16 v[16:31], a[0:3], a[88:91], v[16:31]\n"
"ds_read_b64_tr_b16 a[24:25], v90 offset:61440\n"
"ds_read_b64_tr_b16 a[26:27], v90 offset:62464\n"
"s_waitcnt lgkmcnt(10)\n"
"v_mfma_f32_32x32x16_bf16 v[32:47], a[4:7], a[88:91], v[32:47]\n"
"ds_read_b64_tr_b16 a[28:29], v91 offset:61440\n"
"ds_read_b64_tr_b16 a[30:31], v91 offset:62464\n"
"s_waitcnt lgkmcnt(10)\n"
"v_mfma_f32_32x32x16_bf16 v[48:63], a[8:11], a[88:91], v[48:63]\n"
"v_add_u32 v88, 0x10000, v88\n"
"ds_read_b64_tr_b16 a[0:1], v88\n"
"ds_read_b64_tr_b16 a[2:3], v88 offset:1024\n"
"s_waitcnt lgkmcnt(10)\n"
"v_mfma_f32_32x32x16_bf16 v[64:79], a[12:15], a[88:91], v[64:79]\n"
"v_add_u32 v89, 0x10000, v89\n"
"ds_read_b64_tr_b16 a[4:5], v89\n"
"ds_read_b64_tr_b16 a[6:7], v89 offset:1024\n"
"s_waitcnt lgkmcnt(10)\n"
"v_mfma_f32_32x32x16_bf16 v[16:31], a[16:19], a[92:95], v[16:31]\n"
"v_add_u32 v90, 0x10000, v90\n"
"ds_read_b64_tr_b16 a[8:9], v90\n"
"ds_read_b64_tr_b16 a[10:11], v90 offset:1024\n"
"s_waitcnt lgkmcnt(10)\n"
"v_mfma_f32_32x32x16_bf16 v[32:47], a[20:23], a[92:95], v[32:47]\n"
"v_add_u32 v91, 0x10000, v91\n"
"ds_read_b64_tr_b16 a[12:13], v91\n"
"ds_read_b64_tr_b16 a[14:15], v91 offset:1024\n"
"s_add_u32 s82, s79, 0x8000\n"
"buffer_store_dwordx4 a[200:203], v93, %[rsW], s82 offen sc1\n"
"s_waitcnt lgkmcnt(10)\n"
"v_mfma_f32_32x32x16_bf16 v[48:63], a[24:27], a[92:95], v[48:63]\n"
"ds_read_b64_tr_b16 a[16:17], v88 offset:4096\n"
"ds_read_b64_tr_b16 a[18:19], v88 offset:5120\n"
"s_waitcnt lgkmcnt(10)\n"
"v_mfma_f32_32x32x16_bf16 v[64:79], a[28:31], a[92:95], v[64:79]\n"
"ds_read_b64_tr_b16 a[20:21], v89 offset:4096\n"
"ds_read_b64_tr_b16 a[22:23], v89 offset:5120\n"
"s_barrier\n"
"s_waitcnt lgkmcnt(10)\n"
"v_mfma_f32_32x32x16_bf16 v[16:31], a[0:3], a[96:99], v[16:31]\n"
"ds_read_b64_tr_b16 a[24:25], v90 offset:4096\n"
"ds_read_b64_tr_b16 a[26:27], v90 offset:5120\n"
"s_add_u32 m0, s74, 0x8000\n"
"s_add_u32 s71, %[q0], 0x80000\n"
"buffer_load_dwordx4 v80, %[rsQ], s71 offen lds\n"
"s_waitcnt lgkmcnt(10)\n"
"v_mfma_f32_32x32x16_bf16 v[32:47], a[4:7], a[96:99], v[32:47]\n"
"ds_read_b64_tr_b16 a[28:29], v91 offset:4096\n"
"ds_read_b64_tr_b16 a[30:31], v91 offset:5120\n"
"s_add_u32 s82, s79, 0xc000\n"
"buffer_store_dwordx4 a[204:207], v93, %[rsW], s82 offen sc1\n"
"s_waitcnt lgkmcnt(10)\n"
"v_mfma_f32_32x32x16_bf16 v[48:63], a[8:11], a[96:99], v[48:63]\n"
"ds_read_b64_tr_b16 a[0:1], v88 offset:8192\n"
"ds_read_b64_tr_b16 a[2:3], v88 offset:9216\n"
"s_add_u32 s71, %[q0], 0x83c00\n"
"buffer_load_dwordx4 v81, %[rsQ], s71 offen offset:1024 lds\n"
"s_waitcnt lgkmcnt(10)\n"
"v_mfma_f32_32x32x16_bf16 v[64:79], a[12:15], a[96:99], v[64:79]\n"
"ds_read_b64_tr_b16 a[4:5], v89 offset:8192\n"
"ds_read_b64_tr_b16 a[6:7], v89 offset:9216\n"
"s_waitcnt lgkmcnt(10)\n"
"v_mfma_f32_32x32x16_bf16 v[16:31], a[16:19], a[100:103], v[16:31]\n"
"ds_read_b64_tr_b16 a[8:9], v90 offset:8192\n"
"ds_read_b64_tr_b16 a[10:11], v90 offset:9216\n"
"s_add_u32 s71, %[q0], 0x87800\n"
"buffer_load_dwordx4 v80, %[rsQ], s71 offen offset:2048 lds\n"
"s_waitcnt lgkmcnt(10)\n"
"v_mfma_f32_32x32x16_bf16 v[32:47], a[20:23], a[100:103], v[32:47]\n"
"ds_read_b64_tr_b16 a[12:13], v91 offset:8192\n"
"ds_read_b64_tr_b16 a[14:15], v91 offset:9216\n"
"buffer_store_dwordx4 a[208:211], v92, %[rsW], s80 offen sc1\n"
"s_waitcnt lgkmcnt(10)\n"
"v_mfma_f32_32x32x16_bf16 v[48:63], a[24:27], a[100:103], v[48:63]\n"
"ds_read_b64_tr_b16 a[16:17], v88 offset:12288\n"
"ds_read_b64_tr_b16 a[18:19], v88 offset:13312\n"
"s_add_u32 s71, %[q0], 0x8b400\n"
"buffer_load_dwordx4 v81, %[rsQ], s71 offen offset:3072 lds\n"
"s_waitcnt lgkmcnt(10)\n"
"v_mfma_f32_32x32x16_bf16 v[64:79], a[28:31], a[100:103], v[64:79]\n"
"ds_read_b64_tr_b16 a[20:21], v89 offset:12288\n"
"ds_read_b64_tr_b16 a[22:23], v89 offset:13312\n"
"s_waitcnt lgkmcnt(10)\n"
"v_mfma_f32_32x32x16_bf16 v[16:31], a[0:3], a[104:107], v[16:31]\n"
"ds_read_b64_tr_b16 a[24:25], v90 offset:12288\n"
"ds_read_b64_tr_b16 a[26:27], v90 offset:13312\n"
"s_add_u32 m0, s74, 0x9000\n"
"s_add_u32 s71, %[q0], 0x90000\n"
"buffer_load_dwordx4 v80, %[rsQ], s71 offen lds\n"
"s_waitcnt lgkmcnt(10)\n"
"v_mfma_f32_32x32x16_bf16 v[32:47], a[4:7], a[104:107], v[32:47]\n"
"ds_read_b64_tr_b16 a[28:29], v91 offset:12288\n"
"ds_read_b64_tr_b16 a[30:31], v91 offset:13312\n"
"s_waitcnt lgkmcnt(10)\n"
"v_mfma_f32_32x32x16_bf16 v[48:63], a[8:11], a[104:107], v[48:63]\n"
"ds_read_b64_tr_b16 a[0:1], v88 offset:16384\n"
"ds_read_b64_tr_b16 a[2:3], v88 offset:17408\n"
"s_add_u32 s71, %[q0], 0x93c00\n"
"buffer_load_dwordx4 v81, %[rsQ], s71 offen offset:1024 lds\n"
"s_waitcnt lgkmcnt(10)\n"
"v_mfma_f32_32x32x16_bf16 v[64:79], a[12:15], a[104:107], v[64:79]\n"
"ds_read_b64_tr_b16 a[4:5], v89 offset:16384\n"
"ds_read_b64_tr_b16 a[6:7], v89 offset:17408\n"
"s_add_u32 s82, s80, 0x4000\n"
"buffer_store_dwordx4 a[212:215], v92, %[rsW], s82 offen sc1\n"
"s_waitcnt lgkmcnt(10)\n"
"v_mfma_f32_32x32x16_bf16 v[16:31], a[16:19], a[108:111], v[16:31]\n"
"ds_read_b64_tr_b16 a[8:9], v90 offset:16384\n"
"ds_read_b64_tr_b16 a[10:11], v90 offset:17408\n"
"s_add_u32 s71, %[q0], 0x97800\n"
"buffer_load_dwordx4 v80, %[rsQ], s71 offen offset:2048 lds\n"
"s_waitcnt lgkmcnt(10)\n"
"v_mfma_f32_32x32x16_bf16 v[32:47], a[20:23], a[108:111], v[32:47]\n"
"ds_read_b64_tr_b16 a[12:13], v91 offset:16384\n"
"ds_read_b64_tr_b16 a[14:15], v91 offset:17408\n"
"s_waitcnt lgkmcnt(10)\n"
"v_mfma_f32_32x32x16_bf16 v[48:63], a[24:27], a[108:111], v[48:63]\n"
"ds_read_b64_tr_b16 a[16:17], v88 offset:20480\n"
"ds_read_b64_tr_b16 a[18:19], v88 offset:21504\n"
"s_add_u32 s71, %[q0], 0x9b400\n"
"buffer_load_dwordx4 v81, %[rsQ], s71 offen offset:3072 lds\n"
"s_waitcnt lgkmcnt(10)\n"
"v_mfma_f32_32x32x16_bf16 v[64:79], a[28:31], a[108:111], v[64:79]\n"
"ds_read_b64_tr_b16 a[20:21], v89 offset:20480\n"
"ds_read_b64_tr_b16 a[22:23], v89 offset:21504\n"
"s_add_u32 s82, s80, 0x8000\n"
"buffer_store_dwordx4 a[216:219], v92, %[rsW], s82 offen sc1\n"
"s_waitcnt lgkmcnt(10)\n"
"v_mfma_f32_32x32x16_bf16 v[16:31], a[0:3], a[112:115], v[16:31]\n"
"ds_read_b64_tr_b16 a[24:25], v90 offset:20480\n"
"ds_read_b64_tr_b16 a[26:27], v90 offset:21504\n"
"s_waitcnt lgkmcnt(10)\n"
"v_mfma_f32_32x32x16_bf16 v[32:47], a[4:7], a[112:115], v[32:47]\n"
"ds_read_b64_tr_b16 a[28:29], v91 offset:20480\n"
"ds_read_b64_tr_b16 a[30:31], v91 offset:21504\n"
"s_waitcnt lgkmcnt(10)\n"
"v_mfma_f32_32x32x16_bf16 v[48:63], a[8:11], a[112:115], v[48:63]\n"
"ds_read_b64_tr_b16 a[0:1], v88 offset:24576\n"
"ds_read_b64_tr_b16 a[2:3], v88 offset:25600\n"
"s_waitcnt lgkmcnt(10)\n"
"v_mfma_f32_32x32x16_bf16 v[64:79], a[12:15], a[112:115], v[64:79]\n"
"ds_read_b64_tr_b16 a[4:5], v89 offset:24576\n"
"ds_read_b64_tr_b16 a[6:7], v89 offset:25600\n"
"s_add_u32 s82, s80, 0xc000\n"
"buffer_store_dwordx4 a[220:223], v92, %[rsW], s82 offen sc1\n"
"s_waitcnt lgkmcnt(10)\n"
"v_mfma_f32_32x32x16_bf16 v[16:31], a[16:19], a[116:119], v[16:31]\n"
"ds_read_b64_tr_b16 a[8:9], v90 offset:24576\n"
"ds_read_b64_tr_b16 a[10:11], v90 offset:25600\n"
"s_waitcnt lgkmcnt(10)\n"
"v_mfma_f32_32x32x16_bf16 v[32:47], a[20:23], a[116:119], v[32:47]\n"
"ds_read_b64_tr_b16 a[12:13], v91 offset:24576\n"
"ds_read_b64_tr_b16 a[14:15], v91 offset:25600\n"
"s_waitcnt lgkmcnt(10)\n"
"v_mfma_f32_32x32x16_bf16 v[48:63], a[24:27], a[116:119], v[48:63]\n"
"ds_read_b64_tr_b16 a[16:17], v88 offset:28672\n"
"ds_read_b64_tr_b16 a[18:19], v88 offset:29696\n"
"s_waitcnt lgkmcnt(10)\n"
"v_mfma_f32_32x32x16_bf16 v[64:79], a[28:31], a[116:119], v[64:79]\n"
"ds_read_b64_tr_b16 a[20:21], v89 offset:28672\n"
"ds_read_b64_tr_b16 a[22:23], v89 offset:29696\n"
"s_waitcnt lgkmcnt(10)\n"
"v_mfma_f32_32x32x16_bf16 v[16:31], a[0:3], a[120:123], v[16:31]\n"
"ds_read_b64_tr_b16 a[24:25], v90 offset:28672\n"
"ds_read_b64_tr_b16 a[26:27], v90 offset:29696\n"
"s_waitcnt lgkmcnt(10)\n"
"v_mfma_f32_32x32x16_bf16 v[32:47], a[4:7], a[120:123], v[32:47]\n"
"ds_read_b64_tr_b16 a[28:29], v91 offset:28672\n"
"ds_read_b64_tr_b16 a[30:31], v91 offset:29696\n"
"buffer_store_dwordx4 a[224:227], v93, %[rsW], s80 offen sc1\n"
"s_waitcnt lgkmcnt(10)\n"
"v_mfma_f32_32x32x16_bf16 v[48:63], a[8:11], a[120:123], v[48:63]\n"
"ds_read_b64_tr_b16 a[0:1], v88 offset:32768\n"
"ds_read_b64_tr_b16 a[2:3], v88 offset:33792\n"
"s_waitcnt lgkmcnt(10)\n"
"v_mfma_f32_32x32x16_bf16 v[64:79], a[12:15], a[120:123], v[64:79]\n"
"ds_read_b64_tr_b16 a[4:5], v89 offset:32768\n"
"ds_read_b64_tr_b16 a[6:7], v89 offset:33792\n"
"s_waitcnt lgkmcnt(10)\n"
"v_mfma_f32_32x32x16_bf16 v[16:31], a[16:19], a[124:127], v[16:31]\n"
"ds_read_b64_tr_b16 a[8:9], v90 offset:32768\n"
"ds_read_b64_tr_b16 a[10:11], v90 offset:33792\n"
"s_waitcnt lgkmcnt(10)\n"
"v_mfma_f32_32x32x16_bf16 v[32:47], a[20:23], a[124:127], v[32:47]\n"
"ds_read_b64_tr_b16 a[12:13], v91 offset:32768\n"
"ds_read_b64_tr_b16 a[14:15], v91 offset:33792\n"
"s_add_u32 s82, s80, 0x4000\n"
"buffer_store_dwordx4 a[228:231], v93, %[rsW], s82 offen sc1\n"
"s_waitcnt lgkmcnt(10)\n"
"v_mfma_f32_32x32x16_bf16 v[48:63], a[24:27], a[124:127], v[48:63]\n"
"ds_read_b64_tr_b16 a[16:17], v88 offset:36864\n"
"ds_read_b64_tr_b16 a[18:19], v88 offset:37888\n"
"s_waitcnt lgkmcnt(10)\n"
"v_mfma_f32_32x32x16_bf16 v[64:79], a[28:31], a[124:127], v[64:79]\n"
"ds_read_b64_tr_b16 a[20:21], v89 offset:36864\n"
"ds_read_b64_tr_b16 a[22:23], v89 offset:37888\n"
"s_barrier\n"
"s_waitcnt lgkmcnt(10)\n"
"v_mfma_f32_32x32x16_bf16 v[16:31], a[0:3], a[128:131], v[16:31]\n"
"ds_read_b64_tr_b16 a[24:25], v90 offset:36864\n"
"ds_read_b64_tr_b16 a[26:27], v90 offset:37888\n"
"s_add_u32 m0, s74, 0x10000\n"
"s_add_u32 s71, %[q0], 0x100000\n"
"buffer_load_dwordx4 v80, %[rsQ], s71 offen lds\n"
"s_waitcnt lgkmcnt(10)\n"
"v_mfma_f32_32x32x16_bf16 v[32:47], a[4:7], a[128:131], v[32:47]\n"
"ds_read_b64_tr_b16 a[28:29], v91 offset:36864\n"
"ds_read_b64_tr_b16 a[30:31], v91 offset:37888\n"
"s_add_u32 s82, s80, 0x8000\n"
"buffer_store_dwordx4 a[232:235], v93, %[rsW], s82 offen sc1\n"
"s_waitcnt lgkmcnt(10)\n"
"v_mfma_f32_32x32x16_bf16 v[48:63], a[8:11], a[128:131], v[48:63]\n"
"ds_read_b64_tr_b16 a[0:1], v88 offset:40960\n"
"ds_read_b64_tr_b16 a[2:3], v88 offset:41984\n"
"s_add_u32 s71, %[q0], 0x103c00\n"
"buffer_load_dwordx4 v81, %[rsQ], s71 offen offset:1024 lds\n"
"s_waitcnt lgkmcnt(10)\n"
"v_mfma_f32_32x32x16_bf16 v[64:79], a[12:15], a[128:131], v[64:79]\n"
"ds_read_b64_tr_b16 a[4:5], v89 offset:40960\n"
"ds_read_b64_tr_b16 a[6:7], v89 offset:41984\n"
"s_waitcnt lgkmcnt(10)\n"
"v_mfma_f32_32x32x16_bf16 v[16:31], a[16:19], a[132:135], v[16:31]\n"
"ds_read_b64_tr_b16 a[8:9], v90 offset:40960\n"
"ds_read_b64_tr_b16 a[10:11], v90 offset:41984\n"
"s_add_u32 s71, %[q0], 0x107800\n"
"buffer_load_dwordx4 v80, %[rsQ], s71 offen offset:2048 lds\n"
"s_waitcnt lgkmcnt(10)\n"
"v_mfma_f32_32x32x16_bf16 v[32:47], a[20:23], a[132:135], v[32:47]\n"
"ds_read_b64_tr_b16 a[12:13], v91 offset:40960\n"
"ds_read_b64_tr_b16 a[14:15], v91 offset:41984\n"
"s_waitcnt lgkmcnt(10)\n"
"v_mfma_f32_32x32x16_bf16 v[48:63], a[24:27], a[132:135], v[48:63]\n"
"ds_read_b64_tr_b16 a[16:17], v88 offset:45056\n"
"ds_read_b64_tr_b16 a[18:19], v88 offset:46080\n"
"s_add_u32 s71, %[q0], 0x10b400\n"
"buffer_load_dwordx4 v81, %[rsQ], s71 offen offset:3072 lds\n"
"s_waitcnt lgkmcnt(10)\n"
"v_mfma_f32_32x32x16_bf16 v[64:79], a[28:31], a[132:135], v[64:79]\n"
"ds_read_b64_tr_b16 a[20:21], v89 offset:45056\n"
"ds_read_b64_tr_b16 a[22:23], v89 offset:46080\n"
"s_add_u32 s82, s80, 0xc000\n"
"buffer_store_dwordx4 a[236:239], v93, %[rsW], s82 offen sc1\n"
"s_waitcnt lgkmcnt(10)\n"
"v_mfma_f32_32x32x16_bf16 v[16:31], a[0:3], a[136:139], v[16:31]\n"
"ds_read_b64_tr_b16 a[24:25], v90 offset:45056\n"
"ds_read_b64_tr_b16 a[26:27], v90 offset:46080\n"
"s_add_u32 m0, s74, 0x11000\n"
"s_add_u32 s71, %[q0], 0x110000\n"
"buffer_load_dwordx4 v80, %[rsQ], s71 offen lds\n"
"s_waitcnt lgkmcnt(10)\n"
"v_mfma_f32_32x32x16_bf16 v[32:47], a[4:7], a[136:139], v[32:47]\n"
"ds_read_b64_tr_b16 a[28:29], v91 offset:45056\n"
"ds_read_b64_tr_b16 a[30:31], v91 offset:46080\n"
"s_waitcnt lgkmcnt(10)\n"
"v_mfma_f32_32x32x16_bf16 v[48:63], a[8:11], a[136:139], v[48:63]\n"
"ds_read_b64_tr_b16 a[0:1], v88 offset:49152\n"
"ds_read_b64_tr_b16 a[2:3], v88 offset:50176\n"
"s_add_u32 s71, %[q0], 0x113c00\n"
"buffer_load_dwordx4 v81, %[rsQ], s71 offen offset:1024 lds\n"
"s_waitcnt lgkmcnt(10)\n"
"v_mfma_f32_32x32x16_bf16 v[64:79], a[12:15], a[136:139], v[64:79]\n"
"ds_read_b64_tr_b16 a[4:5], v89 offset:49152\n"
"ds_read_b64_tr_b16 a[6:7], v89 offset:50176\n"
"buffer_store_dwordx4 a[32:35], v92, %[rsW], s81 offen sc1\n"
"s_waitcnt lgkmcnt(10)\n"
"v_mfma_f32_32x32x16_bf16 v[16:31], a[16:19], a[140:143], v[16:31]\n"
"ds_read_b64_tr_b16 a[8:9], v90 offset:49152\n"
"ds_read_b64_tr_b16 a[10:11], v90 offset:50176\n"
"s_add_u32 s71, %[q0], 0x117800\n"
"buffer_load_dwordx4 v80, %[rsQ], s71 offen offset:2048 lds\n"
"s_waitcnt lgkmcnt(10)\n"
"v_mfma_f32_32x32x16_bf16 v[32:47], a[20:23], a[140:143], v[32:47]\n"
"ds_read_b64_tr_b16 a[12:13], v91 offset:49152\n"
"ds_read_b64_tr_b16 a[14:15], v91 offset:50176\n"
"s_waitcnt lgkmcnt(10)\n"
"v_mfma_f32_32x32x16_bf16 v[48:63], a[24:27], a[140:143], v[48:63]\n"
"ds_read_b64_tr_b16 a[16:17], v88 offset:53248\n"
"ds_read_b64_tr_b16 a[18:19], v88 offset:54272\n"
"s_add_u32 s71, %[q0], 0x11b400\n"
"buffer_load_dwordx4 v81, %[rsQ], s71 offen offset:3072 lds\n"
"s_waitcnt lgkmcnt(10)\n"
"v_mfma_f32_32x32x16_bf16 v[64:79], a[28:31], a[140:143], v[64:79]\n"
"ds_read_b64_tr_b16 a[20:21], v89 offset:53248\n"
"ds_read_b64_tr_b16 a[22:23], v89 offset:54272\n"
"s_add_u32 s82, s81, 0x4000\n"
"buffer_store_dwordx4 a[36:39], v92, %[rsW], s82 offen sc1\n"
"s_waitcnt lgkmcnt(10)\n"
"v_mfma_f32_32x32x16_bf16 v[16:31], a[0:3], a[144:147], v[16:31]\n"
"ds_read_b64_tr_b16 a[24:25], v90 offset:53248\n"
"ds_read_b64_tr_b16 a[26:27], v90 offset:54272\n"
"s_waitcnt lgkmcnt(10)\n"
"v_mfma_f32_32x32x16_bf16 v[32:47], a[4:7], a[144:147], v[32:47]\n"
"ds_read_b64_tr_b16 a[28:29], v91 offset:53248\n"
"ds_read_b64_tr_b16 a[30:31], v91 offset:54272\n"
"s_waitcnt lgkmcnt(10)\n"
"v_mfma_f32_32x32x16_bf16 v[48:63], a[8:11], a[144:147], v[48:63]\n"
"ds_read_b64_tr_b16 a[0:1], v88 offset:57344\n"
"ds_read_b64_tr_b16 a[2:3], v88 offset:58368\n"
"s_waitcnt lgkmcnt(10)\n"
"v_mfma_f32_32x32x16_bf16 v[64:79], a[12:15], a[144:147], v[64:79]\n"
"ds_read_b64_tr_b16 a[4:5], v89 offset:57344\n"
"ds_read_b64_tr_b16 a[6:7], v89 offset:58368\n"
"s_waitcnt lgkmcnt(10)\n"
"v_mfma_f32_32x32x16_bf16 v[16:31], a[16:19], a[148:151], v[16:31]\n"
"ds_read_b64_tr_b16 a[8:9], v90 offset:57344\n"
"ds_read_b64_tr_b16 a[10:11], v90 offset:58368\n"
"s_waitcnt lgkmcnt(10)\n"
"v_mfma_f32_32x32x16_bf16 v[32:47], a[20:23], a[148:151], v[32:47]\n"
"ds_read_b64_tr_b16 a[12:13], v91 offset:57344\n"
"ds_read_b64_tr_b16 a[14:15], v91 offset:58368\n"
"s_add_u32 s82, s81, 0x8000\n"
"buffer_store_dwordx4 a[40:43], v92, %[rsW], s82 offen sc1\n"
"s_waitcnt lgkmcnt(10)\n"
"v_mfma_f32_32x32x16_bf16 v[48:63], a[24:27], a[148:151], v[48:63]\n"
"ds_read_b64_tr_b16 a[16:17], v88 offset:61440\n"
"ds_read_b64_tr_b16 a[18:19], v88 offset:62464\n"
"s_waitcnt lgkmcnt(10)\n"
"v_mfma_f32_32x32x16_bf16 v[64:79], a[28:31], a[148:151], v[64:79]\n"
"ds_read_b64_tr_b16 a[20:21], v89 offset:61440\n"
"ds_read_b64_tr_b16 a[22:23], v89 offset:62464\n"
"s_waitcnt lgkmcnt(10)\n"
"v_mfma_f32_32x32x16_bf16 v[16:31], a[0:3], a[152:155], v[16:31]\n"
"ds_read_b64_tr_b16 a[24:25], v90 offset:61440\n"
"ds_read_b64_tr_b16 a[26:27], v90 offset:62464\n"
"s_waitcnt lgkmcnt(10)\n"
"v_mfma_f32_32x32x16_bf16 v[32:47], a[4:7], a[152:155], v[32:47]\n"
"ds_read_b64_tr_b16 a[28:29], v91 offset:61440\n"
"ds_read_b64_tr_b16 a[30:31], v91 offset:62464\n"
"s_add_u32 s82, s81, 0xc000\n"
"buffer_store_dwordx4 a[44:47], v92, %[rsW], s82 offen sc1\n"
"s_waitcnt lgkmcnt(10)\n"
"v_mfma_f32_32x32x16_bf16 v[48:63], a[8:11], a[152:155], v[48:63]\n"
"v_add_u32 v88, 0x10000, v88\n"
"ds_read_b64_tr_b16 a[0:1], v88\n"
"ds_read_b64_tr_b16 a[2:3], v88 offset:1024\n"
"s_waitcnt lgkmcnt(10)\n"
"v_mfma_f32_32x32x16_bf16 v[64:79], a[12:15], a[152:155], v[64:79]\n"
"v_add_u32 v89, 0x10000, v89\n"
"ds_read_b64_tr_b16 a[4:5], v89\n"
"ds_read_b64_tr_b16 a[6:7], v89 offset:1024\n"
"s_waitcnt lgkmcnt(10)\n"
"v_mfma_f32_32x32x16_bf16 v[16:31], a[16:19], a[156:159], v[16:31]\n"
"v_add_u32 v90, 0x10000, v90\n"
"ds_read_b64_tr_b16 a[8:9], v90\n"
"ds_read_b64_tr_b16 a[10:11], v90 offset:1024\n"
"s_waitcnt lgkmcnt(10)\n"
"v_mfma_f32_32x32x16_bf16 v[32:47], a[20:23], a[156:159], v[32:47]\n"
"v_add_u32 v91, 0x10000, v91\n"
"ds_read_b64_tr_b16 a[12:13], v91\n"
"ds_read_b64_tr_b16 a[14:15], v91 offset:1024\n"
"buffer_store_dwordx4 a[48:51], v93, %[rsW], s81 offen sc1\n"
"s_waitcnt lgkmcnt(10)\n"
"v_mfma_f32_32x32x16_bf16 v[48:63], a[24:27], a[156:159], v[48:63]\n"
"ds_read_b64_tr_b16 a[16:17], v88 offset:4096\n"
"ds_read_b64_tr_b16 a[18:19], v88 offset:5120\n"
"s_waitcnt lgkmcnt(10)\n"
"v_mfma_f32_32x32x16_bf16 v[64:79], a[28:31], a[156:159], v[64:79]\n"
"ds_read_b64_tr_b16 a[20:21], v89 offset:4096\n"
"ds_read_b64_tr_b16 a[22:23], v89 offset:5120\n"
"s_barrier\n"
"s_waitcnt lgkmcnt(10)\n"
"v_mfma_f32_32x32x16_bf16 v[16:31], a[0:3], a[160:163], v[16:31]\n"
"ds_read_b64_tr_b16 a[24:25], v90 offset:4096\n"
"ds_read_b64_tr_b16 a[26:27], v90 offset:5120\n"
"s_add_u32 m0, s74, 0x18000\n"
"s_add_u32 s71, %[q0], 0x180000\n"
"buffer_load_dwordx4 v80, %[rsQ], s71 offen lds\n"
"s_waitcnt lgkmcnt(10)\n"
"v_mfma_f32_32x32x16_bf16 v[32:47], a[4:7], a[160:163], v[32:47]\n"
"ds_read_b64_tr_b16 a[28:29], v91 offset:4096\n"
"ds_read_b64_tr_b16 a[30:31], v91 offset:5120\n"
"s_waitcnt lgkmcnt(10)\n"
"v_mfma_f32_32x32x16_bf16 v[48:63], a[8:11], a[160:163], v[48:63]\n"
"ds_read_b64_tr_b16 a[0:1], v88 offset:8192\n"
"ds_read_b64_tr_b16 a[2:3], v88 offset:9216\n"
"s_add_u32 s71, %[q0], 0x183c00\n"
"buffer_load_dwordx4 v81, %[rsQ], s71 offen offset:1024 lds\n"
"s_waitcnt lgkmcnt(10)\n"
"v_mfma_f32_32x32x16_bf16 v[64:79], a[12:15], a[160:163], v[64:79]\n"
"ds_read_b64_tr_b16 a[4:5], v89 offset:8192\n"
"ds_read_b64_tr_b16 a[6:7], v89 offset:9216\n"
"s_add_u32 s82, s81, 0x4000\n"
"buffer_store_dwordx4 a[52:55], v93, %[rsW], s82 offen sc1\n"
"s_waitcnt lgkmcnt(10)\n"
"v_mfma_f32_32x32x16_bf16 v[16:31], a[16:19], a[164:167], v[16:31]\n"
"ds_read_b64_tr_b16 a[8:9], v90 offset:8192\n"
"ds_read_b64_tr_b16 a[10:11], v90 offset:9216\n"
"s_add_u32 s71, %[q0], 0x187800\n"
"buffer_load_dwordx4 v80, %[rsQ], s71 offen offset:2048 lds\n"
"s_waitcnt lgkmcnt(10)\n"
"v_mfma_f32_32x32x16_bf16 v[32:47], a[20:23], a[164:167], v[32:47]\n"
"ds_read_b64_tr_b16 a[12:13], v91 offset:8192\n"
"ds_read_b64_tr_b16 a[14:15], v91 offset:9216\n"
"s_waitcnt lgkmcnt(10)\n"
"v_mfma_f32_32x32x16_bf16 v[48:63], a[24:27], a[164:167], v[48:63]\n"
"ds_read_b64_tr_b16 a[16:17], v88 offset:12288\n"
"ds_read_b64_tr_b16 a[18:19], v88 offset:13312\n"
"s_add_u32 s71, %[q0], 0x18b400\n"
"buffer_load_dwordx4 v81, %[rsQ], s71 offen offset:3072 lds\n"
"s_waitcnt lgkmcnt(10)\n"
"v_mfma_f32_32x32x16_bf16 v[64:79], a[28:31], a[164:167], v[64:79]\n"
"ds_read_b64_tr_b16 a[20:21], v89 offset:12288\n"
"ds_read_b64_tr_b16 a[22:23], v89 offset:13312\n"
"s_add_u32 s82, s81, 0x8000\n"
"buffer_store_dwordx4 a[56:59], v93, %[rsW], s82 offen sc1\n"
"s_waitcnt lgkmcnt(10)\n"
"v_mfma_f32_32x32x16_bf16 v[16:31], a[0:3], a[168:171], v[16:31]\n"
"ds_read_b64_tr_b16 a[24:25], v90 offset:12288\n"
"ds_read_b64_tr_b16 a[26:27], v90 offset:13312\n"
"s_add_u32 m0, s74, 0x19000\n"
"s_add_u32 s71, %[q0], 0x190000\n"
"buffer_load_dwordx4 v80, %[rsQ], s71 offen lds\n"
"s_waitcnt lgkmcnt(10)\n"
"v_mfma_f32_32x32x16_bf16 v[32:47], a[4:7], a[168:171], v[32:47]\n"
"ds_read_b64_tr_b16 a[28:29], v91 offset:12288\n"
"ds_read_b64_tr_b16 a[30:31], v91 offset:13312\n"
"s_waitcnt lgkmcnt(10)\n"
"v_mfma_f32_32x32x16_bf16 v[48:63], a[8:11], a[168:171], v[48:63]\n"
"ds_read_b64_tr_b16 a[0:1], v84\n"
"ds_read_b64_tr_b16 a[2:3], v84 offset:2048\n"
"s_add_u32 s71, %[q0], 0x193c00\n"
"buffer_load_dwordx4 v81, %[rsQ], s71 offen offset:1024 lds\n"
"s_waitcnt lgkmcnt(10)\n"
"v_mfma_f32_32x32x16_bf16 v[64:79], a[12:15], a[168:171], v[64:79]\n"
"ds_read_b64_tr_b16 a[4:5], v85\n"
"ds_read_b64_tr_b16 a[6:7], v85 offset:2048\n"
"s_add_u32 s82, s81, 0xc000\n"
"buffer_store_dwordx4 a[60:63], v93, %[rsW], s82 offen sc1\n"
"s_waitcnt lgkmcnt(10)\n"
"v_mfma_f32_32x32x16_bf16 v[16:31], a[16:19], a[172:175], v[16:31]\n"
"ds_read_b64_tr_b16 a[8:9], v86\n"
"ds_read_b64_tr_b16 a[10:11], v86 offset:2048\n"
"s_add_u32 s71, %[q0], 0x197800\n"
"buffer_load_dwordx4 v80, %[rsQ], s71 offen offset:2048 lds\n"
"s_waitcnt lgkmcnt(10)\n"
"v_mfma_f32_32x32x16_bf16 v[32:47], a[20:23], a[172:175], v[32:47]\n"
"ds_read_b64_tr_b16 a[12:13], v87\n"
"ds_read_b64_tr_b16 a[14:15], v87 offset:2048\n"
"s_waitcnt lgkmcnt(10)\n"
"v_mfma_f32_32x32x16_bf16 v[48:63], a[24:27], a[172:175], v[48:63]\n"
"ds_read_b64_tr_b16 a[16:17], v84 offset:256\n"
"ds_read_b64_tr_b16 a[18:19], v84 offset:2304\n"
"s_add_u32 s71, %[q0], 0x19b400\n"
"buffer_load_dwordx4 v81, %[rsQ], s71 offen offset:3072 lds\n"
"s_waitcnt lgkmcnt(10)\n"
"v_mfma_f32_32x32x16_bf16 v[64:79], a[28:31], a[172:175], v[64:79]\n"
"ds_read_b64_tr_b16 a[20:21], v85 offset:256\n"
"ds_read_b64_tr_b16 a[22:23], v85 offset:2304\n"
"s_waitcnt vmcnt(30)\n"
"s_barrier\n"
"s_mov_b32 s77, 0xbfb8aa3b\n"
"s_waitcnt lgkmcnt(10)\n"
"v_mfma_f32_32x32x16_bf16 v[128:143], a[0:3], a[64:67], 0\n"
"ds_read_b64_tr_b16 a[24:25], v86 offset:256\n"
"ds_read_b64_tr_b16 a[26:27], v86 offset:2304\n"
"s_add_u32 m0, s74, 0x20000\n"
"s_add_u32 s71, %[q0], 0x200000\n"
"buffer_load_dwordx4 v80, %[rsQ], s71 offen lds\n"
"s_waitcnt lgkmcnt(10)\n"
"v_mfma_f32_32x32x16_bf16 v[144:159], a[4:7], a[64:67], 0\n"
"ds_read_b64_tr_b16 a[28:29], v87 offset:256\n"
"ds_read_b64_tr_b16 a[30:31], v87 offset:2304\n"
"s_waitcnt lgkmcnt(10)\n"
"v_mfma_f32_32x32x16_bf16 v[160:175], a[8:11], a[64:67], 0\n"
"ds_read_b64_tr_b16 a[0:1], v84 offset:8192\n"
"ds_read_b64_tr_b16 a[2:3], v84 offset:10240\n"
"s_waitcnt lgkmcnt(10)\n"
"v_mfma_f32_32x32x16_bf16 v[176:191], a[12:15], a[64:67], 0\n"
"ds_read_b64_tr_b16 a[4:5], v85 offset:8192\n"
"ds_read_b64_tr_b16 a[6:7], v85 offset:10240\n"
"v_mul_f32 v96, s77, v16\n"
"v_mul_f32 v97, s77, v17\n"
"s_waitcnt lgkmcnt(10)\n"
"v_mfma_f32_32x32x16_bf16 v[192:207], a[16:19], a[64:67], 0\n"
"ds_read_b64_tr_b16 a[8:9], v86 offset:8192\n"
"ds_read_b64_tr_b16 a[10:11], v86 offset:10240\n"
"s_add_u32 s71, %[q0], 0x203c00\n"
"buffer_load_dwordx4 v81, %[rsQ], s71 offen offset:1024 lds\n"
"s_waitcnt lgkmcnt(10)\n"
"v_mfma_f32_32x32x16_bf16 v[208:223], a[20:23], a[64:67], 0\n"
"ds_read_b64_tr_b16 a[12:13], v87 offset:8192\n"
"ds_read_b64_tr_b16 a[14:15], v87 offset:10240\n"
"v_exp_f32 v96, v96\n"
"v_mul_f32 v98, s77, v18\n"
"s_waitcnt lgkmcnt(10)\n"
"v_mfma_f32_32x32x16_bf16 v[224:239], a[24:27], a[64:67], 0\n"
"ds_read_b64_tr_b16 a[16:17], v84 offset:8448\n"
"ds_read_b64_tr_b16 a[18:19], v84 offset:10496\n"
"v_exp_f32 v97, v97\n"
"v_add_f32 v96, 1.0, v96\n"
"s_waitcnt lgkmcnt(10)\n"
"v_mfma_f32_32x32x16_bf16 v[240:255], a[28:31], a[64:67], 0\n"
"ds_read_b64_tr_b16 a[20:21], v85 offset:8448\n"
"ds_read_b64_tr_b16 a[22:23], v85 offset:10496\n"
"v_mul_f32 v99, s77, v19\n"
"v_exp_f32 v98, v98\n"
"s_waitcnt lgkmcnt(10)\n"
"v_mfma_f32_32x32x16_bf16 v[128:143], a[0:3], a[68:71], v[128:143]\n"
"ds_read_b64_tr_b16 a[24:25], v86 offset:8448\n"
"ds_read_b64_tr_b16 a[26:27], v86 offset:10496\n"
"s_add_u32 s71, %[q0], 0x207800\n"
"buffer_load_dwordx4 v80, %[rsQ], s71 offen offset:2048 lds\n"
"v_add_f32 v97, 1.0, v97\n"
"s_waitcnt lgkmcnt(10)\n"
"v_mfma_f32_32x32x16_bf16 v[144:159], a[4:7], a[68:71], v[144:159]\n"
"ds_read_b64_tr_b16 a[28:29], v87 offset:8448\n"
"ds_read_b64_tr_b16 a[30:31], v87 offset:10496\n"
"v_rcp_f32 v96, v96\n"
"v_mul_f32 v100, s77, v20\n"
"s_waitcnt lgkmcnt(10)\n"
"v_mfma_f32_32x32x16_bf16 v[160:175], a[8:11], a[68:71], v[160:175]\n"
"ds_read_b64_tr_b16 a[0:1], v84 offset:16384\n"
"ds_read_b64_tr_b16 a[2:3], v84 offset:18432\n"
"s_add_u32 s82, %[xoff], 0\n"
"buffer_load_dwordx4 a[64:67], v1, %[rsW], s82 offen sc1\n"
"v_exp_f32 v99, v99\n"
"s_waitcnt lgkmcnt(10)\n"
"v_mfma_f32_32x32x16_bf16 v[176:191], a[12:15], a[68:71], v[176:191]\n"
"ds_read_b64_tr_b16 a[4:5], v85 offset:16384\n"
"ds_read_b64_tr_b16 a[6:7], v85 offset:18432\n"
"v_add_f32 v98, 1.0, v98\n"
"v_rcp_f32 v97, v97\n"
"s_waitcnt lgkmcnt(10)\n"
"v_mfma_f32_32x32x16_bf16 v[192:207], a[16:19], a[68:71], v[192:207]\n"
"ds_read_b64_tr_b16 a[8:9], v86 offset:16384\n"
"ds_read_b64_tr_b16 a[10:11], v86 offset:18432\n"
"s_add_u32 s71, %[q0], 0x20b400\n"
"buffer_load_dwordx4 v81, %[rsQ], s71 offen offset:3072 lds\n"
"v_mul_f32 v96, v96, v16\n"
"s_waitcnt lgkmcnt(10)\n"
"v_mfma_f32_32x32x16_bf16 v[208:223], a[20:23], a[68:71], v[208:223]\n"
"ds_read_b64_tr_b16 a[12:13], v87 offset:16384\n"
"ds_read_b64_tr_b16 a[14:15], v87 offset:18432\n"
"v_mul_f32 v101, s77, v21\n"
"v_exp_f32 v100, v100\n"
"s_waitcnt lgkmcnt(10)\n"
"v_mfma_f32_32x32x16_bf16 v[224:239], a[24:27], a[68:71], v[224:239]\n"
"ds_read_b64_tr_b16 a[16:17], v84 offset:16640\n"
"ds_read_b64_tr_b16 a[18:19], v84 offset:18688\n"
"v_add_f32 v99, 1.0, v99\n"
"v_rcp_f32 v98, v98\n"
"s_waitcnt lgkmcnt(10)\n"
"v_mfma_f32_32x32x16_bf16 v[240:255], a[28:31], a[68:71], v[240:255]\n"
"ds_read_b64_tr_b16 a[20:21], v85 offset:16640\n"
"ds_read_b64_tr_b16 a[22:23], v85 offset:18688\n"
"v_mul_f32 v97, v97, v17\n"
"v_mul_f32 v102, s77, v22\n"
"s_waitcnt lgkmcnt(10)\n"
"v_mfma_f32_32x32x16_bf16 v[128:143], a[0:3], a[72:75], v[128:143]\n"
"ds_read_b64_tr_b16 a[24:25], v86 offset:16640\n"
"ds_read_b64_tr_b16 a[26:27], v86 offset:18688\n"
"s_add_u32 m0, s74, 0x21000\n"
"s_add_u32 s71, %[q0], 0x210000\n"
"buffer_load_dwordx4 v80, %[rsQ], s71 offen lds\n"
"s_waitcnt lgkmcnt(10)\n"
"v_mfma_f32_32x32x16_bf16 v[144:159], a[4:7], a[72:75], v[144:159]\n"
"ds_read_b64_tr_b16 a[28:29], v87 offset:16640\n"
"ds_read_b64_tr_b16 a[30:31], v87 offset:18688\n"
"v_exp_f32 v101, v101\n"
"v_add_f32 v100, 1.0, v100\n"
"s_waitcnt lgkmcnt(10)\n"
"v_mfma_f32_32x32x16_bf16 v[160:175], a[8:11], a[72:75], v[160:175]\n"
"ds_read_b64_tr_b16 a[0:1], v84 offset:24576\n"
"ds_read_b64_tr_b16 a[2:3], v84 offset:26624\n"
"s_add_u32 s82, %[xoff], 0\n"
"buffer_load_dwordx4 a[68:71], v1, %[rsW], s82 offen offset:1024 sc1\n"
"v_rcp_f32 v99, v99\n"
"s_waitcnt lgkmcnt(10)\n"
"v_mfma_f32_32x32x16_bf16 v[176:191], a[12:15], a[72:75], v[176:191]\n"
"ds_read_b64_tr_b16 a[4:5], v85 offset:24576\n"
"ds_read_b64_tr_b16 a[6:7], v85 offset:26624\n"
"v_mul_f32 v98, v98, v18\n"
"v_cvt_pk_bf16_f32 v104, v96, v97\n"
"v_mul_f32 v103, s77, v23\n"
"s_waitcnt lgkmcnt(10)\n"
"v_mfma_f32_32x32x16_bf16 v[192:207], a[16:19], a[72:75], v[192:207]\n"
"ds_read_b64_tr_b16 a[8:9], v86 offset:24576\n"
"ds_read_b64_tr_b16 a[10:11], v86 offset:26624\n"
"s_add_u32 s71, %[q0], 0x213c00\n"
"buffer_load_dwordx4 v81, %[rsQ], s71 offen offset:1024 lds\n"
"s_waitcnt lgkmcnt(10)\n"
"v_mfma_f32_32x32x16_bf16 v[208:223], a[20:23], a[72:75], v[208:223]\n"
"ds_read_b64_tr_b16 a[12:13], v87 offset:24576\n"
"ds_read_b64_tr_b16 a[14:15], v87 offset:26624\n"
"v_exp_f32 v102, v102\n"
"v_add_f32 v101, 1.0, v101\n"
"s_waitcnt lgkmcnt(10)\n"
"v_mfma_f32_32x32x16_bf16 v[224:239], a[24:27], a[72:75], v[224:239]\n"
"ds_read_b64_tr_b16 a[16:17], v84 offset:24832\n"
"ds_read_b64_tr_b16 a[18:19], v84 offset:26880\n"
"v_rcp_f32 v100, v100\n"
"v_mul_f32 v99, v99, v19\n"
"s_waitcnt lgkmcnt(10)\n"
"v_mfma_f32_32x32x16_bf16 v[240:255], a[28:31], a[72:75], v[240:255]\n"
"ds_read_b64_tr_b16 a[20:21], v85 offset:24832\n"
"ds_read_b64_tr_b16 a[22:23], v85 offset:26880\n"
"v_mul_f32 v96, s77, v24\n"
"v_exp_f32 v103, v103\n"
"s_waitcnt lgkmcnt(10)\n"
"v_mfma_f32_32x32x16_bf16 v[128:143], a[0:3], a[76:79], v[128:143]\n"
"ds_read_b64_tr_b16 a[24:25], v86 offset:24832\n"
"ds_read_b64_tr_b16 a[26:27], v86 offset:26880\n"
"s_add_u32 s71, %[q0], 0x217800\n"
"buffer_load_dwordx4 v80, %[rsQ], s71 offen offset:2048 lds\n"
"v_add_f32 v102, 1.0, v102\n"
"s_waitcnt lgkmcnt(10)\n"
"v_mfma_f32_32x32x16_bf16 v[144:159], a[4:7], a[76:79], v[144:159]\n"
"ds_read_b64_tr_b16 a[28:29], v87 offset:24832\n"
"ds_read_b64_tr_b16 a[30:31], v87 offset:26880\n"
"v_rcp_f32 v101, v101\n"
"v_mul_f32 v100, v100, v20\n"
"s_waitcnt lgkmcnt(10)\n"
"v_mfma_f32_32x32x16_bf16 v[160:175], a[8:11], a[76:79], v[160:175]\n"
"ds_read_b64_tr_b16 a[0:1], v84 offset:32768\n"
"ds_read_b64_tr_b16 a[2:3], v84 offset:34816\n"
"s_add_u32 s82, %[xoff], 0\n"
"buffer_load_dwordx4 a[72:75], v1, %[rsW], s82 offen offset:2048 sc1\n"
"v_cvt_pk_bf16_f32 v105, v98, v99\n"
"v_mul_f32 v97, s77, v25\n"
"s_waitcnt lgkmcnt(10)\n"
"v_mfma_f32_32x32x16_bf16 v[176:191], a[12:15], a[76:79], v[176:191]\n"
"ds_read_b64_tr_b16 a[4:5], v85 offset:32768\n"
"ds_read_b64_tr_b16 a[6:7], v85 offset:34816\n"
"v_exp_f32 v96, v96\n"
"v_add_f32 v103, 1.0, v103\n"
"s_waitcnt lgkmcnt(10)\n"
"v_mfma_f32_32x32x16_bf16 v[192:207], a[16:19], a[76:79], v[192:207]\n"
"ds_read_b64_tr_b16 a[8:9], v86 offset:32768\n"
"ds_read_b64_tr_b16 a[10:11], v86 offset:34816\n"
"s_add_u32 s71, %[q0], 0x21b400\n"
"buffer_load_dwordx4 v81, %[rsQ], s71 offen offset:3072 lds\n"
"s_waitcnt lgkmcnt(10)\n"
"v_mfma_f32_32x32x16_bf16 v[208:223], a[20:23], a[76:79], v[208:223]\n"
"ds_read_b64_tr_b16 a[12:13], v87 offset:32768\n"
"ds_read_b64_tr_b16 a[14:15], v87 offset:34816\n"
"v_rcp_f32 v102, v102\n"
"v_mul_f32 v101, v101, v21\n"
"s_waitcnt lgkmcnt(10)\n"
"v_mfma_f32_32x32x16_bf16 v[224:239], a[24:27], a[76:79], v[224:239]\n"
"ds_read_b64_tr_b16 a[16:17], v84 offset:33024\n"
"ds_read_b64_tr_b16 a[18:19], v84 offset:35072\n"
"v_mul_f32 v98, s77, v26\n"
"v_exp_f32 v97, v97\n"
"s_waitcnt lgkmcnt(10)\n"
"v_mfma_f32_32x32x16_bf16 v[240:255], a[28:31], a[76:79], v[240:255]\n"
"ds_read_b64_tr_b16 a[20:21], v85 offset:33024\n"
"ds_read_b64_tr_b16 a[22:23], v85 offset:35072\n"
"v_add_f32 v96, 1.0, v96\n"
"v_rcp_f32 v103, v103\n"
"s_waitcnt vmcnt(26)\n"
"s_waitcnt vmcnt(13)\n"
"buffer_store_dwordx2 v[12:13], off, %[rsW], %[kvf] sc1\n"
"s_barrier\n"
"s_waitcnt lgkmcnt(10)\n"
"v_mfma_f32_32x32x16_bf16 v[128:143], a[0:3], a[80:83], v[128:143]\n"
"ds_read_b64_tr_b16 a[24:25], v86 offset:33024\n"
"ds_read_b64_tr_b16 a[26:27], v86 offset:35072\n"
"s_add_u32 m0, s74, 0\n"
"s_add_u32 s71, %[q0], 0x280000\n"
"buffer_load_dwordx4 v80, %[rsQ], s71 offen lds\n"
"v_mul_f32 v102, v102, v22\n"
"s_waitcnt lgkmcnt(10)\n"
"v_mfma_f32_32x32x16_bf16 v[144:159], a[4:7], a[80:83], v[144:159]\n"
"ds_read_b64_tr_b16 a[28:29], v87 offset:33024\n"
"ds_read_b64_tr_b16 a[30:31], v87 offset:35072\n"
"v_cvt_pk_bf16_f32 v106, v100, v101\n"
"v_mul_f32 v99, s77, v27\n"
"s_waitcnt lgkmcnt(10)\n"
"v_mfma_f32_32x32x16_bf16 v[160:175], a[8:11], a[80:83], v[160:175]\n"
"ds_read_b64_tr_b16 a[0:1], v84 offset:40960\n"
"ds_read_b64_tr_b16 a[2:3], v84 offset:43008\n"
"s_add_u32 s82, %[xoff], 0\n"
"buffer_load_dwordx4 a[76:79], v1, %[rsW], s82 offen offset:3072 sc1\n"
"v_exp_f32 v98, v98\n"
"s_waitcnt lgkmcnt(10)\n"
"v_mfma_f32_32x32x16_bf16 v[176:191], a[12:15], a[80:83], v[176:191]\n"
"ds_read_b64_tr_b16 a[4:5], v85 offset:40960\n"
"ds_read_b64_tr_b16 a[6:7], v85 offset:43008\n"
"v_add_f32 v97, 1.0, v97\n"
"v_rcp_f32 v96, v96\n"
"s_waitcnt lgkmcnt(10)\n"
"v_mfma_f32_32x32x16_bf16 v[192:207], a[16:19], a[80:83], v[192:207]\n"
"ds_read_b64_tr_b16 a[8:9], v86 offset:40960\n"
"ds_read_b64_tr_b16 a[10:11], v86 offset:43008\n"
"s_add_u32 s71, %[q0], 0x283c00\n"
"buffer_load_dwordx4 v81, %[rsQ], s71 offen offset:1024 lds\n"
"v_mul_f32 v103, v103, v23\n"
"s_waitcnt lgkmcnt(10)\n"
"v_mfma_f32_32x32x16_bf16 v[208:223], a[20:23], a[80:83], v[208:223]\n"
"ds_read_b64_tr_b16 a[12:13], v87 offset:40960\n"
"ds_read_b64_tr_b16 a[14:15], v87 offset:43008\n"
"v_mul_f32 v100, s77, v28\n"
"v_exp_f32 v99, v99\n"
"s_waitcnt lgkmcnt(10)\n"
"v_mfma_f32_32x32x16_bf16 v[224:239], a[24:27], a[80:83], v[224:239]\n"
"ds_read_b64_tr_b16 a[16:17], v84 offset:41216\n"
"ds_read_b64_tr_b16 a[18:19], v84 offset:43264\n"
"v_add_f32 v98, 1.0, v98\n"
"v_rcp_f32 v97, v97\n"
"s_waitcnt lgkmcnt(10)\n"
"v_mfma_f32_32x32x16_bf16 v[240:255], a[28:31], a[80:83], v[240:255]\n"
"ds_read_b64_tr_b16 a[20:21], v85 offset:41216\n"
"ds_read_b64_tr_b16 a[22:23], v85 offset:43264\n"
"v_mul_f32 v96, v96, v24\n"
"v_cvt_pk_bf16_f32 v107, v102, v103\n"
"s_waitcnt lgkmcnt(10)\n"
"v_mfma_f32_32x32x16_bf16 v[128:143], a[0:3], a[84:87], v[128:143]\n"
"ds_read_b64_tr_b16 a[24:25], v86 offset:41216\n"
"ds_read_b64_tr_b16 a[26:27], v86 offset:43264\n"
"s_add_u32 s71, %[q0], 0x287800\n"
"buffer_load_dwordx4 v80, %[rsQ], s71 offen offset:2048 lds\n"
"s_waitcnt lgkmcnt(10)\n"
"v_mfma_f32_32x32x16_bf16 v[144:159], a[4:7], a[84:87], v[144:159]\n"
"ds_read_b64_tr_b16 a[28:29], v87 offset:41216\n"
"ds_read_b64_tr_b16 a[30:31], v87 offset:43264\n"
"s_add_u32 s68, %[hoff], 0\n"
"buffer_store_dwordx4 v[104:107], v1, %[rsW], s68 offen sc1\n"
"v_mul_f32 v101, s77, v29\n"
"s_waitcnt lgkmcnt(10)\n"
"v_mfma_f32_32x32x16_bf16 v[160:175], a[8:11], a[84:87], v[160:175]\n"
"ds_read_b64_tr_b16 a[0:1], v84 offset:49152\n"
"ds_read_b64_tr_b16 a[2:3], v84 offset:51200\n"
"s_add_u32 s82, %[xoff], 0x1000\n"
"buffer_load_dwordx4 a[80:83], v1, %[rsW], s82 offen sc1\n"
"v_exp_f32 v100, v100\n"
"s_waitcnt lgkmcnt(10)\n"
"v_mfma_f32_32x32x16_bf16 v[176:191], a[12:15], a[84:87], v[176:191]\n"
"ds_read_b64_tr_b16 a[4:5], v85 offset:49152\n"
"ds_read_b64_tr_b16 a[6:7], v85 offset:51200\n"
"v_add_f32 v99, 1.0, v99\n"
"v_rcp_f32 v98, v98\n"
"s_waitcnt lgkmcnt(10)\n"
"v_mfma_f32_32x32x16_bf16 v[192:207], a[16:19], a[84:87], v[192:207]\n"
"ds_read_b64_tr_b16 a[8:9], v86 offset:49152\n"
"ds_read_b64_tr_b16 a[10:11], v86 offset:51200\n"
"s_add_u32 s71, %[q0], 0x28b400\n"
"buffer_load_dwordx4 v81, %[rsQ], s71 offen offset:3072 lds\n"
"v_mul_f32 v97, v97, v25\n"
"s_waitcnt lgkmcnt(10)\n"
"v_mfma_f32_32x32x16_bf16 v[208:223], a[20:23], a[84:87], v[208:223]\n"
"ds_read_b64_tr_b16 a[12:13], v87 offset:49152\n"
"ds_read_b64_tr_b16 a[14:15], v87 offset:51200\n"
"v_mul_f32 v102, s77, v30\n"
"v_exp_f32 v101, v101\n"
"s_waitcnt lgkmcnt(10)\n"
"v_mfma_f32_32x32x16_bf16 v[224:239], a[24:27], a[84:87], v[224:239]\n"
"ds_read_b64_tr_b16 a[16:17], v84 offset:49408\n"
"ds_read_b64_tr_b16 a[18:19], v84 offset:51456\n"
"v_add_f32 v100, 1.0, v100\n"
"v_rcp_f32 v99, v99\n"
"s_waitcnt lgkmcnt(10)\n"
"v_mfma_f32_32x32x16_bf16 v[240:255], a[28:31], a[84:87], v[240:255]\n"
"ds_read_b64_tr_b16 a[20:21], v85 offset:49408\n"
"ds_read_b64_tr_b16 a[22:23], v85 offset:51456\n"
"v_mul_f32 v98, v98, v26\n"
"v_cvt_pk_bf16_f32 v108, v96, v97\n"
"v_mul_f32 v103, s77, v31\n"
"s_waitcnt lgkmcnt(10)\n"
"v_mfma_f32_32x32x16_bf16 v[128:143], a[0:3], a[88:91], v[128:143]\n"
"ds_read_b64_tr_b16 a[24:25], v86 offset:49408\n"
"ds_read_b64_tr_b16 a[26:27], v86 offset:51456\n"
"s_add_u32 m0, s74, 0x1000\n"
"s_add_u32 s71, %[q0], 0x290000\n"
"buffer_load_dwordx4 v80, %[rsQ], s71 offen lds\n"
"s_waitcnt lgkmcnt(10)\n"
"v_mfma_f32_32x32x16_bf16 v[144:159], a[4:7], a[88:91], v[144:159]\n"
"ds_read_b64_tr_b16 a[28:29], v87 offset:49408\n"
"ds_read_b64_tr_b16 a[30:31], v87 offset:51456\n"
"v_exp_f32 v102, v102\n"
"v_add_f32 v101, 1.0, v101\n"
"s_waitcnt lgkmcnt(10)\n"
"v_mfma_f32_32x32x16_bf16 v[160:175], a[8:11], a[88:91], v[160:175]\n"
"ds_read_b64_tr_b16 a[0:1], v84 offset:57344\n"
"ds_read_b64_tr_b16 a[2:3], v84 offset:59392\n"
"s_add_u32 s82, %[xoff], 0x1000\n"
"buffer_load_dwordx4 a[84:87], v1, %[rsW], s82 offen offset:1024 sc1\n"
"v_rcp_f32 v100, v100\n"
"s_waitcnt lgkmcnt(10)\n"
"v_mfma_f32_32x32x16_bf16 v[176:191], a[12:15], a[88:91], v[176:191]\n"
"ds_read_b64_tr_b16 a[4:5], v85 offset:57344\n"
"ds_read_b64_tr_b16 a[6:7], v85 offset:59392\n"
"v_mul_f32 v99, v99, v27\n"
"v_mul_f32 v96, s77, v32\n"
"s_waitcnt lgkmcnt(10)\n"
"v_mfma_f32_32x32x16_bf16 v[192:207], a[16:19], a[88:91], v[192:207]\n"
"ds_read_b64_tr_b16 a[8:9], v86 offset:57344\n"
"ds_read_b64_tr_b16 a[10:11], v86 offset:59392\n"
"s_add_u32 s71, %[q0], 0x293c00\n"
"buffer_load_dwordx4 v81, %[rsQ], s71 offen offset:1024 lds\n"
"s_waitcnt lgkmcnt(10)\n"
"v_mfma_f32_32x32x16_bf16 v[208:223], a[20:23], a[88:91], v[208:223]\n"
"ds_read_b64_tr_b16 a[12:13], v87 offset:57344\n"
"ds_read_b64_tr_b16 a[14:15], v87 offset:59392\n"
"v_exp_f32 v103, v103\n"
"v_add_f32 v102, 1.0, v102\n"
"s_waitcnt lgkmcnt(10)\n"
"v_mfma_f32_32x32x16_bf16 v[224:239], a[24:27], a[88:91], v[224:239]\n"
"ds_read_b64_tr_b16 a[16:17], v84 offset:57600\n"
"ds_read_b64_tr_b16 a[18:19], v84 offset:59648\n"
"v_rcp_f32 v101, v101\n"
"v_mul_f32 v100, v100, v28\n"
"s_waitcnt lgkmcnt(10)\n"
"v_mfma_f32_32x32x16_bf16 v[240:255], a[28:31], a[88:91], v[240:255]\n"
"ds_read_b64_tr_b16 a[20:21], v85 offset:57600\n"
"ds_read_b64_tr_b16 a[22:23], v85 offset:59648\n"
"v_cvt_pk_bf16_f32 v109, v98, v99\n"
"v_mul_f32 v97, s77, v33\n"
"s_waitcnt lgkmcnt(10)\n"
"v_mfma_f32_32x32x16_bf16 v[128:143], a[0:3], a[92:95], v[128:143]\n"
"ds_read_b64_tr_b16 a[24:25], v86 offset:57600\n"
"ds_read_b64_tr_b16 a[26:27], v86 offset:59648\n"
"s_add_u32 s71, %[q0], 0x297800\n"
"buffer_load_dwordx4 v80, %[rsQ], s71 offen offset:2048 lds\n"
"s_waitcnt lgkmcnt(10)\n"
"v_mfma_f32_32x32x16_bf16 v[144:159], a[4:7], a[92:95], v[144:159]\n"
"ds_read_b64_tr_b16 a[28:29], v87 offset:57600\n"
"ds_read_b64_tr_b16 a[30:31], v87 offset:59648\n"
"v_exp_f32 v96, v96\n"
"v_add_f32 v103, 1.0, v103\n"
"s_waitcnt lgkmcnt(10)\n"
"v_mfma_f32_32x32x16_bf16 v[160:175], a[8:11], a[92:95], v[160:175]\n"
"v_add_u32 v84, 0x10000, v84\n"
"ds_read_b64_tr_b16 a[0:1], v84\n"
"ds_read_b64_tr_b16 a[2:3], v84 offset:2048\n"
"s_add_u32 s82, %[xoff], 0x1000\n"
"buffer_load_dwordx4 a[88:91], v1, %[rsW], s82 offen offset:2048 sc1\n"
"v_rcp_f32 v102, v102\n"
"s_waitcnt lgkmcnt(10)\n"
"v_mfma_f32_32x32x16_bf16 v[176:191], a[12:15], a[92:95], v[176:191]\n"
"v_add_u32 v85, 0x10000, v85\n"
"ds_read_b64_tr_b16 a[4:5], v85\n"
"ds_read_b64_tr_b16 a[6:7], v85 offset:2048\n"
"v_mul_f32 v101, v101, v29\n"
"v_mul_f32 v98, s77, v34\n"
"s_waitcnt lgkmcnt(10)\n"
"v_mfma_f32_32x32x16_bf16 v[192:207], a[16:19], a[92:95], v[192:207]\n"
"v_add_u32 v86, 0x10000, v86\n"
"ds_read_b64_tr_b16 a[8:9], v86\n"
"ds_read_b64_tr_b16 a[10:11], v86 offset:2048\n"
"s_add_u32 s71, %[q0], 0x29b400\n"
"buffer_load_dwordx4 v81, %[rsQ], s71 offen offset:3072 lds\n"
"s_waitcnt lgkmcnt(10)\n"
"v_mfma_f32_32x32x16_bf16 v[208:223], a[20:23], a[92:95], v[208:223]\n"
"v_add_u32 v87, 0x10000, v87\n"
"ds_read_b64_tr_b16 a[12:13], v87\n"
"ds_read_b64_tr_b16 a[14:15], v87 offset:2048\n"
"v_exp_f32 v97, v97\n"
"v_add_f32 v96, 1.0, v96\n"
"s_waitcnt lgkmcnt(10)\n"
"v_mfma_f32_32x32x16_bf16 v[224:239], a[24:27], a[92:95], v[224:239]\n"
"ds_read_b64_tr_b16 a[16:17], v84 offset:256\n"
"ds_read_b64_tr_b16 a[18:19], v84 offset:2304\n"
"v_rcp_f32 v103, v103\n"
"v_mul_f32 v102, v102, v30\n"
"s_waitcnt lgkmcnt(10)\n"
"v_mfma_f32_32x32x16_bf16 v[240:255], a[28:31], a[92:95], v[240:255]\n"
"ds_read_b64_tr_b16 a[20:21], v85 offset:256\n"
"ds_read_b64_tr_b16 a[22:23], v85 offset:2304\n"
"v_cvt_pk_bf16_f32 v110, v100, v101\n"
"v_mul_f32 v99, s77, v35\n"
"s_waitcnt vmcnt(25)\n"
"s_barrier\n"
"s_waitcnt lgkmcnt(10)\n"
"v_mfma_f32_32x32x16_bf16 v[128:143], a[0:3], a[96:99], v[128:143]\n"
"ds_read_b64_tr_b16 a[24:25], v86 offset:256\n"
"ds_read_b64_tr_b16 a[26:27], v86 offset:2304\n"
"s_add_u32 m0, s74, 0x8000\n"
"s_add_u32 s71, %[q0], 0x300000\n"
"buffer_load_dwordx4 v80, %[rsQ], s71 offen lds\n"
"s_waitcnt lgkmcnt(10)\n"
"v_mfma_f32_32x32x16_bf16 v[144:159], a[4:7], a[96:99], v[144:159]\n"
"ds_read_b64_tr_b16 a[28:29], v87 offset:256\n"
"ds_read_b64_tr_b16 a[30:31], v87 offset:2304\n"
"v_exp_f32 v98, v98\n"
"v_add_f32 v97, 1.0, v97\n"
"s_waitcnt lgkmcnt(10)\n"
"v_mfma_f32_32x32x16_bf16 v[160:175], a[8:11], a[96:99], v[160:175]\n"
"ds_read_b64_tr_b16 a[0:1], v84 offset:8192\n"
"ds_read_b64_tr_b16 a[2:3], v84 offset:10240\n"
"s_add_u32 s82, %[xoff], 0x1000\n"
"buffer_load_dwordx4 a[92:95], v1, %[rsW], s82 offen offset:3072 sc1\n"
"v_rcp_f32 v96, v96\n"
"s_waitcnt lgkmcnt(10)\n"
"v_mfma_f32_32x32x16_bf16 v[176:191], a[12:15], a[96:99], v[176:191]\n"
"ds_read_b64_tr_b16 a[4:5], v85 offset:8192\n"
"ds_read_b64_tr_b16 a[6:7], v85 offset:10240\n"
"v_mul_f32 v103, v103, v31\n"
"v_mul_f32 v100, s77, v36\n"
"s_waitcnt lgkmcnt(10)\n"
"v_mfma_f32_32x32x16_bf16 v[192:207], a[16:19], a[96:99], v[192:207]\n"
"ds_read_b64_tr_b16 a[8:9], v86 offset:8192\n"
"ds_read_b64_tr_b16 a[10:11], v86 offset:10240\n"
"s_add_u32 s71, %[q0], 0x303c00\n"
"buffer_load_dwordx4 v81, %[rsQ], s71 offen offset:1024 lds\n"
"s_waitcnt lgkmcnt(10)\n"
"v_mfma_f32_32x32x16_bf16 v[208:223], a[20:23], a[96:99], v[208:223]\n"
"ds_read_b64_tr_b16 a[12:13], v87 offset:8192\n"
"ds_read_b64_tr_b16 a[14:15], v87 offset:10240\n"
"v_exp_f32 v99, v99\n"
"v_add_f32 v98, 1.0, v98\n"
"s_waitcnt lgkmcnt(10)\n"
"v_mfma_f32_32x32x16_bf16 v[224:239], a[24:27], a[96:99], v[224:239]\n"
"ds_read_b64_tr_b16 a[16:17], v84 offset:8448\n"
"ds_read_b64_tr_b16 a[18:19], v84 offset:10496\n"
"v_rcp_f32 v97, v97\n"
"v_mul_f32 v96, v96, v32\n"
"s_waitcnt lgkmcnt(10)\n"
"v_mfma_f32_32x32x16_bf16 v[240:255], a[28:31], a[96:99], v[240:255]\n"
"ds_read_b64_tr_b16 a[20:21], v85 offset:8448\n"
"ds_read_b64_tr_b16 a[22:23], v85 offset:10496\n"
"v_cvt_pk_bf16_f32 v111, v102, v103\n"
"s_add_u32 s68, %[hoff], 0\n"
"buffer_store_dwordx4 v[108:111], v1, %[rsW], s68 offen offset:1024 sc1\n"
"s_waitcnt lgkmcnt(10)\n"
"v_mfma_f32_32x32x16_bf16 v[128:143], a[0:3], a[100:103], v[128:143]\n"
"ds_read_b64_tr_b16 a[24:25], v86 offset:8448\n"
"ds_read_b64_tr_b16 a[26:27], v86 offset:10496\n"
"s_add_u32 s71, %[q0], 0x307800\n"
"buffer_load_dwordx4 v80, %[rsQ], s71 offen offset:2048 lds\n"
"v_mul_f32 v101, s77, v37\n"
"s_waitcnt lgkmcnt(10)\n"
"v_mfma_f32_32x32x16_bf16 v[144:159], a[4:7], a[100:103], v[144:159]\n"
"ds_read_b64_tr_b16 a[28:29], v87 offset:8448\n"
"ds_read_b64_tr_b16 a[30:31], v87 offset:10496\n"
"v_exp_f32 v100, v100\n"
"v_add_f32 v99, 1.0, v99\n"
"s_waitcnt lgkmcnt(10)\n"
"v_mfma_f32_32x32x16_bf16 v[160:175], a[8:11], a[100:103], v[160:175]\n"
"ds_read_b64_tr_b16 a[0:1], v84 offset:16384\n"
"ds_read_b64_tr_b16 a[2:3], v84 offset:18432\n"
"s_add_u32 s82, %[xoff], 0x2000\n"
"buffer_load_dwordx4 a[96:99], v1, %[rsW], s82 offen sc1\n"
"v_rcp_f32 v98, v98\n"
"s_waitcnt lgkmcnt(10)\n"
"v_mfma_f32_32x32x16_bf16 v[176:191], a[12:15], a[100:103], v[176:191]\n"
"ds_read_b64_tr_b16 a[4:5], v85 offset:16384\n"
"ds_read_b64_tr_b16 a[6:7], v85 offset:18432\n"
"v_mul_f32 v97, v97, v33\n"
"v_mul_f32 v102, s77, v38\n"
"s_waitcnt lgkmcnt(10)\n"
"v_mfma_f32_32x32x16_bf16 v[192:207], a[16:19], a[100:103], v[192:207]\n"
"ds_read_b64_tr_b16 a[8:9], v86 offset:16384\n"
"ds_read_b64_tr_b16 a[10:11], v86 offset:18432\n"
"s_add_u32 s71, %[q0], 0x30b400\n"
"buffer_load_dwordx4 v81, %[rsQ], s71 offen offset:3072 lds\n"
"s_waitcnt lgkmcnt(10)\n"
"v_mfma_f32_32x32x16_bf16 v[208:223], a[20:23], a[100:103], v[208:223]\n"
"ds_read_b64_tr_b16 a[12:13], v87 offset:16384\n"
"ds_read_b64_tr_b16 a[14:15], v87 offset:18432\n"
"v_exp_f32 v101, v101\n"
"v_add_f32 v100, 1.0, v100\n"
"s_waitcnt lgkmcnt(10)\n"
"v_mfma_f32_32x32x16_bf16 v[224:239], a[24:27], a[100:103], v[224:239]\n"
"ds_read_b64_tr_b16 a[16:17], v84 offset:16640\n"
"ds_read_b64_tr_b16 a[18:19], v84 offset:18688\n"
"v_rcp_f32 v99, v99\n"
"v_mul_f32 v98, v98, v34\n"
"s_waitcnt lgkmcnt(10)\n"
"v_mfma_f32_32x32x16_bf16 v[240:255], a[28:31], a[100:103], v[240:255]\n"
"ds_read_b64_tr_b16 a[20:21], v85 offset:16640\n"
"ds_read_b64_tr_b16 a[22:23], v85 offset:18688\n"
"v_cvt_pk_bf16_f32 v104, v96, v97\n"
"v_mul_f32 v103, s77, v39\n"
"s_waitcnt lgkmcnt(10)\n"
"v_mfma_f32_32x32x16_bf16 v[128:143], a[0:3], a[104:107], v[128:143]\n"
"ds_read_b64_tr_b16 a[24:25], v86 offset:16640\n"
"ds_read_b64_tr_b16 a[26:27], v86 offset:18688\n"
"s_add_u32 m0, s74, 0x9000\n"
"s_add_u32 s71, %[q0], 0x310000\n"
"buffer_load_dwordx4 v80, %[rsQ], s71 offen lds\n"
"s_waitcnt lgkmcnt(10)\n"
"v_mfma_f32_32x32x16_bf16 v[144:159], a[4:7], a[104:107], v[144:159]\n"
"ds_read_b64_tr_b16 a[28:29], v87 offset:16640\n"
"ds_read_b64_tr_b16 a[30:31], v87 offset:18688\n"
"v_exp_f32 v102, v102\n"
"v_add_f32 v101, 1.0, v101\n"
"s_waitcnt lgkmcnt(10)\n"
"v_mfma_f32_32x32x16_bf16 v[160:175], a[8:11], a[104:107], v[160:175]\n"
"ds_read_b64_tr_b16 a[0:1], v84 offset:24576\n"
"ds_read_b64_tr_b16 a[2:3], v84 offset:26624\n"
"s_add_u32 s82, %[xoff], 0x2000\n"
"buffer_load_dwordx4 a[100:103], v1, %[rsW], s82 offen offset:1024 sc1\n"
"v_rcp_f32 v100, v100\n"
"s_waitcnt lgkmcnt(10)\n"
"v_mfma_f32_32x32x16_bf16 v[176:191], a[12:15], a[104:107], v[176:191]\n"
"ds_read_b64_tr_b16 a[4:5], v85 offset:24576\n"
"ds_read_b64_tr_b16 a[6:7], v85 offset:26624\n"
"v_mul_f32 v99, v99, v35\n"
"v_mul_f32 v96, s77, v40\n"
"s_waitcnt lgkmcnt(10)\n"
"v_mfma_f32_32x32x16_bf16 v[192:207], a[16:19], a[104:107], v[192:207]\n"
"ds_read_b64_tr_b16 a[8:9], v86 offset:24576\n"
"ds_read_b64_tr_b16 a[10:11], v86 offset:26624\n"
"s_add_u32 s71, %[q0], 0x313c00\n"
"buffer_load_dwordx4 v81, %[rsQ], s71 offen offset:1024 lds\n"
"s_waitcnt lgkmcnt(10)\n"
"v_mfma_f32_32x32x16_bf16 v[208:223], a[20:23], a[104:107], v[208:223]\n"
"ds_read_b64_tr_b16 a[12:13], v87 offset:24576\n"
"ds_read_b64_tr_b16 a[14:15], v87 offset:26624\n"
"v_exp_f32 v103, v103\n"
"v_add_f32 v102, 1.0, v102\n"
"s_waitcnt lgkmcnt(10)\n"
"v_mfma_f32_32x32x16_bf16 v[224:239], a[24:27], a[104:107], v[224:239]\n"
"ds_read_b64_tr_b16 a[16:17], v84 offset:24832\n"
"ds_read_b64_tr_b16 a[18:19], v84 offset:26880\n"
"v_rcp_f32 v101, v101\n"
"v_mul_f32 v100, v100, v36\n"
"s_waitcnt lgkmcnt(10)\n"
"v_mfma_f32_32x32x16_bf16 v[240:255], a[28:31], a[104:107], v[240:255]\n"
"ds_read_b64_tr_b16 a[20:21], v85 offset:24832\n"
"ds_read_b64_tr_b16 a[22:23], v85 offset:26880\n"
"v_cvt_pk_bf16_f32 v105, v98, v99\n"
"v_mul_f32 v97, s77, v41\n"
"s_waitcnt lgkmcnt(10)\n"
"v_mfma_f32_32x32x16_bf16 v[128:143], a[0:3], a[108:111], v[128:143]\n"
"ds_read_b64_tr_b16 a[24:25], v86 offset:24832\n"
"ds_read_b64_tr_b16 a[26:27], v86 offset:26880\n"
"s_add_u32 s71, %[q0], 0x317800\n"
"buffer_load_dwordx4 v80, %[rsQ], s71 offen offset:2048 lds\n"
"s_waitcnt lgkmcnt(10)\n"
"v_mfma_f32_32x32x16_bf16 v[144:159], a[4:7], a[108:111], v[144:159]\n"
"ds_read_b64_tr_b16 a[28:29], v87 offset:24832\n"
"ds_read_b64_tr_b16 a[30:31], v87 offset:26880\n"
"v_exp_f32 v96, v96\n"
"v_add_f32 v103, 1.0, v103\n"
"s_waitcnt lgkmcnt(10)\n"
"v_mfma_f32_32x32x16_bf16 v[160:175], a[8:11], a[108:111], v[160:175]\n"
"ds_read_b64_tr_b16 a[0:1], v84 offset:32768\n"
"ds_read_b64_tr_b16 a[2:3], v84 offset:34816\n"
"s_add_u32 s82, %[xoff], 0x2000\n"
"buffer_load_dwordx4 a[104:107], v1, %[rsW], s82 offen offset:2048 sc1\n"
"v_rcp_f32 v102, v102\n"
"s_waitcnt lgkmcnt(10)\n"
"v_mfma_f32_32x32x16_bf16 v[176:191], a[12:15], a[108:111], v[176:191]\n"
"ds_read_b64_tr_b16 a[4:5], v85 offset:32768\n"
"ds_read_b64_tr_b16 a[6:7], v85 offset:34816\n"
"v_mul_f32 v101, v101, v37\n"
"v_mul_f32 v98, s77, v42\n"
"s_waitcnt lgkmcnt(10)\n"
"v_mfma_f32_32x32x16_bf16 v[192:207], a[16:19], a[108:111], v[192:207]\n"
"ds_read_b64_tr_b16 a[8:9], v86 offset:32768\n"
"ds_read_b64_tr_b16 a[10:11], v86 offset:34816\n"
"s_add_u32 s71, %[q0], 0x31b400\n"
"buffer_load_dwordx4 v81, %[rsQ], s71 offen offset:3072 lds\n"
"s_waitcnt lgkmcnt(10)\n"
"v_mfma_f32_32x32x16_bf16 v[208:223], a[20:23], a[108:111], v[208:223]\n"
"ds_read_b64_tr_b16 a[12:13], v87 offset:32768\n"
"ds_read_b64_tr_b16 a[14:15], v87 offset:34816\n"
"v_exp_f32 v97, v97\n"
"v_add_f32 v96, 1.0, v96\n"
"s_waitcnt lgkmcnt(10)\n"
"v_mfma_f32_32x32x16_bf16 v[224:239], a[24:27], a[108:111], v[224:239]\n"
"ds_read_b64_tr_b16 a[16:17], v84 offset:33024\n"
"ds_read_b64_tr_b16 a[18:19], v84 offset:35072\n"
"v_rcp_f32 v103, v103\n"
"v_mul_f32 v102, v102, v38\n"
"s_waitcnt lgkmcnt(10)\n"
"v_mfma_f32_32x32x16_bf16 v[240:255], a[28:31], a[108:111], v[240:255]\n"
"ds_read_b64_tr_b16 a[20:21], v85 offset:33024\n"
"ds_read_b64_tr_b16 a[22:23], v85 offset:35072\n"
"v_cvt_pk_bf16_f32 v106, v100, v101\n"
"v_mul_f32 v99, s77, v43\n"
"s_waitcnt vmcnt(27)\n"
"s_barrier\n"
"s_waitcnt lgkmcnt(10)\n"
"v_mfma_f32_32x32x16_bf16 v[128:143], a[0:3], a[112:115], v[128:143]\n"
"ds_read_b64_tr_b16 a[24:25], v86 offset:33024\n"
"ds_read_b64_tr_b16 a[26:27], v86 offset:35072\n"
"s_add_u32 m0, s74, 0x10000\n"
"s_add_u32 s71, %[q0], 0\n"
"buffer_load_dwordx4 v80, %[rsQ], s71 offen lds\n"
"s_waitcnt lgkmcnt(10)\n"
"v_mfma_f32_32x32x16_bf16 v[144:159], a[4:7], a[112:115], v[144:159]\n"
"ds_read_b64_tr_b16 a[28:29], v87 offset:33024\n"
"ds_read_b64_tr_b16 a[30:31], v87 offset:35072\n"
"v_exp_f32 v98, v98\n"
"v_add_f32 v97, 1.0, v97\n"
"s_waitcnt lgkmcnt(10)\n"
"v_mfma_f32_32x32x16_bf16 v[160:175], a[8:11], a[112:115], v[160:175]\n"
"ds_read_b64_tr_b16 a[0:1], v84 offset:40960\n"
"ds_read_b64_tr_b16 a[2:3], v84 offset:43008\n"
"s_add_u32 s82, %[xoff], 0x2000\n"
"buffer_load_dwordx4 a[108:111], v1, %[rsW], s82 offen offset:3072 sc1\n"
"v_rcp_f32 v96, v96\n"
"s_waitcnt lgkmcnt(10)\n"
"v_mfma_f32_32x32x16_bf16 v[176:191], a[12:15], a[112:115], v[176:191]\n"
"ds_read_b64_tr_b16 a[4:5], v85 offset:40960\n"
"ds_read_b64_tr_b16 a[6:7], v85 offset:43008\n"
"v_mul_f32 v103, v103, v39\n"
"v_mul_f32 v100, s77, v44\n"
"v_exp_f32 v99, v99\n"
"s_waitcnt lgkmcnt(10)\n"
"v_mfma_f32_32x32x16_bf16 v[192:207], a[16:19], a[112:115], v[192:207]\n"
"ds_read_b64_tr_b16 a[8:9], v86 offset:40960\n"
"ds_read_b64_tr_b16 a[10:11], v86 offset:43008\n"
"s_add_u32 s71, %[q0], 0x3c00\n"
"buffer_load_dwordx4 v81, %[rsQ], s71 offen offset:1024 lds\n"
"v_add_f32 v98, 1.0, v98\n"
"s_waitcnt lgkmcnt(10)\n"
"v_mfma_f32_32x32x16_bf16 v[208:223], a[20:23], a[112:115], v[208:223]\n"
"ds_read_b64_tr_b16 a[12:13], v87 offset:40960\n"
"ds_read_b64_tr_b16 a[14:15], v87 offset:43008\n"
"v_rcp_f32 v97, v97\n"
"v_mul_f32 v96, v96, v40\n"
"v_cvt_pk_bf16_f32 v107, v102, v103\n"
"s_waitcnt lgkmcnt(10)\n"
"v_mfma_f32_32x32x16_bf16 v[224:239], a[24:27], a[112:115], v[224:239]\n"
"ds_read_b64_tr_b16 a[16:17], v84 offset:41216\n"
"ds_read_b64_tr_b16 a[18:19], v84 offset:43264\n"
"s_add_u32 s68, %[hoff], 0\n"
"buffer_store_dwordx4 v[104:107], v1, %[rsW], s68 offen offset:2048 sc1\n"
"v_mul_f32 v101, s77, v45\n"
"s_waitcnt lgkmcnt(10)\n"
"v_mfma_f32_32x32x16_bf16 v[240:255], a[28:31], a[112:115], v[240:255]\n"
"ds_read_b64_tr_b16 a[20:21], v85 offset:41216\n"
"ds_read_b64_tr_b16 a[22:23], v85 offset:43264\n"
"v_exp_f32 v100, v100\n"
"v_add_f32 v99, 1.0, v99\n"
"s_waitcnt lgkmcnt(10)\n"
"v_mfma_f32_32x32x16_bf16 v[128:143], a[0:3], a[116:119], v[128:143]\n"
"ds_read_b64_tr_b16 a[24:25], v86 offset:41216\n"
"ds_read_b64_tr_b16 a[26:27], v86 offset:43264\n"
"s_add_u32 s71, %[q0], 0x7800\n"
"buffer_load_dwordx4 v80, %[rsQ], s71 offen offset:2048 lds\n"
"s_waitcnt lgkmcnt(10)\n"
"v_mfma_f32_32x32x16_bf16 v[144:159], a[4:7], a[116:119], v[144:159]\n"
"ds_read_b64_tr_b16 a[28:29], v87 offset:41216\n"
"ds_read_b64_tr_b16 a[30:31], v87 offset:43264\n"
"v_rcp_f32 v98, v98\n"
"v_mul_f32 v97, v97, v41\n"
"v_mul_f32 v102, s77, v46\n"
"s_waitcnt lgkmcnt(10)\n"
"v_mfma_f32_32x32x16_bf16 v[160:175], a[8:11], a[116:119], v[160:175]\n"
"ds_read_b64_tr_b16 a[0:1], v84 offset:49152\n"
"ds_read_b64_tr_b16 a[2:3], v84 offset:51200\n"
"s_add_u32 s82, %[xoff], 0x3000\n"
"buffer_load_dwordx4 a[112:115], v1, %[rsW], s82 offen sc1\n"
"v_exp_f32 v101, v101\n"
"s_waitcnt lgkmcnt(10)\n"
"v_mfma_f32_32x32x16_bf16 v[176:191], a[12:15], a[116:119], v[176:191]\n"
"ds_read_b64_tr_b16 a[4:5], v85 offset:49152\n"
"ds_read_b64_tr_b16 a[6:7], v85 offset:51200\n"
"v_add_f32 v100, 1.0, v100\n"
"v_rcp_f32 v99, v99\n"
"v_mul_f32 v98, v98, v42\n"
"s_waitcnt lgkmcnt(10)\n"
"v_mfma_f32_32x32x16_bf16 v[192:207], a[16:19], a[116:119], v[192:207]\n"
"ds_read_b64_tr_b16 a[8:9], v86 offset:49152\n"
"ds_read_b64_tr_b16 a[10:11], v86 offset:51200\n"
"s_add_u32 s71, %[q0], 0xb400\n"
"buffer_load_dwordx4 v81, %[rsQ], s71 offen offset:3072 lds\n"
"v_cvt_pk_bf16_f32 v108, v96, v97\n"
"s_waitcnt lgkmcnt(10)\n"
"v_mfma_f32_32x32x16_bf16 v[208:223], a[20:23], a[116:119], v[208:223]\n"
"ds_read_b64_tr_b16 a[12:13], v87 offset:49152\n"
"ds_read_b64_tr_b16 a[14:15], v87 offset:51200\n"
"v_mul_f32 v103, s77, v47\n"
"v_exp_f32 v102, v102\n"
"v_add_f32 v101, 1.0, v101\n"
"s_waitcnt lgkmcnt(10)\n"
"v_mfma_f32_32x32x16_bf16 v[224:239], a[24:27], a[116:119], v[224:239]\n"
"ds_read_b64_tr_b16 a[16:17], v84 offset:49408\n"
"ds_read_b64_tr_b16 a[18:19], v84 offset:51456\n"
"v_rcp_f32 v100, v100\n"
"v_mul_f32 v99, v99, v43\n"
"v_mul_f32 v96, s77, v48\n"
"s_waitcnt lgkmcnt(10)\n"
"v_mfma_f32_32x32x16_bf16 v[240:255], a[28:31], a[116:119], v[240:255]\n"
"ds_read_b64_tr_b16 a[20:21], v85 offset:49408\n"
"ds_read_b64_tr_b16 a[22:23], v85 offset:51456\n"
"v_exp_f32 v103, v103\n"
"v_add_f32 v102, 1.0, v102\n"
"s_waitcnt lgkmcnt(10)\n"
"v_mfma_f32_32x32x16_bf16 v[128:143], a[0:3], a[120:123], v[128:143]\n"
"ds_read_b64_tr_b16 a[24:25], v86 offset:49408\n"
"ds_read_b64_tr_b16 a[26:27], v86 offset:51456\n"
"s_add_u32 m0, s74, 0x11000\n"
"s_add_u32 s71, %[q0], 0x10000\n"
"buffer_load_dwordx4 v80, %[rsQ], s71 offen lds\n"
"s_waitcnt lgkmcnt(10)\n"
"v_mfma_f32_32x32x16_bf16 v[144:159], a[4:7], a[120:123], v[144:159]\n"
"ds_read_b64_tr_b16 a[28:29], v87 offset:49408\n"
"ds_read_b64_tr_b16 a[30:31], v87 offset:51456\n"
"v_rcp_f32 v101, v101\n"
"v_mul_f32 v100, v100, v44\n"
"v_cvt_pk_bf16_f32 v109, v98, v99\n"
"s_waitcnt lgkmcnt(10)\n"
"v_mfma_f32_32x32x16_bf16 v[160:175], a[8:11], a[120:123], v[160:175]\n"
"ds_read_b64_tr_b16 a[0:1], v84 offset:57344\n"
"ds_read_b64_tr_b16 a[2:3], v84 offset:59392\n"
"s_add_u32 s82, %[xoff], 0x3000\n"
"buffer_load_dwordx4 a[116:119], v1, %[rsW], s82 offen offset:1024 sc1\n"
"v_mul_f32 v97, s77, v49\n"
"s_waitcnt lgkmcnt(10)\n"
"v_mfma_f32_32x32x16_bf16 v[176:191], a[12:15], a[120:123], v[176:191]\n"
"ds_read_b64_tr_b16 a[4:5], v85 offset:57344\n"
"ds_read_b64_tr_b16 a[6:7], v85 offset:59392\n"
"v_exp_f32 v96, v96\n"
"v_add_f32 v103, 1.0, v103\n"
"s_waitcnt lgkmcnt(10)\n"
"v_mfma_f32_32x32x16_bf16 v[192:207], a[16:19], a[120:123], v[192:207]\n"
"ds_read_b64_tr_b16 a[8:9], v86 offset:57344\n"
"ds_read_b64_tr_b16 a[10:11], v86 offset:59392\n"
"s_add_u32 s71, %[q0], 0x13c00\n"
"buffer_load_dwordx4 v81, %[rsQ], s71 offen offset:1024 lds\n"
"s_waitcnt lgkmcnt(10)\n"
"v_mfma_f32_32x32x16_bf16 v[208:223], a[20:23], a[120:123], v[208:223]\n"
"ds_read_b64_tr_b16 a[12:13], v87 offset:57344\n"
"ds_read_b64_tr_b16 a[14:15], v87 offset:59392\n"
"v_rcp_f32 v102, v102\n"
"v_mul_f32 v101, v101, v45\n"
"v_mul_f32 v98, s77, v50\n"
"s_waitcnt lgkmcnt(10)\n"
"v_mfma_f32_32x32x16_bf16 v[224:239], a[24:27], a[120:123], v[224:239]\n"
"ds_read_b64_tr_b16 a[16:17], v84 offset:57600\n"
"ds_read_b64_tr_b16 a[18:19], v84 offset:59648\n"
"v_exp_f32 v97, v97\n"
"v_add_f32 v96, 1.0, v96\n"
"s_waitcnt lgkmcnt(10)\n"
"v_mfma_f32_32x32x16_bf16 v[240:255], a[28:31], a[120:123], v[240:255]\n"
"ds_read_b64_tr_b16 a[20:21], v85 offset:57600\n"
"ds_read_b64_tr_b16 a[22:23], v85 offset:59648\n"
"v_rcp_f32 v103, v103\n"
"v_mul_f32 v102, v102, v46\n"
"v_cvt_pk_bf16_f32 v110, v100, v101\n"
"s_waitcnt lgkmcnt(10)\n"
"v_mfma_f32_32x32x16_bf16 v[128:143], a[0:3], a[124:127], v[128:143]\n"
"ds_read_b64_tr_b16 a[24:25], v86 offset:57600\n"
"ds_read_b64_tr_b16 a[26:27], v86 offset:59648\n"
"s_add_u32 s71, %[q0], 0x17800\n"
"buffer_load_dwordx4 v80, %[rsQ], s71 offen offset:2048 lds\n"
"v_mul_f32 v99, s77, v51\n"
"s_waitcnt lgkmcnt(10)\n"
"v_mfma_f32_32x32x16_bf16 v[144:159], a[4:7], a[124:127], v[144:159]\n"
"ds_read_b64_tr_b16 a[28:29], v87 offset:57600\n"
"ds_read_b64_tr_b16 a[30:31], v87 offset:59648\n"
"v_exp_f32 v98, v98\n"
"v_add_f32 v97, 1.0, v97\n"
"s_waitcnt lgkmcnt(10)\n"
"v_mfma_f32_32x32x16_bf16 v[160:175], a[8:11], a[124:127], v[160:175]\n"
"v_add_u32 v84, 0x10000, v84\n"
"ds_read_b64_tr_b16 a[0:1], v84\n"
"ds_read_b64_tr_b16 a[2:3], v84 offset:2048\n"
"s_add_u32 s82, %[xoff], 0x3000\n"
"buffer_load_dwordx4 a[120:123], v1, %[rsW], s82 offen offset:2048 sc1\n"
"v_rcp_f32 v96, v96\n"
"s_waitcnt lgkmcnt(10)\n"
"v_mfma_f32_32x32x16_bf16 v[176:191], a[12:15], a[124:127], v[176:191]\n"
"v_add_u32 v85, 0x10000, v85\n"
"ds_read_b64_tr_b16 a[4:5], v85\n"
"ds_read_b64_tr_b16 a[6:7], v85 offset:2048\n"
"v_mul_f32 v103, v103, v47\n"
"v_mul_f32 v100, s77, v52\n"
"v_exp_f32 v99, v99\n"
"s_waitcnt lgkmcnt(10)\n"
"v_mfma_f32_32x32x16_bf16 v[192:207], a[16:19], a[124:127], v[192:207]\n"
"v_add_u32 v86, 0x10000, v86\n"
"ds_read_b64_tr_b16 a[8:9], v86\n"
"ds_read_b64_tr_b16 a[10:11], v86 offset:2048\n"
"s_add_u32 s71, %[q0], 0x1b400\n"
"buffer_load_dwordx4 v81, %[rsQ], s71 offen offset:3072 lds\n"
"v_add_f32 v98, 1.0, v98\n"
"s_waitcnt lgkmcnt(10)\n"
"v_mfma_f32_32x32x16_bf16 v[208:223], a[20:23], a[124:127], v[208:223]\n"
"v_add_u32 v87, 0x10000, v87\n"
"ds_read_b64_tr_b16 a[12:13], v87\n"
"ds_read_b64_tr_b16 a[14:15], v87 offset:2048\n"
"v_rcp_f32 v97, v97\n"
"v_mul_f32 v96, v96, v48\n"
"v_cvt_pk_bf16_f32 v111, v102, v103\n"
"s_waitcnt lgkmcnt(10)\n"
"v_mfma_f32_32x32x16_bf16 v[224:239], a[24:27], a[124:127], v[224:239]\n"
"ds_read_b64_tr_b16 a[16:17], v84 offset:256\n"
"ds_read_b64_tr_b16 a[18:19], v84 offset:2304\n"
"s_add_u32 s68, %[hoff], 0\n"
"buffer_store_dwordx4 v[108:111], v1, %[rsW], s68 offen offset:3072 sc1\n"
"v_mul_f32 v101, s77, v53\n"
"s_waitcnt lgkmcnt(10)\n"
"v_mfma_f32_32x32x16_bf16 v[240:255], a[28:31], a[124:127], v[240:255]\n"
"ds_read_b64_tr_b16 a[20:21], v85 offset:256\n"
"ds_read_b64_tr_b16 a[22:23], v85 offset:2304\n"
"v_exp_f32 v100, v100\n"
"v_add_f32 v99, 1.0, v99\n"
"s_waitcnt vmcnt(27)\n"
"s_barrier\n"
"s_waitcnt lgkmcnt(10)\n"
"v_mfma_f32_32x32x16_bf16 v[128:143], a[0:3], a[128:131], v[128:143]\n"
"ds_read_b64_tr_b16 a[24:25], v86 offset:256\n"
"ds_read_b64_tr_b16 a[26:27], v86 offset:2304\n"
"s_add_u32 m0, s74, 0x18000\n"
"s_add_u32 s71, %[q0], 0x80000\n"
"buffer_load_dwordx4 v80, %[rsQ], s71 offen lds\n"
"s_waitcnt lgkmcnt(10)\n"
"v_mfma_f32_32x32x16_bf16 v[144:159], a[4:7], a[128:131], v[144:159]\n"
"ds_read_b64_tr_b16 a[28:29], v87 offset:256\n"
"ds_read_b64_tr_b16 a[30:31], v87 offset:2304\n"
"v_rcp_f32 v98, v98\n"
"v_mul_f32 v97, v97, v49\n"
"v_mul_f32 v102, s77, v54\n"
"s_waitcnt lgkmcnt(10)\n"
"v_mfma_f32_32x32x16_bf16 v[160:175], a[8:11], a[128:131], v[160:175]\n"
"ds_read_b64_tr_b16 a[0:1], v84 offset:8192\n"
"ds_read_b64_tr_b16 a[2:3], v84 offset:10240\n"
"s_add_u32 s82, %[xoff], 0x3000\n"
"buffer_load_dwordx4 a[124:127], v1, %[rsW], s82 offen offset:3072 sc1\n"
"v_exp_f32 v101, v101\n"
"s_waitcnt lgkmcnt(10)\n"
"v_mfma_f32_32x32x16_bf16 v[176:191], a[12:15], a[128:131], v[176:191]\n"
"ds_read_b64_tr_b16 a[4:5], v85 offset:8192\n"
"ds_read_b64_tr_b16 a[6:7], v85 offset:10240\n"
"v_add_f32 v100, 1.0, v100\n"
"v_rcp_f32 v99, v99\n"
"v_mul_f32 v98, v98, v50\n"
"s_waitcnt lgkmcnt(10)\n"
"v_mfma_f32_32x32x16_bf16 v[192:207], a[16:19], a[128:131], v[192:207]\n"
"ds_read_b64_tr_b16 a[8:9], v86 offset:8192\n"
"ds_read_b64_tr_b16 a[10:11], v86 offset:10240\n"
"s_add_u32 s71, %[q0], 0x83c00\n"
"buffer_load_dwordx4 v81, %[rsQ], s71 offen offset:1024 lds\n"
"v_cvt_pk_bf16_f32 v104, v96, v97\n"
"s_waitcnt lgkmcnt(10)\n"
"v_mfma_f32_32x32x16_bf16 v[208:223], a[20:23], a[128:131], v[208:223]\n"
"ds_read_b64_tr_b16 a[12:13], v87 offset:8192\n"
"ds_read_b64_tr_b16 a[14:15], v87 offset:10240\n"
"v_mul_f32 v103, s77, v55\n"
"v_exp_f32 v102, v102\n"
"v_add_f32 v101, 1.0, v101\n"
"s_waitcnt lgkmcnt(10)\n"
"v_mfma_f32_32x32x16_bf16 v[224:239], a[24:27], a[128:131], v[224:239]\n"
"ds_read_b64_tr_b16 a[16:17], v84 offset:8448\n"
"ds_read_b64_tr_b16 a[18:19], v84 offset:10496\n"
"v_rcp_f32 v100, v100\n"
"v_mul_f32 v99, v99, v51\n"
"v_mul_f32 v96, s77, v56\n"
"s_waitcnt lgkmcnt(10)\n"
"v_mfma_f32_32x32x16_bf16 v[240:255], a[28:31], a[128:131], v[240:255]\n"
"ds_read_b64_tr_b16 a[20:21], v85 offset:8448\n"
"ds_read_b64_tr_b16 a[22:23], v85 offset:10496\n"
"v_exp_f32 v103, v103\n"
"v_add_f32 v102, 1.0, v102\n"
"s_waitcnt lgkmcnt(10)\n"
"v_mfma_f32_32x32x16_bf16 v[128:143], a[0:3], a[132:135], v[128:143]\n"
"ds_read_b64_tr_b16 a[24:25], v86 offset:8448\n"
"ds_read_b64_tr_b16 a[26:27], v86 offset:10496\n"
"s_add_u32 s71, %[q0], 0x87800\n"
"buffer_load_dwordx4 v80, %[rsQ], s71 offen offset:2048 lds\n"
"s_waitcnt lgkmcnt(10)\n"
"v_mfma_f32_32x32x16_bf16 v[144:159], a[4:7], a[132:135], v[144:159]\n"
"ds_read_b64_tr_b16 a[28:29], v87 offset:8448\n"
"ds_read_b64_tr_b16 a[30:31], v87 offset:10496\n"
"v_rcp_f32 v101, v101\n"
"v_mul_f32 v100, v100, v52\n"
"v_cvt_pk_bf16_f32 v105, v98, v99\n"
"s_waitcnt lgkmcnt(10)\n"
"v_mfma_f32_32x32x16_bf16 v[160:175], a[8:11], a[132:135], v[160:175]\n"
"ds_read_b64_tr_b16 a[0:1], v84 offset:16384\n"
"ds_read_b64_tr_b16 a[2:3], v84 offset:18432\n"
"s_add_u32 s82, %[xoff], 0x4000\n"
"buffer_load_dwordx4 a[128:131], v1, %[rsW], s82 offen sc1\n"
"v_mul_f32 v97, s77, v57\n"
"s_waitcnt lgkmcnt(10)\n"
"v_mfma_f32_32x32x16_bf16 v[176:191], a[12:15], a[132:135], v[176:191]\n"
"ds_read_b64_tr_b16 a[4:5], v85 offset:16384\n"
"ds_read_b64_tr_b16 a[6:7], v85 offset:18432\n"
"v_exp_f32 v96, v96\n"
"v_add_f32 v103, 1.0, v103\n"
"s_waitcnt lgkmcnt(10)\n"
"v_mfma_f32_32x32x16_bf16 v[192:207], a[16:19], a[132:135], v[192:207]\n"
"ds_read_b64_tr_b16 a[8:9], v86 offset:16384\n"
"ds_read_b64_tr_b16 a[10:11], v86 offset:18432\n"
"s_add_u32 s71, %[q0], 0x8b400\n"
"buffer_load_dwordx4 v81, %[rsQ], s71 offen offset:3072 lds\n"
"s_waitcnt lgkmcnt(10)\n"
"v_mfma_f32_32x32x16_bf16 v[208:223], a[20:23], a[132:135], v[208:223]\n"
"ds_read_b64_tr_b16 a[12:13], v87 offset:16384\n"
"ds_read_b64_tr_b16 a[14:15], v87 offset:18432\n"
"v_rcp_f32 v102, v102\n"
"v_mul_f32 v101, v101, v53\n"
"v_mul_f32 v98, s77, v58\n"
"s_waitcnt lgkmcnt(10)\n"
"v_mfma_f32_32x32x16_bf16 v[224:239], a[24:27], a[132:135], v[224:239]\n"
"ds_read_b64_tr_b16 a[16:17], v84 offset:16640\n"
"ds_read_b64_tr_b16 a[18:19], v84 offset:18688\n"
"v_exp_f32 v97, v97\n"
"v_add_f32 v96, 1.0, v96\n"
"s_waitcnt lgkmcnt(10)\n"
"v_mfma_f32_32x32x16_bf16 v[240:255], a[28:31], a[132:135], v[240:255]\n"
"ds_read_b64_tr_b16 a[20:21], v85 offset:16640\n"
"ds_read_b64_tr_b16 a[22:23], v85 offset:18688\n"
"v_rcp_f32 v103, v103\n"
"v_mul_f32 v102, v102, v54\n"
"v_cvt_pk_bf16_f32 v106, v100, v101\n"
"s_waitcnt lgkmcnt(10)\n"
"v_mfma_f32_32x32x16_bf16 v[128:143], a[0:3], a[136:139], v[128:143]\n"
"ds_read_b64_tr_b16 a[24:25], v86 offset:16640\n"
"ds_read_b64_tr_b16 a[26:27], v86 offset:18688\n"
"s_add_u32 m0, s74, 0x19000\n"
"s_add_u32 s71, %[q0], 0x90000\n"
"buffer_load_dwordx4 v80, %[rsQ], s71 offen lds\n"
"v_mul_f32 v99, s77, v59\n"
"s_waitcnt lgkmcnt(10)\n"
"v_mfma_f32_32x32x16_bf16 v[144:159], a[4:7], a[136:139], v[144:159]\n"
"ds_read_b64_tr_b16 a[28:29], v87 offset:16640\n"
"ds_read_b64_tr_b16 a[30:31], v87 offset:18688\n"
"v_exp_f32 v98, v98\n"
"v_add_f32 v97, 1.0, v97\n"
"s_waitcnt lgkmcnt(10)\n"
"v_mfma_f32_32x32x16_bf16 v[160:175], a[8:11], a[136:139], v[160:175]\n"
"ds_read_b64_tr_b16 a[0:1], v84 offset:24576\n"
"ds_read_b64_tr_b16 a[2:3], v84 offset:26624\n"
"s_add_u32 s82, %[xoff], 0x4000\n"
"buffer_load_dwordx4 a[132:135], v1, %[rsW], s82 offen offset:1024 sc1\n"
"v_rcp_f32 v96, v96\n"
"s_waitcnt lgkmcnt(10)\n"
"v_mfma_f32_32x32x16_bf16 v[176:191], a[12:15], a[136:139], v[176:191]\n"
"ds_read_b64_tr_b16 a[4:5], v85 offset:24576\n"
"ds_read_b64_tr_b16 a[6:7], v85 offset:26624\n"
"v_mul_f32 v103, v103, v55\n"
"v_mul_f32 v100, s77, v60\n"
"v_exp_f32 v99, v99\n"
"s_waitcnt lgkmcnt(10)\n"
"v_mfma_f32_32x32x16_bf16 v[192:207], a[16:19], a[136:139], v[192:207]\n"
"ds_read_b64_tr_b16 a[8:9], v86 offset:24576\n"
"ds_read_b64_tr_b16 a[10:11], v86 offset:26624\n"
"s_add_u32 s71, %[q0], 0x93c00\n"
"buffer_load_dwordx4 v81, %[rsQ], s71 offen offset:1024 lds\n"
"v_add_f32 v98, 1.0, v98\n"
"s_waitcnt lgkmcnt(10)\n"
"v_mfma_f32_32x32x16_bf16 v[208:223], a[20:23], a[136:139], v[208:223]\n"
"ds_read_b64_tr_b16 a[12:13], v87 offset:24576\n"
"ds_read_b64_tr_b16 a[14:15], v87 offset:26624\n"
"v_rcp_f32 v97, v97\n"
"v_mul_f32 v96, v96, v56\n"
"v_cvt_pk_bf16_f32 v107, v102, v103\n"
"s_waitcnt lgkmcnt(10)\n"
"v_mfma_f32_32x32x16_bf16 v[224:239], a[24:27], a[136:139], v[224:239]\n"
"ds_read_b64_tr_b16 a[16:17], v84 offset:24832\n"
"ds_read_b64_tr_b16 a[18:19], v84 offset:26880\n"
"s_add_u32 s68, %[hoff], 0x1000\n"
"buffer_store_dwordx4 v[104:107], v1, %[rsW], s68 offen sc1\n"
"v_mul_f32 v101, s77, v61\n"
"s_waitcnt lgkmcnt(10)\n"
"v_mfma_f32_32x32x16_bf16 v[240:255], a[28:31], a[136:139], v[240:255]\n"
"ds_read_b64_tr_b16 a[20:21], v85 offset:24832\n"
"ds_read_b64_tr_b16 a[22:23], v85 offset:26880\n"
"v_exp_f32 v100, v100\n"
"v_add_f32 v99, 1.0, v99\n"
"s_waitcnt lgkmcnt(10)\n"
"v_mfma_f32_32x32x16_bf16 v[128:143], a[0:3], a[140:143], v[128:143]\n"
"ds_read_b64_tr_b16 a[24:25], v86 offset:24832\n"
"ds_read_b64_tr_b16 a[26:27], v86 offset:26880\n"
"s_add_u32 s71, %[q0], 0x97800\n"
"buffer_load_dwordx4 v80, %[rsQ], s71 offen offset:2048 lds\n"
"s_waitcnt lgkmcnt(10)\n"
"v_mfma_f32_32x32x16_bf16 v[144:159], a[4:7], a[140:143], v[144:159]\n"
"ds_read_b64_tr_b16 a[28:29], v87 offset:24832\n"
"ds_read_b64_tr_b16 a[30:31], v87 offset:26880\n"
"v_rcp_f32 v98, v98\n"
"v_mul_f32 v97, v97, v57\n"
"v_mul_f32 v102, s77, v62\n"
"s_waitcnt lgkmcnt(10)\n"
"v_mfma_f32_32x32x16_bf16 v[160:175], a[8:11], a[140:143], v[160:175]\n"
"v_add_u32 v84, 0xfffe0000, v84\n"
"ds_read_b64_tr_b16 a[0:1], v84\n"
"ds_read_b64_tr_b16 a[2:3], v84 offset:2048\n"
"s_add_u32 s82, %[xoff], 0x4000\n"
"buffer_load_dwordx4 a[136:139], v1, %[rsW], s82 offen offset:2048 sc1\n"
"v_exp_f32 v101, v101\n"
"s_waitcnt lgkmcnt(10)\n"
"v_mfma_f32_32x32x16_bf16 v[176:191], a[12:15], a[140:143], v[176:191]\n"
"v_add_u32 v85, 0xfffe0000, v85\n"
"ds_read_b64_tr_b16 a[4:5], v85\n"
"ds_read_b64_tr_b16 a[6:7], v85 offset:2048\n"
"v_add_f32 v100, 1.0, v100\n"
"v_rcp_f32 v99, v99\n"
"v_mul_f32 v98, v98, v58\n"
"s_waitcnt lgkmcnt(10)\n"
"v_mfma_f32_32x32x16_bf16 v[192:207], a[16:19], a[140:143], v[192:207]\n"
"v_add_u32 v86, 0xfffe0000, v86\n"
"ds_read_b64_tr_b16 a[8:9], v86\n"
"ds_read_b64_tr_b16 a[10:11], v86 offset:2048\n"
"s_add_u32 s71, %[q0], 0x9b400\n"
"buffer_load_dwordx4 v81, %[rsQ], s71 offen offset:3072 lds\n"
"v_cvt_pk_bf16_f32 v108, v96, v97\n"
"s_waitcnt lgkmcnt(10)\n"
"v_mfma_f32_32x32x16_bf16 v[208:223], a[20:23], a[140:143], v[208:223]\n"
"v_add_u32 v87, 0xfffe0000, v87\n"
"ds_read_b64_tr_b16 a[12:13], v87\n"
"ds_read_b64_tr_b16 a[14:15], v87 offset:2048\n"
"v_mul_f32 v103, s77, v63\n"
"v_exp_f32 v102, v102\n"
"v_add_f32 v101, 1.0, v101\n"
"s_waitcnt lgkmcnt(10)\n"
"v_mfma_f32_32x32x16_bf16 v[224:239], a[24:27], a[140:143], v[224:239]\n"
"ds_read_b64_tr_b16 a[16:17], v84 offset:256\n"
"ds_read_b64_tr_b16 a[18:19], v84 offset:2304\n"
"v_rcp_f32 v100, v100\n"
"v_mul_f32 v99, v99, v59\n"
"v_mul_f32 v96, s77, v64\n"
"s_waitcnt lgkmcnt(10)\n"
"v_mfma_f32_32x32x16_bf16 v[240:255], a[28:31], a[140:143], v[240:255]\n"
"ds_read_b64_tr_b16 a[20:21], v85 offset:256\n"
"ds_read_b64_tr_b16 a[22:23], v85 offset:2304\n"
"v_exp_f32 v103, v103\n"
"v_add_f32 v102, 1.0, v102\n"
"s_waitcnt vmcnt(27)\n"
"s_barrier\n"
"s_waitcnt lgkmcnt(10)\n"
"v_mfma_f32_32x32x16_bf16 v[128:143], a[0:3], a[144:147], v[128:143]\n"
"ds_read_b64_tr_b16 a[24:25], v86 offset:256\n"
"ds_read_b64_tr_b16 a[26:27], v86 offset:2304\n"
"s_add_u32 m0, s74, 0x20000\n"
"s_add_u32 s71, %[q0], 0x100000\n"
"buffer_load_dwordx4 v80, %[rsQ], s71 offen lds\n"
"s_waitcnt lgkmcnt(10)\n"
"v_mfma_f32_32x32x16_bf16 v[144:159], a[4:7], a[144:147], v[144:159]\n"
"ds_read_b64_tr_b16 a[28:29], v87 offset:256\n"
"ds_read_b64_tr_b16 a[30:31], v87 offset:2304\n"
"v_rcp_f32 v101, v101\n"
"v_mul_f32 v100, v100, v60\n"
"v_cvt_pk_bf16_f32 v109, v98, v99\n"
"s_waitcnt lgkmcnt(10)\n"
"v_mfma_f32_32x32x16_bf16 v[160:175], a[8:11], a[144:147], v[160:175]\n"
"ds_read_b64_tr_b16 a[0:1], v84 offset:8192\n"
"ds_read_b64_tr_b16 a[2:3], v84 offset:10240\n"
"s_add_u32 s82, %[xoff], 0x4000\n"
"buffer_load_dwordx4 a[140:143], v1, %[rsW], s82 offen offset:3072 sc1\n"
"v_mul_f32 v97, s77, v65\n"
"s_waitcnt lgkmcnt(10)\n"
"v_mfma_f32_32x32x16_bf16 v[176:191], a[12:15], a[144:147], v[176:191]\n"
"ds_read_b64_tr_b16 a[4:5], v85 offset:8192\n"
"ds_read_b64_tr_b16 a[6:7], v85 offset:10240\n"
"v_exp_f32 v96, v96\n"
"v_add_f32 v103, 1.0, v103\n"
"s_waitcnt lgkmcnt(10)\n"
"v_mfma_f32_32x32x16_bf16 v[192:207], a[16:19], a[144:147], v[192:207]\n"
"ds_read_b64_tr_b16 a[8:9], v86 offset:8192\n"
"ds_read_b64_tr_b16 a[10:11], v86 offset:10240\n"
"s_add_u32 s71, %[q0], 0x103c00\n"
"buffer_load_dwordx4 v81, %[rsQ], s71 offen offset:1024 lds\n"
"s_waitcnt lgkmcnt(10)\n"
"v_mfma_f32_32x32x16_bf16 v[208:223], a[20:23], a[144:147], v[208:223]\n"
"ds_read_b64_tr_b16 a[12:13], v87 offset:8192\n"
"ds_read_b64_tr_b16 a[14:15], v87 offset:10240\n"
"v_rcp_f32 v102, v102\n"
"v_mul_f32 v101, v101, v61\n"
"v_mul_f32 v98, s77, v66\n"
"s_waitcnt lgkmcnt(10)\n"
"v_mfma_f32_32x32x16_bf16 v[224:239], a[24:27], a[144:147], v[224:239]\n"
"ds_read_b64_tr_b16 a[16:17], v84 offset:8448\n"
"ds_read_b64_tr_b16 a[18:19], v84 offset:10496\n"
"v_exp_f32 v97, v97\n"
"v_add_f32 v96, 1.0, v96\n"
"s_waitcnt lgkmcnt(10)\n"
"v_mfma_f32_32x32x16_bf16 v[240:255], a[28:31], a[144:147], v[240:255]\n"
"ds_read_b64_tr_b16 a[20:21], v85 offset:8448\n"
"ds_read_b64_tr_b16 a[22:23], v85 offset:10496\n"
"v_rcp_f32 v103, v103\n"
"v_mul_f32 v102, v102, v62\n"
"v_cvt_pk_bf16_f32 v110, v100, v101\n"
"s_waitcnt lgkmcnt(10)\n"
"v_mfma_f32_32x32x16_bf16 v[128:143], a[0:3], a[148:151], v[128:143]\n"
"ds_read_b64_tr_b16 a[24:25], v86 offset:8448\n"
"ds_read_b64_tr_b16 a[26:27], v86 offset:10496\n"
"s_add_u32 s71, %[q0], 0x107800\n"
"buffer_load_dwordx4 v80, %[rsQ], s71 offen offset:2048 lds\n"
"v_mul_f32 v99, s77, v67\n"
"s_waitcnt lgkmcnt(10)\n"
"v_mfma_f32_32x32x16_bf16 v[144:159], a[4:7], a[148:151], v[144:159]\n"
"ds_read_b64_tr_b16 a[28:29], v87 offset:8448\n"
"ds_read_b64_tr_b16 a[30:31], v87 offset:10496\n"
"v_exp_f32 v98, v98\n"
"v_add_f32 v97, 1.0, v97\n"
"s_waitcnt lgkmcnt(10)\n"
"v_mfma_f32_32x32x16_bf16 v[160:175], a[8:11], a[148:151], v[160:175]\n"
"ds_read_b64_tr_b16 a[0:1], v84 offset:16384\n"
"ds_read_b64_tr_b16 a[2:3], v84 offset:18432\n"
"s_add_u32 s82, %[xoff], 0x5000\n"
"buffer_load_dwordx4 a[144:147], v1, %[rsW], s82 offen sc1\n"
"v_rcp_f32 v96, v96\n"
"s_waitcnt lgkmcnt(10)\n"
"v_mfma_f32_32x32x16_bf16 v[176:191], a[12:15], a[148:151], v[176:191]\n"
"ds_read_b64_tr_b16 a[4:5], v85 offset:16384\n"
"ds_read_b64_tr_b16 a[6:7], v85 offset:18432\n"
"v_mul_f32 v103, v103, v63\n"
"v_mul_f32 v100, s77, v68\n"
"v_exp_f32 v99, v99\n"
"s_waitcnt lgkmcnt(10)\n"
"v_mfma_f32_32x32x16_bf16 v[192:207], a[16:19], a[148:151], v[192:207]\n"
"ds_read_b64_tr_b16 a[8:9], v86 offset:16384\n"
"ds_read_b64_tr_b16 a[10:11], v86 offset:18432\n"
"s_add_u32 s71, %[q0], 0x10b400\n"
"buffer_load_dwordx4 v81, %[rsQ], s71 offen offset:3072 lds\n"
"v_add_f32 v98, 1.0, v98\n"
"s_waitcnt lgkmcnt(10)\n"
"v_mfma_f32_32x32x16_bf16 v[208:223], a[20:23], a[148:151], v[208:223]\n"
"ds_read_b64_tr_b16 a[12:13], v87 offset:16384\n"
"ds_read_b64_tr_b16 a[14:15], v87 offset:18432\n"
"v_rcp_f32 v97, v97\n"
"v_mul_f32 v96, v96, v64\n"
"v_cvt_pk_bf16_f32 v111, v102, v103\n"
"s_waitcnt lgkmcnt(10)\n"
"v_mfma_f32_32x32x16_bf16 v[224:239], a[24:27], a[148:151], v[224:239]\n"
"ds_read_b64_tr_b16 a[16:17], v84 offset:16640\n"
"ds_read_b64_tr_b16 a[18:19], v84 offset:18688\n"
"s_add_u32 s68, %[hoff], 0x1000\n"
"buffer_store_dwordx4 v[108:111], v1, %[rsW], s68 offen offset:1024 sc1\n"
"v_mul_f32 v101, s77, v69\n"
"s_waitcnt lgkmcnt(10)\n"
"v_mfma_f32_32x32x16_bf16 v[240:255], a[28:31], a[148:151], v[240:255]\n"
"ds_read_b64_tr_b16 a[20:21], v85 offset:16640\n"
"ds_read_b64_tr_b16 a[22:23], v85 offset:18688\n"
"v_exp_f32 v100, v100\n"
"v_add_f32 v99, 1.0, v99\n"
"s_waitcnt lgkmcnt(10)\n"
"v_mfma_f32_32x32x16_bf16 v[128:143], a[0:3], a[152:155], v[128:143]\n"
"ds_read_b64_tr_b16 a[24:25], v86 offset:16640\n"
"ds_read_b64_tr_b16 a[26:27], v86 offset:18688\n"
"s_add_u32 m0, s74, 0x21000\n"
"s_add_u32 s71, %[q0], 0x110000\n"
"buffer_load_dwordx4 v80, %[rsQ], s71 offen lds\n"
"s_waitcnt lgkmcnt(10)\n"
"v_mfma_f32_32x32x16_bf16 v[144:159], a[4:7], a[152:155], v[144:159]\n"
"ds_read_b64_tr_b16 a[28:29], v87 offset:16640\n"
"ds_read_b64_tr_b16 a[30:31], v87 offset:18688\n"
"v_rcp_f32 v98, v98\n"
"v_mul_f32 v97, v97, v65\n"
"v_mul_f32 v102, s77, v70\n"
"s_waitcnt lgkmcnt(10)\n"
"v_mfma_f32_32x32x16_bf16 v[160:175], a[8:11], a[152:155], v[160:175]\n"
"ds_read_b64_tr_b16 a[0:1], v84 offset:24576\n"
"ds_read_b64_tr_b16 a[2:3], v84 offset:26624\n"
"s_add_u32 s82, %[xoff], 0x5000\n"
"buffer_load_dwordx4 a[148:151], v1, %[rsW], s82 offen offset:1024 sc1\n"
"v_exp_f32 v101, v101\n"
"s_waitcnt lgkmcnt(10)\n"
"v_mfma_f32_32x32x16_bf16 v[176:191], a[12:15], a[152:155], v[176:191]\n"
"ds_read_b64_tr_b16 a[4:5], v85 offset:24576\n"
"ds_read_b64_tr_b16 a[6:7], v85 offset:26624\n"
"v_add_f32 v100, 1.0, v100\n"
"v_rcp_f32 v99, v99\n"
"v_mul_f32 v98, v98, v66\n"
"s_waitcnt lgkmcnt(10)\n"
"v_mfma_f32_32x32x16_bf16 v[192:207], a[16:19], a[152:155], v[192:207]\n"
"ds_read_b64_tr_b16 a[8:9], v86 offset:24576\n"
"ds_read_b64_tr_b16 a[10:11], v86 offset:26624\n"
"s_add_u32 s71, %[q0], 0x113c00\n"
"buffer_load_dwordx4 v81, %[rsQ], s71 offen offset:1024 lds\n"
"v_cvt_pk_bf16_f32 v104, v96, v97\n"
"s_waitcnt lgkmcnt(10)\n"
"v_mfma_f32_32x32x16_bf16 v[208:223], a[20:23], a[152:155], v[208:223]\n"
"ds_read_b64_tr_b16 a[12:13], v87 offset:24576\n"
"ds_read_b64_tr_b16 a[14:15], v87 offset:26624\n"
"v_mul_f32 v103, s77, v71\n"
"v_exp_f32 v102, v102\n"
"v_add_f32 v101, 1.0, v101\n"
"s_waitcnt lgkmcnt(10)\n"
"v_mfma_f32_32x32x16_bf16 v[224:239], a[24:27], a[152:155], v[224:239]\n"
"ds_read_b64_tr_b16 a[16:17], v84 offset:24832\n"
"ds_read_b64_tr_b16 a[18:19], v84 offset:26880\n"
"v_rcp_f32 v100, v100\n"
"v_mul_f32 v99, v99, v67\n"
"v_mul_f32 v96, s77, v72\n"
"s_waitcnt lgkmcnt(10)\n"
"v_mfma_f32_32x32x16_bf16 v[240:255], a[28:31], a[152:155], v[240:255]\n"
"ds_read_b64_tr_b16 a[20:21], v85 offset:24832\n"
"ds_read_b64_tr_b16 a[22:23], v85 offset:26880\n"
"v_exp_f32 v103, v103\n"
"v_add_f32 v102, 1.0, v102\n"
"s_waitcnt lgkmcnt(10)\n"
"v_mfma_f32_32x32x16_bf16 v[128:143], a[0:3], a[156:159], v[128:143]\n"
"ds_read_b64_tr_b16 a[24:25], v86 offset:24832\n"
"ds_read_b64_tr_b16 a[26:27], v86 offset:26880\n"
"s_add_u32 s71, %[q0], 0x117800\n"
"buffer_load_dwordx4 v80, %[rsQ], s71 offen offset:2048 lds\n"
"s_waitcnt lgkmcnt(10)\n"
"v_mfma_f32_32x32x16_bf16 v[144:159], a[4:7], a[156:159], v[144:159]\n"
"ds_read_b64_tr_b16 a[28:29], v87 offset:24832\n"
"ds_read_b64_tr_b16 a[30:31], v87 offset:26880\n"
"v_rcp_f32 v101, v101\n"
"v_mul_f32 v100, v100, v68\n"
"v_cvt_pk_bf16_f32 v105, v98, v99\n"
"s_waitcnt lgkmcnt(10)\n"
"v_mfma_f32_32x32x16_bf16 v[160:175], a[8:11], a[156:159], v[160:175]\n"
"ds_read_b64_tr_b16 a[0:1], v84 offset:32768\n"
"ds_read_b64_tr_b16 a[2:3], v84 offset:34816\n"
"s_add_u32 s82, %[xoff], 0x5000\n"
"buffer_load_dwordx4 a[152:155], v1, %[rsW], s82 offen offset:2048 sc1\n"
"v_mul_f32 v97, s77, v73\n"
"s_waitcnt lgkmcnt(10)\n"
"v_mfma_f32_32x32x16_bf16 v[176:191], a[12:15], a[156:159], v[176:191]\n"
"ds_read_b64_tr_b16 a[4:5], v85 offset:32768\n"
"ds_read_b64_tr_b16 a[6:7], v85 offset:34816\n"
"v_exp_f32 v96, v96\n"
"v_add_f32 v103, 1.0, v103\n"
"s_waitcnt lgkmcnt(10)\n"
"v_mfma_f32_32x32x16_bf16 v[192:207], a[16:19], a[156:159], v[192:207]\n"
"ds_read_b64_tr_b16 a[8:9], v86 offset:32768\n"
"ds_read_b64_tr_b16 a[10:11], v86 offset:34816\n"
"s_add_u32 s71, %[q0], 0x11b400\n"
"buffer_load_dwordx4 v81, %[rsQ], s71 offen offset:3072 lds\n"
"s_waitcnt lgkmcnt(10)\n"
"v_mfma_f32_32x32x16_bf16 v[208:223], a[20:23], a[156:159], v[208:223]\n"
"ds_read_b64_tr_b16 a[12:13], v87 offset:32768\n"
"ds_read_b64_tr_b16 a[14:15], v87 offset:34816\n"
"v_rcp_f32 v102, v102\n"
"v_mul_f32 v101, v101, v69\n"
"v_mul_f32 v98, s77, v74\n"
"s_waitcnt lgkmcnt(10)\n"
"v_mfma_f32_32x32x16_bf16 v[224:239], a[24:27], a[156:159], v[224:239]\n"
"ds_read_b64_tr_b16 a[16:17], v84 offset:33024\n"
"ds_read_b64_tr_b16 a[18:19], v84 offset:35072\n"
"v_exp_f32 v97, v97\n"
"v_add_f32 v96, 1.0, v96\n"
"s_waitcnt lgkmcnt(10)\n"
"v_mfma_f32_32x32x16_bf16 v[240:255], a[28:31], a[156:159], v[240:255]\n"
"ds_read_b64_tr_b16 a[20:21], v85 offset:33024\n"
"ds_read_b64_tr_b16 a[22:23], v85 offset:35072\n"
"v_rcp_f32 v103, v103\n"
"v_mul_f32 v102, v102, v70\n"
"v_cvt_pk_bf16_f32 v106, v100, v101\n"
"s_waitcnt vmcnt(27)\n"
"s_barrier\n"
"s_waitcnt lgkmcnt(10)\n"
"v_mfma_f32_32x32x16_bf16 v[128:143], a[0:3], a[160:163], v[128:143]\n"
"ds_read_b64_tr_b16 a[24:25], v86 offset:33024\n"
"ds_read_b64_tr_b16 a[26:27], v86 offset:35072\n"
"s_add_u32 m0, s74, 0\n"
"s_add_u32 s71, %[q0], 0x180000\n"
"buffer_load_dwordx4 v80, %[rsQ], s71 offen lds\n"
"v_mul_f32 v99, s77, v75\n"
"s_waitcnt lgkmcnt(10)\n"
"v_mfma_f32_32x32x16_bf16 v[144:159], a[4:7], a[160:163], v[144:159]\n"
"ds_read_b64_tr_b16 a[28:29], v87 offset:33024\n"
"ds_read_b64_tr_b16 a[30:31], v87 offset:35072\n"
"v_exp_f32 v98, v98\n"
"v_add_f32 v97, 1.0, v97\n"
"s_waitcnt lgkmcnt(10)\n"
"v_mfma_f32_32x32x16_bf16 v[160:175], a[8:11], a[160:163], v[160:175]\n"
"ds_read_b64_tr_b16 a[0:1], v84 offset:40960\n"
"ds_read_b64_tr_b16 a[2:3], v84 offset:43008\n"
"s_add_u32 s82, %[xoff], 0x5000\n"
"buffer_load_dwordx4 a[156:159], v1, %[rsW], s82 offen offset:3072 sc1\n"
"v_rcp_f32 v96, v96\n"
"s_waitcnt lgkmcnt(10)\n"
"v_mfma_f32_32x32x16_bf16 v[176:191], a[12:15], a[160:163], v[176:191]\n"
"ds_read_b64_tr_b16 a[4:5], v85 offset:40960\n"
"ds_read_b64_tr_b16 a[6:7], v85 offset:43008\n"
"v_mul_f32 v103, v103, v71\n"
"v_mul_f32 v100, s77, v76\n"
"v_exp_f32 v99, v99\n"
"s_waitcnt lgkmcnt(10)\n"
"v_mfma_f32_32x32x16_bf16 v[192:207], a[16:19], a[160:163], v[192:207]\n"
"ds_read_b64_tr_b16 a[8:9], v86 offset:40960\n"
"ds_read_b64_tr_b16 a[10:11], v86 offset:43008\n"
"s_add_u32 s71, %[q0], 0x183c00\n"
"buffer_load_dwordx4 v81, %[rsQ], s71 offen offset:1024 lds\n"
"v_add_f32 v98, 1.0, v98\n"
"s_waitcnt lgkmcnt(10)\n"
"v_mfma_f32_32x32x16_bf16 v[208:223], a[20:23], a[160:163], v[208:223]\n"
"ds_read_b64_tr_b16 a[12:13], v87 offset:40960\n"
"ds_read_b64_tr_b16 a[14:15], v87 offset:43008\n"
"v_rcp_f32 v97, v97\n"
"v_mul_f32 v96, v96, v72\n"
"v_cvt_pk_bf16_f32 v107, v102, v103\n"
"s_waitcnt lgkmcnt(10)\n"
"v_mfma_f32_32x32x16_bf16 v[224:239], a[24:27], a[160:163], v[224:239]\n"
"ds_read_b64_tr_b16 a[16:17], v84 offset:41216\n"
"ds_read_b64_tr_b16 a[18:19], v84 offset:43264\n"
"s_add_u32 s68, %[hoff], 0x1000\n"
"buffer_store_dwordx4 v[104:107], v1, %[rsW], s68 offen offset:2048 sc1\n"
"v_mul_f32 v101, s77, v77\n"
"s_waitcnt lgkmcnt(10)\n"
"v_mfma_f32_32x32x16_bf16 v[240:255], a[28:31], a[160:163], v[240:255]\n"
"ds_read_b64_tr_b16 a[20:21], v85 offset:41216\n"
"ds_read_b64_tr_b16 a[22:23], v85 offset:43264\n"
"v_exp_f32 v100, v100\n"
"v_add_f32 v99, 1.0, v99\n"
"s_waitcnt lgkmcnt(10)\n"
"v_mfma_f32_32x32x16_bf16 v[128:143], a[0:3], a[164:167], v[128:143]\n"
"ds_read_b64_tr_b16 a[24:25], v86 offset:41216\n"
"ds_read_b64_tr_b16 a[26:27], v86 offset:43264\n"
"s_add_u32 s71, %[q0], 0x187800\n"
"buffer_load_dwordx4 v80, %[rsQ], s71 offen offset:2048 lds\n"
"s_waitcnt lgkmcnt(10)\n"
"v_mfma_f32_32x32x16_bf16 v[144:159], a[4:7], a[164:167], v[144:159]\n"
"ds_read_b64_tr_b16 a[28:29], v87 offset:41216\n"
"ds_read_b64_tr_b16 a[30:31], v87 offset:43264\n"
"v_rcp_f32 v98, v98\n"
"v_mul_f32 v97, v97, v73\n"
"v_mul_f32 v102, s77, v78\n"
"s_waitcnt lgkmcnt(10)\n"
"v_mfma_f32_32x32x16_bf16 v[160:175], a[8:11], a[164:167], v[160:175]\n"
"ds_read_b64_tr_b16 a[0:1], v84 offset:49152\n"
"ds_read_b64_tr_b16 a[2:3], v84 offset:51200\n"
"s_add_u32 s82, %[xoff], 0x6000\n"
"buffer_load_dwordx4 a[160:163], v1, %[rsW], s82 offen sc1\n"
"v_exp_f32 v101, v101\n"
"s_waitcnt lgkmcnt(10)\n"
"v_mfma_f32_32x32x16_bf16 v[176:191], a[12:15], a[164:167], v[176:191]\n"
"ds_read_b64_tr_b16 a[4:5], v85 offset:49152\n"
"ds_read_b64_tr_b16 a[6:7], v85 offset:51200\n"
"v_add_f32 v100, 1.0, v100\n"
"v_rcp_f32 v99, v99\n"
"v_mul_f32 v98, v98, v74\n"
"s_waitcnt lgkmcnt(10)\n"
"v_mfma_f32_32x32x16_bf16 v[192:207], a[16:19], a[164:167], v[192:207]\n"
"ds_read_b64_tr_b16 a[8:9], v86 offset:49152\n"
"ds_read_b64_tr_b16 a[10:11], v86 offset:51200\n"
"s_add_u32 s71, %[q0], 0x18b400\n"
"buffer_load_dwordx4 v81, %[rsQ], s71 offen offset:3072 lds\n"
"v_cvt_pk_bf16_f32 v108, v96, v97\n"
"s_waitcnt lgkmcnt(10)\n"
"v_mfma_f32_32x32x16_bf16 v[208:223], a[20:23], a[164:167], v[208:223]\n"
"ds_read_b64_tr_b16 a[12:13], v87 offset:49152\n"
"ds_read_b64_tr_b16 a[14:15], v87 offset:51200\n"
"v_mul_f32 v103, s77, v79\n"
"v_exp_f32 v102, v102\n"
"v_add_f32 v101, 1.0, v101\n"
"s_waitcnt lgkmcnt(10)\n"
"v_mfma_f32_32x32x16_bf16 v[224:239], a[24:27], a[164:167], v[224:239]\n"
"ds_read_b64_tr_b16 a[16:17], v84 offset:49408\n"
"ds_read_b64_tr_b16 a[18:19], v84 offset:51456\n"
"v_rcp_f32 v100, v100\n"
"v_mul_f32 v99, v99, v75\n"
"s_waitcnt lgkmcnt(10)\n"
"v_mfma_f32_32x32x16_bf16 v[240:255], a[28:31], a[164:167], v[240:255]\n"
"ds_read_b64_tr_b16 a[20:21], v85 offset:49408\n"
"ds_read_b64_tr_b16 a[22:23], v85 offset:51456\n"
"v_exp_f32 v103, v103\n"
"v_add_f32 v102, 1.0, v102\n"
"s_waitcnt lgkmcnt(10)\n"
"v_mfma_f32_32x32x16_bf16 v[128:143], a[0:3], a[168:171], v[128:143]\n"
"ds_read_b64_tr_b16 a[24:25], v86 offset:49408\n"
"ds_read_b64_tr_b16 a[26:27], v86 offset:51456\n"
"s_add_u32 m0, s74, 0x1000\n"
"s_add_u32 s71, %[q0], 0x190000\n"
"buffer_load_dwordx4 v80, %[rsQ], s71 offen lds\n"
"s_waitcnt lgkmcnt(10)\n"
"v_mfma_f32_32x32x16_bf16 v[144:159], a[4:7], a[168:171], v[144:159]\n"
"ds_read_b64_tr_b16 a[28:29], v87 offset:49408\n"
"ds_read_b64_tr_b16 a[30:31], v87 offset:51456\n"
"v_rcp_f32 v101, v101\n"
"v_mul_f32 v100, v100, v76\n"
"v_cvt_pk_bf16_f32 v109, v98, v99\n"
"s_waitcnt lgkmcnt(10)\n"
"v_mfma_f32_32x32x16_bf16 v[160:175], a[8:11], a[168:171], v[160:175]\n"
"ds_read_b64_tr_b16 a[0:1], v84 offset:57344\n"
"ds_read_b64_tr_b16 a[2:3], v84 offset:59392\n"
"s_add_u32 s82, %[xoff], 0x6000\n"
"buffer_load_dwordx4 a[164:167], v1, %[rsW], s82 offen offset:1024 sc1\n"
"v_add_f32 v103, 1.0, v103\n"
"s_waitcnt lgkmcnt(10)\n"
"v_mfma_f32_32x32x16_bf16 v[176:191], a[12:15], a[168:171], v[176:191]\n"
"ds_read_b64_tr_b16 a[4:5], v85 offset:57344\n"
"ds_read_b64_tr_b16 a[6:7], v85 offset:59392\n"
"v_rcp_f32 v102, v102\n"
"v_mul_f32 v101, v101, v77\n"
"s_waitcnt lgkmcnt(10)\n"
"v_mfma_f32_32x32x16_bf16 v[192:207], a[16:19], a[168:171], v[192:207]\n"
"ds_read_b64_tr_b16 a[8:9], v86 offset:57344\n"
"ds_read_b64_tr_b16 a[10:11], v86 offset:59392\n"
"s_add_u32 s71, %[q0], 0x193c00\n"
"buffer_load_dwordx4 v81, %[rsQ], s71 offen offset:1024 lds\n"
"s_waitcnt lgkmcnt(10)\n"
"v_mfma_f32_32x32x16_bf16 v[208:223], a[20:23], a[168:171], v[208:223]\n"
"ds_read_b64_tr_b16 a[12:13], v87 offset:57344\n"
"ds_read_b64_tr_b16 a[14:15], v87 offset:59392\n"
"v_rcp_f32 v103, v103\n"
"v_mul_f32 v102, v102, v78\n"
"v_cvt_pk_bf16_f32 v110, v100, v101\n"
"s_waitcnt lgkmcnt(10)\n"
"v_mfma_f32_32x32x16_bf16 v[224:239], a[24:27], a[168:171], v[224:239]\n"
"ds_read_b64_tr_b16 a[16:17], v84 offset:57600\n"
"ds_read_b64_tr_b16 a[18:19], v84 offset:59648\n"
"v_mul_f32 v103, v103, v79\n"
"v_cvt_pk_bf16_f32 v111, v102, v103\n"
"s_add_u32 s68, %[hoff], 0x1000\n"
"buffer_store_dwordx4 v[108:111], v1, %[rsW], s68 offen offset:3072 sc1\n"
"s_waitcnt lgkmcnt(10)\n"
"v_mfma_f32_32x32x16_bf16 v[240:255], a[28:31], a[168:171], v[240:255]\n"
"ds_read_b64_tr_b16 a[20:21], v85 offset:57600\n"
"ds_read_b64_tr_b16 a[22:23], v85 offset:59648\n"
"s_mov_b32 s68, 0\n"
"s_waitcnt lgkmcnt(10)\n"
"v_mfma_f32_32x32x16_bf16 v[128:143], a[0:3], a[172:175], v[128:143]\n"
"ds_read_b64_tr_b16 a[24:25], v86 offset:57600\n"
"ds_read_b64_tr_b16 a[26:27], v86 offset:59648\n"
"s_add_u32 s71, %[q0], 0x197800\n"
"buffer_load_dwordx4 v80, %[rsQ], s71 offen offset:2048 lds\n"
"s_waitcnt lgkmcnt(10)\n"
"v_mfma_f32_32x32x16_bf16 v[144:159], a[4:7], a[172:175], v[144:159]\n"
"ds_read_b64_tr_b16 a[28:29], v87 offset:57600\n"
"ds_read_b64_tr_b16 a[30:31], v87 offset:59648\n"
"s_waitcnt lgkmcnt(10)\n"
"v_mfma_f32_32x32x16_bf16 v[160:175], a[8:11], a[172:175], v[160:175]\n"
"v_add_u32 v84, 0x10000, v84\n"
"ds_read_b64_tr_b16 a[0:1], v84\n"
"ds_read_b64_tr_b16 a[2:3], v84 offset:2048\n"
"s_add_u32 s82, %[xoff], 0x6000\n"
"buffer_load_dwordx4 a[168:171], v1, %[rsW], s82 offen offset:2048 sc1\n"
"s_waitcnt lgkmcnt(10)\n"
"v_mfma_f32_32x32x16_bf16 v[176:191], a[12:15], a[172:175], v[176:191]\n"
"v_add_u32 v85, 0x10000, v85\n"
"ds_read_b64_tr_b16 a[4:5], v85\n"
"ds_read_b64_tr_b16 a[6:7], v85 offset:2048\n"
"s_waitcnt lgkmcnt(10)\n"
"v_mfma_f32_32x32x16_bf16 v[192:207], a[16:19], a[172:175], v[192:207]\n"
"v_add_u32 v86, 0x10000, v86\n"
"ds_read_b64_tr_b16 a[8:9], v86\n"
"ds_read_b64_tr_b16 a[10:11], v86 offset:2048\n"
"s_add_u32 s71, %[q0], 0x19b400\n"
"buffer_load_dwordx4 v81, %[rsQ], s71 offen offset:3072 lds\n"
"s_waitcnt lgkmcnt(10)\n"
"v_mfma_f32_32x32x16_bf16 v[208:223], a[20:23], a[172:175], v[208:223]\n"
"v_add_u32 v87, 0x10000, v87\n"
"ds_read_b64_tr_b16 a[12:13], v87\n"
"ds_read_b64_tr_b16 a[14:15], v87 offset:2048\n"
"s_waitcnt lgkmcnt(10)\n"
"v_mfma_f32_32x32x16_bf16 v[224:239], a[24:27], a[172:175], v[224:239]\n"
"ds_read_b64_tr_b16 a[16:17], v84 offset:256\n"
"ds_read_b64_tr_b16 a[18:19], v84 offset:2304\n"
"s_waitcnt lgkmcnt(10)\n"
"v_mfma_f32_32x32x16_bf16 v[240:255], a[28:31], a[172:175], v[240:255]\n"
"ds_read_b64_tr_b16 a[20:21], v85 offset:256\n"
"ds_read_b64_tr_b16 a[22:23], v85 offset:2304\n"
"s_waitcnt vmcnt(27)\n"
"s_barrier\n"
"s_add_u32 s82, %[xoff], 0x6000\n"
"buffer_load_dwordx4 a[172:175], v1, %[rsW], s82 offen offset:3072 sc1\n"
"v_cvt_pk_bf16_f32 v16, v128, v129\n"
"v_cvt_pk_bf16_f32 v17, v130, v131\n"
"v_cvt_pk_bf16_f32 v18, v132, v133\n"
"v_cvt_pk_bf16_f32 v19, v134, v135\n"
"v_cvt_pk_bf16_f32 v20, v136, v137\n"
"v_cvt_pk_bf16_f32 v21, v138, v139\n"
"v_cvt_pk_bf16_f32 v22, v140, v141\n"
"v_cvt_pk_bf16_f32 v23, v142, v143\n"
"v_cvt_pk_bf16_f32 v24, v144, v145\n"
"v_cvt_pk_bf16_f32 v25, v146, v147\n"
"v_cvt_pk_bf16_f32 v26, v148, v149\n"
"v_cvt_pk_bf16_f32 v27, v150, v151\n"
"v_cvt_pk_bf16_f32 v28, v152, v153\n"
"v_cvt_pk_bf16_f32 v29, v154, v155\n"
"v_cvt_pk_bf16_f32 v30, v156, v157\n"
"v_cvt_pk_bf16_f32 v31, v158, v159\n"
"v_cvt_pk_bf16_f32 v32, v160, v161\n"
"v_cvt_pk_bf16_f32 v33, v162, v163\n"
"v_cvt_pk_bf16_f32 v34, v164, v165\n"
"v_cvt_pk_bf16_f32 v35, v166, v167\n"
"v_cvt_pk_bf16_f32 v36, v168, v169\n"
"v_cvt_pk_bf16_f32 v37, v170, v171\n"
"v_cvt_pk_bf16_f32 v38, v172, v173\n"
"v_cvt_pk_bf16_f32 v39, v174, v175\n"
"v_cvt_pk_bf16_f32 v40, v176, v177\n"
"v_cvt_pk_bf16_f32 v41, v178, v179\n"
"v_cvt_pk_bf16_f32 v42, v180, v181\n"
"v_cvt_pk_bf16_f32 v43, v182, v183\n"
"v_cvt_pk_bf16_f32 v44, v184, v185\n"
"v_cvt_pk_bf16_f32 v45, v186, v187\n"
"v_cvt_pk_bf16_f32 v46, v188, v189\n"
"v_cvt_pk_bf16_f32 v47, v190, v191\n"
"s_nop 0\n"
"v_cvt_pk_bf16_f32 v48, v192, v193\n"
"v_cvt_pk_bf16_f32 v49, v194, v195\n"
"v_cvt_pk_bf16_f32 v50, v196, v197\n"
"v_cvt_pk_bf16_f32 v51, v198, v199\n"
"v_cvt_pk_bf16_f32 v52, v200, v201\n"
"v_cvt_pk_bf16_f32 v53, v202, v203\n"
"v_cvt_pk_bf16_f32 v54, v204, v205\n"
"v_cvt_pk_bf16_f32 v55, v206, v207\n"
"v_cvt_pk_bf16_f32 v56, v208, v209\n"
"v_cvt_pk_bf16_f32 v57, v210, v211\n"
"v_cvt_pk_bf16_f32 v58, v212, v213\n"
"v_cvt_pk_bf16_f32 v59, v214, v215\n"
"v_cvt_pk_bf16_f32 v60, v216, v217\n"
"v_cvt_pk_bf16_f32 v61, v218, v219\n"
"v_cvt_pk_bf16_f32 v62, v220, v221\n"
"v_cvt_pk_bf16_f32 v63, v222, v223\n"
"v_cvt_pk_bf16_f32 v64, v224, v225\n"
"v_cvt_pk_bf16_f32 v65, v226, v227\n"
"v_cvt_pk_bf16_f32 v66, v228, v229\n"
"v_cvt_pk_bf16_f32 v67, v230, v231\n"
"v_cvt_pk_bf16_f32 v68, v232, v233\n"
"v_cvt_pk_bf16_f32 v69, v234, v235\n"
"v_cvt_pk_bf16_f32 v70, v236, v237\n"
"v_cvt_pk_bf16_f32 v71, v238, v239\n"
"v_cvt_pk_bf16_f32 v72, v240, v241\n"
"v_cvt_pk_bf16_f32 v73, v242, v243\n"
"v_cvt_pk_bf16_f32 v74, v244, v245\n"
"v_cvt_pk_bf16_f32 v75, v246, v247\n"
"v_cvt_pk_bf16_f32 v76, v248, v249\n"
"v_cvt_pk_bf16_f32 v77, v250, v251\n"
"v_cvt_pk_bf16_f32 v78, v252, v253\n"
"v_cvt_pk_bf16_f32 v79, v254, v255\n"
"s_waitcnt lgkmcnt(10)\n"
"v_mfma_f32_32x32x16_bf16 v[128:143], a[0:3], a[64:67], 0\n"
"ds_read_b64_tr_b16 a[24:25], v86 offset:256\n"
"ds_read_b64_tr_b16 a[26:27], v86 offset:2304\n"
"s_add_u32 m0, s74, 0x8000\n"
"s_add_u32 s71, %[q0], 0x200000\n"
"buffer_load_dwordx4 v80, %[rsQ], s71 offen lds\n"
"s_waitcnt lgkmcnt(10)\n"
"v_mfma_f32_32x32x16_bf16 v[144:159], a[4:7], a[64:67], 0\n"
"ds_read_b64_tr_b16 a[28:29], v87 offset:256\n"
"ds_read_b64_tr_b16 a[30:31], v87 offset:2304\n"
"s_waitcnt lgkmcnt(10)\n"
"v_mfma_f32_32x32x16_bf16 v[160:175], a[8:11], a[64:67], 0\n"
"ds_read_b64_tr_b16 a[0:1], v84 offset:8192\n"
"ds_read_b64_tr_b16 a[2:3], v84 offset:10240\n"
"s_waitcnt lgkmcnt(10)\n"
"v_mfma_f32_32x32x16_bf16 v[176:191], a[12:15], a[64:67], 0\n"
"ds_read_b64_tr_b16 a[4:5], v85 offset:8192\n"
"ds_read_b64_tr_b16 a[6:7], v85 offset:10240\n"
"v_accvgpr_write_b32 a176, v16\n"
"v_accvgpr_write_b32 a177, v17\n"
"v_accvgpr_write_b32 a178, v18\n"
"s_waitcnt lgkmcnt(10)\n"
"v_mfma_f32_32x32x16_bf16 v[192:207], a[16:19], a[64:67], 0\n"
"ds_read_b64_tr_b16 a[8:9], v86 offset:8192\n"
"ds_read_b64_tr_b16 a[10:11], v86 offset:10240\n"
"s_add_u32 s71, %[q0], 0x203c00\n"
"buffer_load_dwordx4 v81, %[rsQ], s71 offen offset:1024 lds\n"
"v_accvgpr_write_b32 a179, v19\n"
"s_waitcnt lgkmcnt(10)\n"
"v_mfma_f32_32x32x16_bf16 v[208:223], a[20:23], a[64:67], 0\n"
"ds_read_b64_tr_b16 a[12:13], v87 offset:8192\n"
"ds_read_b64_tr_b16 a[14:15], v87 offset:10240\n"
"v_accvgpr_write_b32 a180, v20\n"
"v_accvgpr_write_b32 a181, v21\n"
"v_accvgpr_write_b32 a182, v22\n"
"s_waitcnt lgkmcnt(10)\n"
"v_mfma_f32_32x32x16_bf16 v[224:239], a[24:27], a[64:67], 0\n"
"ds_read_b64_tr_b16 a[16:17], v84 offset:8448\n"
"ds_read_b64_tr_b16 a[18:19], v84 offset:10496\n"
"v_accvgpr_write_b32 a183, v23\n"
"v_accvgpr_write_b32 a184, v24\n"
"v_accvgpr_write_b32 a185, v25\n"
"s_waitcnt lgkmcnt(10)\n"
"v_mfma_f32_32x32x16_bf16 v[240:255], a[28:31], a[64:67], 0\n"
"ds_read_b64_tr_b16 a[20:21], v85 offset:8448\n"
"ds_read_b64_tr_b16 a[22:23], v85 offset:10496\n"
"v_accvgpr_write_b32 a186, v26\n"
"v_accvgpr_write_b32 a187, v27\n"
"v_accvgpr_write_b32 a188, v28\n"
"s_waitcnt lgkmcnt(10)\n"
"v_mfma_f32_32x32x16_bf16 v[128:143], a[0:3], a[68:71], v[128:143]\n"
"ds_read_b64_tr_b16 a[24:25], v86 offset:8448\n"
"ds_read_b64_tr_b16 a[26:27], v86 offset:10496\n"
"s_add_u32 s71, %[q0], 0x207800\n"
"buffer_load_dwordx4 v80, %[rsQ], s71 offen offset:2048 lds\n"
"v_accvgpr_write_b32 a189, v29\n"
"s_waitcnt lgkmcnt(10)\n"
"v_mfma_f32_32x32x16_bf16 v[144:159], a[4:7], a[68:71], v[144:159]\n"
"ds_read_b64_tr_b16 a[28:29], v87 offset:8448\n"
"ds_read_b64_tr_b16 a[30:31], v87 offset:10496\n"
"v_accvgpr_write_b32 a190, v30\n"
"v_accvgpr_write_b32 a191, v31\n"
"v_accvgpr_write_b32 a192, v32\n"
"s_waitcnt lgkmcnt(10)\n"
"v_mfma_f32_32x32x16_bf16 v[160:175], a[8:11], a[68:71], v[160:175]\n"
"ds_read_b64_tr_b16 a[0:1], v84 offset:16384\n"
"ds_read_b64_tr_b16 a[2:3], v84 offset:18432\n"
"v_accvgpr_write_b32 a193, v33\n"
"v_accvgpr_write_b32 a194, v34\n"
"v_accvgpr_write_b32 a195, v35\n"
"s_waitcnt lgkmcnt(10)\n"
"v_mfma_f32_32x32x16_bf16 v[176:191], a[12:15], a[68:71], v[176:191]\n"
"ds_read_b64_tr_b16 a[4:5], v85 offset:16384\n"
"ds_read_b64_tr_b16 a[6:7], v85 offset:18432\n"
"v_accvgpr_write_b32 a196, v36\n"
"v_accvgpr_write_b32 a197, v37\n"
"v_accvgpr_write_b32 a198, v38\n"
"s_waitcnt lgkmcnt(10)\n"
"v_mfma_f32_32x32x16_bf16 v[192:207], a[16:19], a[68:71], v[192:207]\n"
"ds_read_b64_tr_b16 a[8:9], v86 offset:16384\n"
"ds_read_b64_tr_b16 a[10:11], v86 offset:18432\n"
"s_add_u32 s71, %[q0], 0x20b400\n"
"buffer_load_dwordx4 v81, %[rsQ], s71 offen offset:3072 lds\n"
"v_accvgpr_write_b32 a199, v39\n"
"s_waitcnt lgkmcnt(10)\n"
"v_mfma_f32_32x32x16_bf16 v[208:223], a[20:23], a[68:71], v[208:223]\n"
"ds_read_b64_tr_b16 a[12:13], v87 offset:16384\n"
"ds_read_b64_tr_b16 a[14:15], v87 offset:18432\n"
"v_accvgpr_write_b32 a200, v40\n"
"v_accvgpr_write_b32 a201, v41\n"
"v_accvgpr_write_b32 a202, v42\n"
"s_waitcnt lgkmcnt(10)\n"
"v_mfma_f32_32x32x16_bf16 v[224:239], a[24:27], a[68:71], v[224:239]\n"
"ds_read_b64_tr_b16 a[16:17], v84 offset:16640\n"
"ds_read_b64_tr_b16 a[18:19], v84 offset:18688\n"
"v_accvgpr_write_b32 a203, v43\n"
"v_accvgpr_write_b32 a204, v44\n"
"v_accvgpr_write_b32 a205, v45\n"
"s_waitcnt lgkmcnt(10)\n"
"v_mfma_f32_32x32x16_bf16 v[240:255], a[28:31], a[68:71], v[240:255]\n"
"ds_read_b64_tr_b16 a[20:21], v85 offset:16640\n"
"ds_read_b64_tr_b16 a[22:23], v85 offset:18688\n"
"v_accvgpr_write_b32 a206, v46\n"
"v_accvgpr_write_b32 a207, v47\n"
"v_accvgpr_write_b32 a208, v48\n"
"s_waitcnt lgkmcnt(10)\n"
"v_mfma_f32_32x32x16_bf16 v[128:143], a[0:3], a[72:75], v[128:143]\n"
"ds_read_b64_tr_b16 a[24:25], v86 offset:16640\n"
"ds_read_b64_tr_b16 a[26:27], v86 offset:18688\n"
"s_add_u32 m0, s74, 0x9000\n"
"s_add_u32 s71, %[q0], 0x210000\n"
"buffer_load_dwordx4 v80, %[rsQ], s71 offen lds\n"
"v_accvgpr_write_b32 a209, v49\n"
"s_waitcnt lgkmcnt(10)\n"
"v_mfma_f32_32x32x16_bf16 v[144:159], a[4:7], a[72:75], v[144:159]\n"
"ds_read_b64_tr_b16 a[28:29], v87 offset:16640\n"
"ds_read_b64_tr_b16 a[30:31], v87 offset:18688\n"
"v_accvgpr_write_b32 a210, v50\n"
"v_accvgpr_write_b32 a211, v51\n"
"v_accvgpr_write_b32 a212, v52\n"
"s_waitcnt lgkmcnt(10)\n"
"v_mfma_f32_32x32x16_bf16 v[160:175], a[8:11], a[72:75], v[160:175]\n"
"ds_read_b64_tr_b16 a[0:1], v84 offset:24576\n"
"ds_read_b64_tr_b16 a[2:3], v84 offset:26624\n"
"v_accvgpr_write_b32 a213, v53\n"
"v_accvgpr_write_b32 a214, v54\n"
"v_accvgpr_write_b32 a215, v55\n"
"s_waitcnt lgkmcnt(10)\n"
"v_mfma_f32_32x32x16_bf16 v[176:191], a[12:15], a[72:75], v[176:191]\n"
"ds_read_b64_tr_b16 a[4:5], v85 offset:24576\n"
"ds_read_b64_tr_b16 a[6:7], v85 offset:26624\n"
"v_accvgpr_write_b32 a216, v56\n"
"v_accvgpr_write_b32 a217, v57\n"
"v_accvgpr_write_b32 a218, v58\n"
"s_waitcnt lgkmcnt(10)\n"
"v_mfma_f32_32x32x16_bf16 v[192:207], a[16:19], a[72:75], v[192:207]\n"
"ds_read_b64_tr_b16 a[8:9], v86 offset:24576\n"
"ds_read_b64_tr_b16 a[10:11], v86 offset:26624\n"
"s_add_u32 s71, %[q0], 0x213c00\n"
"buffer_load_dwordx4 v81, %[rsQ], s71 offen offset:1024 lds\n"
"v_accvgpr_write_b32 a219, v59\n"
"s_waitcnt lgkmcnt(10)\n"
"v_mfma_f32_32x32x16_bf16 v[208:223], a[20:23], a[72:75], v[208:223]\n"
"ds_read_b64_tr_b16 a[12:13], v87 offset:24576\n"
"ds_read_b64_tr_b16 a[14:15], v87 offset:26624\n"
"v_accvgpr_write_b32 a220, v60\n"
"v_accvgpr_write_b32 a221, v61\n"
"v_accvgpr_write_b32 a222, v62\n"
"s_waitcnt lgkmcnt(10)\n"
"v_mfma_f32_32x32x16_bf16 v[224:239], a[24:27], a[72:75], v[224:239]\n"
"ds_read_b64_tr_b16 a[16:17], v84 offset:24832\n"
"ds_read_b64_tr_b16 a[18:19], v84 offset:26880\n"
"v_accvgpr_write_b32 a223, v63\n"
"v_accvgpr_write_b32 a224, v64\n"
"v_accvgpr_write_b32 a225, v65\n"
"s_waitcnt lgkmcnt(10)\n"
"v_mfma_f32_32x32x16_bf16 v[240:255], a[28:31], a[72:75], v[240:255]\n"
"ds_read_b64_tr_b16 a[20:21], v85 offset:24832\n"
"ds_read_b64_tr_b16 a[22:23], v85 offset:26880\n"
"v_accvgpr_write_b32 a226, v66\n"
"v_accvgpr_write_b32 a227, v67\n"
"v_accvgpr_write_b32 a228, v68\n"
"s_waitcnt lgkmcnt(10)\n"
"v_mfma_f32_32x32x16_bf16 v[128:143], a[0:3], a[76:79], v[128:143]\n"
"ds_read_b64_tr_b16 a[24:25], v86 offset:24832\n"
"ds_read_b64_tr_b16 a[26:27], v86 offset:26880\n"
"s_add_u32 s71, %[q0], 0x217800\n"
"buffer_load_dwordx4 v80, %[rsQ], s71 offen offset:2048 lds\n"
"v_accvgpr_write_b32 a229, v69\n"
"s_waitcnt lgkmcnt(10)\n"
"v_mfma_f32_32x32x16_bf16 v[144:159], a[4:7], a[76:79], v[144:159]\n"
"ds_read_b64_tr_b16 a[28:29], v87 offset:24832\n"
"ds_read_b64_tr_b16 a[30:31], v87 offset:26880\n"
"v_accvgpr_write_b32 a230, v70\n"
"v_accvgpr_write_b32 a231, v71\n"
"v_accvgpr_write_b32 a232, v72\n"
"s_waitcnt lgkmcnt(10)\n"
"v_mfma_f32_32x32x16_bf16 v[160:175], a[8:11], a[76:79], v[160:175]\n"
"ds_read_b64_tr_b16 a[0:1], v84 offset:32768\n"
"ds_read_b64_tr_b16 a[2:3], v84 offset:34816\n"
"v_accvgpr_write_b32 a233, v73\n"
"v_accvgpr_write_b32 a234, v74\n"
"v_accvgpr_write_b32 a235, v75\n"
"s_waitcnt lgkmcnt(10)\n"
"v_mfma_f32_32x32x16_bf16 v[176:191], a[12:15], a[76:79], v[176:191]\n"
"ds_read_b64_tr_b16 a[4:5], v85 offset:32768\n"
"ds_read_b64_tr_b16 a[6:7], v85 offset:34816\n"
"v_accvgpr_write_b32 a236, v76\n"
"v_accvgpr_write_b32 a237, v77\n"
"v_accvgpr_write_b32 a238, v78\n"
"s_waitcnt lgkmcnt(10)\n"
"v_mfma_f32_32x32x16_bf16 v[192:207], a[16:19], a[76:79], v[192:207]\n"
"ds_read_b64_tr_b16 a[8:9], v86 offset:32768\n"
"ds_read_b64_tr_b16 a[10:11], v86 offset:34816\n"
"s_add_u32 s71, %[q0], 0x21b400\n"
"buffer_load_dwordx4 v81, %[rsQ], s71 offen offset:3072 lds\n"
"v_accvgpr_write_b32 a239, v79\n"
"s_waitcnt lgkmcnt(10)\n"
"v_mfma_f32_32x32x16_bf16 v[208:223], a[20:23], a[76:79], v[208:223]\n"
"ds_read_b64_tr_b16 a[12:13], v87 offset:32768\n"
"ds_read_b64_tr_b16 a[14:15], v87 offset:34816\n"
"s_waitcnt lgkmcnt(10)\n"
"v_mfma_f32_32x32x16_bf16 v[224:239], a[24:27], a[76:79], v[224:239]\n"
"ds_read_b64_tr_b16 a[16:17], v84 offset:33024\n"
"ds_read_b64_tr_b16 a[18:19], v84 offset:35072\n"
"s_waitcnt lgkmcnt(10)\n"
"v_mfma_f32_32x32x16_bf16 v[240:255], a[28:31], a[76:79], v[240:255]\n"
"ds_read_b64_tr_b16 a[20:21], v85 offset:33024\n"
"ds_read_b64_tr_b16 a[22:23], v85 offset:35072\n"
"s_waitcnt vmcnt(23)\n"
"s_barrier\n"
"s_waitcnt lgkmcnt(10)\n"
"v_mfma_f32_32x32x16_bf16 v[128:143], a[0:3], a[80:83], v[128:143]\n"
"ds_read_b64_tr_b16 a[24:25], v86 offset:33024\n"
"ds_read_b64_tr_b16 a[26:27], v86 offset:35072\n"
"s_add_u32 m0, s74, 0x10000\n"
"s_add_u32 s71, %[q0], 0x280000\n"
"buffer_load_dwordx4 v80, %[rsQ], s71 offen lds\n"
"s_waitcnt lgkmcnt(10)\n"
"v_mfma_f32_32x32x16_bf16 v[144:159], a[4:7], a[80:83], v[144:159]\n"
"ds_read_b64_tr_b16 a[28:29], v87 offset:33024\n"
"ds_read_b64_tr_b16 a[30:31], v87 offset:35072\n"
"s_waitcnt lgkmcnt(10)\n"
"v_mfma_f32_32x32x16_bf16 v[160:175], a[8:11], a[80:83], v[160:175]\n"
"ds_read_b64_tr_b16 a[0:1], v84 offset:40960\n"
"ds_read_b64_tr_b16 a[2:3], v84 offset:43008\n"
"s_waitcnt lgkmcnt(10)\n"
"v_mfma_f32_32x32x16_bf16 v[176:191], a[12:15], a[80:83], v[176:191]\n"
"ds_read_b64_tr_b16 a[4:5], v85 offset:40960\n"
"ds_read_b64_tr_b16 a[6:7], v85 offset:43008\n"
"s_waitcnt lgkmcnt(10)\n"
"v_mfma_f32_32x32x16_bf16 v[192:207], a[16:19], a[80:83], v[192:207]\n"
"ds_read_b64_tr_b16 a[8:9], v86 offset:40960\n"
"ds_read_b64_tr_b16 a[10:11], v86 offset:43008\n"
"s_add_u32 s71, %[q0], 0x283c00\n"
"buffer_load_dwordx4 v81, %[rsQ], s71 offen offset:1024 lds\n"
"s_waitcnt lgkmcnt(10)\n"
"v_mfma_f32_32x32x16_bf16 v[208:223], a[20:23], a[80:83], v[208:223]\n"
"ds_read_b64_tr_b16 a[12:13], v87 offset:40960\n"
"ds_read_b64_tr_b16 a[14:15], v87 offset:43008\n"
"s_waitcnt lgkmcnt(10)\n"
"v_mfma_f32_32x32x16_bf16 v[224:239], a[24:27], a[80:83], v[224:239]\n"
"ds_read_b64_tr_b16 a[16:17], v84 offset:41216\n"
"ds_read_b64_tr_b16 a[18:19], v84 offset:43264\n"
"s_waitcnt lgkmcnt(10)\n"
"v_mfma_f32_32x32x16_bf16 v[240:255], a[28:31], a[80:83], v[240:255]\n"
"ds_read_b64_tr_b16 a[20:21], v85 offset:41216\n"
"ds_read_b64_tr_b16 a[22:23], v85 offset:43264\n"
"s_waitcnt lgkmcnt(10)\n"
"v_mfma_f32_32x32x16_bf16 v[128:143], a[0:3], a[84:87], v[128:143]\n"
"ds_read_b64_tr_b16 a[24:25], v86 offset:41216\n"
"ds_read_b64_tr_b16 a[26:27], v86 offset:43264\n"
"s_add_u32 s71, %[q0], 0x287800\n"
"buffer_load_dwordx4 v80, %[rsQ], s71 offen offset:2048 lds\n"
"s_waitcnt lgkmcnt(10)\n"
"v_mfma_f32_32x32x16_bf16 v[144:159], a[4:7], a[84:87], v[144:159]\n"
"ds_read_b64_tr_b16 a[28:29], v87 offset:41216\n"
"ds_read_b64_tr_b16 a[30:31], v87 offset:43264\n"
"s_waitcnt lgkmcnt(10)\n"
"v_mfma_f32_32x32x16_bf16 v[160:175], a[8:11], a[84:87], v[160:175]\n"
"ds_read_b64_tr_b16 a[0:1], v84 offset:49152\n"
"ds_read_b64_tr_b16 a[2:3], v84 offset:51200\n"
"s_waitcnt lgkmcnt(10)\n"
"v_mfma_f32_32x32x16_bf16 v[176:191], a[12:15], a[84:87], v[176:191]\n"
"ds_read_b64_tr_b16 a[4:5], v85 offset:49152\n"
"ds_read_b64_tr_b16 a[6:7], v85 offset:51200\n"
"s_waitcnt lgkmcnt(10)\n"
"v_mfma_f32_32x32x16_bf16 v[192:207], a[16:19], a[84:87], v[192:207]\n"
"ds_read_b64_tr_b16 a[8:9], v86 offset:49152\n"
"ds_read_b64_tr_b16 a[10:11], v86 offset:51200\n"
"s_add_u32 s71, %[q0], 0x28b400\n"
"buffer_load_dwordx4 v81, %[rsQ], s71 offen offset:3072 lds\n"
"s_waitcnt lgkmcnt(10)\n"
"v_mfma_f32_32x32x16_bf16 v[208:223], a[20:23], a[84:87], v[208:223]\n"
"ds_read_b64_tr_b16 a[12:13], v87 offset:49152\n"
"ds_read_b64_tr_b16 a[14:15], v87 offset:51200\n"
"s_waitcnt lgkmcnt(10)\n"
"v_mfma_f32_32x32x16_bf16 v[224:239], a[24:27], a[84:87], v[224:239]\n"
"ds_read_b64_tr_b16 a[16:17], v84 offset:49408\n"
"ds_read_b64_tr_b16 a[18:19], v84 offset:51456\n"
"s_waitcnt lgkmcnt(10)\n"
"v_mfma_f32_32x32x16_bf16 v[240:255], a[28:31], a[84:87], v[240:255]\n"
"ds_read_b64_tr_b16 a[20:21], v85 offset:49408\n"
"ds_read_b64_tr_b16 a[22:23], v85 offset:51456\n"
"s_waitcnt lgkmcnt(10)\n"
"v_mfma_f32_32x32x16_bf16 v[128:143], a[0:3], a[88:91], v[128:143]\n"
"ds_read_b64_tr_b16 a[24:25], v86 offset:49408\n"
"ds_read_b64_tr_b16 a[26:27], v86 offset:51456\n"
"s_add_u32 m0, s74, 0x11000\n"
"s_add_u32 s71, %[q0], 0x290000\n"
"buffer_load_dwordx4 v80, %[rsQ], s71 offen lds\n"
"s_waitcnt lgkmcnt(10)\n"
"v_mfma_f32_32x32x16_bf16 v[144:159], a[4:7], a[88:91], v[144:159]\n"
"ds_read_b64_tr_b16 a[28:29], v87 offset:49408\n"
"ds_read_b64_tr_b16 a[30:31], v87 offset:51456\n"
"s_waitcnt lgkmcnt(10)\n"
"v_mfma_f32_32x32x16_bf16 v[160:175], a[8:11], a[88:91], v[160:175]\n"
"ds_read_b64_tr_b16 a[0:1], v84 offset:57344\n"
"ds_read_b64_tr_b16 a[2:3], v84 offset:59392\n"
"s_waitcnt lgkmcnt(10)\n"
"v_mfma_f32_32x32x16_bf16 v[176:191], a[12:15], a[88:91], v[176:191]\n"
"ds_read_b64_tr_b16 a[4:5], v85 offset:57344\n"
"ds_read_b64_tr_b16 a[6:7], v85 offset:59392\n"
"s_waitcnt lgkmcnt(10)\n"
"v_mfma_f32_32x32x16_bf16 v[192:207], a[16:19], a[88:91], v[192:207]\n"
"ds_read_b64_tr_b16 a[8:9], v86 offset:57344\n"
"ds_read_b64_tr_b16 a[10:11], v86 offset:59392\n"
"s_add_u32 s71, %[q0], 0x293c00\n"
"buffer_load_dwordx4 v81, %[rsQ], s71 offen offset:1024 lds\n"
"s_waitcnt lgkmcnt(10)\n"
"v_mfma_f32_32x32x16_bf16 v[208:223], a[20:23], a[88:91], v[208:223]\n"
"ds_read_b64_tr_b16 a[12:13], v87 offset:57344\n"
"ds_read_b64_tr_b16 a[14:15], v87 offset:59392\n"
"s_waitcnt lgkmcnt(10)\n"
"v_mfma_f32_32x32x16_bf16 v[224:239], a[24:27], a[88:91], v[224:239]\n"
"ds_read_b64_tr_b16 a[16:17], v84 offset:57600\n"
"ds_read_b64_tr_b16 a[18:19], v84 offset:59648\n"
"s_waitcnt lgkmcnt(10)\n"
"v_mfma_f32_32x32x16_bf16 v[240:255], a[28:31], a[88:91], v[240:255]\n"
"ds_read_b64_tr_b16 a[20:21], v85 offset:57600\n"
"ds_read_b64_tr_b16 a[22:23], v85 offset:59648\n"
"s_waitcnt lgkmcnt(10)\n"
"v_mfma_f32_32x32x16_bf16 v[128:143], a[0:3], a[92:95], v[128:143]\n"
"ds_read_b64_tr_b16 a[24:25], v86 offset:57600\n"
"ds_read_b64_tr_b16 a[26:27], v86 offset:59648\n"
"s_add_u32 s71, %[q0], 0x297800\n"
"buffer_load_dwordx4 v80, %[rsQ], s71 offen offset:2048 lds\n"
"s_waitcnt lgkmcnt(10)\n"
"v_mfma_f32_32x32x16_bf16 v[144:159], a[4:7], a[92:95], v[144:159]\n"
"ds_read_b64_tr_b16 a[28:29], v87 offset:57600\n"
"ds_read_b64_tr_b16 a[30:31], v87 offset:59648\n"
"s_waitcnt lgkmcnt(10)\n"
"v_mfma_f32_32x32x16_bf16 v[160:175], a[8:11], a[92:95], v[160:175]\n"
"v_add_u32 v84, 0x10000, v84\n"
"ds_read_b64_tr_b16 a[0:1], v84\n"
"ds_read_b64_tr_b16 a[2:3], v84 offset:2048\n"
"s_waitcnt lgkmcnt(10)\n"
"v_mfma_f32_32x32x16_bf16 v[176:191], a[12:15], a[92:95], v[176:191]\n"
"v_add_u32 v85, 0x10000, v85\n"
"ds_read_b64_tr_b16 a[4:5], v85\n"
"ds_read_b64_tr_b16 a[6:7], v85 offset:2048\n"
"s_waitcnt lgkmcnt(10)\n"
"v_mfma_f32_32x32x16_bf16 v[192:207], a[16:19], a[92:95], v[192:207]\n"
"v_add_u32 v86, 0x10000, v86\n"
"ds_read_b64_tr_b16 a[8:9], v86\n"
"ds_read_b64_tr_b16 a[10:11], v86 offset:2048\n"
"s_add_u32 s71, %[q0], 0x29b400\n"
"buffer_load_dwordx4 v81, %[rsQ], s71 offen offset:3072 lds\n"
"s_waitcnt lgkmcnt(10)\n"
"v_mfma_f32_32x32x16_bf16 v[208:223], a[20:23], a[92:95], v[208:223]\n"
"v_add_u32 v87, 0x10000, v87\n"
"ds_read_b64_tr_b16 a[12:13], v87\n"
"ds_read_b64_tr_b16 a[14:15], v87 offset:2048\n"
"s_waitcnt lgkmcnt(10)\n"
"v_mfma_f32_32x32x16_bf16 v[224:239], a[24:27], a[92:95], v[224:239]\n"
"ds_read_b64_tr_b16 a[16:17], v84 offset:256\n"
"ds_read_b64_tr_b16 a[18:19], v84 offset:2304\n"
"s_waitcnt lgkmcnt(10)\n"
"v_mfma_f32_32x32x16_bf16 v[240:255], a[28:31], a[92:95], v[240:255]\n"
"ds_read_b64_tr_b16 a[20:21], v85 offset:256\n"
"ds_read_b64_tr_b16 a[22:23], v85 offset:2304\n"
"s_waitcnt vmcnt(17)\n"
"s_barrier\n"
"s_waitcnt lgkmcnt(10)\n"
"v_mfma_f32_32x32x16_bf16 v[128:143], a[0:3], a[96:99], v[128:143]\n"
"ds_read_b64_tr_b16 a[24:25], v86 offset:256\n"
"ds_read_b64_tr_b16 a[26:27], v86 offset:2304\n"
"s_add_u32 m0, s74, 0x18000\n"
"s_add_u32 s71, %[q0], 0x300000\n"
"buffer_load_dwordx4 v80, %[rsQ], s71 offen lds\n"
"s_waitcnt lgkmcnt(10)\n"
"v_mfma_f32_32x32x16_bf16 v[144:159], a[4:7], a[96:99], v[144:159]\n"
"ds_read_b64_tr_b16 a[28:29], v87 offset:256\n"
"ds_read_b64_tr_b16 a[30:31], v87 offset:2304\n"
"s_waitcnt lgkmcnt(10)\n"
"v_mfma_f32_32x32x16_bf16 v[160:175], a[8:11], a[96:99], v[160:175]\n"
"ds_read_b64_tr_b16 a[0:1], v84 offset:8192\n"
"ds_read_b64_tr_b16 a[2:3], v84 offset:10240\n"
"s_waitcnt lgkmcnt(10)\n"
"v_mfma_f32_32x32x16_bf16 v[176:191], a[12:15], a[96:99], v[176:191]\n"
"ds_read_b64_tr_b16 a[4:5], v85 offset:8192\n"
"ds_read_b64_tr_b16 a[6:7], v85 offset:10240\n"
"s_waitcnt lgkmcnt(10)\n"
"v_mfma_f32_32x32x16_bf16 v[192:207], a[16:19], a[96:99], v[192:207]\n"
"ds_read_b64_tr_b16 a[8:9], v86 offset:8192\n"
"ds_read_b64_tr_b16 a[10:11], v86 offset:10240\n"
"s_add_u32 s71, %[q0], 0x303c00\n"
"buffer_load_dwordx4 v81, %[rsQ], s71 offen offset:1024 lds\n"
"s_waitcnt lgkmcnt(10)\n"
"v_mfma_f32_32x32x16_bf16 v[208:223], a[20:23], a[96:99], v[208:223]\n"
"ds_read_b64_tr_b16 a[12:13], v87 offset:8192\n"
"ds_read_b64_tr_b16 a[14:15], v87 offset:10240\n"
"s_waitcnt lgkmcnt(10)\n"
"v_mfma_f32_32x32x16_bf16 v[224:239], a[24:27], a[96:99], v[224:239]\n"
"ds_read_b64_tr_b16 a[16:17], v84 offset:8448\n"
"ds_read_b64_tr_b16 a[18:19], v84 offset:10496\n"
"s_waitcnt lgkmcnt(10)\n"
"v_mfma_f32_32x32x16_bf16 v[240:255], a[28:31], a[96:99], v[240:255]\n"
"ds_read_b64_tr_b16 a[20:21], v85 offset:8448\n"
"ds_read_b64_tr_b16 a[22:23], v85 offset:10496\n"
"s_waitcnt lgkmcnt(10)\n"
"v_mfma_f32_32x32x16_bf16 v[128:143], a[0:3], a[100:103], v[128:143]\n"
"ds_read_b64_tr_b16 a[24:25], v86 offset:8448\n"
"ds_read_b64_tr_b16 a[26:27], v86 offset:10496\n"
"s_add_u32 s71, %[q0], 0x307800\n"
"buffer_load_dwordx4 v80, %[rsQ], s71 offen offset:2048 lds\n"
"s_waitcnt lgkmcnt(10)\n"
"v_mfma_f32_32x32x16_bf16 v[144:159], a[4:7], a[100:103], v[144:159]\n"
"ds_read_b64_tr_b16 a[28:29], v87 offset:8448\n"
"ds_read_b64_tr_b16 a[30:31], v87 offset:10496\n"
"s_waitcnt lgkmcnt(10)\n"
"v_mfma_f32_32x32x16_bf16 v[160:175], a[8:11], a[100:103], v[160:175]\n"
"ds_read_b64_tr_b16 a[0:1], v84 offset:16384\n"
"ds_read_b64_tr_b16 a[2:3], v84 offset:18432\n"
"s_waitcnt lgkmcnt(10)\n"
"v_mfma_f32_32x32x16_bf16 v[176:191], a[12:15], a[100:103], v[176:191]\n"
"ds_read_b64_tr_b16 a[4:5], v85 offset:16384\n"
"ds_read_b64_tr_b16 a[6:7], v85 offset:18432\n"
"s_waitcnt lgkmcnt(10)\n"
"v_mfma_f32_32x32x16_bf16 v[192:207], a[16:19], a[100:103], v[192:207]\n"
"ds_read_b64_tr_b16 a[8:9], v86 offset:16384\n"
"ds_read_b64_tr_b16 a[10:11], v86 offset:18432\n"
"s_add_u32 s71, %[q0], 0x30b400\n"
"buffer_load_dwordx4 v81, %[rsQ], s71 offen offset:3072 lds\n"
"s_waitcnt lgkmcnt(10)\n"
"v_mfma_f32_32x32x16_bf16 v[208:223], a[20:23], a[100:103], v[208:223]\n"
"ds_read_b64_tr_b16 a[12:13], v87 offset:16384\n"
"ds_read_b64_tr_b16 a[14:15], v87 offset:18432\n"
"s_waitcnt lgkmcnt(10)\n"
"v_mfma_f32_32x32x16_bf16 v[224:239], a[24:27], a[100:103], v[224:239]\n"
"ds_read_b64_tr_b16 a[16:17], v84 offset:16640\n"
"ds_read_b64_tr_b16 a[18:19], v84 offset:18688\n"
"s_waitcnt lgkmcnt(10)\n"
"v_mfma_f32_32x32x16_bf16 v[240:255], a[28:31], a[100:103], v[240:255]\n"
"ds_read_b64_tr_b16 a[20:21], v85 offset:16640\n"
"ds_read_b64_tr_b16 a[22:23], v85 offset:18688\n"
"s_waitcnt lgkmcnt(10)\n"
"v_mfma_f32_32x32x16_bf16 v[128:143], a[0:3], a[104:107], v[128:143]\n"
"ds_read_b64_tr_b16 a[24:25], v86 offset:16640\n"
"ds_read_b64_tr_b16 a[26:27], v86 offset:18688\n"
"s_add_u32 m0, s74, 0x19000\n"
"s_add_u32 s71, %[q0], 0x310000\n"
"buffer_load_dwordx4 v80, %[rsQ], s71 offen lds\n"
"s_waitcnt lgkmcnt(10)\n"
"v_mfma_f32_32x32x16_bf16 v[144:159], a[4:7], a[104:107], v[144:159]\n"
"ds_read_b64_tr_b16 a[28:29], v87 offset:16640\n"
"ds_read_b64_tr_b16 a[30:31], v87 offset:18688\n"
"s_waitcnt lgkmcnt(10)\n"
"v_mfma_f32_32x32x16_bf16 v[160:175], a[8:11], a[104:107], v[160:175]\n"
"ds_read_b64_tr_b16 a[0:1], v84 offset:24576\n"
"ds_read_b64_tr_b16 a[2:3], v84 offset:26624\n"
"s_waitcnt lgkmcnt(10)\n"
"v_mfma_f32_32x32x16_bf16 v[176:191], a[12:15], a[104:107], v[176:191]\n"
"ds_read_b64_tr_b16 a[4:5], v85 offset:24576\n"
"ds_read_b64_tr_b16 a[6:7], v85 offset:26624\n"
"s_waitcnt lgkmcnt(10)\n"
"v_mfma_f32_32x32x16_bf16 v[192:207], a[16:19], a[104:107], v[192:207]\n"
"ds_read_b64_tr_b16 a[8:9], v86 offset:24576\n"
"ds_read_b64_tr_b16 a[10:11], v86 offset:26624\n"
"s_add_u32 s71, %[q0], 0x313c00\n"
"buffer_load_dwordx4 v81, %[rsQ], s71 offen offset:1024 lds\n"
"s_waitcnt lgkmcnt(10)\n"
"v_mfma_f32_32x32x16_bf16 v[208:223], a[20:23], a[104:107], v[208:223]\n"
"ds_read_b64_tr_b16 a[12:13], v87 offset:24576\n"
"ds_read_b64_tr_b16 a[14:15], v87 offset:26624\n"
"s_waitcnt lgkmcnt(10)\n"
"v_mfma_f32_32x32x16_bf16 v[224:239], a[24:27], a[104:107], v[224:239]\n"
"ds_read_b64_tr_b16 a[16:17], v84 offset:24832\n"
"ds_read_b64_tr_b16 a[18:19], v84 offset:26880\n"
"s_waitcnt lgkmcnt(10)\n"
"v_mfma_f32_32x32x16_bf16 v[240:255], a[28:31], a[104:107], v[240:255]\n"
"ds_read_b64_tr_b16 a[20:21], v85 offset:24832\n"
"ds_read_b64_tr_b16 a[22:23], v85 offset:26880\n"
"s_waitcnt lgkmcnt(10)\n"
"v_mfma_f32_32x32x16_bf16 v[128:143], a[0:3], a[108:111], v[128:143]\n"
"ds_read_b64_tr_b16 a[24:25], v86 offset:24832\n"
"ds_read_b64_tr_b16 a[26:27], v86 offset:26880\n"
"s_add_u32 s71, %[q0], 0x317800\n"
"buffer_load_dwordx4 v80, %[rsQ], s71 offen offset:2048 lds\n"
"s_waitcnt lgkmcnt(10)\n"
"v_mfma_f32_32x32x16_bf16 v[144:159], a[4:7], a[108:111], v[144:159]\n"
"ds_read_b64_tr_b16 a[28:29], v87 offset:24832\n"
"ds_read_b64_tr_b16 a[30:31], v87 offset:26880\n"
"s_waitcnt lgkmcnt(10)\n"
"v_mfma_f32_32x32x16_bf16 v[160:175], a[8:11], a[108:111], v[160:175]\n"
"v_add_u32 v84, 0xfffe0000, v84\n"
"ds_read_b64_tr_b16 a[0:1], v84\n"
"ds_read_b64_tr_b16 a[2:3], v84 offset:2048\n"
"s_waitcnt lgkmcnt(10)\n"
"v_mfma_f32_32x32x16_bf16 v[176:191], a[12:15], a[108:111], v[176:191]\n"
"v_add_u32 v85, 0xfffe0000, v85\n"
"ds_read_b64_tr_b16 a[4:5], v85\n"
"ds_read_b64_tr_b16 a[6:7], v85 offset:2048\n"
"s_waitcnt lgkmcnt(10)\n"
"v_mfma_f32_32x32x16_bf16 v[192:207], a[16:19], a[108:111], v[192:207]\n"
"v_add_u32 v86, 0xfffe0000, v86\n"
"ds_read_b64_tr_b16 a[8:9], v86\n"
"ds_read_b64_tr_b16 a[10:11], v86 offset:2048\n"
"s_add_u32 s71, %[q0], 0x31b400\n"
"buffer_load_dwordx4 v81, %[rsQ], s71 offen offset:3072 lds\n"
"s_waitcnt lgkmcnt(10)\n"
"v_mfma_f32_32x32x16_bf16 v[208:223], a[20:23], a[108:111], v[208:223]\n"
"v_add_u32 v87, 0xfffe0000, v87\n"
"ds_read_b64_tr_b16 a[12:13], v87\n"
"ds_read_b64_tr_b16 a[14:15], v87 offset:2048\n"
"s_waitcnt lgkmcnt(10)\n"
"v_mfma_f32_32x32x16_bf16 v[224:239], a[24:27], a[108:111], v[224:239]\n"
"ds_read_b64_tr_b16 a[16:17], v84 offset:256\n"
"ds_read_b64_tr_b16 a[18:19], v84 offset:2304\n"
"s_waitcnt lgkmcnt(10)\n"
"v_mfma_f32_32x32x16_bf16 v[240:255], a[28:31], a[108:111], v[240:255]\n"
"ds_read_b64_tr_b16 a[20:21], v85 offset:256\n"
"ds_read_b64_tr_b16 a[22:23], v85 offset:2304\n"
"s_waitcnt vmcnt(16)\n"
"s_barrier\n"
"s_waitcnt lgkmcnt(10)\n"
"v_mfma_f32_32x32x16_bf16 v[128:143], a[0:3], a[112:115], v[128:143]\n"
"ds_read_b64_tr_b16 a[24:25], v86 offset:256\n"
"ds_read_b64_tr_b16 a[26:27], v86 offset:2304\n"
"s_waitcnt lgkmcnt(10)\n"
"v_mfma_f32_32x32x16_bf16 v[144:159], a[4:7], a[112:115], v[144:159]\n"
"ds_read_b64_tr_b16 a[28:29], v87 offset:256\n"
"ds_read_b64_tr_b16 a[30:31], v87 offset:2304\n"
"s_waitcnt lgkmcnt(10)\n"
"v_mfma_f32_32x32x16_bf16 v[160:175], a[8:11], a[112:115], v[160:175]\n"
"ds_read_b64_tr_b16 a[0:1], v84 offset:8192\n"
"ds_read_b64_tr_b16 a[2:3], v84 offset:10240\n"
"s_waitcnt lgkmcnt(10)\n"
"v_mfma_f32_32x32x16_bf16 v[176:191], a[12:15], a[112:115], v[176:191]\n"
"ds_read_b64_tr_b16 a[4:5], v85 offset:8192\n"
"ds_read_b64_tr_b16 a[6:7], v85 offset:10240\n"
"s_waitcnt lgkmcnt(10)\n"
"v_mfma_f32_32x32x16_bf16 v[192:207], a[16:19], a[112:115], v[192:207]\n"
"ds_read_b64_tr_b16 a[8:9], v86 offset:8192\n"
"ds_read_b64_tr_b16 a[10:11], v86 offset:10240\n"
"s_waitcnt lgkmcnt(10)\n"
"v_mfma_f32_32x32x16_bf16 v[208:223], a[20:23], a[112:115], v[208:223]\n"
"ds_read_b64_tr_b16 a[12:13], v87 offset:8192\n"
"ds_read_b64_tr_b16 a[14:15], v87 offset:10240\n"
"s_waitcnt lgkmcnt(10)\n"
"v_mfma_f32_32x32x16_bf16 v[224:239], a[24:27], a[112:115], v[224:239]\n"
"ds_read_b64_tr_b16 a[16:17], v84 offset:8448\n"
"ds_read_b64_tr_b16 a[18:19], v84 offset:10496\n"
"s_waitcnt lgkmcnt(10)\n"
"v_mfma_f32_32x32x16_bf16 v[240:255], a[28:31], a[112:115], v[240:255]\n"
"ds_read_b64_tr_b16 a[20:21], v85 offset:8448\n"
"ds_read_b64_tr_b16 a[22:23], v85 offset:10496\n"
"s_waitcnt lgkmcnt(10)\n"
"v_mfma_f32_32x32x16_bf16 v[128:143], a[0:3], a[116:119], v[128:143]\n"
"ds_read_b64_tr_b16 a[24:25], v86 offset:8448\n"
"ds_read_b64_tr_b16 a[26:27], v86 offset:10496\n"
"s_waitcnt lgkmcnt(10)\n"
"v_mfma_f32_32x32x16_bf16 v[144:159], a[4:7], a[116:119], v[144:159]\n"
"ds_read_b64_tr_b16 a[28:29], v87 offset:8448\n"
"ds_read_b64_tr_b16 a[30:31], v87 offset:10496\n"
"s_waitcnt lgkmcnt(10)\n"
"v_mfma_f32_32x32x16_bf16 v[160:175], a[8:11], a[116:119], v[160:175]\n"
"ds_read_b64_tr_b16 a[0:1], v84 offset:16384\n"
"ds_read_b64_tr_b16 a[2:3], v84 offset:18432\n"
"s_waitcnt lgkmcnt(10)\n"
"v_mfma_f32_32x32x16_bf16 v[176:191], a[12:15], a[116:119], v[176:191]\n"
"ds_read_b64_tr_b16 a[4:5], v85 offset:16384\n"
"ds_read_b64_tr_b16 a[6:7], v85 offset:18432\n"
"s_waitcnt lgkmcnt(10)\n"
"v_mfma_f32_32x32x16_bf16 v[192:207], a[16:19], a[116:119], v[192:207]\n"
"ds_read_b64_tr_b16 a[8:9], v86 offset:16384\n"
"ds_read_b64_tr_b16 a[10:11], v86 offset:18432\n"
"s_waitcnt lgkmcnt(10)\n"
"v_mfma_f32_32x32x16_bf16 v[208:223], a[20:23], a[116:119], v[208:223]\n"
"ds_read_b64_tr_b16 a[12:13], v87 offset:16384\n"
"ds_read_b64_tr_b16 a[14:15], v87 offset:18432\n"
"s_waitcnt lgkmcnt(10)\n"
"v_mfma_f32_32x32x16_bf16 v[224:239], a[24:27], a[116:119], v[224:239]\n"
"ds_read_b64_tr_b16 a[16:17], v84 offset:16640\n"
"ds_read_b64_tr_b16 a[18:19], v84 offset:18688\n"
"s_waitcnt lgkmcnt(10)\n"
"v_mfma_f32_32x32x16_bf16 v[240:255], a[28:31], a[116:119], v[240:255]\n"
"ds_read_b64_tr_b16 a[20:21], v85 offset:16640\n"
"ds_read_b64_tr_b16 a[22:23], v85 offset:18688\n"
"s_waitcnt lgkmcnt(10)\n"
"v_mfma_f32_32x32x16_bf16 v[128:143], a[0:3], a[120:123], v[128:143]\n"
"ds_read_b64_tr_b16 a[24:25], v86 offset:16640\n"
"ds_read_b64_tr_b16 a[26:27], v86 offset:18688\n"
"s_waitcnt lgkmcnt(10)\n"
"v_mfma_f32_32x32x16_bf16 v[144:159], a[4:7], a[120:123], v[144:159]\n"
"ds_read_b64_tr_b16 a[28:29], v87 offset:16640\n"
"ds_read_b64_tr_b16 a[30:31], v87 offset:18688\n"
"s_waitcnt lgkmcnt(10)\n"
"v_mfma_f32_32x32x16_bf16 v[160:175], a[8:11], a[120:123], v[160:175]\n"
"ds_read_b64_tr_b16 a[0:1], v84 offset:24576\n"
"ds_read_b64_tr_b16 a[2:3], v84 offset:26624\n"
"s_waitcnt lgkmcnt(10)\n"
"v_mfma_f32_32x32x16_bf16 v[176:191], a[12:15], a[120:123], v[176:191]\n"
"ds_read_b64_tr_b16 a[4:5], v85 offset:24576\n"
"ds_read_b64_tr_b16 a[6:7], v85 offset:26624\n"
"s_waitcnt lgkmcnt(10)\n"
"v_mfma_f32_32x32x16_bf16 v[192:207], a[16:19], a[120:123], v[192:207]\n"
"ds_read_b64_tr_b16 a[8:9], v86 offset:24576\n"
"ds_read_b64_tr_b16 a[10:11], v86 offset:26624\n"
"s_waitcnt lgkmcnt(10)\n"
"v_mfma_f32_32x32x16_bf16 v[208:223], a[20:23], a[120:123], v[208:223]\n"
"ds_read_b64_tr_b16 a[12:13], v87 offset:24576\n"
"ds_read_b64_tr_b16 a[14:15], v87 offset:26624\n"
"s_waitcnt lgkmcnt(10)\n"
"v_mfma_f32_32x32x16_bf16 v[224:239], a[24:27], a[120:123], v[224:239]\n"
"ds_read_b64_tr_b16 a[16:17], v84 offset:24832\n"
"ds_read_b64_tr_b16 a[18:19], v84 offset:26880\n"
"s_waitcnt lgkmcnt(10)\n"
"v_mfma_f32_32x32x16_bf16 v[240:255], a[28:31], a[120:123], v[240:255]\n"
"ds_read_b64_tr_b16 a[20:21], v85 offset:24832\n"
"ds_read_b64_tr_b16 a[22:23], v85 offset:26880\n"
"s_waitcnt lgkmcnt(10)\n"
"v_mfma_f32_32x32x16_bf16 v[128:143], a[0:3], a[124:127], v[128:143]\n"
"ds_read_b64_tr_b16 a[24:25], v86 offset:24832\n"
"ds_read_b64_tr_b16 a[26:27], v86 offset:26880\n"
"s_waitcnt lgkmcnt(10)\n"
"v_mfma_f32_32x32x16_bf16 v[144:159], a[4:7], a[124:127], v[144:159]\n"
"ds_read_b64_tr_b16 a[28:29], v87 offset:24832\n"
"ds_read_b64_tr_b16 a[30:31], v87 offset:26880\n"
"s_waitcnt lgkmcnt(10)\n"
"v_mfma_f32_32x32x16_bf16 v[160:175], a[8:11], a[124:127], v[160:175]\n"
"ds_read_b64_tr_b16 a[0:1], v84 offset:32768\n"
"ds_read_b64_tr_b16 a[2:3], v84 offset:34816\n"
"s_waitcnt lgkmcnt(10)\n"
"v_mfma_f32_32x32x16_bf16 v[176:191], a[12:15], a[124:127], v[176:191]\n"
"ds_read_b64_tr_b16 a[4:5], v85 offset:32768\n"
"ds_read_b64_tr_b16 a[6:7], v85 offset:34816\n"
"s_waitcnt lgkmcnt(10)\n"
"v_mfma_f32_32x32x16_bf16 v[192:207], a[16:19], a[124:127], v[192:207]\n"
"ds_read_b64_tr_b16 a[8:9], v86 offset:32768\n"
"ds_read_b64_tr_b16 a[10:11], v86 offset:34816\n"
"s_waitcnt lgkmcnt(10)\n"
"v_mfma_f32_32x32x16_bf16 v[208:223], a[20:23], a[124:127], v[208:223]\n"
"ds_read_b64_tr_b16 a[12:13], v87 offset:32768\n"
"ds_read_b64_tr_b16 a[14:15], v87 offset:34816\n"
"s_waitcnt lgkmcnt(10)\n"
"v_mfma_f32_32x32x16_bf16 v[224:239], a[24:27], a[124:127], v[224:239]\n"
"ds_read_b64_tr_b16 a[16:17], v84 offset:33024\n"
"ds_read_b64_tr_b16 a[18:19], v84 offset:35072\n"
"s_waitcnt lgkmcnt(10)\n"
"v_mfma_f32_32x32x16_bf16 v[240:255], a[28:31], a[124:127], v[240:255]\n"
"ds_read_b64_tr_b16 a[20:21], v85 offset:33024\n"
"ds_read_b64_tr_b16 a[22:23], v85 offset:35072\n"
"s_waitcnt vmcnt(8)\n"
"s_barrier\n"
"s_waitcnt lgkmcnt(10)\n"
"v_mfma_f32_32x32x16_bf16 v[128:143], a[0:3], a[128:131], v[128:143]\n"
"ds_read_b64_tr_b16 a[24:25], v86 offset:33024\n"
"ds_read_b64_tr_b16 a[26:27], v86 offset:35072\n"
"s_waitcnt lgkmcnt(10)\n"
"v_mfma_f32_32x32x16_bf16 v[144:159], a[4:7], a[128:131], v[144:159]\n"
"ds_read_b64_tr_b16 a[28:29], v87 offset:33024\n"
"ds_read_b64_tr_b16 a[30:31], v87 offset:35072\n"
"s_waitcnt lgkmcnt(10)\n"
"v_mfma_f32_32x32x16_bf16 v[160:175], a[8:11], a[128:131], v[160:175]\n"
"ds_read_b64_tr_b16 a[0:1], v84 offset:40960\n"
"ds_read_b64_tr_b16 a[2:3], v84 offset:43008\n"
"buffer_load_dwordx2 v[14:15], v11, %[rsW], %[kvl] offen sc1\n"
"s_waitcnt lgkmcnt(10)\n"
"v_mfma_f32_32x32x16_bf16 v[176:191], a[12:15], a[128:131], v[176:191]\n"
"ds_read_b64_tr_b16 a[4:5], v85 offset:40960\n"
"ds_read_b64_tr_b16 a[6:7], v85 offset:43008\n"
"s_waitcnt lgkmcnt(10)\n"
"v_mfma_f32_32x32x16_bf16 v[192:207], a[16:19], a[128:131], v[192:207]\n"
"ds_read_b64_tr_b16 a[8:9], v86 offset:40960\n"
"ds_read_b64_tr_b16 a[10:11], v86 offset:43008\n"
"s_waitcnt lgkmcnt(10)\n"
"v_mfma_f32_32x32x16_bf16 v[208:223], a[20:23], a[128:131], v[208:223]\n"
"ds_read_b64_tr_b16 a[12:13], v87 offset:40960\n"
"ds_read_b64_tr_b16 a[14:15], v87 offset:43008\n"
"s_waitcnt lgkmcnt(10)\n"
"v_mfma_f32_32x32x16_bf16 v[224:239], a[24:27], a[128:131], v[224:239]\n"
"ds_read_b64_tr_b16 a[16:17], v84 offset:41216\n"
"ds_read_b64_tr_b16 a[18:19], v84 offset:43264\n"
"s_waitcnt lgkmcnt(10)\n"
"v_mfma_f32_32x32x16_bf16 v[240:255], a[28:31], a[128:131], v[240:255]\n"
"ds_read_b64_tr_b16 a[20:21], v85 offset:41216\n"
"ds_read_b64_tr_b16 a[22:23], v85 offset:43264\n"
"s_waitcnt lgkmcnt(10)\n"
"v_mfma_f32_32x32x16_bf16 v[128:143], a[0:3], a[132:135], v[128:143]\n"
"ds_read_b64_tr_b16 a[24:25], v86 offset:41216\n"
"ds_read_b64_tr_b16 a[26:27], v86 offset:43264\n"
"s_waitcnt lgkmcnt(10)\n"
"v_mfma_f32_32x32x16_bf16 v[144:159], a[4:7], a[132:135], v[144:159]\n"
"ds_read_b64_tr_b16 a[28:29], v87 offset:41216\n"
"ds_read_b64_tr_b16 a[30:31], v87 offset:43264\n"
"s_waitcnt lgkmcnt(10)\n"
"v_mfma_f32_32x32x16_bf16 v[160:175], a[8:11], a[132:135], v[160:175]\n"
"ds_read_b64_tr_b16 a[0:1], v84 offset:49152\n"
"ds_read_b64_tr_b16 a[2:3], v84 offset:51200\n"
"s_waitcnt lgkmcnt(10)\n"
"v_mfma_f32_32x32x16_bf16 v[176:191], a[12:15], a[132:135], v[176:191]\n"
"ds_read_b64_tr_b16 a[4:5], v85 offset:49152\n"
"ds_read_b64_tr_b16 a[6:7], v85 offset:51200\n"
"s_waitcnt lgkmcnt(10)\n"
"v_mfma_f32_32x32x16_bf16 v[192:207], a[16:19], a[132:135], v[192:207]\n"
"ds_read_b64_tr_b16 a[8:9], v86 offset:49152\n"
"ds_read_b64_tr_b16 a[10:11], v86 offset:51200\n"
"s_waitcnt lgkmcnt(10)\n"
"v_mfma_f32_32x32x16_bf16 v[208:223], a[20:23], a[132:135], v[208:223]\n"
"ds_read_b64_tr_b16 a[12:13], v87 offset:49152\n"
"ds_read_b64_tr_b16 a[14:15], v87 offset:51200\n"
"s_waitcnt lgkmcnt(10)\n"
"v_mfma_f32_32x32x16_bf16 v[224:239], a[24:27], a[132:135], v[224:239]\n"
"ds_read_b64_tr_b16 a[16:17], v84 offset:49408\n"
"ds_read_b64_tr_b16 a[18:19], v84 offset:51456\n"
"s_waitcnt lgkmcnt(10)\n"
"v_mfma_f32_32x32x16_bf16 v[240:255], a[28:31], a[132:135], v[240:255]\n"
"ds_read_b64_tr_b16 a[20:21], v85 offset:49408\n"
"ds_read_b64_tr_b16 a[22:23], v85 offset:51456\n"
"s_waitcnt lgkmcnt(10)\n"
"v_mfma_f32_32x32x16_bf16 v[128:143], a[0:3], a[136:139], v[128:143]\n"
"ds_read_b64_tr_b16 a[24:25], v86 offset:49408\n"
"ds_read_b64_tr_b16 a[26:27], v86 offset:51456\n"
"s_waitcnt lgkmcnt(10)\n"
"v_mfma_f32_32x32x16_bf16 v[144:159], a[4:7], a[136:139], v[144:159]\n"
"ds_read_b64_tr_b16 a[28:29], v87 offset:49408\n"
"ds_read_b64_tr_b16 a[30:31], v87 offset:51456\n"
"s_waitcnt lgkmcnt(10)\n"
"v_mfma_f32_32x32x16_bf16 v[160:175], a[8:11], a[136:139], v[160:175]\n"
"ds_read_b64_tr_b16 a[0:1], v84 offset:57344\n"
"ds_read_b64_tr_b16 a[2:3], v84 offset:59392\n"
"s_waitcnt lgkmcnt(10)\n"
"v_mfma_f32_32x32x16_bf16 v[176:191], a[12:15], a[136:139], v[176:191]\n"
"ds_read_b64_tr_b16 a[4:5], v85 offset:57344\n"
"ds_read_b64_tr_b16 a[6:7], v85 offset:59392\n"
"s_waitcnt lgkmcnt(10)\n"
"v_mfma_f32_32x32x16_bf16 v[192:207], a[16:19], a[136:139], v[192:207]\n"
"ds_read_b64_tr_b16 a[8:9], v86 offset:57344\n"
"ds_read_b64_tr_b16 a[10:11], v86 offset:59392\n"
"s_waitcnt lgkmcnt(10)\n"
"v_mfma_f32_32x32x16_bf16 v[208:223], a[20:23], a[136:139], v[208:223]\n"
"ds_read_b64_tr_b16 a[12:13], v87 offset:57344\n"
"ds_read_b64_tr_b16 a[14:15], v87 offset:59392\n"
"s_waitcnt lgkmcnt(10)\n"
"v_mfma_f32_32x32x16_bf16 v[224:239], a[24:27], a[136:139], v[224:239]\n"
"ds_read_b64_tr_b16 a[16:17], v84 offset:57600\n"
"ds_read_b64_tr_b16 a[18:19], v84 offset:59648\n"
"s_waitcnt lgkmcnt(10)\n"
"v_mfma_f32_32x32x16_bf16 v[240:255], a[28:31], a[136:139], v[240:255]\n"
"ds_read_b64_tr_b16 a[20:21], v85 offset:57600\n"
"ds_read_b64_tr_b16 a[22:23], v85 offset:59648\n"
"s_waitcnt lgkmcnt(10)\n"
"v_mfma_f32_32x32x16_bf16 v[128:143], a[0:3], a[140:143], v[128:143]\n"
"ds_read_b64_tr_b16 a[24:25], v86 offset:57600\n"
"ds_read_b64_tr_b16 a[26:27], v86 offset:59648\n"
"s_waitcnt lgkmcnt(10)\n"
"v_mfma_f32_32x32x16_bf16 v[144:159], a[4:7], a[140:143], v[144:159]\n"
"ds_read_b64_tr_b16 a[28:29], v87 offset:57600\n"
"ds_read_b64_tr_b16 a[30:31], v87 offset:59648\n"
"s_waitcnt lgkmcnt(10)\n"
"v_mfma_f32_32x32x16_bf16 v[160:175], a[8:11], a[140:143], v[160:175]\n"
"v_add_u32 v84, 0x10000, v84\n"
"ds_read_b64_tr_b16 a[0:1], v84\n"
"ds_read_b64_tr_b16 a[2:3], v84 offset:2048\n"
"s_waitcnt lgkmcnt(10)\n"
"v_mfma_f32_32x32x16_bf16 v[176:191], a[12:15], a[140:143], v[176:191]\n"
"v_add_u32 v85, 0x10000, v85\n"
"ds_read_b64_tr_b16 a[4:5], v85\n"
"ds_read_b64_tr_b16 a[6:7], v85 offset:2048\n"
"s_waitcnt lgkmcnt(10)\n"
"v_mfma_f32_32x32x16_bf16 v[192:207], a[16:19], a[140:143], v[192:207]\n"
"v_add_u32 v86, 0x10000, v86\n"
"ds_read_b64_tr_b16 a[8:9], v86\n"
"ds_read_b64_tr_b16 a[10:11], v86 offset:2048\n"
"s_waitcnt lgkmcnt(10)\n"
"v_mfma_f32_32x32x16_bf16 v[208:223], a[20:23], a[140:143], v[208:223]\n"
"v_add_u32 v87, 0x10000, v87\n"
"ds_read_b64_tr_b16 a[12:13], v87\n"
"ds_read_b64_tr_b16 a[14:15], v87 offset:2048\n"
"s_waitcnt lgkmcnt(10)\n"
"v_mfma_f32_32x32x16_bf16 v[224:239], a[24:27], a[140:143], v[224:239]\n"
"ds_read_b64_tr_b16 a[16:17], v84 offset:256\n"
"ds_read_b64_tr_b16 a[18:19], v84 offset:2304\n"
"s_waitcnt lgkmcnt(10)\n"
"v_mfma_f32_32x32x16_bf16 v[240:255], a[28:31], a[140:143], v[240:255]\n"
"ds_read_b64_tr_b16 a[20:21], v85 offset:256\n"
"ds_read_b64_tr_b16 a[22:23], v85 offset:2304\n"
"s_waitcnt vmcnt(0)\n"
"v_cmp_ne_u64 vcc, %[tag], v[14:15]\n"
"s_nop 0\n"
"s_cbranch_vccnz KVWH%=\n"
"KVGH%=:\n"
"s_barrier\n"
"s_waitcnt lgkmcnt(10)\n"
"v_mfma_f32_32x32x16_bf16 v[128:143], a[0:3], a[144:147], v[128:143]\n"
"ds_read_b64_tr_b16 a[24:25], v86 offset:256\n"
"ds_read_b64_tr_b16 a[26:27], v86 offset:2304\n"
"s_add_u32 m0, s74, 0x20000\n"
"s_add_u32 s71, %[ks], 0\n"
"buffer_load_dwordx4 v1, %[rsW], s71 offen sc1 lds\n"
"s_waitcnt lgkmcnt(10)\n"
"v_mfma_f32_32x32x16_bf16 v[144:159], a[4:7], a[144:147], v[144:159]\n"
"ds_read_b64_tr_b16 a[28:29], v87 offset:256\n"
"ds_read_b64_tr_b16 a[30:31], v87 offset:2304\n"
"buffer_load_dwordx4 v1, %[rsW], s71 offen offset:1024 sc1 lds\n"
"s_waitcnt lgkmcnt(10)\n"
"v_mfma_f32_32x32x16_bf16 v[160:175], a[8:11], a[144:147], v[160:175]\n"
"ds_read_b64_tr_b16 a[0:1], v84 offset:8192\n"
"ds_read_b64_tr_b16 a[2:3], v84 offset:10240\n"
"buffer_load_dwordx4 v1, %[rsW], s71 offen offset:2048 sc1 lds\n"
"s_waitcnt lgkmcnt(10)\n"
"v_mfma_f32_32x32x16_bf16 v[176:191], a[12:15], a[144:147], v[176:191]\n"
"ds_read_b64_tr_b16 a[4:5], v85 offset:8192\n"
"ds_read_b64_tr_b16 a[6:7], v85 offset:10240\n"
"buffer_load_dwordx4 v1, %[rsW], s71 offen offset:3072 sc1 lds\n"
"s_waitcnt lgkmcnt(10)\n"
"v_mfma_f32_32x32x16_bf16 v[192:207], a[16:19], a[144:147], v[192:207]\n"
"ds_read_b64_tr_b16 a[8:9], v86 offset:8192\n"
"ds_read_b64_tr_b16 a[10:11], v86 offset:10240\n"
"s_add_u32 m0, s74, 0x21000\n"
"s_add_u32 s71, %[ks], 0x1000\n"
"buffer_load_dwordx4 v1, %[rsW], s71 offen sc1 lds\n"
"s_waitcnt lgkmcnt(10)\n"
"v_mfma_f32_32x32x16_bf16 v[208:223], a[20:23], a[144:147], v[208:223]\n"
"ds_read_b64_tr_b16 a[12:13], v87 offset:8192\n"
"ds_read_b64_tr_b16 a[14:15], v87 offset:10240\n"
"buffer_load_dwordx4 v1, %[rsW], s71 offen offset:1024 sc1 lds\n"
"s_waitcnt lgkmcnt(10)\n"
"v_mfma_f32_32x32x16_bf16 v[224:239], a[24:27], a[144:147], v[224:239]\n"
"ds_read_b64_tr_b16 a[16:17], v84 offset:8448\n"
"ds_read_b64_tr_b16 a[18:19], v84 offset:10496\n"
"buffer_load_dwordx4 v1, %[rsW], s71 offen offset:2048 sc1 lds\n"
"s_waitcnt lgkmcnt(10)\n"
"v_mfma_f32_32x32x16_bf16 v[240:255], a[28:31], a[144:147], v[240:255]\n"
"ds_read_b64_tr_b16 a[20:21], v85 offset:8448\n"
"ds_read_b64_tr_b16 a[22:23], v85 offset:10496\n"
"buffer_load_dwordx4 v1, %[rsW], s71 offen offset:3072 sc1 lds\n"
"s_waitcnt lgkmcnt(10)\n"
"v_mfma_f32_32x32x16_bf16 v[128:143], a[0:3], a[148:151], v[128:143]\n"
"ds_read_b64_tr_b16 a[24:25], v86 offset:8448\n"
"ds_read_b64_tr_b16 a[26:27], v86 offset:10496\n"
"s_waitcnt lgkmcnt(10)\n"
"v_mfma_f32_32x32x16_bf16 v[144:159], a[4:7], a[148:151], v[144:159]\n"
"ds_read_b64_tr_b16 a[28:29], v87 offset:8448\n"
"ds_read_b64_tr_b16 a[30:31], v87 offset:10496\n"
"s_waitcnt lgkmcnt(10)\n"
"v_mfma_f32_32x32x16_bf16 v[160:175], a[8:11], a[148:151], v[160:175]\n"
"ds_read_b64_tr_b16 a[0:1], v84 offset:16384\n"
"ds_read_b64_tr_b16 a[2:3], v84 offset:18432\n"
"s_waitcnt lgkmcnt(10)\n"
"v_mfma_f32_32x32x16_bf16 v[176:191], a[12:15], a[148:151], v[176:191]\n"
"ds_read_b64_tr_b16 a[4:5], v85 offset:16384\n"
"ds_read_b64_tr_b16 a[6:7], v85 offset:18432\n"
"s_waitcnt lgkmcnt(10)\n"
"v_mfma_f32_32x32x16_bf16 v[192:207], a[16:19], a[148:151], v[192:207]\n"
"ds_read_b64_tr_b16 a[8:9], v86 offset:16384\n"
"ds_read_b64_tr_b16 a[10:11], v86 offset:18432\n"
"s_waitcnt lgkmcnt(10)\n"
"v_mfma_f32_32x32x16_bf16 v[208:223], a[20:23], a[148:151], v[208:223]\n"
"ds_read_b64_tr_b16 a[12:13], v87 offset:16384\n"
"ds_read_b64_tr_b16 a[14:15], v87 offset:18432\n"
"s_waitcnt lgkmcnt(10)\n"
"v_mfma_f32_32x32x16_bf16 v[224:239], a[24:27], a[148:151], v[224:239]\n"
"ds_read_b64_tr_b16 a[16:17], v84 offset:16640\n"
"ds_read_b64_tr_b16 a[18:19], v84 offset:18688\n"
"s_waitcnt lgkmcnt(10)\n"
"v_mfma_f32_32x32x16_bf16 v[240:255], a[28:31], a[148:151], v[240:255]\n"
"ds_read_b64_tr_b16 a[20:21], v85 offset:16640\n"
"ds_read_b64_tr_b16 a[22:23], v85 offset:18688\n"
"s_waitcnt lgkmcnt(10)\n"
"v_mfma_f32_32x32x16_bf16 v[128:143], a[0:3], a[152:155], v[128:143]\n"
"ds_read_b64_tr_b16 a[24:25], v86 offset:16640\n"
"ds_read_b64_tr_b16 a[26:27], v86 offset:18688\n"
"s_add_u32 m0, s74, 0\n"
"s_add_u32 s71, %[ks], 0x8000\n"
"buffer_load_dwordx4 v1, %[rsW], s71 offen sc1 lds\n"
"s_waitcnt lgkmcnt(10)\n"
"v_mfma_f32_32x32x16_bf16 v[144:159], a[4:7], a[152:155], v[144:159]\n"
"ds_read_b64_tr_b16 a[28:29], v87 offset:16640\n"
"ds_read_b64_tr_b16 a[30:31], v87 offset:18688\n"
"s_waitcnt lgkmcnt(10)\n"
"v_mfma_f32_32x32x16_bf16 v[160:175], a[8:11], a[152:155], v[160:175]\n"
"ds_read_b64_tr_b16 a[0:1], v84 offset:24576\n"
"ds_read_b64_tr_b16 a[2:3], v84 offset:26624\n"
"buffer_load_dwordx4 v1, %[rsW], s71 offen offset:1024 sc1 lds\n"
"s_waitcnt lgkmcnt(10)\n"
"v_mfma_f32_32x32x16_bf16 v[176:191], a[12:15], a[152:155], v[176:191]\n"
"ds_read_b64_tr_b16 a[4:5], v85 offset:24576\n"
"ds_read_b64_tr_b16 a[6:7], v85 offset:26624\n"
"s_waitcnt lgkmcnt(10)\n"
"v_mfma_f32_32x32x16_bf16 v[192:207], a[16:19], a[152:155], v[192:207]\n"
"ds_read_b64_tr_b16 a[8:9], v86 offset:24576\n"
"ds_read_b64_tr_b16 a[10:11], v86 offset:26624\n"
"buffer_load_dwordx4 v1, %[rsW], s71 offen offset:2048 sc1 lds\n"
"s_waitcnt lgkmcnt(10)\n"
"v_mfma_f32_32x32x16_bf16 v[208:223], a[20:23], a[152:155], v[208:223]\n"
"ds_read_b64_tr_b16 a[12:13], v87 offset:24576\n"
"ds_read_b64_tr_b16 a[14:15], v87 offset:26624\n"
"s_waitcnt lgkmcnt(10)\n"
"v_mfma_f32_32x32x16_bf16 v[224:239], a[24:27], a[152:155], v[224:239]\n"
"ds_read_b64_tr_b16 a[16:17], v84 offset:24832\n"
"ds_read_b64_tr_b16 a[18:19], v84 offset:26880\n"
"buffer_load_dwordx4 v1, %[rsW], s71 offen offset:3072 sc1 lds\n"
"s_waitcnt lgkmcnt(10)\n"
"v_mfma_f32_32x32x16_bf16 v[240:255], a[28:31], a[152:155], v[240:255]\n"
"ds_read_b64_tr_b16 a[20:21], v85 offset:24832\n"
"ds_read_b64_tr_b16 a[22:23], v85 offset:26880\n"
"s_waitcnt lgkmcnt(10)\n"
"v_mfma_f32_32x32x16_bf16 v[128:143], a[0:3], a[156:159], v[128:143]\n"
"ds_read_b64_tr_b16 a[24:25], v86 offset:24832\n"
"ds_read_b64_tr_b16 a[26:27], v86 offset:26880\n"
"s_add_u32 m0, s74, 0x1000\n"
"s_add_u32 s71, %[ks], 0x9000\n"
"buffer_load_dwordx4 v1, %[rsW], s71 offen sc1 lds\n"
"s_waitcnt lgkmcnt(10)\n"
"v_mfma_f32_32x32x16_bf16 v[144:159], a[4:7], a[156:159], v[144:159]\n"
"ds_read_b64_tr_b16 a[28:29], v87 offset:24832\n"
"ds_read_b64_tr_b16 a[30:31], v87 offset:26880\n"
"s_waitcnt lgkmcnt(10)\n"
"v_mfma_f32_32x32x16_bf16 v[160:175], a[8:11], a[156:159], v[160:175]\n"
"ds_read_b64_tr_b16 a[0:1], v84 offset:32768\n"
"ds_read_b64_tr_b16 a[2:3], v84 offset:34816\n"
"buffer_load_dwordx4 v1, %[rsW], s71 offen offset:1024 sc1 lds\n"
"s_waitcnt lgkmcnt(10)\n"
"v_mfma_f32_32x32x16_bf16 v[176:191], a[12:15], a[156:159], v[176:191]\n"
"ds_read_b64_tr_b16 a[4:5], v85 offset:32768\n"
"ds_read_b64_tr_b16 a[6:7], v85 offset:34816\n"
"s_waitcnt lgkmcnt(10)\n"
"v_mfma_f32_32x32x16_bf16 v[192:207], a[16:19], a[156:159], v[192:207]\n"
"ds_read_b64_tr_b16 a[8:9], v86 offset:32768\n"
"ds_read_b64_tr_b16 a[10:11], v86 offset:34816\n"
"buffer_load_dwordx4 v1, %[rsW], s71 offen offset:2048 sc1 lds\n"
"s_waitcnt lgkmcnt(10)\n"
"v_mfma_f32_32x32x16_bf16 v[208:223], a[20:23], a[156:159], v[208:223]\n"
"ds_read_b64_tr_b16 a[12:13], v87 offset:32768\n"
"ds_read_b64_tr_b16 a[14:15], v87 offset:34816\n"
"s_waitcnt lgkmcnt(10)\n"
"v_mfma_f32_32x32x16_bf16 v[224:239], a[24:27], a[156:159], v[224:239]\n"
"ds_read_b64_tr_b16 a[16:17], v84 offset:33024\n"
"ds_read_b64_tr_b16 a[18:19], v84 offset:35072\n"
"buffer_load_dwordx4 v1, %[rsW], s71 offen offset:3072 sc1 lds\n"
"s_waitcnt lgkmcnt(10)\n"
"v_mfma_f32_32x32x16_bf16 v[240:255], a[28:31], a[156:159], v[240:255]\n"
"ds_read_b64_tr_b16 a[20:21], v85 offset:33024\n"
"ds_read_b64_tr_b16 a[22:23], v85 offset:35072\n"
"s_waitcnt vmcnt(8)\n"
"s_barrier\n"
"s_waitcnt lgkmcnt(10)\n"
"v_mfma_f32_32x32x16_bf16 v[128:143], a[0:3], a[160:163], v[128:143]\n"
"ds_read_b64_tr_b16 a[24:25], v86 offset:33024\n"
"ds_read_b64_tr_b16 a[26:27], v86 offset:35072\n"
"s_add_u32 m0, s74, 0x8000\n"
"s_add_u32 s71, %[vs], 0\n"
"buffer_load_dwordx4 v1, %[rsW], s71 offen sc1 lds\n"
"s_waitcnt lgkmcnt(10)\n"
"v_mfma_f32_32x32x16_bf16 v[144:159], a[4:7], a[160:163], v[144:159]\n"
"ds_read_b64_tr_b16 a[28:29], v87 offset:33024\n"
"ds_read_b64_tr_b16 a[30:31], v87 offset:35072\n"
"s_waitcnt lgkmcnt(10)\n"
"v_mfma_f32_32x32x16_bf16 v[160:175], a[8:11], a[160:163], v[160:175]\n"
"ds_read_b64_tr_b16 a[0:1], v84 offset:40960\n"
"ds_read_b64_tr_b16 a[2:3], v84 offset:43008\n"
"buffer_load_dwordx4 v1, %[rsW], s71 offen offset:1024 sc1 lds\n"
"s_waitcnt lgkmcnt(10)\n"
"v_mfma_f32_32x32x16_bf16 v[176:191], a[12:15], a[160:163], v[176:191]\n"
"ds_read_b64_tr_b16 a[4:5], v85 offset:40960\n"
"ds_read_b64_tr_b16 a[6:7], v85 offset:43008\n"
"s_waitcnt lgkmcnt(10)\n"
"v_mfma_f32_32x32x16_bf16 v[192:207], a[16:19], a[160:163], v[192:207]\n"
"ds_read_b64_tr_b16 a[8:9], v86 offset:40960\n"
"ds_read_b64_tr_b16 a[10:11], v86 offset:43008\n"
"buffer_load_dwordx4 v1, %[rsW], s71 offen offset:2048 sc1 lds\n"
"s_waitcnt lgkmcnt(10)\n"
"v_mfma_f32_32x32x16_bf16 v[208:223], a[20:23], a[160:163], v[208:223]\n"
"ds_read_b64_tr_b16 a[12:13], v87 offset:40960\n"
"ds_read_b64_tr_b16 a[14:15], v87 offset:43008\n"
"s_waitcnt lgkmcnt(10)\n"
"v_mfma_f32_32x32x16_bf16 v[224:239], a[24:27], a[160:163], v[224:239]\n"
"ds_read_b64_tr_b16 a[16:17], v84 offset:41216\n"
"ds_read_b64_tr_b16 a[18:19], v84 offset:43264\n"
"buffer_load_dwordx4 v1, %[rsW], s71 offen offset:3072 sc1 lds\n"
"s_waitcnt lgkmcnt(10)\n"
"v_mfma_f32_32x32x16_bf16 v[240:255], a[28:31], a[160:163], v[240:255]\n"
"ds_read_b64_tr_b16 a[20:21], v85 offset:41216\n"
"ds_read_b64_tr_b16 a[22:23], v85 offset:43264\n"
"s_waitcnt lgkmcnt(10)\n"
"v_mfma_f32_32x32x16_bf16 v[128:143], a[0:3], a[164:167], v[128:143]\n"
"ds_read_b64_tr_b16 a[24:25], v86 offset:41216\n"
"ds_read_b64_tr_b16 a[26:27], v86 offset:43264\n"
"s_add_u32 m0, s74, 0x9000\n"
"s_add_u32 s71, %[vs], 0x1000\n"
"buffer_load_dwordx4 v1, %[rsW], s71 offen sc1 lds\n"
"s_waitcnt lgkmcnt(10)\n"
"v_mfma_f32_32x32x16_bf16 v[144:159], a[4:7], a[164:167], v[144:159]\n"
"ds_read_b64_tr_b16 a[28:29], v87 offset:41216\n"
"ds_read_b64_tr_b16 a[30:31], v87 offset:43264\n"
"s_waitcnt lgkmcnt(10)\n"
"v_mfma_f32_32x32x16_bf16 v[160:175], a[8:11], a[164:167], v[160:175]\n"
"ds_read_b64_tr_b16 a[0:1], v84 offset:49152\n"
"ds_read_b64_tr_b16 a[2:3], v84 offset:51200\n"
"buffer_load_dwordx4 v1, %[rsW], s71 offen offset:1024 sc1 lds\n"
"s_waitcnt lgkmcnt(10)\n"
"v_mfma_f32_32x32x16_bf16 v[176:191], a[12:15], a[164:167], v[176:191]\n"
"ds_read_b64_tr_b16 a[4:5], v85 offset:49152\n"
"ds_read_b64_tr_b16 a[6:7], v85 offset:51200\n"
"s_waitcnt lgkmcnt(10)\n"
"v_mfma_f32_32x32x16_bf16 v[192:207], a[16:19], a[164:167], v[192:207]\n"
"ds_read_b64_tr_b16 a[8:9], v86 offset:49152\n"
"ds_read_b64_tr_b16 a[10:11], v86 offset:51200\n"
"buffer_load_dwordx4 v1, %[rsW], s71 offen offset:2048 sc1 lds\n"
"s_waitcnt lgkmcnt(10)\n"
"v_mfma_f32_32x32x16_bf16 v[208:223], a[20:23], a[164:167], v[208:223]\n"
"ds_read_b64_tr_b16 a[12:13], v87 offset:49152\n"
"ds_read_b64_tr_b16 a[14:15], v87 offset:51200\n"
"s_waitcnt lgkmcnt(10)\n"
"v_mfma_f32_32x32x16_bf16 v[224:239], a[24:27], a[164:167], v[224:239]\n"
"ds_read_b64_tr_b16 a[16:17], v84 offset:49408\n"
"ds_read_b64_tr_b16 a[18:19], v84 offset:51456\n"
"buffer_load_dwordx4 v1, %[rsW], s71 offen offset:3072 sc1 lds\n"
"s_waitcnt lgkmcnt(10)\n"
"v_mfma_f32_32x32x16_bf16 v[240:255], a[28:31], a[164:167], v[240:255]\n"
"ds_read_b64_tr_b16 a[20:21], v85 offset:49408\n"
"ds_read_b64_tr_b16 a[22:23], v85 offset:51456\n"
"s_waitcnt lgkmcnt(10)\n"
"v_mfma_f32_32x32x16_bf16 v[128:143], a[0:3], a[168:171], v[128:143]\n"
"ds_read_b64_tr_b16 a[24:25], v86 offset:49408\n"
"ds_read_b64_tr_b16 a[26:27], v86 offset:51456\n"
"s_add_u32 m0, s74, 0x10000\n"
"s_add_u32 s71, %[ks], 0x10000\n"
"buffer_load_dwordx4 v1, %[rsW], s71 offen sc1 lds\n"
"s_waitcnt lgkmcnt(10)\n"
"v_mfma_f32_32x32x16_bf16 v[144:159], a[4:7], a[168:171], v[144:159]\n"
"ds_read_b64_tr_b16 a[28:29], v87 offset:49408\n"
"ds_read_b64_tr_b16 a[30:31], v87 offset:51456\n"
"s_waitcnt lgkmcnt(10)\n"
"v_mfma_f32_32x32x16_bf16 v[160:175], a[8:11], a[168:171], v[160:175]\n"
"ds_read_b64_tr_b16 a[0:1], v84 offset:57344\n"
"ds_read_b64_tr_b16 a[2:3], v84 offset:59392\n"
"buffer_load_dwordx4 v1, %[rsW], s71 offen offset:1024 sc1 lds\n"
"s_waitcnt lgkmcnt(10)\n"
"v_mfma_f32_32x32x16_bf16 v[176:191], a[12:15], a[168:171], v[176:191]\n"
"ds_read_b64_tr_b16 a[4:5], v85 offset:57344\n"
"ds_read_b64_tr_b16 a[6:7], v85 offset:59392\n"
"s_waitcnt lgkmcnt(10)\n"
"v_mfma_f32_32x32x16_bf16 v[192:207], a[16:19], a[168:171], v[192:207]\n"
"ds_read_b64_tr_b16 a[8:9], v86 offset:57344\n"
"ds_read_b64_tr_b16 a[10:11], v86 offset:59392\n"
"buffer_load_dwordx4 v1, %[rsW], s71 offen offset:2048 sc1 lds\n"
"s_waitcnt lgkmcnt(10)\n"
"v_mfma_f32_32x32x16_bf16 v[208:223], a[20:23], a[168:171], v[208:223]\n"
"ds_read_b64_tr_b16 a[12:13], v87 offset:57344\n"
"ds_read_b64_tr_b16 a[14:15], v87 offset:59392\n"
"s_waitcnt lgkmcnt(10)\n"
"v_mfma_f32_32x32x16_bf16 v[224:239], a[24:27], a[168:171], v[224:239]\n"
"ds_read_b64_tr_b16 a[16:17], v84 offset:57600\n"
"ds_read_b64_tr_b16 a[18:19], v84 offset:59648\n"
"buffer_load_dwordx4 v1, %[rsW], s71 offen offset:3072 sc1 lds\n"
"s_waitcnt lgkmcnt(10)\n"
"v_mfma_f32_32x32x16_bf16 v[240:255], a[28:31], a[168:171], v[240:255]\n"
"ds_read_b64_tr_b16 a[20:21], v85 offset:57600\n"
"ds_read_b64_tr_b16 a[22:23], v85 offset:59648\n"
"s_waitcnt lgkmcnt(10)\n"
"v_mfma_f32_32x32x16_bf16 v[128:143], a[0:3], a[172:175], v[128:143]\n"
"ds_read_b64_tr_b16 a[24:25], v86 offset:57600\n"
"ds_read_b64_tr_b16 a[26:27], v86 offset:59648\n"
"s_add_u32 m0, s74, 0x11000\n"
"s_add_u32 s71, %[ks], 0x11000\n"
"buffer_load_dwordx4 v1, %[rsW], s71 offen sc1 lds\n"
"s_waitcnt lgkmcnt(10)\n"
"v_mfma_f32_32x32x16_bf16 v[144:159], a[4:7], a[172:175], v[144:159]\n"
"ds_read_b64_tr_b16 a[28:29], v87 offset:57600\n"
"ds_read_b64_tr_b16 a[30:31], v87 offset:59648\n"
"s_waitcnt lgkmcnt(10)\n"
"v_mfma_f32_32x32x16_bf16 v[160:175], a[8:11], a[172:175], v[160:175]\n"
"ds_read_b128 a[0:3], v2\n"
"buffer_load_dwordx4 v1, %[rsW], s71 offen offset:1024 sc1 lds\n"
"s_waitcnt lgkmcnt(9)\n"
"v_mfma_f32_32x32x16_bf16 v[176:191], a[12:15], a[172:175], v[176:191]\n"
"ds_read_b128 a[4:7], v2 offset:1024\n"
"s_waitcnt lgkmcnt(8)\n"
"v_mfma_f32_32x32x16_bf16 v[192:207], a[16:19], a[172:175], v[192:207]\n"
"ds_read_b128 a[8:11], v2 offset:2048\n"
"buffer_load_dwordx4 v1, %[rsW], s71 offen offset:2048 sc1 lds\n"
"v_cvt_pk_bf16_f32 v80, v128, v129\n"
"v_cvt_pk_bf16_f32 v81, v130, v131\n"
"v_cvt_pk_bf16_f32 v82, v132, v133\n"
"v_cvt_pk_bf16_f32 v83, v134, v135\n"
"s_waitcnt lgkmcnt(7)\n"
"v_mfma_f32_32x32x16_bf16 v[208:223], a[20:23], a[172:175], v[208:223]\n"
"ds_read_b128 a[12:15], v2 offset:3072\n"
"v_cvt_pk_bf16_f32 v84, v136, v137\n"
"v_cvt_pk_bf16_f32 v85, v138, v139\n"
"v_cvt_pk_bf16_f32 v86, v140, v141\n"
"v_cvt_pk_bf16_f32 v87, v142, v143\n"
"s_waitcnt lgkmcnt(6)\n"
"v_mfma_f32_32x32x16_bf16 v[224:239], a[24:27], a[172:175], v[224:239]\n"
"ds_read_b128 a[16:19], v2 offset:4096\n"
"buffer_load_dwordx4 v1, %[rsW], s71 offen offset:3072 sc1 lds\n"
"v_cvt_pk_bf16_f32 v88, v144, v145\n"
"v_cvt_pk_bf16_f32 v89, v146, v147\n"
"v_cvt_pk_bf16_f32 v90, v148, v149\n"
"v_cvt_pk_bf16_f32 v91, v150, v151\n"
"s_waitcnt lgkmcnt(5)\n"
"v_mfma_f32_32x32x16_bf16 v[240:255], a[28:31], a[172:175], v[240:255]\n"
"ds_read_b128 a[20:23], v2 offset:5120\n"
"v_cvt_pk_bf16_f32 v92, v152, v153\n"
"v_cvt_pk_bf16_f32 v93, v154, v155\n"
"v_cvt_pk_bf16_f32 v94, v156, v157\n"
"v_cvt_pk_bf16_f32 v95, v158, v159\n"
"s_waitcnt vmcnt(16)\n"
"s_barrier\n"
"s_branch T0%=\n"
"RESUME%=:\n"
"v_accvgpr_read_b32 v80, a176\n"
"v_accvgpr_read_b32 v81, a177\n"
"v_accvgpr_read_b32 v82, a178\n"
"v_accvgpr_read_b32 v83, a179\n"
"v_accvgpr_read_b32 v84, a180\n"
"v_accvgpr_read_b32 v85, a181\n"
"v_accvgpr_read_b32 v86, a182\n"
"v_accvgpr_read_b32 v87, a183\n"
"v_accvgpr_read_b32 v88, a184\n"
"v_accvgpr_read_b32 v89, a185\n"
"v_accvgpr_read_b32 v90, a186\n"
"v_accvgpr_read_b32 v91, a187\n"
"v_accvgpr_read_b32 v92, a188\n"
"v_accvgpr_read_b32 v93, a189\n"
"v_accvgpr_read_b32 v94, a190\n"
"v_accvgpr_read_b32 v95, a191\n"
"v_accvgpr_read_b32 v96, a192\n"
"v_accvgpr_read_b32 v97, a193\n"
"v_accvgpr_read_b32 v98, a194\n"
"v_accvgpr_read_b32 v99, a195\n"
"v_accvgpr_read_b32 v100, a196\n"
"v_accvgpr_read_b32 v101, a197\n"
"v_accvgpr_read_b32 v102, a198\n"
"v_accvgpr_read_b32 v103, a199\n"
"v_accvgpr_read_b32 v104, a200\n"
"v_accvgpr_read_b32 v105, a201\n"
"v_accvgpr_read_b32 v106, a202\n"
"v_accvgpr_read_b32 v107, a203\n"
"v_accvgpr_read_b32 v108, a204\n"
"v_accvgpr_read_b32 v109, a205\n"
"v_accvgpr_read_b32 v110, a206\n"
"v_accvgpr_read_b32 v111, a207\n"
"v_accvgpr_read_b32 v112, a208\n"
"v_accvgpr_read_b32 v113, a209\n"
"v_accvgpr_read_b32 v114, a210\n"
"v_accvgpr_read_b32 v115, a211\n"
"v_accvgpr_read_b32 v116, a212\n"
"v_accvgpr_read_b32 v117, a213\n"
"v_accvgpr_read_b32 v118, a214\n"
"v_accvgpr_read_b32 v119, a215\n"
"v_accvgpr_read_b32 v120, a216\n"
"v_accvgpr_read_b32 v121, a217\n"
"v_accvgpr_read_b32 v122, a218\n"
"v_accvgpr_read_b32 v123, a219\n"
"v_accvgpr_read_b32 v124, a220\n"
"v_accvgpr_read_b32 v125, a221\n"
"v_accvgpr_read_b32 v126, a222\n"
"v_accvgpr_read_b32 v127, a223\n"
"v_accvgpr_mov_b32 a32, a224\n"
"v_accvgpr_mov_b32 a33, a225\n"
"v_accvgpr_mov_b32 a34, a226\n"
"v_accvgpr_mov_b32 a35, a227\n"
"v_accvgpr_mov_b32 a36, a228\n"
"v_accvgpr_mov_b32 a37, a229\n"
"v_accvgpr_mov_b32 a38, a230\n"
"v_accvgpr_mov_b32 a39, a231\n"
"v_accvgpr_mov_b32 a40, a232\n"
"v_accvgpr_mov_b32 a41, a233\n"
"v_accvgpr_mov_b32 a42, a234\n"
"v_accvgpr_mov_b32 a43, a235\n"
"v_accvgpr_mov_b32 a44, a236\n"
"v_accvgpr_mov_b32 a45, a237\n"
"v_accvgpr_mov_b32 a46, a238\n"
"v_accvgpr_mov_b32 a47, a239\n"
"s_waitcnt vmcnt(17)\n"
"s_barrier\n"
"ds_read_b128 a[0:3], v2\n"
"ds_read_b128 a[4:7], v2 offset:1024\n"
"ds_read_b128 a[8:11], v2 offset:2048\n"
"ds_read_b128 a[12:15], v2 offset:3072\n"
"ds_read_b128 a[16:19], v2 offset:4096\n"
"ds_read_b128 a[20:23], v2 offset:5120\n"
"s_waitcnt lgkmcnt(5)\n"
"v_mfma_f32_32x32x16_bf16 v[32:47], a[0:3], v[80:83], 0\n"
"ds_read_b128 a[24:27], v2 offset:6144\n"
"s_waitcnt lgkmcnt(5)\n"
"v_mfma_f32_32x32x16_bf16 v[32:47], a[4:7], v[84:87], v[32:47]\n"
"ds_read_b128 a[28:31], v2 offset:7168\n"
"s_waitcnt lgkmcnt(5)\n"
"v_mfma_f32_32x32x16_bf16 v[32:47], a[8:11], v[88:91], v[32:47]\n"
"ds_read_b128 a[0:3], v2 offset:8192\n"
"s_waitcnt lgkmcnt(5)\n"
"v_mfma_f32_32x32x16_bf16 v[32:47], a[12:15], v[92:95], v[32:47]\n"
"ds_read_b128 a[4:7], v2 offset:9216\n"
"s_waitcnt lgkmcnt(5)\n"
"v_mfma_f32_32x32x16_bf16 v[32:47], a[16:19], v[96:99], v[32:47]\n"
"ds_read_b128 a[8:11], v2 offset:10240\n"
"s_waitcnt lgkmcnt(5)\n"
"v_mfma_f32_32x32x16_bf16 v[32:47], a[20:23], v[100:103], v[32:47]\n"
"ds_read_b128 a[12:15], v2 offset:11264\n"
"s_waitcnt lgkmcnt(5)\n"
"v_mfma_f32_32x32x16_bf16 v[32:47], a[24:27], v[104:107], v[32:47]\n"
"ds_read_b128 a[16:19], v2 offset:12288\n"
"s_waitcnt lgkmcnt(5)\n"
"v_mfma_f32_32x32x16_bf16 v[32:47], a[28:31], v[108:111], v[32:47]\n"
"ds_read_b128 a[20:23], v2 offset:13312\n"
"s_waitcnt lgkmcnt(5)\n"
"v_mfma_f32_32x32x16_bf16 v[32:47], a[0:3], v[112:115], v[32:47]\n"
"ds_read_b128 a[24:27], v2 offset:14336\n"
"s_waitcnt lgkmcnt(5)\n"
"v_mfma_f32_32x32x16_bf16 v[32:47], a[4:7], v[116:119], v[32:47]\n"
"ds_read_b128 a[28:31], v2 offset:15360\n"
"s_waitcnt lgkmcnt(5)\n"
"v_mfma_f32_32x32x16_bf16 v[32:47], a[8:11], v[120:123], v[32:47]\n"
"ds_read_b128 a[0:3], v2 offset:16384\n"
"s_waitcnt lgkmcnt(5)\n"
"v_mfma_f32_32x32x16_bf16 v[32:47], a[12:15], v[124:127], v[32:47]\n"
"ds_read_b128 a[4:7], v2 offset:17408\n"
"s_waitcnt lgkmcnt(5)\n"
"v_mfma_f32_32x32x16_bf16 v[32:47], a[16:19], a[32:35], v[32:47]\n"
"ds_read_b128 a[8:11], v2 offset:18432\n"
"s_waitcnt lgkmcnt(5)\n"
"v_mfma_f32_32x32x16_bf16 v[32:47], a[20:23], a[36:39], v[32:47]\n"
"ds_read_b128 a[12:15], v2 offset:19456\n"
"s_waitcnt lgkmcnt(5)\n"
"v_mfma_f32_32x32x16_bf16 v[32:47], a[24:27], a[40:43], v[32:47]\n"
"ds_read_b128 a[16:19], v2 offset:20480\n"
"s_waitcnt lgkmcnt(5)\n"
"v_mfma_f32_32x32x16_bf16 v[32:47], a[28:31], a[44:47], v[32:47]\n"
"ds_read_b128 a[20:23], v2 offset:21504\n"
"s_waitcnt lgkmcnt(5)\n"
"v_mfma_f32_32x32x16_bf16 v[48:63], a[0:3], v[80:83], 0\n"
"ds_read_b128 a[24:27], v2 offset:22528\n"
"s_waitcnt lgkmcnt(5)\n"
"v_mfma_f32_32x32x16_bf16 v[48:63], a[4:7], v[84:87], v[48:63]\n"
"ds_read_b128 a[28:31], v2 offset:23552\n"
"s_waitcnt lgkmcnt(5)\n"
"v_mfma_f32_32x32x16_bf16 v[48:63], a[8:11], v[88:91], v[48:63]\n"
"ds_read_b128 a[0:3], v2 offset:24576\n"
"s_waitcnt lgkmcnt(5)\n"
"v_mfma_f32_32x32x16_bf16 v[48:63], a[12:15], v[92:95], v[48:63]\n"
"ds_read_b128 a[4:7], v2 offset:25600\n"
"s_waitcnt lgkmcnt(5)\n"
"v_mfma_f32_32x32x16_bf16 v[48:63], a[16:19], v[96:99], v[48:63]\n"
"ds_read_b128 a[8:11], v2 offset:26624\n"
"s_waitcnt lgkmcnt(5)\n"
"v_mfma_f32_32x32x16_bf16 v[48:63], a[20:23], v[100:103], v[48:63]\n"
"ds_read_b128 a[12:15], v2 offset:27648\n"
"s_waitcnt lgkmcnt(5)\n"
"v_mfma_f32_32x32x16_bf16 v[48:63], a[24:27], v[104:107], v[48:63]\n"
"ds_read_b128 a[16:19], v2 offset:28672\n"
"v_max_f32 v6, v32, v33\n"
"s_waitcnt lgkmcnt(5)\n"
"v_mfma_f32_32x32x16_bf16 v[48:63], a[28:31], v[108:111], v[48:63]\n"
"ds_read_b128 a[20:23], v2 offset:29696\n"
"v_max3_f32 v6, v6, v34, v35\n"
"s_waitcnt lgkmcnt(5)\n"
"v_mfma_f32_32x32x16_bf16 v[48:63], a[0:3], v[112:115], v[48:63]\n"
"ds_read_b128 a[24:27], v2 offset:30720\n"
"v_max3_f32 v6, v6, v36, v37\n"
"s_waitcnt lgkmcnt(5)\n"
"v_mfma_f32_32x32x16_bf16 v[48:63], a[4:7], v[116:119], v[48:63]\n"
"ds_read_b128 a[28:31], v2 offset:31744\n"
"v_max3_f32 v6, v6, v38, v39\n"
"s_waitcnt lgkmcnt(5)\n"
"v_mfma_f32_32x32x16_bf16 v[48:63], a[8:11], v[120:123], v[48:63]\n"
"v_mov_b32 v2, v1\n"
"ds_read_b128 a[0:3], v2\n"
"v_max3_f32 v6, v6, v40, v41\n"
"s_waitcnt lgkmcnt(5)\n"
"v_mfma_f32_32x32x16_bf16 v[48:63], a[12:15], v[124:127], v[48:63]\n"
"ds_read_b128 a[4:7], v2 offset:1024\n"
"v_max3_f32 v6, v6, v42, v43\n"
"s_waitcnt lgkmcnt(5)\n"
"v_mfma_f32_32x32x16_bf16 v[48:63], a[16:19], a[32:35], v[48:63]\n"
"ds_read_b128 a[8:11], v2 offset:2048\n"
"v_max3_f32 v6, v6, v44, v45\n"
"s_waitcnt lgkmcnt(5)\n"
"v_mfma_f32_32x32x16_bf16 v[48:63], a[20:23], a[36:39], v[48:63]\n"
"ds_read_b128 a[12:15], v2 offset:3072\n"
"v_max3_f32 v6, v6, v46, v47\n"
"s_waitcnt lgkmcnt(5)\n"
"v_mfma_f32_32x32x16_bf16 v[48:63], a[24:27], a[40:43], v[48:63]\n"
"ds_read_b128 a[16:19], v2 offset:4096\n"
"s_waitcnt lgkmcnt(5)\n"
"v_mfma_f32_32x32x16_bf16 v[48:63], a[28:31], a[44:47], v[48:63]\n"
"ds_read_b128 a[20:23], v2 offset:5120\n"
"s_cmp_eq_u32 %[tm], 0\n"
"s_cbranch_scc1 T0MASK%=\n"
"s_nop 8\n"
"s_branch T0SM%=\n"
"USTEPF%=:\n"
"s_waitcnt lgkmcnt(5)\n"
"v_mfma_f32_32x32x16_bf16 v[128:143], a[0:3], v[64:67], 0\n"
"ds_read_b128 a[24:27], v3 offset:6144\n"
"v_add_u32 v2, s66, v1\n"
"s_mov_b32 s77, s66\n"
"s_mov_b32 s66, s67\n"
"s_waitcnt lgkmcnt(5)\n"
"v_mfma_f32_32x32x16_bf16 v[144:159], a[4:7], v[64:67], 0\n"
"ds_read_b128 a[28:31], v3 offset:7168\n"
"s_mov_b32 s67, s68\n"
"s_mov_b32 s68, s77\n"
"s_add_u32 s70, s70, 1\n"
"s_waitcnt lgkmcnt(5)\n"
"v_mfma_f32_32x32x16_bf16 v[160:175], a[8:11], v[64:67], 0\n"
"ds_read_b128 a[0:3], v3 offset:8192\n"
"s_mov_b32 s73, 0\n"
"s_waitcnt lgkmcnt(5)\n"
"v_mfma_f32_32x32x16_bf16 v[176:191], a[12:15], v[64:67], 0\n"
"ds_read_b128 a[4:7], v3 offset:9216\n"
"v_max3_f32 v6, v6, v48, v49\n"
"v_max3_f32 v6, v6, v50, v51\n"
"v_max3_f32 v6, v6, v52, v53\n"
"s_waitcnt lgkmcnt(5)\n"
"v_mfma_f32_32x32x16_bf16 v[192:207], a[16:19], v[64:67], 0\n"
"ds_read_b128 a[8:11], v3 offset:10240\n"
"v_max3_f32 v6, v6, v54, v55\n"
"v_max3_f32 v6, v6, v56, v57\n"
"v_max3_f32 v6, v6, v58, v59\n"
"s_waitcnt lgkmcnt(5)\n"
"v_mfma_f32_32x32x16_bf16 v[208:223], a[20:23], v[64:67], 0\n"
"ds_read_b128 a[12:15], v3 offset:11264\n"
"v_max3_f32 v6, v6, v60, v61\n"
"v_max3_f32 v6, v6, v62, v63\n"
"s_waitcnt lgkmcnt(5)\n"
"v_mfma_f32_32x32x16_bf16 v[224:239], a[24:27], v[64:67], 0\n"
"ds_read_b128 a[16:19], v3 offset:12288\n"
"v_mov_b32 v10, v6\n"
"v_subrev_u32 v7, 64, v7\n"
"s_nop 0\n"
"v_permlane32_swap_b32 v10, v6\n"
"s_waitcnt lgkmcnt(5)\n"
"v_mfma_f32_32x32x16_bf16 v[240:255], a[28:31], v[64:67], 0\n"
"ds_read_b128 a[20:23], v3 offset:13312\n"
"v_max_f32 v6, v10, v6\n"
"v_cmp_lt_f32 vcc, 0x41c00000, v6\n"
"s_branch USTEPJ%=\n"
"USTEPR%=:\n"
"v_max_f32 v10, 0, v6\n"
"v_add_f32 v4, v4, v10\n"
"v_sub_f32 v9, 0, v10\n"
"v_exp_f32 v9, v9\n"
"v_sub_f32 v32, v32, v10\n"
"v_sub_f32 v48, v48, v10\n"
"v_mul_f32 v16, -1.0, v4\n"
"v_sub_f32 v33, v33, v10\n"
"v_sub_f32 v49, v49, v10\n"
"v_mul_f32 v17, -1.0, v4\n"
"v_sub_f32 v34, v34, v10\n"
"v_sub_f32 v50, v50, v10\n"
"v_mul_f32 v18, -1.0, v4\n"
"v_sub_f32 v35, v35, v10\n"
"v_sub_f32 v51, v51, v10\n"
"v_mul_f32 v19, -1.0, v4\n"
"v_sub_f32 v36, v36, v10\n"
"v_sub_f32 v52, v52, v10\n"
"v_mul_f32 v20, -1.0, v4\n"
"v_sub_f32 v37, v37, v10\n"
"v_sub_f32 v53, v53, v10\n"
"v_mul_f32 v21, -1.0, v4\n"
"v_sub_f32 v38, v38, v10\n"
"v_sub_f32 v54, v54, v10\n"
"v_mul_f32 v22, -1.0, v4\n"
"v_sub_f32 v39, v39, v10\n"
"v_sub_f32 v55, v55, v10\n"
"v_mul_f32 v23, -1.0, v4\n"
"v_sub_f32 v40, v40, v10\n"
"v_sub_f32 v56, v56, v10\n"
"v_mul_f32 v24, -1.0, v4\n"
"v_sub_f32 v41, v41, v10\n"
"v_sub_f32 v57, v57, v10\n"
"v_mul_f32 v25, -1.0, v4\n"
"v_sub_f32 v42, v42, v10\n"
"v_sub_f32 v58, v58, v10\n"
"v_mul_f32 v26, -1.0, v4\n"
"v_sub_f32 v43, v43, v10\n"
"v_sub_f32 v59, v59, v10\n"
"v_mul_f32 v27, -1.0, v4\n"
"v_sub_f32 v44, v44, v10\n"
"v_sub_f32 v60, v60, v10\n"
"v_mul_f32 v28, -1.0, v4\n"
"v_sub_f32 v45, v45, v10\n"
"v_sub_f32 v61, v61, v10\n"
"v_mul_f32 v29, -1.0, v4\n"
"v_sub_f32 v46, v46, v10\n"
"v_sub_f32 v62, v62, v10\n"
"v_mul_f32 v30, -1.0, v4\n"
"v_sub_f32 v47, v47, v10\n"
"v_sub_f32 v63, v63, v10\n"
"v_mul_f32 v31, -1.0, v4\n"
"v_mul_f32 v5, v5, v9\n"
"s_mov_b32 s72, 1\n"
"s_branch USTEPRB%=\n"
"USTEPS%=:\n"
"s_nop 15\n"
"v_mul_f32 v128, v128, v9\n"
"v_mul_f32 v129, v129, v9\n"
"v_mul_f32 v130, v130, v9\n"
"v_mul_f32 v131, v131, v9\n"
"v_mul_f32 v132, v132, v9\n"
"v_mul_f32 v133, v133, v9\n"
"v_mul_f32 v134, v134, v9\n"
"v_mul_f32 v135, v135, v9\n"
"v_mul_f32 v136, v136, v9\n"
"v_mul_f32 v137, v137, v9\n"
"v_mul_f32 v138, v138, v9\n"
"v_mul_f32 v139, v139, v9\n"
"v_mul_f32 v140, v140, v9\n"
"v_mul_f32 v141, v141, v9\n"
"v_mul_f32 v142, v142, v9\n"
"v_mul_f32 v143, v143, v9\n"
"v_mul_f32 v144, v144, v9\n"
"v_mul_f32 v145, v145, v9\n"
"v_mul_f32 v146, v146, v9\n"
"v_mul_f32 v147, v147, v9\n"
"v_mul_f32 v148, v148, v9\n"
"v_mul_f32 v149, v149, v9\n"
"v_mul_f32 v150, v150, v9\n"
"v_mul_f32 v151, v151, v9\n"
"v_mul_f32 v152, v152, v9\n"
"v_mul_f32 v153, v153, v9\n"
"v_mul_f32 v154, v154, v9\n"
"v_mul_f32 v155, v155, v9\n"
"v_mul_f32 v156, v156, v9\n"
"v_mul_f32 v157, v157, v9\n"
"v_mul_f32 v158, v158, v9\n"
"v_mul_f32 v159, v159, v9\n"
"v_mul_f32 v160, v160, v9\n"
"v_mul_f32 v161, v161, v9\n"
"v_mul_f32 v162, v162, v9\n"
"v_mul_f32 v163, v163, v9\n"
"v_mul_f32 v164, v164, v9\n"
"v_mul_f32 v165, v165, v9\n"
"v_mul_f32 v166, v166, v9\n"
"v_mul_f32 v167, v167, v9\n"
"v_mul_f32 v168, v168, v9\n"
"v_mul_f32 v169, v169, v9\n"
"v_mul_f32 v170, v170, v9\n"
"v_mul_f32 v171, v171, v9\n"
"v_mul_f32 v172, v172, v9\n"
"v_mul_f32 v173, v173, v9\n"
"v_mul_f32 v174, v174, v9\n"
"v_mul_f32 v175, v175, v9\n"
"v_mul_f32 v176, v176, v9\n"
"v_mul_f32 v177, v177, v9\n"
"v_mul_f32 v178, v178, v9\n"
"v_mul_f32 v179, v179, v9\n"
"v_mul_f32 v180, v180, v9\n"
"v_mul_f32 v181, v181, v9\n"
"v_mul_f32 v182, v182, v9\n"
"v_mul_f32 v183, v183, v9\n"
"v_mul_f32 v184, v184, v9\n"
"v_mul_f32 v185, v185, v9\n"
"v_mul_f32 v186, v186, v9\n"
"v_mul_f32 v187, v187, v9\n"
"v_mul_f32 v188, v188, v9\n"
"v_mul_f32 v189, v189, v9\n"
"v_mul_f32 v190, v190, v9\n"
"v_mul_f32 v191, v191, v9\n"
"v_mul_f32 v192, v192, v9\n"
"v_mul_f32 v193, v193, v9\n"
"v_mul_f32 v194, v194, v9\n"
"v_mul_f32 v195, v195, v9\n"
"v_mul_f32 v196, v196, v9\n"
"v_mul_f32 v197, v197, v9\n"
"v_mul_f32 v198, v198, v9\n"
"v_mul_f32 v199, v199, v9\n"
"v_mul_f32 v200, v200, v9\n"
"v_mul_f32 v201, v201, v9\n"
"v_mul_f32 v202, v202, v9\n"
"v_mul_f32 v203, v203, v9\n"
"v_mul_f32 v204, v204, v9\n"
"v_mul_f32 v205, v205, v9\n"
"v_mul_f32 v206, v206, v9\n"
"v_mul_f32 v207, v207, v9\n"
"v_mul_f32 v208, v208, v9\n"
"v_mul_f32 v209, v209, v9\n"
"v_mul_f32 v210, v210, v9\n"
"v_mul_f32 v211, v211, v9\n"
"v_mul_f32 v212, v212, v9\n"
"v_mul_f32 v213, v213, v9\n"
"v_mul_f32 v214, v214, v9\n"
"v_mul_f32 v215, v215, v9\n"
"v_mul_f32 v216, v216, v9\n"
"v_mul_f32 v217, v217, v9\n"
"v_mul_f32 v218, v218, v9\n"
"v_mul_f32 v219, v219, v9\n"
"v_mul_f32 v220, v220, v9\n"
"v_mul_f32 v221, v221, v9\n"
"v_mul_f32 v222, v222, v9\n"
"v_mul_f32 v223, v223, v9\n"
"v_mul_f32 v224, v224, v9\n"
"v_mul_f32 v225, v225, v9\n"
"v_mul_f32 v226, v226, v9\n"
"v_mul_f32 v227, v227, v9\n"
"v_mul_f32 v228, v228, v9\n"
"v_mul_f32 v229, v229, v9\n"
"v_mul_f32 v230, v230, v9\n"
"v_mul_f32 v231, v231, v9\n"
"v_mul_f32 v232, v232, v9\n"
"v_mul_f32 v233, v233, v9\n"
"v_mul_f32 v234, v234, v9\n"
"v_mul_f32 v235, v235, v9\n"
"v_mul_f32 v236, v236, v9\n"
"v_mul_f32 v237, v237, v9\n"
"v_mul_f32 v238, v238, v9\n"
"v_mul_f32 v239, v239, v9\n"
"v_mul_f32 v240, v240, v9\n"
"v_mul_f32 v241, v241, v9\n"
"v_mul_f32 v242, v242, v9\n"
"v_mul_f32 v243, v243, v9\n"
"v_mul_f32 v244, v244, v9\n"
"v_mul_f32 v245, v245, v9\n"
"v_mul_f32 v246, v246, v9\n"
"v_mul_f32 v247, v247, v9\n"
"v_mul_f32 v248, v248, v9\n"
"v_mul_f32 v249, v249, v9\n"
"v_mul_f32 v250, v250, v9\n"
"v_mul_f32 v251, v251, v9\n"
"v_mul_f32 v252, v252, v9\n"
"v_mul_f32 v253, v253, v9\n"
"v_mul_f32 v254, v254, v9\n"
"v_mul_f32 v255, v255, v9\n"
"s_mov_b32 s72, 0\n"
"s_branch USTEPSB%=\n"
"MSTEPF%=:\n"
"s_waitcnt lgkmcnt(5)\n"
"v_mfma_f32_32x32x16_bf16 v[128:143], a[0:3], v[64:67], 0\n"
"ds_read_b128 a[24:27], v3 offset:6144\n"
"v_add_u32 v2, s66, v1\n"
"s_mov_b32 s77, s66\n"
"s_mov_b32 s66, s67\n"
"s_waitcnt lgkmcnt(5)\n"
"v_mfma_f32_32x32x16_bf16 v[144:159], a[4:7], v[64:67], 0\n"
"ds_read_b128 a[28:31], v3 offset:7168\n"
"s_mov_b32 s67, s68\n"
"s_mov_b32 s68, s77\n"
"s_add_u32 s70, s70, 1\n"
"s_waitcnt lgkmcnt(5)\n"
"v_mfma_f32_32x32x16_bf16 v[160:175], a[8:11], v[64:67], 0\n"
"ds_read_b128 a[0:3], v3 offset:8192\n"
"s_mov_b32 s73, 0\n"
"s_waitcnt lgkmcnt(5)\n"
"v_mfma_f32_32x32x16_bf16 v[176:191], a[12:15], v[64:67], 0\n"
"ds_read_b128 a[4:7], v3 offset:9216\n"
"v_cmp_lt_i32_e64 s[78:79], v7, 32\n"
"v_cmp_lt_i32_e64 s[80:81], v7, 33\n"
"v_cmp_lt_i32_e64 s[82:83], v7, 34\n"
"v_cndmask_b32_e64 v48, v48, v8, s[78:79]\n"
"v_cmp_lt_i32_e64 s[78:79], v7, 35\n"
"v_cndmask_b32_e64 v49, v49, v8, s[80:81]\n"
"v_cmp_lt_i32_e64 s[80:81], v7, 48\n"
"v_cndmask_b32_e64 v50, v50, v8, s[82:83]\n"
"v_cmp_lt_i32_e64 s[82:83], v7, 49\n"
"v_cndmask_b32_e64 v51, v51, v8, s[78:79]\n"
"v_cmp_lt_i32_e64 s[78:79], v7, 50\n"
"v_cndmask_b32_e64 v52, v52, v8, s[80:81]\n"
"v_cmp_lt_i32_e64 s[80:81], v7, 51\n"
"v_cndmask_b32_e64 v53, v53, v8, s[82:83]\n"
"s_waitcnt lgkmcnt(5)\n"
"v_mfma_f32_32x32x16_bf16 v[192:207], a[16:19], v[64:67], 0\n"
"ds_read_b128 a[8:11], v3 offset:10240\n"
"v_cmp_lt_i32_e64 s[82:83], v7, 40\n"
"v_cndmask_b32_e64 v54, v54, v8, s[78:79]\n"
"v_cmp_lt_i32_e64 s[78:79], v7, 41\n"
"v_cndmask_b32_e64 v55, v55, v8, s[80:81]\n"
"v_cmp_lt_i32_e64 s[80:81], v7, 42\n"
"v_cndmask_b32_e64 v56, v56, v8, s[82:83]\n"
"v_cmp_lt_i32_e64 s[82:83], v7, 43\n"
"v_cndmask_b32_e64 v57, v57, v8, s[78:79]\n"
"v_cmp_lt_i32_e64 s[78:79], v7, 56\n"
"v_cndmask_b32_e64 v58, v58, v8, s[80:81]\n"
"v_cmp_lt_i32_e64 s[80:81], v7, 57\n"
"v_cndmask_b32_e64 v59, v59, v8, s[82:83]\n"
"v_cmp_lt_i32_e64 s[82:83], v7, 58\n"
"s_waitcnt lgkmcnt(5)\n"
"v_mfma_f32_32x32x16_bf16 v[208:223], a[20:23], v[64:67], 0\n"
"ds_read_b128 a[12:15], v3 offset:11264\n"
"v_cndmask_b32_e64 v60, v60, v8, s[78:79]\n"
"v_cmp_lt_i32_e64 s[78:79], v7, 59\n"
"v_cndmask_b32_e64 v61, v61, v8, s[80:81]\n"
"v_cndmask_b32_e64 v62, v62, v8, s[82:83]\n"
"v_cndmask_b32_e64 v63, v63, v8, s[78:79]\n"
"v_max3_f32 v6, v6, v48, v49\n"
"v_max3_f32 v6, v6, v50, v51\n"
"v_max3_f32 v6, v6, v52, v53\n"
"v_max3_f32 v6, v6, v54, v55\n"
"v_max3_f32 v6, v6, v56, v57\n"
"v_max3_f32 v6, v6, v58, v59\n"
"v_max3_f32 v6, v6, v60, v61\n"
"v_max3_f32 v6, v6, v62, v63\n"
"s_waitcnt lgkmcnt(5)\n"
"v_mfma_f32_32x32x16_bf16 v[224:239], a[24:27], v[64:67], 0\n"
"ds_read_b128 a[16:19], v3 offset:12288\n"
"v_mov_b32 v10, v6\n"
"v_subrev_u32 v7, 64, v7\n"
"s_nop 0\n"
"v_permlane32_swap_b32 v10, v6\n"
"s_waitcnt lgkmcnt(5)\n"
"v_mfma_f32_32x32x16_bf16 v[240:255], a[28:31], v[64:67], 0\n"
"ds_read_b128 a[20:23], v3 offset:13312\n"
"v_max_f32 v6, v10, v6\n"
"v_cmp_lt_f32 vcc, 0x41c00000, v6\n"
"s_branch MSTEPJ%=\n"
"MSTEPR%=:\n"
"v_max_f32 v10, 0, v6\n"
"v_add_f32 v4, v4, v10\n"
"v_sub_f32 v9, 0, v10\n"
"v_exp_f32 v9, v9\n"
"v_sub_f32 v32, v32, v10\n"
"v_sub_f32 v48, v48, v10\n"
"v_mul_f32 v16, -1.0, v4\n"
"v_sub_f32 v33, v33, v10\n"
"v_sub_f32 v49, v49, v10\n"
"v_mul_f32 v17, -1.0, v4\n"
"v_sub_f32 v34, v34, v10\n"
"v_sub_f32 v50, v50, v10\n"
"v_mul_f32 v18, -1.0, v4\n"
"v_sub_f32 v35, v35, v10\n"
"v_sub_f32 v51, v51, v10\n"
"v_mul_f32 v19, -1.0, v4\n"
"v_sub_f32 v36, v36, v10\n"
"v_sub_f32 v52, v52, v10\n"
"v_mul_f32 v20, -1.0, v4\n"
"v_sub_f32 v37, v37, v10\n"
"v_sub_f32 v53, v53, v10\n"
"v_mul_f32 v21, -1.0, v4\n"
"v_sub_f32 v38, v38, v10\n"
"v_sub_f32 v54, v54, v10\n"
"v_mul_f32 v22, -1.0, v4\n"
"v_sub_f32 v39, v39, v10\n"
"v_sub_f32 v55, v55, v10\n"
"v_mul_f32 v23, -1.0, v4\n"
"v_sub_f32 v40, v40, v10\n"
"v_sub_f32 v56, v56, v10\n"
"v_mul_f32 v24, -1.0, v4\n"
"v_sub_f32 v41, v41, v10\n"
"v_sub_f32 v57, v57, v10\n"
"v_mul_f32 v25, -1.0, v4\n"
"v_sub_f32 v42, v42, v10\n"
"v_sub_f32 v58, v58, v10\n"
"v_mul_f32 v26, -1.0, v4\n"
"v_sub_f32 v43, v43, v10\n"
"v_sub_f32 v59, v59, v10\n"
"v_mul_f32 v27, -1.0, v4\n"
"v_sub_f32 v44, v44, v10\n"
"v_sub_f32 v60, v60, v10\n"
"v_mul_f32 v28, -1.0, v4\n"
"v_sub_f32 v45, v45, v10\n"
"v_sub_f32 v61, v61, v10\n"
"v_mul_f32 v29, -1.0, v4\n"
"v_sub_f32 v46, v46, v10\n"
"v_sub_f32 v62, v62, v10\n"
"v_mul_f32 v30, -1.0, v4\n"
"v_sub_f32 v47, v47, v10\n"
"v_sub_f32 v63, v63, v10\n"
"v_mul_f32 v31, -1.0, v4\n"
"v_mul_f32 v5, v5, v9\n"
"s_mov_b32 s72, 1\n"
"s_branch MSTEPRB%=\n"
"MSTEPS%=:\n"
"s_nop 15\n"
"v_mul_f32 v128, v128, v9\n"
"v_mul_f32 v129, v129, v9\n"
"v_mul_f32 v130, v130, v9\n"
"v_mul_f32 v131, v131, v9\n"
"v_mul_f32 v132, v132, v9\n"
"v_mul_f32 v133, v133, v9\n"
"v_mul_f32 v134, v134, v9\n"
"v_mul_f32 v135, v135, v9\n"
"v_mul_f32 v136, v136, v9\n"
"v_mul_f32 v137, v137, v9\n"
"v_mul_f32 v138, v138, v9\n"
"v_mul_f32 v139, v139, v9\n"
"v_mul_f32 v140, v140, v9\n"
"v_mul_f32 v141, v141, v9\n"
"v_mul_f32 v142, v142, v9\n"
"v_mul_f32 v143, v143, v9\n"
"v_mul_f32 v144, v144, v9\n"
"v_mul_f32 v145, v145, v9\n"
"v_mul_f32 v146, v146, v9\n"
"v_mul_f32 v147, v147, v9\n"
"v_mul_f32 v148, v148, v9\n"
"v_mul_f32 v149, v149, v9\n"
"v_mul_f32 v150, v150, v9\n"
"v_mul_f32 v151, v151, v9\n"
"v_mul_f32 v152, v152, v9\n"
"v_mul_f32 v153, v153, v9\n"
"v_mul_f32 v154, v154, v9\n"
"v_mul_f32 v155, v155, v9\n"
"v_mul_f32 v156, v156, v9\n"
"v_mul_f32 v157, v157, v9\n"
"v_mul_f32 v158, v158, v9\n"
"v_mul_f32 v159, v159, v9\n"
"v_mul_f32 v160, v160, v9\n"
"v_mul_f32 v161, v161, v9\n"
"v_mul_f32 v162, v162, v9\n"
"v_mul_f32 v163, v163, v9\n"
"v_mul_f32 v164, v164, v9\n"
"v_mul_f32 v165, v165, v9\n"
"v_mul_f32 v166, v166, v9\n"
"v_mul_f32 v167, v167, v9\n"
"v_mul_f32 v168, v168, v9\n"
"v_mul_f32 v169, v169, v9\n"
"v_mul_f32 v170, v170, v9\n"
"v_mul_f32 v171, v171, v9\n"
"v_mul_f32 v172, v172, v9\n"
"v_mul_f32 v173, v173, v9\n"
"v_mul_f32 v174, v174, v9\n"
"v_mul_f32 v175, v175, v9\n"
"v_mul_f32 v176, v176, v9\n"
"v_mul_f32 v177, v177, v9\n"
"v_mul_f32 v178, v178, v9\n"
"v_mul_f32 v179, v179, v9\n"
"v_mul_f32 v180, v180, v9\n"
"v_mul_f32 v181, v181, v9\n"
"v_mul_f32 v182, v182, v9\n"
"v_mul_f32 v183, v183, v9\n"
"v_mul_f32 v184, v184, v9\n"
"v_mul_f32 v185, v185, v9\n"
"v_mul_f32 v186, v186, v9\n"
"v_mul_f32 v187, v187, v9\n"
"v_mul_f32 v188, v188, v9\n"
"v_mul_f32 v189, v189, v9\n"
"v_mul_f32 v190, v190, v9\n"
"v_mul_f32 v191, v191, v9\n"
"v_mul_f32 v192, v192, v9\n"
"v_mul_f32 v193, v193, v9\n"
"v_mul_f32 v194, v194, v9\n"
"v_mul_f32 v195, v195, v9\n"
"v_mul_f32 v196, v196, v9\n"
"v_mul_f32 v197, v197, v9\n"
"v_mul_f32 v198, v198, v9\n"
"v_mul_f32 v199, v199, v9\n"
"v_mul_f32 v200, v200, v9\n"
"v_mul_f32 v201, v201, v9\n"
"v_mul_f32 v202, v202, v9\n"
"v_mul_f32 v203, v203, v9\n"
"v_mul_f32 v204, v204, v9\n"
"v_mul_f32 v205, v205, v9\n"
"v_mul_f32 v206, v206, v9\n"
"v_mul_f32 v207, v207, v9\n"
"v_mul_f32 v208, v208, v9\n"
"v_mul_f32 v209, v209, v9\n"
"v_mul_f32 v210, v210, v9\n"
"v_mul_f32 v211, v211, v9\n"
"v_mul_f32 v212, v212, v9\n"
"v_mul_f32 v213, v213, v9\n"
"v_mul_f32 v214, v214, v9\n"
"v_mul_f32 v215, v215, v9\n"
"v_mul_f32 v216, v216, v9\n"
"v_mul_f32 v217, v217, v9\n"
"v_mul_f32 v218, v218, v9\n"
"v_mul_f32 v219, v219, v9\n"
"v_mul_f32 v220, v220, v9\n"
"v_mul_f32 v221, v221, v9\n"
"v_mul_f32 v222, v222, v9\n"
"v_mul_f32 v223, v223, v9\n"
"v_mul_f32 v224, v224, v9\n"
"v_mul_f32 v225, v225, v9\n"
"v_mul_f32 v226, v226, v9\n"
"v_mul_f32 v227, v227, v9\n"
"v_mul_f32 v228, v228, v9\n"
"v_mul_f32 v229, v229, v9\n"
"v_mul_f32 v230, v230, v9\n"
"v_mul_f32 v231, v231, v9\n"
"v_mul_f32 v232, v232, v9\n"
"v_mul_f32 v233, v233, v9\n"
"v_mul_f32 v234, v234, v9\n"
"v_mul_f32 v235, v235, v9\n"
"v_mul_f32 v236, v236, v9\n"
"v_mul_f32 v237, v237, v9\n"
"v_mul_f32 v238, v238, v9\n"
"v_mul_f32 v239, v239, v9\n"
"v_mul_f32 v240, v240, v9\n"
"v_mul_f32 v241, v241, v9\n"
"v_mul_f32 v242, v242, v9\n"
"v_mul_f32 v243, v243, v9\n"
"v_mul_f32 v244, v244, v9\n"
"v_mul_f32 v245, v245, v9\n"
"v_mul_f32 v246, v246, v9\n"
"v_mul_f32 v247, v247, v9\n"
"v_mul_f32 v248, v248, v9\n"
"v_mul_f32 v249, v249, v9\n"
"v_mul_f32 v250, v250, v9\n"
"v_mul_f32 v251, v251, v9\n"
"v_mul_f32 v252, v252, v9\n"
"v_mul_f32 v253, v253, v9\n"
"v_mul_f32 v254, v254, v9\n"
"v_mul_f32 v255, v255, v9\n"
"s_mov_b32 s72, 0\n"
"s_branch MSTEPSB%=\n"
"T0MASK%=:\n"
"s_nop 9\n"
"v_cmp_lt_i32_e64 s[78:79], v7, 0\n"
"v_cmp_lt_i32_e64 s[80:81], v7, 1\n"
"v_cmp_lt_i32_e64 s[82:83], v7, 2\n"
"v_cndmask_b32_e64 v32, v32, v8, s[78:79]\n"
"v_cmp_lt_i32_e64 s[78:79], v7, 3\n"
"v_cndmask_b32_e64 v33, v33, v8, s[80:81]\n"
"v_cmp_lt_i32_e64 s[80:81], v7, 16\n"
"v_cndmask_b32_e64 v34, v34, v8, s[82:83]\n"
"v_cmp_lt_i32_e64 s[82:83], v7, 17\n"
"v_cndmask_b32_e64 v35, v35, v8, s[78:79]\n"
"v_cmp_lt_i32_e64 s[78:79], v7, 18\n"
"v_cndmask_b32_e64 v36, v36, v8, s[80:81]\n"
"v_cmp_lt_i32_e64 s[80:81], v7, 19\n"
"v_cndmask_b32_e64 v37, v37, v8, s[82:83]\n"
"v_cmp_lt_i32_e64 s[82:83], v7, 8\n"
"v_cndmask_b32_e64 v38, v38, v8, s[78:79]\n"
"v_cmp_lt_i32_e64 s[78:79], v7, 9\n"
"v_cndmask_b32_e64 v39, v39, v8, s[80:81]\n"
"v_cmp_lt_i32_e64 s[80:81], v7, 10\n"
"v_cndmask_b32_e64 v40, v40, v8, s[82:83]\n"
"v_cmp_lt_i32_e64 s[82:83], v7, 11\n"
"v_cndmask_b32_e64 v41, v41, v8, s[78:79]\n"
"v_cmp_lt_i32_e64 s[78:79], v7, 24\n"
"v_cndmask_b32_e64 v42, v42, v8, s[80:81]\n"
"v_cmp_lt_i32_e64 s[80:81], v7, 25\n"
"v_cndmask_b32_e64 v43, v43, v8, s[82:83]\n"
"v_cmp_lt_i32_e64 s[82:83], v7, 26\n"
"v_cndmask_b32_e64 v44, v44, v8, s[78:79]\n"
"v_cmp_lt_i32_e64 s[78:79], v7, 27\n"
"v_cndmask_b32_e64 v45, v45, v8, s[80:81]\n"
"v_cndmask_b32_e64 v46, v46, v8, s[82:83]\n"
"v_cndmask_b32_e64 v47, v47, v8, s[78:79]\n"
"v_cmp_lt_i32_e64 s[78:79], v7, 32\n"
"v_cmp_lt_i32_e64 s[80:81], v7, 33\n"
"v_cmp_lt_i32_e64 s[82:83], v7, 34\n"
"v_cndmask_b32_e64 v48, v48, v8, s[78:79]\n"
"v_cmp_lt_i32_e64 s[78:79], v7, 35\n"
"v_cndmask_b32_e64 v49, v49, v8, s[80:81]\n"
"v_cmp_lt_i32_e64 s[80:81], v7, 48\n"
"v_cndmask_b32_e64 v50, v50, v8, s[82:83]\n"
"v_cmp_lt_i32_e64 s[82:83], v7, 49\n"
"v_cndmask_b32_e64 v51, v51, v8, s[78:79]\n"
"v_cmp_lt_i32_e64 s[78:79], v7, 50\n"
"v_cndmask_b32_e64 v52, v52, v8, s[80:81]\n"
"v_cmp_lt_i32_e64 s[80:81], v7, 51\n"
"v_cndmask_b32_e64 v53, v53, v8, s[82:83]\n"
"v_cmp_lt_i32_e64 s[82:83], v7, 40\n"
"v_cndmask_b32_e64 v54, v54, v8, s[78:79]\n"
"v_cmp_lt_i32_e64 s[78:79], v7, 41\n"
"v_cndmask_b32_e64 v55, v55, v8, s[80:81]\n"
"v_cmp_lt_i32_e64 s[80:81], v7, 42\n"
"v_cndmask_b32_e64 v56, v56, v8, s[82:83]\n"
"v_cmp_lt_i32_e64 s[82:83], v7, 43\n"
"v_cndmask_b32_e64 v57, v57, v8, s[78:79]\n"
"v_cmp_lt_i32_e64 s[78:79], v7, 56\n"
"v_cndmask_b32_e64 v58, v58, v8, s[80:81]\n"
"v_cmp_lt_i32_e64 s[80:81], v7, 57\n"
"v_cndmask_b32_e64 v59, v59, v8, s[82:83]\n"
"v_cmp_lt_i32_e64 s[82:83], v7, 58\n"
"v_cndmask_b32_e64 v60, v60, v8, s[78:79]\n"
"v_cmp_lt_i32_e64 s[78:79], v7, 59\n"
"v_cndmask_b32_e64 v61, v61, v8, s[80:81]\n"
"v_cndmask_b32_e64 v62, v62, v8, s[82:83]\n"
"v_cndmask_b32_e64 v63, v63, v8, s[78:79]\n"
"v_max_f32 v6, v32, v33\n"
"v_max3_f32 v6, v6, v34, v35\n"
"v_max3_f32 v6, v6, v36, v37\n"
"v_max3_f32 v6, v6, v38, v39\n"
"v_max3_f32 v6, v6, v40, v41\n"
"v_max3_f32 v6, v6, v42, v43\n"
"v_max3_f32 v6, v6, v44, v45\n"
"v_max3_f32 v6, v6, v46, v47\n"
"s_branch T0SM%=\n"
"FIN%=:\n"
      :
      : [rsW] "s"(rsW), [rsQ] "s"(rsQ), [rs1] "s"(rs1), [w] "s"(w), [xoff] "s"(xoff), [q0] "s"(q0), [f0] "s"(f0), [ks] "s"(ks), [vs] "s"(vs), [n] "s"(n), [tm] "s"(tm),
        [nk0] "s"(nk0), [mode] "s"(mode), [hoff] "s"(hoff), [coff] "s"(coff), [poff] "s"(poff), [flag] "s"(flag), [stoff] "s"(stoff),
        [tag] "s"(tag), [kvf] "s"(kvf), [kvl] "s"(kvl), [out] "s"(out), [stp] "s"(stp)
      : CL2);
  }
}

  asm volatile("" : "+s"(ap), "+s"(b), "+s"(w));
  {
  const int xc = b & 7, j0 = b >> 3, mh = j0 >> 4, qt = (j0 & 1) ? 15 - ((j0 & 15) >> 1) : (j0 & 15) >> 1, j = qt * 2 + mh;
  const rsrc_t rsO = mkrsrc(ap->wo, H * D * M * 2), rs2 = mkrsrc(ap->w2, F * M * 2), rsW = mkrsrc(ap->ws, (unsigned)WS_END);
  const rsrc_t rsX = mkrsrc(ap->x, S * M * 2), rsY = mkrsrc(ap->out, S * M * 2);
  const unsigned arow = (unsigned)WS_AF + (qt * 4 + w) * (KS3 * 1024), a1 = arow + (2 * xc + mh) * 16 * 1024, a1b = arow + (2 * xc + 1 - mh) * 16 * 1024, a2 = arow + (256 + xc * 14) * 1024;
  const unsigned so = (2 * xc + mh) * 256 * (M * 2) + mh * 448, sob = (2 * xc + 1 - mh) * 256 * (M * 2) + mh * 448, s2 = xc * 224 * (M * 2) + mh * 448;
  const int lo = (w >> 1) ^ 1, w3 = (w & (w >> 1)) ^ 1;
  const unsigned s11 = s2 + lo * (3 * 64 * M * 2);
  const unsigned pt = (unsigned)WS_P3 + j * (unsigned)P3_TILE, pst = pt + 2 * w * 57344 + xc * 7168, pl1 = pt + xc * 57344 + w * 1024, pl2 = pl1 + w3 * 4096;
  const unsigned fl = WS_K3F + j * 512, fst = fl + w * 128 + xc * 8, fld = fl + (xc >> 1) * 128;
  const unsigned x1 = ((qt * 128 + 16 * xc) * M + mh * 224 + 32 * w) * 2, x2 = x1 + w3 * 256;
  const unsigned l0 = w * 7168, stoff = (unsigned)WS_STAMP + 524288 + (b * 4 + w) * 128;
  const unsigned aff = WS_AFF + ((2 * xc + mh) * 16 + qt) * 128 + w * 8, afl = WS_AFF + ((2 * xc + 1 - mh) * 16 + qt) * 128 + w * 8, ep = b | w;
  static_assert(WS_EPOCH == 0x5F00000, "literal in the asm");
  asm volatile(
"s_add_u32 s64, %[so], 0\n"
"s_add_u32 m0, %[l0], 0\n"
"s_nop 0\n"
"buffer_load_dwordx4 %[vo0], %[rsO], s64 offen lds\n"
"buffer_load_dwordx4 %[vo1], %[rsO], s64 offen offset:1024 lds\n"
"buffer_load_dwordx4 %[vo2], %[rsO], s64 offen offset:2048 lds\n"
"buffer_load_dwordx4 %[vo3], %[rsO], s64 offen offset:3072 lds\n"
"s_add_u32 m0, %[l0], 0x1000\n"
"s_nop 0\n"
"buffer_load_dwordx4 %[vo4], %[rsO], s64 offen lds\n"
"buffer_load_dwordx4 %[vo5], %[rsO], s64 offen offset:1024 lds\n"
"buffer_load_dwordx4 %[vo6], %[rsO], s64 offen offset:2048 lds\n"
"s_add_u32 s64, %[so], 0xe000\n"
"s_add_u32 m0, %[l0], 0x7000\n"
"s_nop 0\n"
"buffer_load_dwordx4 %[vo0], %[rsO], s64 offen lds\n"
"buffer_load_dwordx4 %[vo1], %[rsO], s64 offen offset:1024 lds\n"
"buffer_load_dwordx4 %[vo2], %[rsO], s64 offen offset:2048 lds\n"
"buffer_load_dwordx4 %[vo3], %[rsO], s64 offen offset:3072 lds\n"
"s_add_u32 m0, %[l0], 0x8000\n"
"s_nop 0\n"
"buffer_load_dwordx4 %[vo4], %[rsO], s64 offen lds\n"
"buffer_load_dwordx4 %[vo5], %[rsO], s64 offen offset:1024 lds\n"
"buffer_load_dwordx4 %[vo6], %[rsO], s64 offen offset:2048 lds\n"
"v_mov_b32 v16, %[tbe]\n"
"v_mov_b32 v17, %[tbo]\n"
"v_mov_b64 v[18:19], %[tag]\n"
"s_waitcnt vmcnt(14)\n"
"buffer_store_dwordx2 v[18:19], off, %[rsW], %[aff] sc1\n"
"s_add_u32 s65, %[a1], 0\n"
"buffer_load_dwordx4 a[0:3], %[va], %[rsW], s65 offen sc1\n"
"buffer_load_dwordx4 a[4:7], %[va], %[rsW], s65 offen offset:256 sc1\n"
"buffer_load_dwordx4 a[8:11], %[va], %[rsW], s65 offen offset:2048 sc1\n"
"buffer_load_dwordx4 a[12:15], %[va], %[rsW], s65 offen offset:2304 sc1\n"
"s_add_u32 s64, %[so], 0x1c000\n"
"s_add_u32 m0, %[l0], 0xe000\n"
"s_nop 0\n"
"buffer_load_dwordx4 %[vo0], %[rsO], s64 offen lds\n"
"buffer_load_dwordx4 %[vo1], %[rsO], s64 offen offset:1024 lds\n"
"buffer_load_dwordx4 %[vo2], %[rsO], s64 offen offset:2048 lds\n"
"buffer_load_dwordx4 %[vo3], %[rsO], s64 offen offset:3072 lds\n"
"s_add_u32 m0, %[l0], 0xf000\n"
"s_nop 0\n"
"buffer_load_dwordx4 %[vo4], %[rsO], s64 offen lds\n"
"buffer_load_dwordx4 %[vo5], %[rsO], s64 offen offset:1024 lds\n"
"buffer_load_dwordx4 %[vo6], %[rsO], s64 offen offset:2048 lds\n"
"s_add_u32 s65, %[a1], 0x1000\n"
"buffer_load_dwordx4 a[16:19], %[va], %[rsW], s65 offen sc1\n"
"buffer_load_dwordx4 a[20:23], %[va], %[rsW], s65 offen offset:256 sc1\n"
"buffer_load_dwordx4 a[24:27], %[va], %[rsW], s65 offen offset:2048 sc1\n"
"buffer_load_dwordx4 a[28:31], %[va], %[rsW], s65 offen offset:2304 sc1\n"
"s_add_u32 s64, %[so], 0x2a000\n"
"s_add_u32 m0, %[l0], 0x15000\n"
"s_nop 0\n"
"buffer_load_dwordx4 %[vo0], %[rsO], s64 offen lds\n"
"buffer_load_dwordx4 %[vo1], %[rsO], s64 offen offset:1024 lds\n"
"buffer_load_dwordx4 %[vo2], %[rsO], s64 offen offset:2048 lds\n"
"buffer_load_dwordx4 %[vo3], %[rsO], s64 offen offset:3072 lds\n"
"s_add_u32 m0, %[l0], 0x16000\n"
"s_nop 0\n"
"buffer_load_dwordx4 %[vo4], %[rsO], s64 offen lds\n"
"buffer_load_dwordx4 %[vo5], %[rsO], s64 offen offset:1024 lds\n"
"buffer_load_dwordx4 %[vo6], %[rsO], s64 offen offset:2048 lds\n"
"s_add_u32 s65, %[a1], 0x2000\n"
"buffer_load_dwordx4 a[32:35], %[va], %[rsW], s65 offen sc1\n"
"buffer_load_dwordx4 a[36:39], %[va], %[rsW], s65 offen offset:256 sc1\n"
"buffer_load_dwordx4 a[40:43], %[va], %[rsW], s65 offen offset:2048 sc1\n"
"buffer_load_dwordx4 a[44:47], %[va], %[rsW], s65 offen offset:2304 sc1\n"
"buffer_load_dwordx4 v[20:23], %[vx], %[rsX], %[x1] offen\n"
"buffer_load_dwordx4 v[24:27], %[vx], %[rsX], %[x2] offen\n"
"s_waitcnt vmcnt(36)\n"
"s_barrier\n"
"ds_read_b64_tr_b16 v[32:33], v16\n"
"ds_read_b64_tr_b16 v[34:35], v16 offset:3584\n"
"ds_read_b64_tr_b16 v[36:37], v17\n"
"ds_read_b64_tr_b16 v[38:39], v17 offset:3584\n"
"ds_read_b64_tr_b16 v[40:41], v16 offset:64\n"
"ds_read_b64_tr_b16 v[42:43], v16 offset:3648\n"
"ds_read_b64_tr_b16 v[44:45], v17 offset:64\n"
"ds_read_b64_tr_b16 v[46:47], v17 offset:3648\n"
"ds_read_b64_tr_b16 v[48:49], v16 offset:128\n"
"ds_read_b64_tr_b16 v[50:51], v16 offset:3712\n"
"ds_read_b64_tr_b16 v[52:53], v17 offset:128\n"
"ds_read_b64_tr_b16 v[54:55], v17 offset:3712\n"
"s_waitcnt lgkmcnt(8)\n"
"s_waitcnt vmcnt(27)\n"
"v_mfma_f32_16x16x32_bf16 v[128:131], v[32:35], a[0:3], 0\n"
"ds_read_b64_tr_b16 v[56:57], v16 offset:192\n"
"s_waitcnt vmcnt(26)\n"
"v_mfma_f32_16x16x32_bf16 v[132:135], v[32:35], a[4:7], 0\n"
"ds_read_b64_tr_b16 v[58:59], v16 offset:3776\n"
"v_mfma_f32_16x16x32_bf16 v[136:139], v[36:39], a[0:3], 0\n"
"ds_read_b64_tr_b16 v[60:61], v17 offset:192\n"
"v_mfma_f32_16x16x32_bf16 v[140:143], v[36:39], a[4:7], 0\n"
"ds_read_b64_tr_b16 v[62:63], v17 offset:3776\n"
"s_waitcnt lgkmcnt(8)\n"
"v_mfma_f32_16x16x32_bf16 v[144:147], v[40:43], a[0:3], 0\n"
"ds_read_b64_tr_b16 v[32:33], v16 offset:256\n"
"v_mfma_f32_16x16x32_bf16 v[148:151], v[40:43], a[4:7], 0\n"
"ds_read_b64_tr_b16 v[34:35], v16 offset:3840\n"
"v_mfma_f32_16x16x32_bf16 v[152:155], v[44:47], a[0:3], 0\n"
"ds_read_b64_tr_b16 v[36:37], v17 offset:256\n"
"v_mfma_f32_16x16x32_bf16 v[156:159], v[44:47], a[4:7], 0\n"
"ds_read_b64_tr_b16 v[38:39], v17 offset:3840\n"
"s_waitcnt lgkmcnt(8)\n"
"v_mfma_f32_16x16x32_bf16 v[160:163], v[48:51], a[0:3], 0\n"
"ds_read_b64_tr_b16 v[40:41], v16 offset:320\n"
"v_mfma_f32_16x16x32_bf16 v[164:167], v[48:51], a[4:7], 0\n"
"ds_read_b64_tr_b16 v[42:43], v16 offset:3904\n"
"v_mfma_f32_16x16x32_bf16 v[168:171], v[52:55], a[0:3], 0\n"
"ds_read_b64_tr_b16 v[44:45], v17 offset:320\n"
"v_mfma_f32_16x16x32_bf16 v[172:175], v[52:55], a[4:7], 0\n"
"ds_read_b64_tr_b16 v[46:47], v17 offset:3904\n"
"s_waitcnt lgkmcnt(8)\n"
"v_mfma_f32_16x16x32_bf16 v[176:179], v[56:59], a[0:3], 0\n"
"ds_read_b64_tr_b16 v[48:49], v16 offset:384\n"
"v_mfma_f32_16x16x32_bf16 v[180:183], v[56:59], a[4:7], 0\n"
"ds_read_b64_tr_b16 v[50:51], v16 offset:3968\n"
"v_mfma_f32_16x16x32_bf16 v[184:187], v[60:63], a[0:3], 0\n"
"ds_read_b64_tr_b16 v[52:53], v17 offset:384\n"
"v_mfma_f32_16x16x32_bf16 v[188:191], v[60:63], a[4:7], 0\n"
"ds_read_b64_tr_b16 v[54:55], v17 offset:3968\n"
"s_waitcnt lgkmcnt(8)\n"
"v_mfma_f32_16x16x32_bf16 v[192:195], v[32:35], a[0:3], 0\n"
"ds_read_b64_tr_b16 v[56:57], v16 offset:14336\n"
"v_mfma_f32_16x16x32_bf16 v[196:199], v[32:35], a[4:7], 0\n"
"ds_read_b64_tr_b16 v[58:59], v16 offset:17920\n"
"v_mfma_f32_16x16x32_bf16 v[200:203], v[36:39], a[0:3], 0\n"
"ds_read_b64_tr_b16 v[60:61], v17 offset:14336\n"
"v_mfma_f32_16x16x32_bf16 v[204:207], v[36:39], a[4:7], 0\n"
"ds_read_b64_tr_b16 v[62:63], v17 offset:17920\n"
"s_waitcnt lgkmcnt(8)\n"
"v_mfma_f32_16x16x32_bf16 v[208:211], v[40:43], a[0:3], 0\n"
"ds_read_b64_tr_b16 v[32:33], v16 offset:14400\n"
"v_mfma_f32_16x16x32_bf16 v[212:215], v[40:43], a[4:7], 0\n"
"ds_read_b64_tr_b16 v[34:35], v16 offset:17984\n"
"v_mfma_f32_16x16x32_bf16 v[216:219], v[44:47], a[0:3], 0\n"
"ds_read_b64_tr_b16 v[36:37], v17 offset:14400\n"
"v_mfma_f32_16x16x32_bf16 v[220:223], v[44:47], a[4:7], 0\n"
"ds_read_b64_tr_b16 v[38:39], v17 offset:17984\n"
"s_waitcnt lgkmcnt(8)\n"
"v_mfma_f32_16x16x32_bf16 v[224:227], v[48:51], a[0:3], 0\n"
"ds_read_b64_tr_b16 v[40:41], v16 offset:14464\n"
"v_mfma_f32_16x16x32_bf16 v[228:231], v[48:51], a[4:7], 0\n"
"ds_read_b64_tr_b16 v[42:43], v16 offset:18048\n"
"v_mfma_f32_16x16x32_bf16 v[232:235], v[52:55], a[0:3], 0\n"
"ds_read_b64_tr_b16 v[44:45], v17 offset:14464\n"
"v_mfma_f32_16x16x32_bf16 v[236:239], v[52:55], a[4:7], 0\n"
"ds_read_b64_tr_b16 v[46:47], v17 offset:18048\n"
"s_waitcnt lgkmcnt(8)\n"
"s_waitcnt vmcnt(25)\n"
"v_mfma_f32_16x16x32_bf16 v[128:131], v[56:59], a[8:11], v[128:131]\n"
"ds_read_b64_tr_b16 v[48:49], v16 offset:14528\n"
"s_waitcnt vmcnt(24)\n"
"v_mfma_f32_16x16x32_bf16 v[132:135], v[56:59], a[12:15], v[132:135]\n"
"ds_read_b64_tr_b16 v[50:51], v16 offset:18112\n"
"v_mfma_f32_16x16x32_bf16 v[136:139], v[60:63], a[8:11], v[136:139]\n"
"ds_read_b64_tr_b16 v[52:53], v17 offset:14528\n"
"v_mfma_f32_16x16x32_bf16 v[140:143], v[60:63], a[12:15], v[140:143]\n"
"ds_read_b64_tr_b16 v[54:55], v17 offset:18112\n"
"s_waitcnt lgkmcnt(8)\n"
"v_mfma_f32_16x16x32_bf16 v[144:147], v[32:35], a[8:11], v[144:147]\n"
"ds_read_b64_tr_b16 v[56:57], v16 offset:14592\n"
"v_mfma_f32_16x16x32_bf16 v[148:151], v[32:35], a[12:15], v[148:151]\n"
"ds_read_b64_tr_b16 v[58:59], v16 offset:18176\n"
"v_mfma_f32_16x16x32_bf16 v[152:155], v[36:39], a[8:11], v[152:155]\n"
"ds_read_b64_tr_b16 v[60:61], v17 offset:14592\n"
"v_mfma_f32_16x16x32_bf16 v[156:159], v[36:39], a[12:15], v[156:159]\n"
"ds_read_b64_tr_b16 v[62:63], v17 offset:18176\n"
"s_waitcnt lgkmcnt(8)\n"
"v_mfma_f32_16x16x32_bf16 v[160:163], v[40:43], a[8:11], v[160:163]\n"
"ds_read_b64_tr_b16 v[32:33], v16 offset:14656\n"
"v_mfma_f32_16x16x32_bf16 v[164:167], v[40:43], a[12:15], v[164:167]\n"
"ds_read_b64_tr_b16 v[34:35], v16 offset:18240\n"
"v_mfma_f32_16x16x32_bf16 v[168:171], v[44:47], a[8:11], v[168:171]\n"
"ds_read_b64_tr_b16 v[36:37], v17 offset:14656\n"
"v_mfma_f32_16x16x32_bf16 v[172:175], v[44:47], a[12:15], v[172:175]\n"
"ds_read_b64_tr_b16 v[38:39], v17 offset:18240\n"
"s_waitcnt lgkmcnt(8)\n"
"v_mfma_f32_16x16x32_bf16 v[176:179], v[48:51], a[8:11], v[176:179]\n"
"ds_read_b64_tr_b16 v[40:41], v16 offset:14720\n"
"v_mfma_f32_16x16x32_bf16 v[180:183], v[48:51], a[12:15], v[180:183]\n"
"ds_read_b64_tr_b16 v[42:43], v16 offset:18304\n"
"v_mfma_f32_16x16x32_bf16 v[184:187], v[52:55], a[8:11], v[184:187]\n"
"ds_read_b64_tr_b16 v[44:45], v17 offset:14720\n"
"v_mfma_f32_16x16x32_bf16 v[188:191], v[52:55], a[12:15], v[188:191]\n"
"ds_read_b64_tr_b16 v[46:47], v17 offset:18304\n"
"s_waitcnt lgkmcnt(8)\n"
"v_mfma_f32_16x16x32_bf16 v[192:195], v[56:59], a[8:11], v[192:195]\n"
"s_waitcnt vmcnt(24)\n"
"s_barrier\n"
"v_add_u32 v16, 0x7000, v16\n"
"v_add_u32 v17, 0x7000, v17\n"
"ds_read_b64_tr_b16 v[48:49], v16\n"
"v_mfma_f32_16x16x32_bf16 v[196:199], v[56:59], a[12:15], v[196:199]\n"
"ds_read_b64_tr_b16 v[50:51], v16 offset:3584\n"
"v_mfma_f32_16x16x32_bf16 v[200:203], v[60:63], a[8:11], v[200:203]\n"
"ds_read_b64_tr_b16 v[52:53], v17\n"
"v_mfma_f32_16x16x32_bf16 v[204:207], v[60:63], a[12:15], v[204:207]\n"
"ds_read_b64_tr_b16 v[54:55], v17 offset:3584\n"
"buffer_load_dwordx2 v[28:29], off, %[rsW], %[afl] sc1\n"
"s_waitcnt lgkmcnt(8)\n"
"v_mfma_f32_16x16x32_bf16 v[208:211], v[32:35], a[8:11], v[208:211]\n"
"ds_read_b64_tr_b16 v[56:57], v16 offset:64\n"
"v_mfma_f32_16x16x32_bf16 v[212:215], v[32:35], a[12:15], v[212:215]\n"
"ds_read_b64_tr_b16 v[58:59], v16 offset:3648\n"
"s_add_u32 s64, %[sob], 0\n"
"v_mfma_f32_16x16x32_bf16 v[216:219], v[36:39], a[8:11], v[216:219]\n"
"ds_read_b64_tr_b16 v[60:61], v17 offset:64\n"
"v_mfma_f32_16x16x32_bf16 v[220:223], v[36:39], a[12:15], v[220:223]\n"
"ds_read_b64_tr_b16 v[62:63], v17 offset:3648\n"
"s_add_u32 m0, %[l0], 0x1c000\n"
"s_waitcnt lgkmcnt(8)\n"
"v_mfma_f32_16x16x32_bf16 v[224:227], v[40:43], a[8:11], v[224:227]\n"
"ds_read_b64_tr_b16 v[32:33], v16 offset:128\n"
"v_mfma_f32_16x16x32_bf16 v[228:231], v[40:43], a[12:15], v[228:231]\n"
"ds_read_b64_tr_b16 v[34:35], v16 offset:3712\n"
"buffer_load_dwordx4 %[vo0], %[rsO], s64 offen lds\n"
"v_mfma_f32_16x16x32_bf16 v[232:235], v[44:47], a[8:11], v[232:235]\n"
"ds_read_b64_tr_b16 v[36:37], v17 offset:128\n"
"v_mfma_f32_16x16x32_bf16 v[236:239], v[44:47], a[12:15], v[236:239]\n"
"ds_read_b64_tr_b16 v[38:39], v17 offset:3712\n"
"s_waitcnt lgkmcnt(8)\n"
"s_waitcnt vmcnt(18)\n"
"v_mfma_f32_16x16x32_bf16 v[128:131], v[48:51], a[16:19], v[128:131]\n"
"ds_read_b64_tr_b16 v[40:41], v16 offset:192\n"
"s_waitcnt vmcnt(17)\n"
"v_mfma_f32_16x16x32_bf16 v[132:135], v[48:51], a[20:23], v[132:135]\n"
"ds_read_b64_tr_b16 v[42:43], v16 offset:3776\n"
"buffer_load_dwordx4 %[vo1], %[rsO], s64 offen offset:1024 lds\n"
"v_mfma_f32_16x16x32_bf16 v[136:139], v[52:55], a[16:19], v[136:139]\n"
"ds_read_b64_tr_b16 v[44:45], v17 offset:192\n"
"v_mfma_f32_16x16x32_bf16 v[140:143], v[52:55], a[20:23], v[140:143]\n"
"ds_read_b64_tr_b16 v[46:47], v17 offset:3776\n"
"s_waitcnt lgkmcnt(8)\n"
"v_mfma_f32_16x16x32_bf16 v[144:147], v[56:59], a[16:19], v[144:147]\n"
"ds_read_b64_tr_b16 v[48:49], v16 offset:256\n"
"v_mfma_f32_16x16x32_bf16 v[148:151], v[56:59], a[20:23], v[148:151]\n"
"ds_read_b64_tr_b16 v[50:51], v16 offset:3840\n"
"buffer_load_dwordx4 %[vo2], %[rsO], s64 offen offset:2048 lds\n"
"v_mfma_f32_16x16x32_bf16 v[152:155], v[60:63], a[16:19], v[152:155]\n"
"ds_read_b64_tr_b16 v[52:53], v17 offset:256\n"
"v_mfma_f32_16x16x32_bf16 v[156:159], v[60:63], a[20:23], v[156:159]\n"
"ds_read_b64_tr_b16 v[54:55], v17 offset:3840\n"
"s_waitcnt lgkmcnt(8)\n"
"v_mfma_f32_16x16x32_bf16 v[160:163], v[32:35], a[16:19], v[160:163]\n"
"ds_read_b64_tr_b16 v[56:57], v16 offset:320\n"
"v_mfma_f32_16x16x32_bf16 v[164:167], v[32:35], a[20:23], v[164:167]\n"
"ds_read_b64_tr_b16 v[58:59], v16 offset:3904\n"
"buffer_load_dwordx4 %[vo3], %[rsO], s64 offen offset:3072 lds\n"
"v_mfma_f32_16x16x32_bf16 v[168:171], v[36:39], a[16:19], v[168:171]\n"
"ds_read_b64_tr_b16 v[60:61], v17 offset:320\n"
"v_mfma_f32_16x16x32_bf16 v[172:175], v[36:39], a[20:23], v[172:175]\n"
"ds_read_b64_tr_b16 v[62:63], v17 offset:3904\n"
"s_add_u32 m0, %[l0], 0x1d000\n"
"s_waitcnt lgkmcnt(8)\n"
"v_mfma_f32_16x16x32_bf16 v[176:179], v[40:43], a[16:19], v[176:179]\n"
"ds_read_b64_tr_b16 v[32:33], v16 offset:384\n"
"v_mfma_f32_16x16x32_bf16 v[180:183], v[40:43], a[20:23], v[180:183]\n"
"ds_read_b64_tr_b16 v[34:35], v16 offset:3968\n"
"buffer_load_dwordx4 %[vo4], %[rsO], s64 offen lds\n"
"v_mfma_f32_16x16x32_bf16 v[184:187], v[44:47], a[16:19], v[184:187]\n"
"ds_read_b64_tr_b16 v[36:37], v17 offset:384\n"
"v_mfma_f32_16x16x32_bf16 v[188:191], v[44:47], a[20:23], v[188:191]\n"
"ds_read_b64_tr_b16 v[38:39], v17 offset:3968\n"
"s_waitcnt lgkmcnt(8)\n"
"v_mfma_f32_16x16x32_bf16 v[192:195], v[48:51], a[16:19], v[192:195]\n"
"ds_read_b64_tr_b16 v[40:41], v16 offset:14336\n"
"v_mfma_f32_16x16x32_bf16 v[196:199], v[48:51], a[20:23], v[196:199]\n"
"ds_read_b64_tr_b16 v[42:43], v16 offset:17920\n"
"buffer_load_dwordx4 %[vo5], %[rsO], s64 offen offset:1024 lds\n"
"v_mfma_f32_16x16x32_bf16 v[200:203], v[52:55], a[16:19], v[200:203]\n"
"ds_read_b64_tr_b16 v[44:45], v17 offset:14336\n"
"v_mfma_f32_16x16x32_bf16 v[204:207], v[52:55], a[20:23], v[204:207]\n"
"ds_read_b64_tr_b16 v[46:47], v17 offset:17920\n"
"s_waitcnt lgkmcnt(8)\n"
"v_mfma_f32_16x16x32_bf16 v[208:211], v[56:59], a[16:19], v[208:211]\n"
"ds_read_b64_tr_b16 v[48:49], v16 offset:14400\n"
"v_mfma_f32_16x16x32_bf16 v[212:215], v[56:59], a[20:23], v[212:215]\n"
"ds_read_b64_tr_b16 v[50:51], v16 offset:17984\n"
"v_mfma_f32_16x16x32_bf16 v[216:219], v[60:63], a[16:19], v[216:219]\n"
"ds_read_b64_tr_b16 v[52:53], v17 offset:14400\n"
"v_mfma_f32_16x16x32_bf16 v[220:223], v[60:63], a[20:23], v[220:223]\n"
"ds_read_b64_tr_b16 v[54:55], v17 offset:17984\n"
"buffer_load_dwordx4 %[vo6], %[rsO], s64 offen offset:2048 lds\n"
"s_waitcnt lgkmcnt(8)\n"
"v_mfma_f32_16x16x32_bf16 v[224:227], v[32:35], a[16:19], v[224:227]\n"
"ds_read_b64_tr_b16 v[56:57], v16 offset:14464\n"
"v_mfma_f32_16x16x32_bf16 v[228:231], v[32:35], a[20:23], v[228:231]\n"
"ds_read_b64_tr_b16 v[58:59], v16 offset:18048\n"
"s_add_u32 s65, %[a1], 0x3000\n"
"v_mfma_f32_16x16x32_bf16 v[232:235], v[36:39], a[16:19], v[232:235]\n"
"ds_read_b64_tr_b16 v[60:61], v17 offset:14464\n"
"v_mfma_f32_16x16x32_bf16 v[236:239], v[36:39], a[20:23], v[236:239]\n"
"ds_read_b64_tr_b16 v[62:63], v17 offset:18048\n"
"buffer_load_dwordx4 a[48:51], %[va], %[rsW], s65 offen sc1\n"
"s_waitcnt lgkmcnt(8)\n"
"s_waitcnt vmcnt(23)\n"
"v_mfma_f32_16x16x32_bf16 v[128:131], v[40:43], a[24:27], v[128:131]\n"
"ds_read_b64_tr_b16 v[32:33], v16 offset:14528\n"
"s_waitcnt vmcnt(22)\n"
"v_mfma_f32_16x16x32_bf16 v[132:135], v[40:43], a[28:31], v[132:135]\n"
"ds_read_b64_tr_b16 v[34:35], v16 offset:18112\n"
"v_mfma_f32_16x16x32_bf16 v[136:139], v[44:47], a[24:27], v[136:139]\n"
"ds_read_b64_tr_b16 v[36:37], v17 offset:14528\n"
"v_mfma_f32_16x16x32_bf16 v[140:143], v[44:47], a[28:31], v[140:143]\n"
"ds_read_b64_tr_b16 v[38:39], v17 offset:18112\n"
"buffer_load_dwordx4 a[52:55], %[va], %[rsW], s65 offen offset:256 sc1\n"
"s_waitcnt lgkmcnt(8)\n"
"v_mfma_f32_16x16x32_bf16 v[144:147], v[48:51], a[24:27], v[144:147]\n"
"ds_read_b64_tr_b16 v[40:41], v16 offset:14592\n"
"v_mfma_f32_16x16x32_bf16 v[148:151], v[48:51], a[28:31], v[148:151]\n"
"ds_read_b64_tr_b16 v[42:43], v16 offset:18176\n"
"v_mfma_f32_16x16x32_bf16 v[152:155], v[52:55], a[24:27], v[152:155]\n"
"ds_read_b64_tr_b16 v[44:45], v17 offset:14592\n"
"v_mfma_f32_16x16x32_bf16 v[156:159], v[52:55], a[28:31], v[156:159]\n"
"ds_read_b64_tr_b16 v[46:47], v17 offset:18176\n"
"buffer_load_dwordx4 a[56:59], %[va], %[rsW], s65 offen offset:2048 sc1\n"
"s_waitcnt lgkmcnt(8)\n"
"v_mfma_f32_16x16x32_bf16 v[160:163], v[56:59], a[24:27], v[160:163]\n"
"ds_read_b64_tr_b16 v[48:49], v16 offset:14656\n"
"v_mfma_f32_16x16x32_bf16 v[164:167], v[56:59], a[28:31], v[164:167]\n"
"ds_read_b64_tr_b16 v[50:51], v16 offset:18240\n"
"v_mfma_f32_16x16x32_bf16 v[168:171], v[60:63], a[24:27], v[168:171]\n"
"ds_read_b64_tr_b16 v[52:53], v17 offset:14656\n"
"v_mfma_f32_16x16x32_bf16 v[172:175], v[60:63], a[28:31], v[172:175]\n"
"ds_read_b64_tr_b16 v[54:55], v17 offset:18240\n"
"buffer_load_dwordx4 a[60:63], %[va], %[rsW], s65 offen offset:2304 sc1\n"
"s_waitcnt lgkmcnt(8)\n"
"v_mfma_f32_16x16x32_bf16 v[176:179], v[32:35], a[24:27], v[176:179]\n"
"ds_read_b64_tr_b16 v[56:57], v16 offset:14720\n"
"v_mfma_f32_16x16x32_bf16 v[180:183], v[32:35], a[28:31], v[180:183]\n"
"ds_read_b64_tr_b16 v[58:59], v16 offset:18304\n"
"v_mfma_f32_16x16x32_bf16 v[184:187], v[36:39], a[24:27], v[184:187]\n"
"ds_read_b64_tr_b16 v[60:61], v17 offset:14720\n"
"v_mfma_f32_16x16x32_bf16 v[188:191], v[36:39], a[28:31], v[188:191]\n"
"ds_read_b64_tr_b16 v[62:63], v17 offset:18304\n"
"s_waitcnt lgkmcnt(8)\n"
"v_mfma_f32_16x16x32_bf16 v[192:195], v[40:43], a[24:27], v[192:195]\n"
"s_waitcnt vmcnt(25)\n"
"s_barrier\n"
"v_add_u32 v16, 0x7000, v16\n"
"v_add_u32 v17, 0x7000, v17\n"
"s_waitcnt vmcnt(11)\n"
"v_cmp_ne_u64 vcc, %[tag], v[28:29]\n"
"s_nop 0\n"
"s_cbranch_vccnz AFW%=\n"
"AFG%=:\n"
"ds_read_b64_tr_b16 v[32:33], v16\n"
"v_mfma_f32_16x16x32_bf16 v[196:199], v[40:43], a[28:31], v[196:199]\n"
"ds_read_b64_tr_b16 v[34:35], v16 offset:3584\n"
"s_add_u32 s64, %[sob], 0xe000\n"
"v_mfma_f32_16x16x32_bf16 v[200:203], v[44:47], a[24:27], v[200:203]\n"
"ds_read_b64_tr_b16 v[36:37], v17\n"
"v_mfma_f32_16x16x32_bf16 v[204:207], v[44:47], a[28:31], v[204:207]\n"
"ds_read_b64_tr_b16 v[38:39], v17 offset:3584\n"
"s_add_u32 m0, %[l0], 0\n"
"s_waitcnt lgkmcnt(8)\n"
"v_mfma_f32_16x16x32_bf16 v[208:211], v[48:51], a[24:27], v[208:211]\n"
"ds_read_b64_tr_b16 v[40:41], v16 offset:64\n"
"v_mfma_f32_16x16x32_bf16 v[212:215], v[48:51], a[28:31], v[212:215]\n"
"ds_read_b64_tr_b16 v[42:43], v16 offset:3648\n"
"buffer_load_dwordx4 %[vo0], %[rsO], s64 offen lds\n"
"v_mfma_f32_16x16x32_bf16 v[216:219], v[52:55], a[24:27], v[216:219]\n"
"ds_read_b64_tr_b16 v[44:45], v17 offset:64\n"
"v_mfma_f32_16x16x32_bf16 v[220:223], v[52:55], a[28:31], v[220:223]\n"
"ds_read_b64_tr_b16 v[46:47], v17 offset:3648\n"
"s_waitcnt lgkmcnt(8)\n"
"v_mfma_f32_16x16x32_bf16 v[224:227], v[56:59], a[24:27], v[224:227]\n"
"ds_read_b64_tr_b16 v[48:49], v16 offset:128\n"
"v_mfma_f32_16x16x32_bf16 v[228:231], v[56:59], a[28:31], v[228:231]\n"
"ds_read_b64_tr_b16 v[50:51], v16 offset:3712\n"
"buffer_load_dwordx4 %[vo1], %[rsO], s64 offen offset:1024 lds\n"
"v_mfma_f32_16x16x32_bf16 v[232:235], v[60:63], a[24:27], v[232:235]\n"
"ds_read_b64_tr_b16 v[52:53], v17 offset:128\n"
"v_mfma_f32_16x16x32_bf16 v[236:239], v[60:63], a[28:31], v[236:239]\n"
"ds_read_b64_tr_b16 v[54:55], v17 offset:3712\n"
"s_waitcnt lgkmcnt(8)\n"
"v_mfma_f32_16x16x32_bf16 v[128:131], v[32:35], a[32:35], v[128:131]\n"
"ds_read_b64_tr_b16 v[56:57], v16 offset:192\n"
"v_mfma_f32_16x16x32_bf16 v[132:135], v[32:35], a[36:39], v[132:135]\n"
"ds_read_b64_tr_b16 v[58:59], v16 offset:3776\n"
"buffer_load_dwordx4 %[vo2], %[rsO], s64 offen offset:2048 lds\n"
"v_mfma_f32_16x16x32_bf16 v[136:139], v[36:39], a[32:35], v[136:139]\n"
"ds_read_b64_tr_b16 v[60:61], v17 offset:192\n"
"v_mfma_f32_16x16x32_bf16 v[140:143], v[36:39], a[36:39], v[140:143]\n"
"ds_read_b64_tr_b16 v[62:63], v17 offset:3776\n"
"s_waitcnt lgkmcnt(8)\n"
"v_mfma_f32_16x16x32_bf16 v[144:147], v[40:43], a[32:35], v[144:147]\n"
"ds_read_b64_tr_b16 v[32:33], v16 offset:256\n"
"v_mfma_f32_16x16x32_bf16 v[148:151], v[40:43], a[36:39], v[148:151]\n"
"ds_read_b64_tr_b16 v[34:35], v16 offset:3840\n"
"v_mfma_f32_16x16x32_bf16 v[152:155], v[44:47], a[32:35], v[152:155]\n"
"ds_read_b64_tr_b16 v[36:37], v17 offset:256\n"
"v_mfma_f32_16x16x32_bf16 v[156:159], v[44:47], a[36:39], v[156:159]\n"
"ds_read_b64_tr_b16 v[38:39], v17 offset:3840\n"
"buffer_load_dwordx4 %[vo3], %[rsO], s64 offen offset:3072 lds\n"
"s_waitcnt lgkmcnt(8)\n"
"v_mfma_f32_16x16x32_bf16 v[160:163], v[48:51], a[32:35], v[160:163]\n"
"ds_read_b64_tr_b16 v[40:41], v16 offset:320\n"
"v_mfma_f32_16x16x32_bf16 v[164:167], v[48:51], a[36:39], v[164:167]\n"
"ds_read_b64_tr_b16 v[42:43], v16 offset:3904\n"
"s_add_u32 m0, %[l0], 0x1000\n"
"v_mfma_f32_16x16x32_bf16 v[168:171], v[52:55], a[32:35], v[168:171]\n"
"ds_read_b64_tr_b16 v[44:45], v17 offset:320\n"
"v_mfma_f32_16x16x32_bf16 v[172:175], v[52:55], a[36:39], v[172:175]\n"
"ds_read_b64_tr_b16 v[46:47], v17 offset:3904\n"
"buffer_load_dwordx4 %[vo4], %[rsO], s64 offen lds\n"
"s_waitcnt lgkmcnt(8)\n"
"v_mfma_f32_16x16x32_bf16 v[176:179], v[56:59], a[32:35], v[176:179]\n"
"ds_read_b64_tr_b16 v[48:49], v16 offset:384\n"
"v_mfma_f32_16x16x32_bf16 v[180:183], v[56:59], a[36:39], v[180:183]\n"
"ds_read_b64_tr_b16 v[50:51], v16 offset:3968\n"
"v_mfma_f32_16x16x32_bf16 v[184:187], v[60:63], a[32:35], v[184:187]\n"
"ds_read_b64_tr_b16 v[52:53], v17 offset:384\n"
"v_mfma_f32_16x16x32_bf16 v[188:191], v[60:63], a[36:39], v[188:191]\n"
"ds_read_b64_tr_b16 v[54:55], v17 offset:3968\n"
"buffer_load_dwordx4 %[vo5], %[rsO], s64 offen offset:1024 lds\n"
"s_waitcnt lgkmcnt(8)\n"
"v_mfma_f32_16x16x32_bf16 v[192:195], v[32:35], a[32:35], v[192:195]\n"
"ds_read_b64_tr_b16 v[56:57], v16 offset:14336\n"
"v_mfma_f32_16x16x32_bf16 v[196:199], v[32:35], a[36:39], v[196:199]\n"
"ds_read_b64_tr_b16 v[58:59], v16 offset:17920\n"
"v_mfma_f32_16x16x32_bf16 v[200:203], v[36:39], a[32:35], v[200:203]\n"
"ds_read_b64_tr_b16 v[60:61], v17 offset:14336\n"
"v_mfma_f32_16x16x32_bf16 v[204:207], v[36:39], a[36:39], v[204:207]\n"
"ds_read_b64_tr_b16 v[62:63], v17 offset:17920\n"
"s_waitcnt lgkmcnt(8)\n"
"v_mfma_f32_16x16x32_bf16 v[208:211], v[40:43], a[32:35], v[208:211]\n"
"ds_read_b64_tr_b16 v[32:33], v16 offset:14400\n"
"v_mfma_f32_16x16x32_bf16 v[212:215], v[40:43], a[36:39], v[212:215]\n"
"ds_read_b64_tr_b16 v[34:35], v16 offset:17984\n"
"buffer_load_dwordx4 %[vo6], %[rsO], s64 offen offset:2048 lds\n"
"v_mfma_f32_16x16x32_bf16 v[216:219], v[44:47], a[32:35], v[216:219]\n"
"ds_read_b64_tr_b16 v[36:37], v17 offset:14400\n"
"v_mfma_f32_16x16x32_bf16 v[220:223], v[44:47], a[36:39], v[220:223]\n"
"ds_read_b64_tr_b16 v[38:39], v17 offset:17984\n"
"s_add_u32 s65, %[a1b], 0\n"
"s_waitcnt lgkmcnt(8)\n"
"v_mfma_f32_16x16x32_bf16 v[224:227], v[48:51], a[32:35], v[224:227]\n"
"ds_read_b64_tr_b16 v[40:41], v16 offset:14464\n"
"v_mfma_f32_16x16x32_bf16 v[228:231], v[48:51], a[36:39], v[228:231]\n"
"ds_read_b64_tr_b16 v[42:43], v16 offset:18048\n"
"buffer_load_dwordx4 a[64:67], %[va], %[rsW], s65 offen sc1\n"
"v_mfma_f32_16x16x32_bf16 v[232:235], v[52:55], a[32:35], v[232:235]\n"
"ds_read_b64_tr_b16 v[44:45], v17 offset:14464\n"
"v_mfma_f32_16x16x32_bf16 v[236:239], v[52:55], a[36:39], v[236:239]\n"
"ds_read_b64_tr_b16 v[46:47], v17 offset:18048\n"
"s_waitcnt lgkmcnt(8)\n"
"v_mfma_f32_16x16x32_bf16 v[128:131], v[56:59], a[40:43], v[128:131]\n"
"ds_read_b64_tr_b16 v[48:49], v16 offset:14528\n"
"v_mfma_f32_16x16x32_bf16 v[132:135], v[56:59], a[44:47], v[132:135]\n"
"ds_read_b64_tr_b16 v[50:51], v16 offset:18112\n"
"v_mfma_f32_16x16x32_bf16 v[136:139], v[60:63], a[40:43], v[136:139]\n"
"ds_read_b64_tr_b16 v[52:53], v17 offset:14528\n"
"v_mfma_f32_16x16x32_bf16 v[140:143], v[60:63], a[44:47], v[140:143]\n"
"ds_read_b64_tr_b16 v[54:55], v17 offset:18112\n"
"buffer_load_dwordx4 a[68:71], %[va], %[rsW], s65 offen offset:256 sc1\n"
"s_waitcnt lgkmcnt(8)\n"
"v_mfma_f32_16x16x32_bf16 v[144:147], v[32:35], a[40:43], v[144:147]\n"
"ds_read_b64_tr_b16 v[56:57], v16 offset:14592\n"
"v_mfma_f32_16x16x32_bf16 v[148:151], v[32:35], a[44:47], v[148:151]\n"
"ds_read_b64_tr_b16 v[58:59], v16 offset:18176\n"
"v_mfma_f32_16x16x32_bf16 v[152:155], v[36:39], a[40:43], v[152:155]\n"
"ds_read_b64_tr_b16 v[60:61], v17 offset:14592\n"
"v_mfma_f32_16x16x32_bf16 v[156:159], v[36:39], a[44:47], v[156:159]\n"
"ds_read_b64_tr_b16 v[62:63], v17 offset:18176\n"
"buffer_load_dwordx4 a[72:75], %[va], %[rsW], s65 offen offset:2048 sc1\n"
"s_waitcnt lgkmcnt(8)\n"
"v_mfma_f32_16x16x32_bf16 v[160:163], v[40:43], a[40:43], v[160:163]\n"
"ds_read_b64_tr_b16 v[32:33], v16 offset:14656\n"
"v_mfma_f32_16x16x32_bf16 v[164:167], v[40:43], a[44:47], v[164:167]\n"
"ds_read_b64_tr_b16 v[34:35], v16 offset:18240\n"
"v_mfma_f32_16x16x32_bf16 v[168:171], v[44:47], a[40:43], v[168:171]\n"
"ds_read_b64_tr_b16 v[36:37], v17 offset:14656\n"
"v_mfma_f32_16x16x32_bf16 v[172:175], v[44:47], a[44:47], v[172:175]\n"
"ds_read_b64_tr_b16 v[38:39], v17 offset:18240\n"
"buffer_load_dwordx4 a[76:79], %[va], %[rsW], s65 offen offset:2304 sc1\n"
"s_waitcnt lgkmcnt(8)\n"
"v_mfma_f32_16x16x32_bf16 v[176:179], v[48:51], a[40:43], v[176:179]\n"
"ds_read_b64_tr_b16 v[40:41], v16 offset:14720\n"
"v_mfma_f32_16x16x32_bf16 v[180:183], v[48:51], a[44:47], v[180:183]\n"
"ds_read_b64_tr_b16 v[42:43], v16 offset:18304\n"
"v_mfma_f32_16x16x32_bf16 v[184:187], v[52:55], a[40:43], v[184:187]\n"
"ds_read_b64_tr_b16 v[44:45], v17 offset:14720\n"
"v_mfma_f32_16x16x32_bf16 v[188:191], v[52:55], a[44:47], v[188:191]\n"
"ds_read_b64_tr_b16 v[46:47], v17 offset:18304\n"
"s_waitcnt lgkmcnt(8)\n"
"v_mfma_f32_16x16x32_bf16 v[192:195], v[56:59], a[40:43], v[192:195]\n"
"s_waitcnt vmcnt(22)\n"
"s_barrier\n"
"v_add_u32 v16, 0x7000, v16\n"
"v_add_u32 v17, 0x7000, v17\n"
"ds_read_b64_tr_b16 v[48:49], v16\n"
"v_mfma_f32_16x16x32_bf16 v[196:199], v[56:59], a[44:47], v[196:199]\n"
"ds_read_b64_tr_b16 v[50:51], v16 offset:3584\n"
"s_add_u32 s64, %[sob], 0x1c000\n"
"v_mfma_f32_16x16x32_bf16 v[200:203], v[60:63], a[40:43], v[200:203]\n"
"ds_read_b64_tr_b16 v[52:53], v17\n"
"v_mfma_f32_16x16x32_bf16 v[204:207], v[60:63], a[44:47], v[204:207]\n"
"ds_read_b64_tr_b16 v[54:55], v17 offset:3584\n"
"s_add_u32 m0, %[l0], 0x7000\n"
"s_waitcnt lgkmcnt(8)\n"
"v_mfma_f32_16x16x32_bf16 v[208:211], v[32:35], a[40:43], v[208:211]\n"
"ds_read_b64_tr_b16 v[56:57], v16 offset:64\n"
"v_mfma_f32_16x16x32_bf16 v[212:215], v[32:35], a[44:47], v[212:215]\n"
"ds_read_b64_tr_b16 v[58:59], v16 offset:3648\n"
"buffer_load_dwordx4 %[vo0], %[rsO], s64 offen lds\n"
"v_mfma_f32_16x16x32_bf16 v[216:219], v[36:39], a[40:43], v[216:219]\n"
"ds_read_b64_tr_b16 v[60:61], v17 offset:64\n"
"v_mfma_f32_16x16x32_bf16 v[220:223], v[36:39], a[44:47], v[220:223]\n"
"ds_read_b64_tr_b16 v[62:63], v17 offset:3648\n"
"s_waitcnt lgkmcnt(8)\n"
"v_mfma_f32_16x16x32_bf16 v[224:227], v[40:43], a[40:43], v[224:227]\n"
"ds_read_b64_tr_b16 v[32:33], v16 offset:128\n"
"v_mfma_f32_16x16x32_bf16 v[228:231], v[40:43], a[44:47], v[228:231]\n"
"ds_read_b64_tr_b16 v[34:35], v16 offset:3712\n"
"buffer_load_dwordx4 %[vo1], %[rsO], s64 offen offset:1024 lds\n"
"v_mfma_f32_16x16x32_bf16 v[232:235], v[44:47], a[40:43], v[232:235]\n"
"ds_read_b64_tr_b16 v[36:37], v17 offset:128\n"
"v_mfma_f32_16x16x32_bf16 v[236:239], v[44:47], a[44:47], v[236:239]\n"
"ds_read_b64_tr_b16 v[38:39], v17 offset:3712\n"
"s_waitcnt lgkmcnt(8)\n"
"s_waitcnt vmcnt(16)\n"
"v_mfma_f32_16x16x32_bf16 v[128:131], v[48:51], a[48:51], v[128:131]\n"
"ds_read_b64_tr_b16 v[40:41], v16 offset:192\n"
"s_waitcnt vmcnt(15)\n"
"v_mfma_f32_16x16x32_bf16 v[132:135], v[48:51], a[52:55], v[132:135]\n"
"ds_read_b64_tr_b16 v[42:43], v16 offset:3776\n"
"buffer_load_dwordx4 %[vo2], %[rsO], s64 offen offset:2048 lds\n"
"v_mfma_f32_16x16x32_bf16 v[136:139], v[52:55], a[48:51], v[136:139]\n"
"ds_read_b64_tr_b16 v[44:45], v17 offset:192\n"
"v_mfma_f32_16x16x32_bf16 v[140:143], v[52:55], a[52:55], v[140:143]\n"
"ds_read_b64_tr_b16 v[46:47], v17 offset:3776\n"
"s_waitcnt lgkmcnt(8)\n"
"v_mfma_f32_16x16x32_bf16 v[144:147], v[56:59], a[48:51], v[144:147]\n"
"ds_read_b64_tr_b16 v[48:49], v16 offset:256\n"
"v_mfma_f32_16x16x32_bf16 v[148:151], v[56:59], a[52:55], v[148:151]\n"
"ds_read_b64_tr_b16 v[50:51], v16 offset:3840\n"
"v_mfma_f32_16x16x32_bf16 v[152:155], v[60:63], a[48:51], v[152:155]\n"
"ds_read_b64_tr_b16 v[52:53], v17 offset:256\n"
"v_mfma_f32_16x16x32_bf16 v[156:159], v[60:63], a[52:55], v[156:159]\n"
"ds_read_b64_tr_b16 v[54:55], v17 offset:3840\n"
"buffer_load_dwordx4 %[vo3], %[rsO], s64 offen offset:3072 lds\n"
"s_waitcnt lgkmcnt(8)\n"
"v_mfma_f32_16x16x32_bf16 v[160:163], v[32:35], a[48:51], v[160:163]\n"
"ds_read_b64_tr_b16 v[56:57], v16 offset:320\n"
"v_mfma_f32_16x16x32_bf16 v[164:167], v[32:35], a[52:55], v[164:167]\n"
"ds_read_b64_tr_b16 v[58:59], v16 offset:3904\n"
"s_add_u32 m0, %[l0], 0x8000\n"
"v_mfma_f32_16x16x32_bf16 v[168:171], v[36:39], a[48:51], v[168:171]\n"
"ds_read_b64_tr_b16 v[60:61], v17 offset:320\n"
"v_mfma_f32_16x16x32_bf16 v[172:175], v[36:39], a[52:55], v[172:175]\n"
"ds_read_b64_tr_b16 v[62:63], v17 offset:3904\n"
"buffer_load_dwordx4 %[vo4], %[rsO], s64 offen lds\n"
"s_waitcnt lgkmcnt(8)\n"
"v_mfma_f32_16x16x32_bf16 v[176:179], v[40:43], a[48:51], v[176:179]\n"
"ds_read_b64_tr_b16 v[32:33], v16 offset:384\n"
"v_mfma_f32_16x16x32_bf16 v[180:183], v[40:43], a[52:55], v[180:183]\n"
"ds_read_b64_tr_b16 v[34:35], v16 offset:3968\n"
"v_mfma_f32_16x16x32_bf16 v[184:187], v[44:47], a[48:51], v[184:187]\n"
"ds_read_b64_tr_b16 v[36:37], v17 offset:384\n"
"v_mfma_f32_16x16x32_bf16 v[188:191], v[44:47], a[52:55], v[188:191]\n"
"ds_read_b64_tr_b16 v[38:39], v17 offset:3968\n"
"buffer_load_dwordx4 %[vo5], %[rsO], s64 offen offset:1024 lds\n"
"s_waitcnt lgkmcnt(8)\n"
"v_mfma_f32_16x16x32_bf16 v[192:195], v[48:51], a[48:51], v[192:195]\n"
"ds_read_b64_tr_b16 v[40:41], v16 offset:14336\n"
"v_mfma_f32_16x16x32_bf16 v[196:199], v[48:51], a[52:55], v[196:199]\n"
"ds_read_b64_tr_b16 v[42:43], v16 offset:17920\n"
"v_mfma_f32_16x16x32_bf16 v[200:203], v[52:55], a[48:51], v[200:203]\n"
"ds_read_b64_tr_b16 v[44:45], v17 offset:14336\n"
"v_mfma_f32_16x16x32_bf16 v[204:207], v[52:55], a[52:55], v[204:207]\n"
"ds_read_b64_tr_b16 v[46:47], v17 offset:17920\n"
"s_waitcnt lgkmcnt(8)\n"
"v_mfma_f32_16x16x32_bf16 v[208:211], v[56:59], a[48:51], v[208:211]\n"
"ds_read_b64_tr_b16 v[48:49], v16 offset:14400\n"
"v_mfma_f32_16x16x32_bf16 v[212:215], v[56:59], a[52:55], v[212:215]\n"
"ds_read_b64_tr_b16 v[50:51], v16 offset:17984\n"
"buffer_load_dwordx4 %[vo6], %[rsO], s64 offen offset:2048 lds\n"
"v_mfma_f32_16x16x32_bf16 v[216:219], v[60:63], a[48:51], v[216:219]\n"
"ds_read_b64_tr_b16 v[52:53], v17 offset:14400\n"
"v_mfma_f32_16x16x32_bf16 v[220:223], v[60:63], a[52:55], v[220:223]\n"
"ds_read_b64_tr_b16 v[54:55], v17 offset:17984\n"
"s_add_u32 s65, %[a1b], 0x1000\n"
"s_waitcnt lgkmcnt(8)\n"
"v_mfma_f32_16x16x32_bf16 v[224:227], v[32:35], a[48:51], v[224:227]\n"
"ds_read_b64_tr_b16 v[56:57], v16 offset:14464\n"
"v_mfma_f32_16x16x32_bf16 v[228:231], v[32:35], a[52:55], v[228:231]\n"
"ds_read_b64_tr_b16 v[58:59], v16 offset:18048\n"
"buffer_load_dwordx4 a[80:83], %[va], %[rsW], s65 offen sc1\n"
"v_mfma_f32_16x16x32_bf16 v[232:235], v[36:39], a[48:51], v[232:235]\n"
"ds_read_b64_tr_b16 v[60:61], v17 offset:14464\n"
"v_mfma_f32_16x16x32_bf16 v[236:239], v[36:39], a[52:55], v[236:239]\n"
"ds_read_b64_tr_b16 v[62:63], v17 offset:18048\n"
"s_waitcnt lgkmcnt(8)\n"
"s_waitcnt vmcnt(20)\n"
"v_mfma_f32_16x16x32_bf16 v[128:131], v[40:43], a[56:59], v[128:131]\n"
"ds_read_b64_tr_b16 v[32:33], v16 offset:14528\n"
"s_waitcnt vmcnt(19)\n"
"v_mfma_f32_16x16x32_bf16 v[132:135], v[40:43], a[60:63], v[132:135]\n"
"ds_read_b64_tr_b16 v[34:35], v16 offset:18112\n"
"v_mfma_f32_16x16x32_bf16 v[136:139], v[44:47], a[56:59], v[136:139]\n"
"ds_read_b64_tr_b16 v[36:37], v17 offset:14528\n"
"v_mfma_f32_16x16x32_bf16 v[140:143], v[44:47], a[60:63], v[140:143]\n"
"ds_read_b64_tr_b16 v[38:39], v17 offset:18112\n"
"buffer_load_dwordx4 a[84:87], %[va], %[rsW], s65 offen offset:256 sc1\n"
"s_waitcnt lgkmcnt(8)\n"
"v_mfma_f32_16x16x32_bf16 v[144:147], v[48:51], a[56:59], v[144:147]\n"
"ds_read_b64_tr_b16 v[40:41], v16 offset:14592\n"
"v_mfma_f32_16x16x32_bf16 v[148:151], v[48:51], a[60:63], v[148:151]\n"
"ds_read_b64_tr_b16 v[42:43], v16 offset:18176\n"
"v_mfma_f32_16x16x32_bf16 v[152:155], v[52:55], a[56:59], v[152:155]\n"
"ds_read_b64_tr_b16 v[44:45], v17 offset:14592\n"
"v_mfma_f32_16x16x32_bf16 v[156:159], v[52:55], a[60:63], v[156:159]\n"
"ds_read_b64_tr_b16 v[46:47], v17 offset:18176\n"
"buffer_load_dwordx4 a[88:91], %[va], %[rsW], s65 offen offset:2048 sc1\n"
"s_waitcnt lgkmcnt(8)\n"
"v_mfma_f32_16x16x32_bf16 v[160:163], v[56:59], a[56:59], v[160:163]\n"
"ds_read_b64_tr_b16 v[48:49], v16 offset:14656\n"
"v_mfma_f32_16x16x32_bf16 v[164:167], v[56:59], a[60:63], v[164:167]\n"
"ds_read_b64_tr_b16 v[50:51], v16 offset:18240\n"
"v_mfma_f32_16x16x32_bf16 v[168:171], v[60:63], a[56:59], v[168:171]\n"
"ds_read_b64_tr_b16 v[52:53], v17 offset:14656\n"
"v_mfma_f32_16x16x32_bf16 v[172:175], v[60:63], a[60:63], v[172:175]\n"
"ds_read_b64_tr_b16 v[54:55], v17 offset:18240\n"
"buffer_load_dwordx4 a[92:95], %[va], %[rsW], s65 offen offset:2304 sc1\n"
"s_waitcnt lgkmcnt(8)\n"
"v_mfma_f32_16x16x32_bf16 v[176:179], v[32:35], a[56:59], v[176:179]\n"
"ds_read_b64_tr_b16 v[56:57], v16 offset:14720\n"
"v_mfma_f32_16x16x32_bf16 v[180:183], v[32:35], a[60:63], v[180:183]\n"
"ds_read_b64_tr_b16 v[58:59], v16 offset:18304\n"
"v_mfma_f32_16x16x32_bf16 v[184:187], v[36:39], a[56:59], v[184:187]\n"
"ds_read_b64_tr_b16 v[60:61], v17 offset:14720\n"
"v_mfma_f32_16x16x32_bf16 v[188:191], v[36:39], a[60:63], v[188:191]\n"
"ds_read_b64_tr_b16 v[62:63], v17 offset:18304\n"
"s_waitcnt lgkmcnt(8)\n"
"v_mfma_f32_16x16x32_bf16 v[192:195], v[40:43], a[56:59], v[192:195]\n"
"s_waitcnt vmcnt(22)\n"
"s_barrier\n"
"v_add_u32 v16, 0x7000, v16\n"
"v_add_u32 v17, 0x7000, v17\n"
"ds_read_b64_tr_b16 v[32:33], v16\n"
"v_mfma_f32_16x16x32_bf16 v[196:199], v[40:43], a[60:63], v[196:199]\n"
"ds_read_b64_tr_b16 v[34:35], v16 offset:3584\n"
"s_add_u32 s64, %[sob], 0x2a000\n"
"v_mfma_f32_16x16x32_bf16 v[200:203], v[44:47], a[56:59], v[200:203]\n"
"ds_read_b64_tr_b16 v[36:37], v17\n"
"v_mfma_f32_16x16x32_bf16 v[204:207], v[44:47], a[60:63], v[204:207]\n"
"ds_read_b64_tr_b16 v[38:39], v17 offset:3584\n"
"s_add_u32 m0, %[l0], 0xe000\n"
"s_waitcnt lgkmcnt(8)\n"
"v_mfma_f32_16x16x32_bf16 v[208:211], v[48:51], a[56:59], v[208:211]\n"
"ds_read_b64_tr_b16 v[40:41], v16 offset:64\n"
"v_mfma_f32_16x16x32_bf16 v[212:215], v[48:51], a[60:63], v[212:215]\n"
"ds_read_b64_tr_b16 v[42:43], v16 offset:3648\n"
"buffer_load_dwordx4 %[vo0], %[rsO], s64 offen lds\n"
"v_mfma_f32_16x16x32_bf16 v[216:219], v[52:55], a[56:59], v[216:219]\n"
"ds_read_b64_tr_b16 v[44:45], v17 offset:64\n"
"v_mfma_f32_16x16x32_bf16 v[220:223], v[52:55], a[60:63], v[220:223]\n"
"ds_read_b64_tr_b16 v[46:47], v17 offset:3648\n"
"s_waitcnt lgkmcnt(8)\n"
"v_mfma_f32_16x16x32_bf16 v[224:227], v[56:59], a[56:59], v[224:227]\n"
"ds_read_b64_tr_b16 v[48:49], v16 offset:128\n"
"v_mfma_f32_16x16x32_bf16 v[228:231], v[56:59], a[60:63], v[228:231]\n"
"ds_read_b64_tr_b16 v[50:51], v16 offset:3712\n"
"buffer_load_dwordx4 %[vo1], %[rsO], s64 offen offset:1024 lds\n"
"v_mfma_f32_16x16x32_bf16 v[232:235], v[60:63], a[56:59], v[232:235]\n"
"ds_read_b64_tr_b16 v[52:53], v17 offset:128\n"
"v_mfma_f32_16x16x32_bf16 v[236:239], v[60:63], a[60:63], v[236:239]\n"
"ds_read_b64_tr_b16 v[54:55], v17 offset:3712\n"
"s_waitcnt lgkmcnt(8)\n"
"s_waitcnt vmcnt(16)\n"
"v_mfma_f32_16x16x32_bf16 v[128:131], v[32:35], a[64:67], v[128:131]\n"
"ds_read_b64_tr_b16 v[56:57], v16 offset:192\n"
"s_waitcnt vmcnt(15)\n"
"v_mfma_f32_16x16x32_bf16 v[132:135], v[32:35], a[68:71], v[132:135]\n"
"ds_read_b64_tr_b16 v[58:59], v16 offset:3776\n"
"buffer_load_dwordx4 %[vo2], %[rsO], s64 offen offset:2048 lds\n"
"v_mfma_f32_16x16x32_bf16 v[136:139], v[36:39], a[64:67], v[136:139]\n"
"ds_read_b64_tr_b16 v[60:61], v17 offset:192\n"
"v_mfma_f32_16x16x32_bf16 v[140:143], v[36:39], a[68:71], v[140:143]\n"
"ds_read_b64_tr_b16 v[62:63], v17 offset:3776\n"
"s_waitcnt lgkmcnt(8)\n"
"v_mfma_f32_16x16x32_bf16 v[144:147], v[40:43], a[64:67], v[144:147]\n"
"ds_read_b64_tr_b16 v[32:33], v16 offset:256\n"
"v_mfma_f32_16x16x32_bf16 v[148:151], v[40:43], a[68:71], v[148:151]\n"
"ds_read_b64_tr_b16 v[34:35], v16 offset:3840\n"
"v_mfma_f32_16x16x32_bf16 v[152:155], v[44:47], a[64:67], v[152:155]\n"
"ds_read_b64_tr_b16 v[36:37], v17 offset:256\n"
"v_mfma_f32_16x16x32_bf16 v[156:159], v[44:47], a[68:71], v[156:159]\n"
"ds_read_b64_tr_b16 v[38:39], v17 offset:3840\n"
"buffer_load_dwordx4 %[vo3], %[rsO], s64 offen offset:3072 lds\n"
"s_waitcnt lgkmcnt(8)\n"
"v_mfma_f32_16x16x32_bf16 v[160:163], v[48:51], a[64:67], v[160:163]\n"
"ds_read_b64_tr_b16 v[40:41], v16 offset:320\n"
"v_mfma_f32_16x16x32_bf16 v[164:167], v[48:51], a[68:71], v[164:167]\n"
"ds_read_b64_tr_b16 v[42:43], v16 offset:3904\n"
"s_add_u32 m0, %[l0], 0xf000\n"
"v_mfma_f32_16x16x32_bf16 v[168:171], v[52:55], a[64:67], v[168:171]\n"
"ds_read_b64_tr_b16 v[44:45], v17 offset:320\n"
"v_mfma_f32_16x16x32_bf16 v[172:175], v[52:55], a[68:71], v[172:175]\n"
"ds_read_b64_tr_b16 v[46:47], v17 offset:3904\n"
"buffer_load_dwordx4 %[vo4], %[rsO], s64 offen lds\n"
"s_waitcnt lgkmcnt(8)\n"
"v_mfma_f32_16x16x32_bf16 v[176:179], v[56:59], a[64:67], v[176:179]\n"
"ds_read_b64_tr_b16 v[48:49], v16 offset:384\n"
"v_mfma_f32_16x16x32_bf16 v[180:183], v[56:59], a[68:71], v[180:183]\n"
"ds_read_b64_tr_b16 v[50:51], v16 offset:3968\n"
"v_mfma_f32_16x16x32_bf16 v[184:187], v[60:63], a[64:67], v[184:187]\n"
"ds_read_b64_tr_b16 v[52:53], v17 offset:384\n"
"v_mfma_f32_16x16x32_bf16 v[188:191], v[60:63], a[68:71], v[188:191]\n"
"ds_read_b64_tr_b16 v[54:55], v17 offset:3968\n"
"buffer_load_dwordx4 %[vo5], %[rsO], s64 offen offset:1024 lds\n"
"s_waitcnt lgkmcnt(8)\n"
"v_mfma_f32_16x16x32_bf16 v[192:195], v[32:35], a[64:67], v[192:195]\n"
"ds_read_b64_tr_b16 v[56:57], v16 offset:14336\n"
"v_mfma_f32_16x16x32_bf16 v[196:199], v[32:35], a[68:71], v[196:199]\n"
"ds_read_b64_tr_b16 v[58:59], v16 offset:17920\n"
"v_mfma_f32_16x16x32_bf16 v[200:203], v[36:39], a[64:67], v[200:203]\n"
"ds_read_b64_tr_b16 v[60:61], v17 offset:14336\n"
"v_mfma_f32_16x16x32_bf16 v[204:207], v[36:39], a[68:71], v[204:207]\n"
"ds_read_b64_tr_b16 v[62:63], v17 offset:17920\n"
"s_waitcnt lgkmcnt(8)\n"
"v_mfma_f32_16x16x32_bf16 v[208:211], v[40:43], a[64:67], v[208:211]\n"
"ds_read_b64_tr_b16 v[32:33], v16 offset:14400\n"
"v_mfma_f32_16x16x32_bf16 v[212:215], v[40:43], a[68:71], v[212:215]\n"
"ds_read_b64_tr_b16 v[34:35], v16 offset:17984\n"
"buffer_load_dwordx4 %[vo6], %[rsO], s64 offen offset:2048 lds\n"
"v_mfma_f32_16x16x32_bf16 v[216:219], v[44:47], a[64:67], v[216:219]\n"
"ds_read_b64_tr_b16 v[36:37], v17 offset:14400\n"
"v_mfma_f32_16x16x32_bf16 v[220:223], v[44:47], a[68:71], v[220:223]\n"
"ds_read_b64_tr_b16 v[38:39], v17 offset:17984\n"
"s_add_u32 s65, %[a1b], 0x2000\n"
"s_waitcnt lgkmcnt(8)\n"
"v_mfma_f32_16x16x32_bf16 v[224:227], v[48:51], a[64:67], v[224:227]\n"
"ds_read_b64_tr_b16 v[40:41], v16 offset:14464\n"
"v_mfma_f32_16x16x32_bf16 v[228:231], v[48:51], a[68:71], v[228:231]\n"
"ds_read_b64_tr_b16 v[42:43], v16 offset:18048\n"
"buffer_load_dwordx4 a[96:99], %[va], %[rsW], s65 offen sc1\n"
"v_mfma_f32_16x16x32_bf16 v[232:235], v[52:55], a[64:67], v[232:235]\n"
"ds_read_b64_tr_b16 v[44:45], v17 offset:14464\n"
"v_mfma_f32_16x16x32_bf16 v[236:239], v[52:55], a[68:71], v[236:239]\n"
"ds_read_b64_tr_b16 v[46:47], v17 offset:18048\n"
"s_waitcnt lgkmcnt(8)\n"
"s_waitcnt vmcnt(20)\n"
"v_mfma_f32_16x16x32_bf16 v[128:131], v[56:59], a[72:75], v[128:131]\n"
"ds_read_b64_tr_b16 v[48:49], v16 offset:14528\n"
"s_waitcnt vmcnt(19)\n"
"v_mfma_f32_16x16x32_bf16 v[132:135], v[56:59], a[76:79], v[132:135]\n"
"ds_read_b64_tr_b16 v[50:51], v16 offset:18112\n"
"v_mfma_f32_16x16x32_bf16 v[136:139], v[60:63], a[72:75], v[136:139]\n"
"ds_read_b64_tr_b16 v[52:53], v17 offset:14528\n"
"v_mfma_f32_16x16x32_bf16 v[140:143], v[60:63], a[76:79], v[140:143]\n"
"ds_read_b64_tr_b16 v[54:55], v17 offset:18112\n"
"buffer_load_dwordx4 a[100:103], %[va], %[rsW], s65 offen offset:256 sc1\n"
"s_waitcnt lgkmcnt(8)\n"
"v_mfma_f32_16x16x32_bf16 v[144:147], v[32:35], a[72:75], v[144:147]\n"
"ds_read_b64_tr_b16 v[56:57], v16 offset:14592\n"
"v_mfma_f32_16x16x32_bf16 v[148:151], v[32:35], a[76:79], v[148:151]\n"
"ds_read_b64_tr_b16 v[58:59], v16 offset:18176\n"
"v_mfma_f32_16x16x32_bf16 v[152:155], v[36:39], a[72:75], v[152:155]\n"
"ds_read_b64_tr_b16 v[60:61], v17 offset:14592\n"
"v_mfma_f32_16x16x32_bf16 v[156:159], v[36:39], a[76:79], v[156:159]\n"
"ds_read_b64_tr_b16 v[62:63], v17 offset:18176\n"
"buffer_load_dwordx4 a[104:107], %[va], %[rsW], s65 offen offset:2048 sc1\n"
"s_waitcnt lgkmcnt(8)\n"
"v_mfma_f32_16x16x32_bf16 v[160:163], v[40:43], a[72:75], v[160:163]\n"
"ds_read_b64_tr_b16 v[32:33], v16 offset:14656\n"
"v_mfma_f32_16x16x32_bf16 v[164:167], v[40:43], a[76:79], v[164:167]\n"
"ds_read_b64_tr_b16 v[34:35], v16 offset:18240\n"
"v_mfma_f32_16x16x32_bf16 v[168:171], v[44:47], a[72:75], v[168:171]\n"
"ds_read_b64_tr_b16 v[36:37], v17 offset:14656\n"
"v_mfma_f32_16x16x32_bf16 v[172:175], v[44:47], a[76:79], v[172:175]\n"
"ds_read_b64_tr_b16 v[38:39], v17 offset:18240\n"
"buffer_load_dwordx4 a[108:111], %[va], %[rsW], s65 offen offset:2304 sc1\n"
"s_waitcnt lgkmcnt(8)\n"
"v_mfma_f32_16x16x32_bf16 v[176:179], v[48:51], a[72:75], v[176:179]\n"
"ds_read_b64_tr_b16 v[40:41], v16 offset:14720\n"
"v_mfma_f32_16x16x32_bf16 v[180:183], v[48:51], a[76:79], v[180:183]\n"
"ds_read_b64_tr_b16 v[42:43], v16 offset:18304\n"
"v_mfma_f32_16x16x32_bf16 v[184:187], v[52:55], a[72:75], v[184:187]\n"
"ds_read_b64_tr_b16 v[44:45], v17 offset:14720\n"
"v_mfma_f32_16x16x32_bf16 v[188:191], v[52:55], a[76:79], v[188:191]\n"
"ds_read_b64_tr_b16 v[46:47], v17 offset:18304\n"
"s_waitcnt lgkmcnt(8)\n"
"v_mfma_f32_16x16x32_bf16 v[192:195], v[56:59], a[72:75], v[192:195]\n"
"s_waitcnt vmcnt(22)\n"
"s_barrier\n"
"v_add_u32 v16, 0xfffe4000, v16\n"
"v_add_u32 v17, 0xfffe4000, v17\n"
"ds_read_b64_tr_b16 v[48:49], v16\n"
"v_mfma_f32_16x16x32_bf16 v[196:199], v[56:59], a[76:79], v[196:199]\n"
"ds_read_b64_tr_b16 v[50:51], v16 offset:3584\n"
"s_add_u32 s64, %[s2], 0\n"
"v_mfma_f32_16x16x32_bf16 v[200:203], v[60:63], a[72:75], v[200:203]\n"
"ds_read_b64_tr_b16 v[52:53], v17\n"
"v_mfma_f32_16x16x32_bf16 v[204:207], v[60:63], a[76:79], v[204:207]\n"
"ds_read_b64_tr_b16 v[54:55], v17 offset:3584\n"
"s_add_u32 m0, %[l0], 0x15000\n"
"s_waitcnt lgkmcnt(8)\n"
"v_mfma_f32_16x16x32_bf16 v[208:211], v[32:35], a[72:75], v[208:211]\n"
"ds_read_b64_tr_b16 v[56:57], v16 offset:64\n"
"v_mfma_f32_16x16x32_bf16 v[212:215], v[32:35], a[76:79], v[212:215]\n"
"ds_read_b64_tr_b16 v[58:59], v16 offset:3648\n"
"buffer_load_dwordx4 %[vo0], %[rs2], s64 offen lds\n"
"v_mfma_f32_16x16x32_bf16 v[216:219], v[36:39], a[72:75], v[216:219]\n"
"ds_read_b64_tr_b16 v[60:61], v17 offset:64\n"
"v_mfma_f32_16x16x32_bf16 v[220:223], v[36:39], a[76:79], v[220:223]\n"
"ds_read_b64_tr_b16 v[62:63], v17 offset:3648\n"
"s_waitcnt lgkmcnt(8)\n"
"v_mfma_f32_16x16x32_bf16 v[224:227], v[40:43], a[72:75], v[224:227]\n"
"ds_read_b64_tr_b16 v[32:33], v16 offset:128\n"
"v_mfma_f32_16x16x32_bf16 v[228:231], v[40:43], a[76:79], v[228:231]\n"
"ds_read_b64_tr_b16 v[34:35], v16 offset:3712\n"
"buffer_load_dwordx4 %[vo1], %[rs2], s64 offen offset:1024 lds\n"
"v_mfma_f32_16x16x32_bf16 v[232:235], v[44:47], a[72:75], v[232:235]\n"
"ds_read_b64_tr_b16 v[36:37], v17 offset:128\n"
"v_mfma_f32_16x16x32_bf16 v[236:239], v[44:47], a[76:79], v[236:239]\n"
"ds_read_b64_tr_b16 v[38:39], v17 offset:3712\n"
"s_waitcnt lgkmcnt(8)\n"
"s_waitcnt vmcnt(16)\n"
"v_mfma_f32_16x16x32_bf16 v[128:131], v[48:51], a[80:83], v[128:131]\n"
"ds_read_b64_tr_b16 v[40:41], v16 offset:192\n"
"s_waitcnt vmcnt(15)\n"
"v_mfma_f32_16x16x32_bf16 v[132:135], v[48:51], a[84:87], v[132:135]\n"
"ds_read_b64_tr_b16 v[42:43], v16 offset:3776\n"
"buffer_load_dwordx4 %[vo2], %[rs2], s64 offen offset:2048 lds\n"
"v_mfma_f32_16x16x32_bf16 v[136:139], v[52:55], a[80:83], v[136:139]\n"
"ds_read_b64_tr_b16 v[44:45], v17 offset:192\n"
"v_mfma_f32_16x16x32_bf16 v[140:143], v[52:55], a[84:87], v[140:143]\n"
"ds_read_b64_tr_b16 v[46:47], v17 offset:3776\n"
"s_waitcnt lgkmcnt(8)\n"
"v_mfma_f32_16x16x32_bf16 v[144:147], v[56:59], a[80:83], v[144:147]\n"
"ds_read_b64_tr_b16 v[48:49], v16 offset:256\n"
"v_mfma_f32_16x16x32_bf16 v[148:151], v[56:59], a[84:87], v[148:151]\n"
"ds_read_b64_tr_b16 v[50:51], v16 offset:3840\n"
"v_mfma_f32_16x16x32_bf16 v[152:155], v[60:63], a[80:83], v[152:155]\n"
"ds_read_b64_tr_b16 v[52:53], v17 offset:256\n"
"v_mfma_f32_16x16x32_bf16 v[156:159], v[60:63], a[84:87], v[156:159]\n"
"ds_read_b64_tr_b16 v[54:55], v17 offset:3840\n"
"buffer_load_dwordx4 %[vo3], %[rs2], s64 offen offset:3072 lds\n"
"s_waitcnt lgkmcnt(8)\n"
"v_mfma_f32_16x16x32_bf16 v[160:163], v[32:35], a[80:83], v[160:163]\n"
"ds_read_b64_tr_b16 v[56:57], v16 offset:320\n"
"v_mfma_f32_16x16x32_bf16 v[164:167], v[32:35], a[84:87], v[164:167]\n"
"ds_read_b64_tr_b16 v[58:59], v16 offset:3904\n"
"s_add_u32 m0, %[l0], 0x16000\n"
"v_mfma_f32_16x16x32_bf16 v[168:171], v[36:39], a[80:83], v[168:171]\n"
"ds_read_b64_tr_b16 v[60:61], v17 offset:320\n"
"v_mfma_f32_16x16x32_bf16 v[172:175], v[36:39], a[84:87], v[172:175]\n"
"ds_read_b64_tr_b16 v[62:63], v17 offset:3904\n"
"buffer_load_dwordx4 %[vo4], %[rs2], s64 offen lds\n"
"s_waitcnt lgkmcnt(8)\n"
"v_mfma_f32_16x16x32_bf16 v[176:179], v[40:43], a[80:83], v[176:179]\n"
"ds_read_b64_tr_b16 v[32:33], v16 offset:384\n"
"v_mfma_f32_16x16x32_bf16 v[180:183], v[40:43], a[84:87], v[180:183]\n"
"ds_read_b64_tr_b16 v[34:35], v16 offset:3968\n"
"v_mfma_f32_16x16x32_bf16 v[184:187], v[44:47], a[80:83], v[184:187]\n"
"ds_read_b64_tr_b16 v[36:37], v17 offset:384\n"
"v_mfma_f32_16x16x32_bf16 v[188:191], v[44:47], a[84:87], v[188:191]\n"
"ds_read_b64_tr_b16 v[38:39], v17 offset:3968\n"
"buffer_load_dwordx4 %[vo5], %[rs2], s64 offen offset:1024 lds\n"
"s_waitcnt lgkmcnt(8)\n"
"v_mfma_f32_16x16x32_bf16 v[192:195], v[48:51], a[80:83], v[192:195]\n"
"ds_read_b64_tr_b16 v[40:41], v16 offset:14336\n"
"v_mfma_f32_16x16x32_bf16 v[196:199], v[48:51], a[84:87], v[196:199]\n"
"ds_read_b64_tr_b16 v[42:43], v16 offset:17920\n"
"v_mfma_f32_16x16x32_bf16 v[200:203], v[52:55], a[80:83], v[200:203]\n"
"ds_read_b64_tr_b16 v[44:45], v17 offset:14336\n"
"v_mfma_f32_16x16x32_bf16 v[204:207], v[52:55], a[84:87], v[204:207]\n"
"ds_read_b64_tr_b16 v[46:47], v17 offset:17920\n"
"s_waitcnt lgkmcnt(8)\n"
"v_mfma_f32_16x16x32_bf16 v[208:211], v[56:59], a[80:83], v[208:211]\n"
"ds_read_b64_tr_b16 v[48:49], v16 offset:14400\n"
"v_mfma_f32_16x16x32_bf16 v[212:215], v[56:59], a[84:87], v[212:215]\n"
"ds_read_b64_tr_b16 v[50:51], v16 offset:17984\n"
"buffer_load_dwordx4 %[vo6], %[rs2], s64 offen offset:2048 lds\n"
"v_mfma_f32_16x16x32_bf16 v[216:219], v[60:63], a[80:83], v[216:219]\n"
"ds_read_b64_tr_b16 v[52:53], v17 offset:14400\n"
"v_mfma_f32_16x16x32_bf16 v[220:223], v[60:63], a[84:87], v[220:223]\n"
"ds_read_b64_tr_b16 v[54:55], v17 offset:17984\n"
"s_add_u32 s65, %[a1b], 0x3000\n"
"s_waitcnt lgkmcnt(8)\n"
"v_mfma_f32_16x16x32_bf16 v[224:227], v[32:35], a[80:83], v[224:227]\n"
"ds_read_b64_tr_b16 v[56:57], v16 offset:14464\n"
"v_mfma_f32_16x16x32_bf16 v[228:231], v[32:35], a[84:87], v[228:231]\n"
"ds_read_b64_tr_b16 v[58:59], v16 offset:18048\n"
"buffer_load_dwordx4 a[112:115], %[va], %[rsW], s65 offen sc1\n"
"v_mfma_f32_16x16x32_bf16 v[232:235], v[36:39], a[80:83], v[232:235]\n"
"ds_read_b64_tr_b16 v[60:61], v17 offset:14464\n"
"v_mfma_f32_16x16x32_bf16 v[236:239], v[36:39], a[84:87], v[236:239]\n"
"ds_read_b64_tr_b16 v[62:63], v17 offset:18048\n"
"s_waitcnt lgkmcnt(8)\n"
"s_waitcnt vmcnt(20)\n"
"v_mfma_f32_16x16x32_bf16 v[128:131], v[40:43], a[88:91], v[128:131]\n"
"ds_read_b64_tr_b16 v[32:33], v16 offset:14528\n"
"s_waitcnt vmcnt(19)\n"
"v_mfma_f32_16x16x32_bf16 v[132:135], v[40:43], a[92:95], v[132:135]\n"
"ds_read_b64_tr_b16 v[34:35], v16 offset:18112\n"
"v_mfma_f32_16x16x32_bf16 v[136:139], v[44:47], a[88:91], v[136:139]\n"
"ds_read_b64_tr_b16 v[36:37], v17 offset:14528\n"
"v_mfma_f32_16x16x32_bf16 v[140:143], v[44:47], a[92:95], v[140:143]\n"
"ds_read_b64_tr_b16 v[38:39], v17 offset:18112\n"
"buffer_load_dwordx4 a[116:119], %[va], %[rsW], s65 offen offset:256 sc1\n"
"s_waitcnt lgkmcnt(8)\n"
"v_mfma_f32_16x16x32_bf16 v[144:147], v[48:51], a[88:91], v[144:147]\n"
"ds_read_b64_tr_b16 v[40:41], v16 offset:14592\n"
"v_mfma_f32_16x16x32_bf16 v[148:151], v[48:51], a[92:95], v[148:151]\n"
"ds_read_b64_tr_b16 v[42:43], v16 offset:18176\n"
"v_mfma_f32_16x16x32_bf16 v[152:155], v[52:55], a[88:91], v[152:155]\n"
"ds_read_b64_tr_b16 v[44:45], v17 offset:14592\n"
"v_mfma_f32_16x16x32_bf16 v[156:159], v[52:55], a[92:95], v[156:159]\n"
"ds_read_b64_tr_b16 v[46:47], v17 offset:18176\n"
"buffer_load_dwordx4 a[120:123], %[va], %[rsW], s65 offen offset:2048 sc1\n"
"s_waitcnt lgkmcnt(8)\n"
"v_mfma_f32_16x16x32_bf16 v[160:163], v[56:59], a[88:91], v[160:163]\n"
"ds_read_b64_tr_b16 v[48:49], v16 offset:14656\n"
"v_mfma_f32_16x16x32_bf16 v[164:167], v[56:59], a[92:95], v[164:167]\n"
"ds_read_b64_tr_b16 v[50:51], v16 offset:18240\n"
"v_mfma_f32_16x16x32_bf16 v[168:171], v[60:63], a[88:91], v[168:171]\n"
"ds_read_b64_tr_b16 v[52:53], v17 offset:14656\n"
"v_mfma_f32_16x16x32_bf16 v[172:175], v[60:63], a[92:95], v[172:175]\n"
"ds_read_b64_tr_b16 v[54:55], v17 offset:18240\n"
"buffer_load_dwordx4 a[124:127], %[va], %[rsW], s65 offen offset:2304 sc1\n"
"s_waitcnt lgkmcnt(8)\n"
"v_mfma_f32_16x16x32_bf16 v[176:179], v[32:35], a[88:91], v[176:179]\n"
"ds_read_b64_tr_b16 v[56:57], v16 offset:14720\n"
"v_mfma_f32_16x16x32_bf16 v[180:183], v[32:35], a[92:95], v[180:183]\n"
"ds_read_b64_tr_b16 v[58:59], v16 offset:18304\n"
"v_mfma_f32_16x16x32_bf16 v[184:187], v[36:39], a[88:91], v[184:187]\n"
"ds_read_b64_tr_b16 v[60:61], v17 offset:14720\n"
"v_mfma_f32_16x16x32_bf16 v[188:191], v[36:39], a[92:95], v[188:191]\n"
"ds_read_b64_tr_b16 v[62:63], v17 offset:18304\n"
"s_waitcnt lgkmcnt(8)\n"
"v_mfma_f32_16x16x32_bf16 v[192:195], v[40:43], a[88:91], v[192:195]\n"
"s_waitcnt vmcnt(22)\n"
"s_barrier\n"
"v_add_u32 v16, 0x7000, v16\n"
"v_add_u32 v17, 0x7000, v17\n"
"ds_read_b64_tr_b16 v[32:33], v16\n"
"v_mfma_f32_16x16x32_bf16 v[196:199], v[40:43], a[92:95], v[196:199]\n"
"ds_read_b64_tr_b16 v[34:35], v16 offset:3584\n"
"s_add_u32 s64, %[s2], 0xe000\n"
"v_mfma_f32_16x16x32_bf16 v[200:203], v[44:47], a[88:91], v[200:203]\n"
"ds_read_b64_tr_b16 v[36:37], v17\n"
"v_mfma_f32_16x16x32_bf16 v[204:207], v[44:47], a[92:95], v[204:207]\n"
"ds_read_b64_tr_b16 v[38:39], v17 offset:3584\n"
"s_add_u32 m0, %[l0], 0x1c000\n"
"s_waitcnt lgkmcnt(8)\n"
"v_mfma_f32_16x16x32_bf16 v[208:211], v[48:51], a[88:91], v[208:211]\n"
"ds_read_b64_tr_b16 v[40:41], v16 offset:64\n"
"v_mfma_f32_16x16x32_bf16 v[212:215], v[48:51], a[92:95], v[212:215]\n"
"ds_read_b64_tr_b16 v[42:43], v16 offset:3648\n"
"buffer_load_dwordx4 %[vo0], %[rs2], s64 offen lds\n"
"v_mfma_f32_16x16x32_bf16 v[216:219], v[52:55], a[88:91], v[216:219]\n"
"ds_read_b64_tr_b16 v[44:45], v17 offset:64\n"
"v_mfma_f32_16x16x32_bf16 v[220:223], v[52:55], a[92:95], v[220:223]\n"
"ds_read_b64_tr_b16 v[46:47], v17 offset:3648\n"
"s_waitcnt lgkmcnt(8)\n"
"v_mfma_f32_16x16x32_bf16 v[224:227], v[56:59], a[88:91], v[224:227]\n"
"ds_read_b64_tr_b16 v[48:49], v16 offset:128\n"
"v_mfma_f32_16x16x32_bf16 v[228:231], v[56:59], a[92:95], v[228:231]\n"
"ds_read_b64_tr_b16 v[50:51], v16 offset:3712\n"
"buffer_load_dwordx4 %[vo1], %[rs2], s64 offen offset:1024 lds\n"
"v_mfma_f32_16x16x32_bf16 v[232:235], v[60:63], a[88:91], v[232:235]\n"
"ds_read_b64_tr_b16 v[52:53], v17 offset:128\n"
"v_mfma_f32_16x16x32_bf16 v[236:239], v[60:63], a[92:95], v[236:239]\n"
"ds_read_b64_tr_b16 v[54:55], v17 offset:3712\n"
"s_waitcnt lgkmcnt(8)\n"
"s_waitcnt vmcnt(16)\n"
"v_mfma_f32_16x16x32_bf16 v[128:131], v[32:35], a[96:99], v[128:131]\n"
"ds_read_b64_tr_b16 v[56:57], v16 offset:192\n"
"s_waitcnt vmcnt(15)\n"
"v_mfma_f32_16x16x32_bf16 v[132:135], v[32:35], a[100:103], v[132:135]\n"
"ds_read_b64_tr_b16 v[58:59], v16 offset:3776\n"
"buffer_load_dwordx4 %[vo2], %[rs2], s64 offen offset:2048 lds\n"
"v_mfma_f32_16x16x32_bf16 v[136:139], v[36:39], a[96:99], v[136:139]\n"
"ds_read_b64_tr_b16 v[60:61], v17 offset:192\n"
"v_mfma_f32_16x16x32_bf16 v[140:143], v[36:39], a[100:103], v[140:143]\n"
"ds_read_b64_tr_b16 v[62:63], v17 offset:3776\n"
"s_waitcnt lgkmcnt(8)\n"
"v_mfma_f32_16x16x32_bf16 v[144:147], v[40:43], a[96:99], v[144:147]\n"
"ds_read_b64_tr_b16 v[32:33], v16 offset:256\n"
"v_mfma_f32_16x16x32_bf16 v[148:151], v[40:43], a[100:103], v[148:151]\n"
"ds_read_b64_tr_b16 v[34:35], v16 offset:3840\n"
"v_mfma_f32_16x16x32_bf16 v[152:155], v[44:47], a[96:99], v[152:155]\n"
"ds_read_b64_tr_b16 v[36:37], v17 offset:256\n"
"v_mfma_f32_16x16x32_bf16 v[156:159], v[44:47], a[100:103], v[156:159]\n"
"ds_read_b64_tr_b16 v[38:39], v17 offset:3840\n"
"buffer_load_dwordx4 %[vo3], %[rs2], s64 offen offset:3072 lds\n"
"s_waitcnt lgkmcnt(8)\n"
"v_mfma_f32_16x16x32_bf16 v[160:163], v[48:51], a[96:99], v[160:163]\n"
"ds_read_b64_tr_b16 v[40:41], v16 offset:320\n"
"v_mfma_f32_16x16x32_bf16 v[164:167], v[48:51], a[100:103], v[164:167]\n"
"ds_read_b64_tr_b16 v[42:43], v16 offset:3904\n"
"s_add_u32 m0, %[l0], 0x1d000\n"
"v_mfma_f32_16x16x32_bf16 v[168:171], v[52:55], a[96:99], v[168:171]\n"
"ds_read_b64_tr_b16 v[44:45], v17 offset:320\n"
"v_mfma_f32_16x16x32_bf16 v[172:175], v[52:55], a[100:103], v[172:175]\n"
"ds_read_b64_tr_b16 v[46:47], v17 offset:3904\n"
"buffer_load_dwordx4 %[vo4], %[rs2], s64 offen lds\n"
"s_waitcnt lgkmcnt(8)\n"
"v_mfma_f32_16x16x32_bf16 v[176:179], v[56:59], a[96:99], v[176:179]\n"
"ds_read_b64_tr_b16 v[48:49], v16 offset:384\n"
"v_mfma_f32_16x16x32_bf16 v[180:183], v[56:59], a[100:103], v[180:183]\n"
"ds_read_b64_tr_b16 v[50:51], v16 offset:3968\n"
"v_mfma_f32_16x16x32_bf16 v[184:187], v[60:63], a[96:99], v[184:187]\n"
"ds_read_b64_tr_b16 v[52:53], v17 offset:384\n"
"v_mfma_f32_16x16x32_bf16 v[188:191], v[60:63], a[100:103], v[188:191]\n"
"ds_read_b64_tr_b16 v[54:55], v17 offset:3968\n"
"buffer_load_dwordx4 %[vo5], %[rs2], s64 offen offset:1024 lds\n"
"s_waitcnt lgkmcnt(8)\n"
"v_mfma_f32_16x16x32_bf16 v[192:195], v[32:35], a[96:99], v[192:195]\n"
"ds_read_b64_tr_b16 v[56:57], v16 offset:14336\n"
"v_mfma_f32_16x16x32_bf16 v[196:199], v[32:35], a[100:103], v[196:199]\n"
"ds_read_b64_tr_b16 v[58:59], v16 offset:17920\n"
"v_mfma_f32_16x16x32_bf16 v[200:203], v[36:39], a[96:99], v[200:203]\n"
"ds_read_b64_tr_b16 v[60:61], v17 offset:14336\n"
"v_mfma_f32_16x16x32_bf16 v[204:207], v[36:39], a[100:103], v[204:207]\n"
"ds_read_b64_tr_b16 v[62:63], v17 offset:17920\n"
"s_waitcnt lgkmcnt(8)\n"
"v_mfma_f32_16x16x32_bf16 v[208:211], v[40:43], a[96:99], v[208:211]\n"
"ds_read_b64_tr_b16 v[32:33], v16 offset:14400\n"
"v_mfma_f32_16x16x32_bf16 v[212:215], v[40:43], a[100:103], v[212:215]\n"
"ds_read_b64_tr_b16 v[34:35], v16 offset:17984\n"
"buffer_load_dwordx4 %[vo6], %[rs2], s64 offen offset:2048 lds\n"
"v_mfma_f32_16x16x32_bf16 v[216:219], v[44:47], a[96:99], v[216:219]\n"
"ds_read_b64_tr_b16 v[36:37], v17 offset:14400\n"
"v_mfma_f32_16x16x32_bf16 v[220:223], v[44:47], a[100:103], v[220:223]\n"
"ds_read_b64_tr_b16 v[38:39], v17 offset:17984\n"
"s_add_u32 s65, %[a2], 0\n"
"s_waitcnt lgkmcnt(8)\n"
"v_mfma_f32_16x16x32_bf16 v[224:227], v[48:51], a[96:99], v[224:227]\n"
"ds_read_b64_tr_b16 v[40:41], v16 offset:14464\n"
"v_mfma_f32_16x16x32_bf16 v[228:231], v[48:51], a[100:103], v[228:231]\n"
"ds_read_b64_tr_b16 v[42:43], v16 offset:18048\n"
"buffer_load_dwordx4 a[128:131], %[va], %[rsW], s65 offen sc1\n"
"v_mfma_f32_16x16x32_bf16 v[232:235], v[52:55], a[96:99], v[232:235]\n"
"ds_read_b64_tr_b16 v[44:45], v17 offset:14464\n"
"v_mfma_f32_16x16x32_bf16 v[236:239], v[52:55], a[100:103], v[236:239]\n"
"ds_read_b64_tr_b16 v[46:47], v17 offset:18048\n"
"s_waitcnt lgkmcnt(8)\n"
"s_waitcnt vmcnt(20)\n"
"v_mfma_f32_16x16x32_bf16 v[128:131], v[56:59], a[104:107], v[128:131]\n"
"ds_read_b64_tr_b16 v[48:49], v16 offset:14528\n"
"s_waitcnt vmcnt(19)\n"
"v_mfma_f32_16x16x32_bf16 v[132:135], v[56:59], a[108:111], v[132:135]\n"
"ds_read_b64_tr_b16 v[50:51], v16 offset:18112\n"
"v_mfma_f32_16x16x32_bf16 v[136:139], v[60:63], a[104:107], v[136:139]\n"
"ds_read_b64_tr_b16 v[52:53], v17 offset:14528\n"
"v_mfma_f32_16x16x32_bf16 v[140:143], v[60:63], a[108:111], v[140:143]\n"
"ds_read_b64_tr_b16 v[54:55], v17 offset:18112\n"
"buffer_load_dwordx4 a[132:135], %[va], %[rsW], s65 offen offset:256 sc1\n"
"s_waitcnt lgkmcnt(8)\n"
"v_mfma_f32_16x16x32_bf16 v[144:147], v[32:35], a[104:107], v[144:147]\n"
"ds_read_b64_tr_b16 v[56:57], v16 offset:14592\n"
"v_mfma_f32_16x16x32_bf16 v[148:151], v[32:35], a[108:111], v[148:151]\n"
"ds_read_b64_tr_b16 v[58:59], v16 offset:18176\n"
"v_mfma_f32_16x16x32_bf16 v[152:155], v[36:39], a[104:107], v[152:155]\n"
"ds_read_b64_tr_b16 v[60:61], v17 offset:14592\n"
"v_mfma_f32_16x16x32_bf16 v[156:159], v[36:39], a[108:111], v[156:159]\n"
"ds_read_b64_tr_b16 v[62:63], v17 offset:18176\n"
"buffer_load_dwordx4 a[136:139], %[va], %[rsW], s65 offen offset:2048 sc1\n"
"s_waitcnt lgkmcnt(8)\n"
"v_mfma_f32_16x16x32_bf16 v[160:163], v[40:43], a[104:107], v[160:163]\n"
"ds_read_b64_tr_b16 v[32:33], v16 offset:14656\n"
"v_mfma_f32_16x16x32_bf16 v[164:167], v[40:43], a[108:111], v[164:167]\n"
"ds_read_b64_tr_b16 v[34:35], v16 offset:18240\n"
"v_mfma_f32_16x16x32_bf16 v[168:171], v[44:47], a[104:107], v[168:171]\n"
"ds_read_b64_tr_b16 v[36:37], v17 offset:14656\n"
"v_mfma_f32_16x16x32_bf16 v[172:175], v[44:47], a[108:111], v[172:175]\n"
"ds_read_b64_tr_b16 v[38:39], v17 offset:18240\n"
"buffer_load_dwordx4 a[140:143], %[va], %[rsW], s65 offen offset:2304 sc1\n"
"s_waitcnt lgkmcnt(8)\n"
"v_mfma_f32_16x16x32_bf16 v[176:179], v[48:51], a[104:107], v[176:179]\n"
"ds_read_b64_tr_b16 v[40:41], v16 offset:14720\n"
"v_mfma_f32_16x16x32_bf16 v[180:183], v[48:51], a[108:111], v[180:183]\n"
"ds_read_b64_tr_b16 v[42:43], v16 offset:18304\n"
"v_mfma_f32_16x16x32_bf16 v[184:187], v[52:55], a[104:107], v[184:187]\n"
"ds_read_b64_tr_b16 v[44:45], v17 offset:14720\n"
"v_mfma_f32_16x16x32_bf16 v[188:191], v[52:55], a[108:111], v[188:191]\n"
"ds_read_b64_tr_b16 v[46:47], v17 offset:18304\n"
"s_waitcnt lgkmcnt(8)\n"
"v_mfma_f32_16x16x32_bf16 v[192:195], v[56:59], a[104:107], v[192:195]\n"
"s_waitcnt vmcnt(22)\n"
"s_barrier\n"
"v_add_u32 v16, 0x7000, v16\n"
"v_add_u32 v17, 0x7000, v17\n"
"ds_read_b64_tr_b16 v[48:49], v16\n"
"v_mfma_f32_16x16x32_bf16 v[196:199], v[56:59], a[108:111], v[196:199]\n"
"ds_read_b64_tr_b16 v[50:51], v16 offset:3584\n"
"s_add_u32 s64, %[s2], 0x1c000\n"
"v_mfma_f32_16x16x32_bf16 v[200:203], v[60:63], a[104:107], v[200:203]\n"
"ds_read_b64_tr_b16 v[52:53], v17\n"
"v_mfma_f32_16x16x32_bf16 v[204:207], v[60:63], a[108:111], v[204:207]\n"
"ds_read_b64_tr_b16 v[54:55], v17 offset:3584\n"
"s_add_u32 m0, %[l0], 0\n"
"s_waitcnt lgkmcnt(8)\n"
"v_mfma_f32_16x16x32_bf16 v[208:211], v[32:35], a[104:107], v[208:211]\n"
"ds_read_b64_tr_b16 v[56:57], v16 offset:64\n"
"v_mfma_f32_16x16x32_bf16 v[212:215], v[32:35], a[108:111], v[212:215]\n"
"ds_read_b64_tr_b16 v[58:59], v16 offset:3648\n"
"buffer_load_dwordx4 %[vo0], %[rs2], s64 offen lds\n"
"v_mfma_f32_16x16x32_bf16 v[216:219], v[36:39], a[104:107], v[216:219]\n"
"ds_read_b64_tr_b16 v[60:61], v17 offset:64\n"
"v_mfma_f32_16x16x32_bf16 v[220:223], v[36:39], a[108:111], v[220:223]\n"
"ds_read_b64_tr_b16 v[62:63], v17 offset:3648\n"
"s_waitcnt lgkmcnt(8)\n"
"v_mfma_f32_16x16x32_bf16 v[224:227], v[40:43], a[104:107], v[224:227]\n"
"ds_read_b64_tr_b16 v[32:33], v16 offset:128\n"
"v_mfma_f32_16x16x32_bf16 v[228:231], v[40:43], a[108:111], v[228:231]\n"
"ds_read_b64_tr_b16 v[34:35], v16 offset:3712\n"
"buffer_load_dwordx4 %[vo1], %[rs2], s64 offen offset:1024 lds\n"
"v_mfma_f32_16x16x32_bf16 v[232:235], v[44:47], a[104:107], v[232:235]\n"
"ds_read_b64_tr_b16 v[36:37], v17 offset:128\n"
"v_mfma_f32_16x16x32_bf16 v[236:239], v[44:47], a[108:111], v[236:239]\n"
"ds_read_b64_tr_b16 v[38:39], v17 offset:3712\n"
"s_waitcnt lgkmcnt(8)\n"
"s_waitcnt vmcnt(16)\n"
"v_mfma_f32_16x16x32_bf16 v[128:131], v[48:51], a[112:115], v[128:131]\n"
"ds_read_b64_tr_b16 v[40:41], v16 offset:192\n"
"s_waitcnt vmcnt(15)\n"
"v_mfma_f32_16x16x32_bf16 v[132:135], v[48:51], a[116:119], v[132:135]\n"
"ds_read_b64_tr_b16 v[42:43], v16 offset:3776\n"
"buffer_load_dwordx4 %[vo2], %[rs2], s64 offen offset:2048 lds\n"
"v_mfma_f32_16x16x32_bf16 v[136:139], v[52:55], a[112:115], v[136:139]\n"
"ds_read_b64_tr_b16 v[44:45], v17 offset:192\n"
"v_mfma_f32_16x16x32_bf16 v[140:143], v[52:55], a[116:119], v[140:143]\n"
"ds_read_b64_tr_b16 v[46:47], v17 offset:3776\n"
"s_waitcnt lgkmcnt(8)\n"
"v_mfma_f32_16x16x32_bf16 v[144:147], v[56:59], a[112:115], v[144:147]\n"
"ds_read_b64_tr_b16 v[48:49], v16 offset:256\n"
"v_mfma_f32_16x16x32_bf16 v[148:151], v[56:59], a[116:119], v[148:151]\n"
"ds_read_b64_tr_b16 v[50:51], v16 offset:3840\n"
"v_mfma_f32_16x16x32_bf16 v[152:155], v[60:63], a[112:115], v[152:155]\n"
"ds_read_b64_tr_b16 v[52:53], v17 offset:256\n"
"v_mfma_f32_16x16x32_bf16 v[156:159], v[60:63], a[116:119], v[156:159]\n"
"ds_read_b64_tr_b16 v[54:55], v17 offset:3840\n"
"buffer_load_dwordx4 %[vo3], %[rs2], s64 offen offset:3072 lds\n"
"s_waitcnt lgkmcnt(8)\n"
"v_mfma_f32_16x16x32_bf16 v[160:163], v[32:35], a[112:115], v[160:163]\n"
"ds_read_b64_tr_b16 v[56:57], v16 offset:320\n"
"v_mfma_f32_16x16x32_bf16 v[164:167], v[32:35], a[116:119], v[164:167]\n"
"ds_read_b64_tr_b16 v[58:59], v16 offset:3904\n"
"s_add_u32 m0, %[l0], 0x1000\n"
"v_mfma_f32_16x16x32_bf16 v[168:171], v[36:39], a[112:115], v[168:171]\n"
"ds_read_b64_tr_b16 v[60:61], v17 offset:320\n"
"v_mfma_f32_16x16x32_bf16 v[172:175], v[36:39], a[116:119], v[172:175]\n"
"ds_read_b64_tr_b16 v[62:63], v17 offset:3904\n"
"buffer_load_dwordx4 %[vo4], %[rs2], s64 offen lds\n"
"s_waitcnt lgkmcnt(8)\n"
"v_mfma_f32_16x16x32_bf16 v[176:179], v[40:43], a[112:115], v[176:179]\n"
"ds_read_b64_tr_b16 v[32:33], v16 offset:384\n"
"v_mfma_f32_16x16x32_bf16 v[180:183], v[40:43], a[116:119], v[180:183]\n"
"ds_read_b64_tr_b16 v[34:35], v16 offset:3968\n"
"v_mfma_f32_16x16x32_bf16 v[184:187], v[44:47], a[112:115], v[184:187]\n"
"ds_read_b64_tr_b16 v[36:37], v17 offset:384\n"
"v_mfma_f32_16x16x32_bf16 v[188:191], v[44:47], a[116:119], v[188:191]\n"
"ds_read_b64_tr_b16 v[38:39], v17 offset:3968\n"
"buffer_load_dwordx4 %[vo5], %[rs2], s64 offen offset:1024 lds\n"
"s_waitcnt lgkmcnt(8)\n"
"v_mfma_f32_16x16x32_bf16 v[192:195], v[48:51], a[112:115], v[192:195]\n"
"ds_read_b64_tr_b16 v[40:41], v16 offset:14336\n"
"v_mfma_f32_16x16x32_bf16 v[196:199], v[48:51], a[116:119], v[196:199]\n"
"ds_read_b64_tr_b16 v[42:43], v16 offset:17920\n"
"v_mfma_f32_16x16x32_bf16 v[200:203], v[52:55], a[112:115], v[200:203]\n"
"ds_read_b64_tr_b16 v[44:45], v17 offset:14336\n"
"v_mfma_f32_16x16x32_bf16 v[204:207], v[52:55], a[116:119], v[204:207]\n"
"ds_read_b64_tr_b16 v[46:47], v17 offset:17920\n"
"s_waitcnt lgkmcnt(8)\n"
"v_mfma_f32_16x16x32_bf16 v[208:211], v[56:59], a[112:115], v[208:211]\n"
"ds_read_b64_tr_b16 v[48:49], v16 offset:14400\n"
"v_mfma_f32_16x16x32_bf16 v[212:215], v[56:59], a[116:119], v[212:215]\n"
"ds_read_b64_tr_b16 v[50:51], v16 offset:17984\n"
"buffer_load_dwordx4 %[vo6], %[rs2], s64 offen offset:2048 lds\n"
"v_mfma_f32_16x16x32_bf16 v[216:219], v[60:63], a[112:115], v[216:219]\n"
"ds_read_b64_tr_b16 v[52:53], v17 offset:14400\n"
"v_mfma_f32_16x16x32_bf16 v[220:223], v[60:63], a[116:119], v[220:223]\n"
"ds_read_b64_tr_b16 v[54:55], v17 offset:17984\n"
"s_add_u32 s65, %[a2], 0x1000\n"
"s_waitcnt lgkmcnt(8)\n"
"v_mfma_f32_16x16x32_bf16 v[224:227], v[32:35], a[112:115], v[224:227]\n"
"ds_read_b64_tr_b16 v[56:57], v16 offset:14464\n"
"v_mfma_f32_16x16x32_bf16 v[228:231], v[32:35], a[116:119], v[228:231]\n"
"ds_read_b64_tr_b16 v[58:59], v16 offset:18048\n"
"buffer_load_dwordx4 a[144:147], %[va], %[rsW], s65 offen sc1\n"
"v_mfma_f32_16x16x32_bf16 v[232:235], v[36:39], a[112:115], v[232:235]\n"
"ds_read_b64_tr_b16 v[60:61], v17 offset:14464\n"
"v_mfma_f32_16x16x32_bf16 v[236:239], v[36:39], a[116:119], v[236:239]\n"
"ds_read_b64_tr_b16 v[62:63], v17 offset:18048\n"
"s_waitcnt lgkmcnt(8)\n"
"s_waitcnt vmcnt(20)\n"
"v_mfma_f32_16x16x32_bf16 v[128:131], v[40:43], a[120:123], v[128:131]\n"
"ds_read_b64_tr_b16 v[32:33], v16 offset:14528\n"
"s_waitcnt vmcnt(19)\n"
"v_mfma_f32_16x16x32_bf16 v[132:135], v[40:43], a[124:127], v[132:135]\n"
"ds_read_b64_tr_b16 v[34:35], v16 offset:18112\n"
"v_mfma_f32_16x16x32_bf16 v[136:139], v[44:47], a[120:123], v[136:139]\n"
"ds_read_b64_tr_b16 v[36:37], v17 offset:14528\n"
"v_mfma_f32_16x16x32_bf16 v[140:143], v[44:47], a[124:127], v[140:143]\n"
"ds_read_b64_tr_b16 v[38:39], v17 offset:18112\n"
"buffer_load_dwordx4 a[148:151], %[va], %[rsW], s65 offen offset:256 sc1\n"
"s_waitcnt lgkmcnt(8)\n"
"v_mfma_f32_16x16x32_bf16 v[144:147], v[48:51], a[120:123], v[144:147]\n"
"ds_read_b64_tr_b16 v[40:41], v16 offset:14592\n"
"v_mfma_f32_16x16x32_bf16 v[148:151], v[48:51], a[124:127], v[148:151]\n"
"ds_read_b64_tr_b16 v[42:43], v16 offset:18176\n"
"v_mfma_f32_16x16x32_bf16 v[152:155], v[52:55], a[120:123], v[152:155]\n"
"ds_read_b64_tr_b16 v[44:45], v17 offset:14592\n"
"v_mfma_f32_16x16x32_bf16 v[156:159], v[52:55], a[124:127], v[156:159]\n"
"ds_read_b64_tr_b16 v[46:47], v17 offset:18176\n"
"buffer_load_dwordx4 a[152:155], %[va], %[rsW], s65 offen offset:2048 sc1\n"
"s_waitcnt lgkmcnt(8)\n"
"v_mfma_f32_16x16x32_bf16 v[160:163], v[56:59], a[120:123], v[160:163]\n"
"ds_read_b64_tr_b16 v[48:49], v16 offset:14656\n"
"v_mfma_f32_16x16x32_bf16 v[164:167], v[56:59], a[124:127], v[164:167]\n"
"ds_read_b64_tr_b16 v[50:51], v16 offset:18240\n"
"v_mfma_f32_16x16x32_bf16 v[168:171], v[60:63], a[120:123], v[168:171]\n"
"ds_read_b64_tr_b16 v[52:53], v17 offset:14656\n"
"v_mfma_f32_16x16x32_bf16 v[172:175], v[60:63], a[124:127], v[172:175]\n"
"ds_read_b64_tr_b16 v[54:55], v17 offset:18240\n"
"buffer_load_dwordx4 a[156:159], %[va], %[rsW], s65 offen offset:2304 sc1\n"
"s_waitcnt lgkmcnt(8)\n"
"v_mfma_f32_16x16x32_bf16 v[176:179], v[32:35], a[120:123], v[176:179]\n"
"ds_read_b64_tr_b16 v[56:57], v16 offset:14720\n"
"v_mfma_f32_16x16x32_bf16 v[180:183], v[32:35], a[124:127], v[180:183]\n"
"ds_read_b64_tr_b16 v[58:59], v16 offset:18304\n"
"v_mfma_f32_16x16x32_bf16 v[184:187], v[36:39], a[120:123], v[184:187]\n"
"ds_read_b64_tr_b16 v[60:61], v17 offset:14720\n"
"v_mfma_f32_16x16x32_bf16 v[188:191], v[36:39], a[124:127], v[188:191]\n"
"ds_read_b64_tr_b16 v[62:63], v17 offset:18304\n"
"s_waitcnt lgkmcnt(8)\n"
"v_mfma_f32_16x16x32_bf16 v[192:195], v[40:43], a[120:123], v[192:195]\n"
"s_waitcnt vmcnt(22)\n"
"s_barrier\n"
"v_add_u32 v16, 0x7000, v16\n"
"v_add_u32 v17, 0x7000, v17\n"
"ds_read_b64_tr_b16 v[32:33], v16\n"
"v_mfma_f32_16x16x32_bf16 v[196:199], v[40:43], a[124:127], v[196:199]\n"
"ds_read_b64_tr_b16 v[34:35], v16 offset:3584\n"
"s_mov_b32 s64, %[s11]\n"
"v_mfma_f32_16x16x32_bf16 v[200:203], v[44:47], a[120:123], v[200:203]\n"
"ds_read_b64_tr_b16 v[36:37], v17\n"
"v_mfma_f32_16x16x32_bf16 v[204:207], v[44:47], a[124:127], v[204:207]\n"
"ds_read_b64_tr_b16 v[38:39], v17 offset:3584\n"
"s_add_u32 m0, %[l0], 0x7000\n"
"s_waitcnt lgkmcnt(8)\n"
"v_mfma_f32_16x16x32_bf16 v[208:211], v[48:51], a[120:123], v[208:211]\n"
"ds_read_b64_tr_b16 v[40:41], v16 offset:64\n"
"v_mfma_f32_16x16x32_bf16 v[212:215], v[48:51], a[124:127], v[212:215]\n"
"ds_read_b64_tr_b16 v[42:43], v16 offset:3648\n"
"buffer_load_dwordx4 %[vo0], %[rs2], s64 offen lds\n"
"v_mfma_f32_16x16x32_bf16 v[216:219], v[52:55], a[120:123], v[216:219]\n"
"ds_read_b64_tr_b16 v[44:45], v17 offset:64\n"
"v_mfma_f32_16x16x32_bf16 v[220:223], v[52:55], a[124:127], v[220:223]\n"
"ds_read_b64_tr_b16 v[46:47], v17 offset:3648\n"
"s_waitcnt lgkmcnt(8)\n"
"v_mfma_f32_16x16x32_bf16 v[224:227], v[56:59], a[120:123], v[224:227]\n"
"ds_read_b64_tr_b16 v[48:49], v16 offset:128\n"
"v_mfma_f32_16x16x32_bf16 v[228:231], v[56:59], a[124:127], v[228:231]\n"
"ds_read_b64_tr_b16 v[50:51], v16 offset:3712\n"
"buffer_load_dwordx4 %[vo1], %[rs2], s64 offen offset:1024 lds\n"
"v_mfma_f32_16x16x32_bf16 v[232:235], v[60:63], a[120:123], v[232:235]\n"
"ds_read_b64_tr_b16 v[52:53], v17 offset:128\n"
"v_mfma_f32_16x16x32_bf16 v[236:239], v[60:63], a[124:127], v[236:239]\n"
"ds_read_b64_tr_b16 v[54:55], v17 offset:3712\n"
"s_waitcnt lgkmcnt(8)\n"
"s_waitcnt vmcnt(16)\n"
"v_mfma_f32_16x16x32_bf16 v[128:131], v[32:35], a[128:131], v[128:131]\n"
"ds_read_b64_tr_b16 v[56:57], v16 offset:192\n"
"s_waitcnt vmcnt(15)\n"
"v_mfma_f32_16x16x32_bf16 v[132:135], v[32:35], a[132:135], v[132:135]\n"
"ds_read_b64_tr_b16 v[58:59], v16 offset:3776\n"
"buffer_load_dwordx4 %[vo2], %[rs2], s64 offen offset:2048 lds\n"
"v_mfma_f32_16x16x32_bf16 v[136:139], v[36:39], a[128:131], v[136:139]\n"
"ds_read_b64_tr_b16 v[60:61], v17 offset:192\n"
"v_mfma_f32_16x16x32_bf16 v[140:143], v[36:39], a[132:135], v[140:143]\n"
"ds_read_b64_tr_b16 v[62:63], v17 offset:3776\n"
"s_waitcnt lgkmcnt(8)\n"
"v_mfma_f32_16x16x32_bf16 v[144:147], v[40:43], a[128:131], v[144:147]\n"
"ds_read_b64_tr_b16 v[32:33], v16 offset:256\n"
"v_mfma_f32_16x16x32_bf16 v[148:151], v[40:43], a[132:135], v[148:151]\n"
"ds_read_b64_tr_b16 v[34:35], v16 offset:3840\n"
"v_mfma_f32_16x16x32_bf16 v[152:155], v[44:47], a[128:131], v[152:155]\n"
"ds_read_b64_tr_b16 v[36:37], v17 offset:256\n"
"v_mfma_f32_16x16x32_bf16 v[156:159], v[44:47], a[132:135], v[156:159]\n"
"ds_read_b64_tr_b16 v[38:39], v17 offset:3840\n"
"buffer_load_dwordx4 %[vo3], %[rs2], s64 offen offset:3072 lds\n"
"s_waitcnt lgkmcnt(8)\n"
"v_mfma_f32_16x16x32_bf16 v[160:163], v[48:51], a[128:131], v[160:163]\n"
"ds_read_b64_tr_b16 v[40:41], v16 offset:320\n"
"v_mfma_f32_16x16x32_bf16 v[164:167], v[48:51], a[132:135], v[164:167]\n"
"ds_read_b64_tr_b16 v[42:43], v16 offset:3904\n"
"s_add_u32 m0, %[l0], 0x8000\n"
"v_mfma_f32_16x16x32_bf16 v[168:171], v[52:55], a[128:131], v[168:171]\n"
"ds_read_b64_tr_b16 v[44:45], v17 offset:320\n"
"v_mfma_f32_16x16x32_bf16 v[172:175], v[52:55], a[132:135], v[172:175]\n"
"ds_read_b64_tr_b16 v[46:47], v17 offset:3904\n"
"buffer_load_dwordx4 %[vo4], %[rs2], s64 offen lds\n"
"s_waitcnt lgkmcnt(8)\n"
"v_mfma_f32_16x16x32_bf16 v[176:179], v[56:59], a[128:131], v[176:179]\n"
"ds_read_b64_tr_b16 v[48:49], v16 offset:384\n"
"v_mfma_f32_16x16x32_bf16 v[180:183], v[56:59], a[132:135], v[180:183]\n"
"ds_read_b64_tr_b16 v[50:51], v16 offset:3968\n"
"v_mfma_f32_16x16x32_bf16 v[184:187], v[60:63], a[128:131], v[184:187]\n"
"ds_read_b64_tr_b16 v[52:53], v17 offset:384\n"
"v_mfma_f32_16x16x32_bf16 v[188:191], v[60:63], a[132:135], v[188:191]\n"
"ds_read_b64_tr_b16 v[54:55], v17 offset:3968\n"
"buffer_load_dwordx4 %[vo5], %[rs2], s64 offen offset:1024 lds\n"
"s_waitcnt lgkmcnt(8)\n"
"v_mfma_f32_16x16x32_bf16 v[192:195], v[32:35], a[128:131], v[192:195]\n"
"ds_read_b64_tr_b16 v[56:57], v16 offset:14336\n"
"v_mfma_f32_16x16x32_bf16 v[196:199], v[32:35], a[132:135], v[196:199]\n"
"ds_read_b64_tr_b16 v[58:59], v16 offset:17920\n"
"v_mfma_f32_16x16x32_bf16 v[200:203], v[36:39], a[128:131], v[200:203]\n"
"ds_read_b64_tr_b16 v[60:61], v17 offset:14336\n"
"v_mfma_f32_16x16x32_bf16 v[204:207], v[36:39], a[132:135], v[204:207]\n"
"ds_read_b64_tr_b16 v[62:63], v17 offset:17920\n"
"s_waitcnt lgkmcnt(8)\n"
"v_mfma_f32_16x16x32_bf16 v[208:211], v[40:43], a[128:131], v[208:211]\n"
"ds_read_b64_tr_b16 v[32:33], v16 offset:14400\n"
"v_mfma_f32_16x16x32_bf16 v[212:215], v[40:43], a[132:135], v[212:215]\n"
"ds_read_b64_tr_b16 v[34:35], v16 offset:17984\n"
"buffer_load_dwordx4 %[vo6], %[rs2], s64 offen offset:2048 lds\n"
"v_mfma_f32_16x16x32_bf16 v[216:219], v[44:47], a[128:131], v[216:219]\n"
"ds_read_b64_tr_b16 v[36:37], v17 offset:14400\n"
"v_mfma_f32_16x16x32_bf16 v[220:223], v[44:47], a[132:135], v[220:223]\n"
"ds_read_b64_tr_b16 v[38:39], v17 offset:17984\n"
"s_add_u32 s65, %[a2], 0x2000\n"
"s_waitcnt lgkmcnt(8)\n"
"v_mfma_f32_16x16x32_bf16 v[224:227], v[48:51], a[128:131], v[224:227]\n"
"ds_read_b64_tr_b16 v[40:41], v16 offset:14464\n"
"v_mfma_f32_16x16x32_bf16 v[228:231], v[48:51], a[132:135], v[228:231]\n"
"ds_read_b64_tr_b16 v[42:43], v16 offset:18048\n"
"buffer_load_dwordx4 a[160:163], %[va], %[rsW], s65 offen sc1\n"
"v_mfma_f32_16x16x32_bf16 v[232:235], v[52:55], a[128:131], v[232:235]\n"
"ds_read_b64_tr_b16 v[44:45], v17 offset:14464\n"
"v_mfma_f32_16x16x32_bf16 v[236:239], v[52:55], a[132:135], v[236:239]\n"
"ds_read_b64_tr_b16 v[46:47], v17 offset:18048\n"
"s_waitcnt lgkmcnt(8)\n"
"s_waitcnt vmcnt(20)\n"
"v_mfma_f32_16x16x32_bf16 v[128:131], v[56:59], a[136:139], v[128:131]\n"
"ds_read_b64_tr_b16 v[48:49], v16 offset:14528\n"
"s_waitcnt vmcnt(19)\n"
"v_mfma_f32_16x16x32_bf16 v[132:135], v[56:59], a[140:143], v[132:135]\n"
"ds_read_b64_tr_b16 v[50:51], v16 offset:18112\n"
"v_mfma_f32_16x16x32_bf16 v[136:139], v[60:63], a[136:139], v[136:139]\n"
"ds_read_b64_tr_b16 v[52:53], v17 offset:14528\n"
"v_mfma_f32_16x16x32_bf16 v[140:143], v[60:63], a[140:143], v[140:143]\n"
"ds_read_b64_tr_b16 v[54:55], v17 offset:18112\n"
"buffer_load_dwordx4 a[164:167], %[va], %[rsW], s65 offen offset:256 sc1\n"
"s_waitcnt lgkmcnt(8)\n"
"v_mfma_f32_16x16x32_bf16 v[144:147], v[32:35], a[136:139], v[144:147]\n"
"ds_read_b64_tr_b16 v[56:57], v16 offset:14592\n"
"v_mfma_f32_16x16x32_bf16 v[148:151], v[32:35], a[140:143], v[148:151]\n"
"ds_read_b64_tr_b16 v[58:59], v16 offset:18176\n"
"v_mfma_f32_16x16x32_bf16 v[152:155], v[36:39], a[136:139], v[152:155]\n"
"ds_read_b64_tr_b16 v[60:61], v17 offset:14592\n"
"v_mfma_f32_16x16x32_bf16 v[156:159], v[36:39], a[140:143], v[156:159]\n"
"ds_read_b64_tr_b16 v[62:63], v17 offset:18176\n"
"buffer_load_dwordx4 a[168:171], %[va], %[rsW], s65 offen offset:2048 sc1\n"
"s_waitcnt lgkmcnt(8)\n"
"v_mfma_f32_16x16x32_bf16 v[160:163], v[40:43], a[136:139], v[160:163]\n"
"ds_read_b64_tr_b16 v[32:33], v16 offset:14656\n"
"v_mfma_f32_16x16x32_bf16 v[164:167], v[40:43], a[140:143], v[164:167]\n"
"ds_read_b64_tr_b16 v[34:35], v16 offset:18240\n"
"v_mfma_f32_16x16x32_bf16 v[168:171], v[44:47], a[136:139], v[168:171]\n"
"ds_read_b64_tr_b16 v[36:37], v17 offset:14656\n"
"v_mfma_f32_16x16x32_bf16 v[172:175], v[44:47], a[140:143], v[172:175]\n"
"ds_read_b64_tr_b16 v[38:39], v17 offset:18240\n"
"buffer_load_dwordx4 a[172:175], %[va], %[rsW], s65 offen offset:2304 sc1\n"
"s_waitcnt lgkmcnt(8)\n"
"v_mfma_f32_16x16x32_bf16 v[176:179], v[48:51], a[136:139], v[176:179]\n"
"ds_read_b64_tr_b16 v[40:41], v16 offset:14720\n"
"v_mfma_f32_16x16x32_bf16 v[180:183], v[48:51], a[140:143], v[180:183]\n"
"ds_read_b64_tr_b16 v[42:43], v16 offset:18304\n"
"v_mfma_f32_16x16x32_bf16 v[184:187], v[52:55], a[136:139], v[184:187]\n"
"ds_read_b64_tr_b16 v[44:45], v17 offset:14720\n"
"v_mfma_f32_16x16x32_bf16 v[188:191], v[52:55], a[140:143], v[188:191]\n"
"ds_read_b64_tr_b16 v[46:47], v17 offset:18304\n"
"s_waitcnt lgkmcnt(8)\n"
"v_mfma_f32_16x16x32_bf16 v[192:195], v[56:59], a[136:139], v[192:195]\n"
"s_waitcnt vmcnt(22)\n"
"s_barrier\n"
"v_add_u32 v16, 0x7000, v16\n"
"v_add_u32 v17, 0x7000, v17\n"
"ds_read_b64_tr_b16 v[48:49], v16\n"
"v_mfma_f32_16x16x32_bf16 v[196:199], v[56:59], a[140:143], v[196:199]\n"
"ds_read_b64_tr_b16 v[50:51], v16 offset:3584\n"
"s_add_u32 s65, %[a2], 0x3000\n"
"v_mfma_f32_16x16x32_bf16 v[200:203], v[60:63], a[136:139], v[200:203]\n"
"ds_read_b64_tr_b16 v[52:53], v17\n"
"v_mfma_f32_16x16x32_bf16 v[204:207], v[60:63], a[140:143], v[204:207]\n"
"ds_read_b64_tr_b16 v[54:55], v17 offset:3584\n"
"buffer_load_dwordx4 a[176:179], %[va], %[rsW], s65 offen sc1\n"
"s_waitcnt lgkmcnt(8)\n"
"v_mfma_f32_16x16x32_bf16 v[208:211], v[32:35], a[136:139], v[208:211]\n"
"ds_read_b64_tr_b16 v[56:57], v16 offset:64\n"
"v_mfma_f32_16x16x32_bf16 v[212:215], v[32:35], a[140:143], v[212:215]\n"
"ds_read_b64_tr_b16 v[58:59], v16 offset:3648\n"
"v_mfma_f32_16x16x32_bf16 v[216:219], v[36:39], a[136:139], v[216:219]\n"
"ds_read_b64_tr_b16 v[60:61], v17 offset:64\n"
"v_mfma_f32_16x16x32_bf16 v[220:223], v[36:39], a[140:143], v[220:223]\n"
"ds_read_b64_tr_b16 v[62:63], v17 offset:3648\n"
"s_waitcnt lgkmcnt(8)\n"
"v_mfma_f32_16x16x32_bf16 v[224:227], v[40:43], a[136:139], v[224:227]\n"
"ds_read_b64_tr_b16 v[32:33], v16 offset:128\n"
"v_mfma_f32_16x16x32_bf16 v[228:231], v[40:43], a[140:143], v[228:231]\n"
"ds_read_b64_tr_b16 v[34:35], v16 offset:3712\n"
"buffer_load_dwordx4 a[180:183], %[va], %[rsW], s65 offen offset:256 sc1\n"
"v_mfma_f32_16x16x32_bf16 v[232:235], v[44:47], a[136:139], v[232:235]\n"
"ds_read_b64_tr_b16 v[36:37], v17 offset:128\n"
"v_mfma_f32_16x16x32_bf16 v[236:239], v[44:47], a[140:143], v[236:239]\n"
"ds_read_b64_tr_b16 v[38:39], v17 offset:3712\n"
"s_waitcnt lgkmcnt(8)\n"
"s_waitcnt vmcnt(16)\n"
"v_mfma_f32_16x16x32_bf16 v[128:131], v[48:51], a[144:147], v[128:131]\n"
"ds_read_b64_tr_b16 v[40:41], v16 offset:192\n"
"s_waitcnt vmcnt(15)\n"
"v_mfma_f32_16x16x32_bf16 v[132:135], v[48:51], a[148:151], v[132:135]\n"
"ds_read_b64_tr_b16 v[42:43], v16 offset:3776\n"
"v_mfma_f32_16x16x32_bf16 v[136:139], v[52:55], a[144:147], v[136:139]\n"
"ds_read_b64_tr_b16 v[44:45], v17 offset:192\n"
"v_mfma_f32_16x16x32_bf16 v[140:143], v[52:55], a[148:151], v[140:143]\n"
"ds_read_b64_tr_b16 v[46:47], v17 offset:3776\n"
"s_waitcnt lgkmcnt(8)\n"
"v_mfma_f32_16x16x32_bf16 v[144:147], v[56:59], a[144:147], v[144:147]\n"
"ds_read_b64_tr_b16 v[48:49], v16 offset:256\n"
"v_mfma_f32_16x16x32_bf16 v[148:151], v[56:59], a[148:151], v[148:151]\n"
"ds_read_b64_tr_b16 v[50:51], v16 offset:3840\n"
"v_mfma_f32_16x16x32_bf16 v[152:155], v[60:63], a[144:147], v[152:155]\n"
"ds_read_b64_tr_b16 v[52:53], v17 offset:256\n"
"v_mfma_f32_16x16x32_bf16 v[156:159], v[60:63], a[148:151], v[156:159]\n"
"ds_read_b64_tr_b16 v[54:55], v17 offset:3840\n"
"s_waitcnt lgkmcnt(8)\n"
"v_mfma_f32_16x16x32_bf16 v[160:163], v[32:35], a[144:147], v[160:163]\n"
"ds_read_b64_tr_b16 v[56:57], v16 offset:320\n"
"v_mfma_f32_16x16x32_bf16 v[164:167], v[32:35], a[148:151], v[164:167]\n"
"ds_read_b64_tr_b16 v[58:59], v16 offset:3904\n"
"v_mfma_f32_16x16x32_bf16 v[168:171], v[36:39], a[144:147], v[168:171]\n"
"ds_read_b64_tr_b16 v[60:61], v17 offset:320\n"
"v_mfma_f32_16x16x32_bf16 v[172:175], v[36:39], a[148:151], v[172:175]\n"
"ds_read_b64_tr_b16 v[62:63], v17 offset:3904\n"
"s_waitcnt lgkmcnt(8)\n"
"v_mfma_f32_16x16x32_bf16 v[176:179], v[40:43], a[144:147], v[176:179]\n"
"ds_read_b64_tr_b16 v[32:33], v16 offset:384\n"
"v_mfma_f32_16x16x32_bf16 v[180:183], v[40:43], a[148:151], v[180:183]\n"
"ds_read_b64_tr_b16 v[34:35], v16 offset:3968\n"
"v_mfma_f32_16x16x32_bf16 v[184:187], v[44:47], a[144:147], v[184:187]\n"
"ds_read_b64_tr_b16 v[36:37], v17 offset:384\n"
"v_mfma_f32_16x16x32_bf16 v[188:191], v[44:47], a[148:151], v[188:191]\n"
"ds_read_b64_tr_b16 v[38:39], v17 offset:3968\n"
"s_waitcnt lgkmcnt(8)\n"
"v_mfma_f32_16x16x32_bf16 v[192:195], v[48:51], a[144:147], v[192:195]\n"
"ds_read_b64_tr_b16 v[40:41], v16 offset:14336\n"
"v_mfma_f32_16x16x32_bf16 v[196:199], v[48:51], a[148:151], v[196:199]\n"
"ds_read_b64_tr_b16 v[42:43], v16 offset:17920\n"
"v_mfma_f32_16x16x32_bf16 v[200:203], v[52:55], a[144:147], v[200:203]\n"
"ds_read_b64_tr_b16 v[44:45], v17 offset:14336\n"
"v_mfma_f32_16x16x32_bf16 v[204:207], v[52:55], a[148:151], v[204:207]\n"
"ds_read_b64_tr_b16 v[46:47], v17 offset:17920\n"
"s_waitcnt lgkmcnt(8)\n"
"v_mfma_f32_16x16x32_bf16 v[208:211], v[56:59], a[144:147], v[208:211]\n"
"ds_read_b64_tr_b16 v[48:49], v16 offset:14400\n"
"v_mfma_f32_16x16x32_bf16 v[212:215], v[56:59], a[148:151], v[212:215]\n"
"ds_read_b64_tr_b16 v[50:51], v16 offset:17984\n"
"v_mfma_f32_16x16x32_bf16 v[216:219], v[60:63], a[144:147], v[216:219]\n"
"ds_read_b64_tr_b16 v[52:53], v17 offset:14400\n"
"v_mfma_f32_16x16x32_bf16 v[220:223], v[60:63], a[148:151], v[220:223]\n"
"ds_read_b64_tr_b16 v[54:55], v17 offset:17984\n"
"s_waitcnt lgkmcnt(8)\n"
"v_mfma_f32_16x16x32_bf16 v[224:227], v[32:35], a[144:147], v[224:227]\n"
"ds_read_b64_tr_b16 v[56:57], v16 offset:14464\n"
"v_mfma_f32_16x16x32_bf16 v[228:231], v[32:35], a[148:151], v[228:231]\n"
"ds_read_b64_tr_b16 v[58:59], v16 offset:18048\n"
"v_mfma_f32_16x16x32_bf16 v[232:235], v[36:39], a[144:147], v[232:235]\n"
"ds_read_b64_tr_b16 v[60:61], v17 offset:14464\n"
"v_mfma_f32_16x16x32_bf16 v[236:239], v[36:39], a[148:151], v[236:239]\n"
"ds_read_b64_tr_b16 v[62:63], v17 offset:18048\n"
"s_waitcnt lgkmcnt(8)\n"
"s_waitcnt vmcnt(14)\n"
"v_mfma_f32_16x16x32_bf16 v[128:131], v[40:43], a[152:155], v[128:131]\n"
"ds_read_b64_tr_b16 v[32:33], v16 offset:14528\n"
"s_waitcnt vmcnt(13)\n"
"v_mfma_f32_16x16x32_bf16 v[132:135], v[40:43], a[156:159], v[132:135]\n"
"ds_read_b64_tr_b16 v[34:35], v16 offset:18112\n"
"v_mfma_f32_16x16x32_bf16 v[136:139], v[44:47], a[152:155], v[136:139]\n"
"ds_read_b64_tr_b16 v[36:37], v17 offset:14528\n"
"v_mfma_f32_16x16x32_bf16 v[140:143], v[44:47], a[156:159], v[140:143]\n"
"ds_read_b64_tr_b16 v[38:39], v17 offset:18112\n"
"s_waitcnt lgkmcnt(8)\n"
"v_mfma_f32_16x16x32_bf16 v[144:147], v[48:51], a[152:155], v[144:147]\n"
"ds_read_b64_tr_b16 v[40:41], v16 offset:14592\n"
"v_mfma_f32_16x16x32_bf16 v[148:151], v[48:51], a[156:159], v[148:151]\n"
"ds_read_b64_tr_b16 v[42:43], v16 offset:18176\n"
"v_mfma_f32_16x16x32_bf16 v[152:155], v[52:55], a[152:155], v[152:155]\n"
"ds_read_b64_tr_b16 v[44:45], v17 offset:14592\n"
"v_mfma_f32_16x16x32_bf16 v[156:159], v[52:55], a[156:159], v[156:159]\n"
"ds_read_b64_tr_b16 v[46:47], v17 offset:18176\n"
"s_waitcnt lgkmcnt(8)\n"
"v_mfma_f32_16x16x32_bf16 v[160:163], v[56:59], a[152:155], v[160:163]\n"
"ds_read_b64_tr_b16 v[48:49], v16 offset:14656\n"
"v_mfma_f32_16x16x32_bf16 v[164:167], v[56:59], a[156:159], v[164:167]\n"
"ds_read_b64_tr_b16 v[50:51], v16 offset:18240\n"
"v_mfma_f32_16x16x32_bf16 v[168:171], v[60:63], a[152:155], v[168:171]\n"
"ds_read_b64_tr_b16 v[52:53], v17 offset:14656\n"
"v_mfma_f32_16x16x32_bf16 v[172:175], v[60:63], a[156:159], v[172:175]\n"
"ds_read_b64_tr_b16 v[54:55], v17 offset:18240\n"
"s_waitcnt lgkmcnt(8)\n"
"v_mfma_f32_16x16x32_bf16 v[176:179], v[32:35], a[152:155], v[176:179]\n"
"ds_read_b64_tr_b16 v[56:57], v16 offset:14720\n"
"v_mfma_f32_16x16x32_bf16 v[180:183], v[32:35], a[156:159], v[180:183]\n"
"ds_read_b64_tr_b16 v[58:59], v16 offset:18304\n"
"v_mfma_f32_16x16x32_bf16 v[184:187], v[36:39], a[152:155], v[184:187]\n"
"ds_read_b64_tr_b16 v[60:61], v17 offset:14720\n"
"v_mfma_f32_16x16x32_bf16 v[188:191], v[36:39], a[156:159], v[188:191]\n"
"ds_read_b64_tr_b16 v[62:63], v17 offset:18304\n"
"s_waitcnt lgkmcnt(8)\n"
"v_mfma_f32_16x16x32_bf16 v[192:195], v[40:43], a[152:155], v[192:195]\n"
"s_waitcnt vmcnt(13)\n"
"s_barrier\n"
"v_add_u32 v16, 0xfffe4000, v16\n"
"v_add_u32 v17, 0xfffe4000, v17\n"
"ds_read_b64_tr_b16 v[32:33], v16\n"
"v_mfma_f32_16x16x32_bf16 v[196:199], v[40:43], a[156:159], v[196:199]\n"
"ds_read_b64_tr_b16 v[34:35], v16 offset:3584\n"
"v_mfma_f32_16x16x32_bf16 v[200:203], v[44:47], a[152:155], v[200:203]\n"
"ds_read_b64_tr_b16 v[36:37], v17\n"
"v_mfma_f32_16x16x32_bf16 v[204:207], v[44:47], a[156:159], v[204:207]\n"
"ds_read_b64_tr_b16 v[38:39], v17 offset:3584\n"
"s_waitcnt lgkmcnt(8)\n"
"v_mfma_f32_16x16x32_bf16 v[208:211], v[48:51], a[152:155], v[208:211]\n"
"ds_read_b64_tr_b16 v[40:41], v16 offset:64\n"
"v_mfma_f32_16x16x32_bf16 v[212:215], v[48:51], a[156:159], v[212:215]\n"
"ds_read_b64_tr_b16 v[42:43], v16 offset:3648\n"
"v_mfma_f32_16x16x32_bf16 v[216:219], v[52:55], a[152:155], v[216:219]\n"
"ds_read_b64_tr_b16 v[44:45], v17 offset:64\n"
"v_mfma_f32_16x16x32_bf16 v[220:223], v[52:55], a[156:159], v[220:223]\n"
"ds_read_b64_tr_b16 v[46:47], v17 offset:3648\n"
"s_waitcnt lgkmcnt(8)\n"
"v_mfma_f32_16x16x32_bf16 v[224:227], v[56:59], a[152:155], v[224:227]\n"
"ds_read_b64_tr_b16 v[48:49], v16 offset:128\n"
"v_mfma_f32_16x16x32_bf16 v[228:231], v[56:59], a[156:159], v[228:231]\n"
"ds_read_b64_tr_b16 v[50:51], v16 offset:3712\n"
"v_mfma_f32_16x16x32_bf16 v[232:235], v[60:63], a[152:155], v[232:235]\n"
"ds_read_b64_tr_b16 v[52:53], v17 offset:128\n"
"v_mfma_f32_16x16x32_bf16 v[236:239], v[60:63], a[156:159], v[236:239]\n"
"ds_read_b64_tr_b16 v[54:55], v17 offset:3712\n"
"s_waitcnt lgkmcnt(8)\n"
"s_waitcnt vmcnt(5)\n"
"v_mfma_f32_16x16x32_bf16 v[128:131], v[32:35], a[160:163], v[128:131]\n"
"ds_read_b64_tr_b16 v[56:57], v16 offset:192\n"
"s_waitcnt vmcnt(4)\n"
"v_mfma_f32_16x16x32_bf16 v[132:135], v[32:35], a[164:167], v[132:135]\n"
"ds_read_b64_tr_b16 v[58:59], v16 offset:3776\n"
"v_mfma_f32_16x16x32_bf16 v[136:139], v[36:39], a[160:163], v[136:139]\n"
"ds_read_b64_tr_b16 v[60:61], v17 offset:192\n"
"v_mfma_f32_16x16x32_bf16 v[140:143], v[36:39], a[164:167], v[140:143]\n"
"ds_read_b64_tr_b16 v[62:63], v17 offset:3776\n"
"s_waitcnt lgkmcnt(8)\n"
"v_mfma_f32_16x16x32_bf16 v[144:147], v[40:43], a[160:163], v[144:147]\n"
"ds_read_b64_tr_b16 v[32:33], v16 offset:256\n"
"v_mfma_f32_16x16x32_bf16 v[148:151], v[40:43], a[164:167], v[148:151]\n"
"ds_read_b64_tr_b16 v[34:35], v16 offset:3840\n"
"v_mfma_f32_16x16x32_bf16 v[152:155], v[44:47], a[160:163], v[152:155]\n"
"ds_read_b64_tr_b16 v[36:37], v17 offset:256\n"
"v_mfma_f32_16x16x32_bf16 v[156:159], v[44:47], a[164:167], v[156:159]\n"
"ds_read_b64_tr_b16 v[38:39], v17 offset:3840\n"
"s_waitcnt lgkmcnt(8)\n"
"v_mfma_f32_16x16x32_bf16 v[160:163], v[48:51], a[160:163], v[160:163]\n"
"ds_read_b64_tr_b16 v[40:41], v16 offset:320\n"
"v_mfma_f32_16x16x32_bf16 v[164:167], v[48:51], a[164:167], v[164:167]\n"
"ds_read_b64_tr_b16 v[42:43], v16 offset:3904\n"
"v_mfma_f32_16x16x32_bf16 v[168:171], v[52:55], a[160:163], v[168:171]\n"
"ds_read_b64_tr_b16 v[44:45], v17 offset:320\n"
"v_mfma_f32_16x16x32_bf16 v[172:175], v[52:55], a[164:167], v[172:175]\n"
"ds_read_b64_tr_b16 v[46:47], v17 offset:3904\n"
"s_waitcnt lgkmcnt(8)\n"
"v_mfma_f32_16x16x32_bf16 v[176:179], v[56:59], a[160:163], v[176:179]\n"
"ds_read_b64_tr_b16 v[48:49], v16 offset:384\n"
"v_mfma_f32_16x16x32_bf16 v[180:183], v[56:59], a[164:167], v[180:183]\n"
"ds_read_b64_tr_b16 v[50:51], v16 offset:3968\n"
"v_mfma_f32_16x16x32_bf16 v[184:187], v[60:63], a[160:163], v[184:187]\n"
"ds_read_b64_tr_b16 v[52:53], v17 offset:384\n"
"v_mfma_f32_16x16x32_bf16 v[188:191], v[60:63], a[164:167], v[188:191]\n"
"ds_read_b64_tr_b16 v[54:55], v17 offset:3968\n"
"s_waitcnt lgkmcnt(8)\n"
"v_mfma_f32_16x16x32_bf16 v[192:195], v[32:35], a[160:163], v[192:195]\n"
"ds_read_b64_tr_b16 v[56:57], v16 offset:14336\n"
"v_mfma_f32_16x16x32_bf16 v[196:199], v[32:35], a[164:167], v[196:199]\n"
"ds_read_b64_tr_b16 v[58:59], v16 offset:17920\n"
"v_mfma_f32_16x16x32_bf16 v[200:203], v[36:39], a[160:163], v[200:203]\n"
"ds_read_b64_tr_b16 v[60:61], v17 offset:14336\n"
"v_mfma_f32_16x16x32_bf16 v[204:207], v[36:39], a[164:167], v[204:207]\n"
"ds_read_b64_tr_b16 v[62:63], v17 offset:17920\n"
"s_waitcnt lgkmcnt(8)\n"
"v_mfma_f32_16x16x32_bf16 v[208:211], v[40:43], a[160:163], v[208:211]\n"
"ds_read_b64_tr_b16 v[32:33], v16 offset:14400\n"
"v_mfma_f32_16x16x32_bf16 v[212:215], v[40:43], a[164:167], v[212:215]\n"
"ds_read_b64_tr_b16 v[34:35], v16 offset:17984\n"
"v_mfma_f32_16x16x32_bf16 v[216:219], v[44:47], a[160:163], v[216:219]\n"
"ds_read_b64_tr_b16 v[36:37], v17 offset:14400\n"
"v_mfma_f32_16x16x32_bf16 v[220:223], v[44:47], a[164:167], v[220:223]\n"
"ds_read_b64_tr_b16 v[38:39], v17 offset:17984\n"
"s_waitcnt lgkmcnt(8)\n"
"v_mfma_f32_16x16x32_bf16 v[224:227], v[48:51], a[160:163], v[224:227]\n"
"ds_read_b64_tr_b16 v[40:41], v16 offset:14464\n"
"v_mfma_f32_16x16x32_bf16 v[228:231], v[48:51], a[164:167], v[228:231]\n"
"ds_read_b64_tr_b16 v[42:43], v16 offset:18048\n"
"v_mfma_f32_16x16x32_bf16 v[232:235], v[52:55], a[160:163], v[232:235]\n"
"ds_read_b64_tr_b16 v[44:45], v17 offset:14464\n"
"v_mfma_f32_16x16x32_bf16 v[236:239], v[52:55], a[164:167], v[236:239]\n"
"ds_read_b64_tr_b16 v[46:47], v17 offset:18048\n"
"s_waitcnt lgkmcnt(8)\n"
"s_waitcnt vmcnt(3)\n"
"v_mfma_f32_16x16x32_bf16 v[128:131], v[56:59], a[168:171], v[128:131]\n"
"ds_read_b64_tr_b16 v[48:49], v16 offset:14528\n"
"s_waitcnt vmcnt(2)\n"
"v_mfma_f32_16x16x32_bf16 v[132:135], v[56:59], a[172:175], v[132:135]\n"
"ds_read_b64_tr_b16 v[50:51], v16 offset:18112\n"
"v_mfma_f32_16x16x32_bf16 v[136:139], v[60:63], a[168:171], v[136:139]\n"
"ds_read_b64_tr_b16 v[52:53], v17 offset:14528\n"
"v_mfma_f32_16x16x32_bf16 v[140:143], v[60:63], a[172:175], v[140:143]\n"
"ds_read_b64_tr_b16 v[54:55], v17 offset:18112\n"
"s_waitcnt lgkmcnt(8)\n"
"v_mfma_f32_16x16x32_bf16 v[144:147], v[32:35], a[168:171], v[144:147]\n"
"ds_read_b64_tr_b16 v[56:57], v16 offset:14592\n"
"v_mfma_f32_16x16x32_bf16 v[148:151], v[32:35], a[172:175], v[148:151]\n"
"ds_read_b64_tr_b16 v[58:59], v16 offset:18176\n"
"v_mfma_f32_16x16x32_bf16 v[152:155], v[36:39], a[168:171], v[152:155]\n"
"ds_read_b64_tr_b16 v[60:61], v17 offset:14592\n"
"v_mfma_f32_16x16x32_bf16 v[156:159], v[36:39], a[172:175], v[156:159]\n"
"ds_read_b64_tr_b16 v[62:63], v17 offset:18176\n"
"s_waitcnt lgkmcnt(8)\n"
"v_mfma_f32_16x16x32_bf16 v[160:163], v[40:43], a[168:171], v[160:163]\n"
"ds_read_b64_tr_b16 v[32:33], v16 offset:14656\n"
"v_mfma_f32_16x16x32_bf16 v[164:167], v[40:43], a[172:175], v[164:167]\n"
"ds_read_b64_tr_b16 v[34:35], v16 offset:18240\n"
"v_mfma_f32_16x16x32_bf16 v[168:171], v[44:47], a[168:171], v[168:171]\n"
"ds_read_b64_tr_b16 v[36:37], v17 offset:14656\n"
"v_mfma_f32_16x16x32_bf16 v[172:175], v[44:47], a[172:175], v[172:175]\n"
"ds_read_b64_tr_b16 v[38:39], v17 offset:18240\n"
"s_waitcnt lgkmcnt(8)\n"
"v_mfma_f32_16x16x32_bf16 v[176:179], v[48:51], a[168:171], v[176:179]\n"
"ds_read_b64_tr_b16 v[40:41], v16 offset:14720\n"
"v_mfma_f32_16x16x32_bf16 v[180:183], v[48:51], a[172:175], v[180:183]\n"
"ds_read_b64_tr_b16 v[42:43], v16 offset:18304\n"
"v_mfma_f32_16x16x32_bf16 v[184:187], v[52:55], a[168:171], v[184:187]\n"
"ds_read_b64_tr_b16 v[44:45], v17 offset:14720\n"
"v_mfma_f32_16x16x32_bf16 v[188:191], v[52:55], a[172:175], v[188:191]\n"
"ds_read_b64_tr_b16 v[46:47], v17 offset:18304\n"
"s_waitcnt lgkmcnt(8)\n"
"v_mfma_f32_16x16x32_bf16 v[192:195], v[56:59], a[168:171], v[192:195]\n"
"s_waitcnt vmcnt(2)\n"
"s_barrier\n"
"v_add_u32 v16, 0x7000, v16\n"
"v_add_u32 v17, 0x7000, v17\n"
"ds_read_b64_tr_b16 v[48:49], v16\n"
"v_mfma_f32_16x16x32_bf16 v[196:199], v[56:59], a[172:175], v[196:199]\n"
"ds_read_b64_tr_b16 v[50:51], v16 offset:3584\n"
"v_mfma_f32_16x16x32_bf16 v[200:203], v[60:63], a[168:171], v[200:203]\n"
"ds_read_b64_tr_b16 v[52:53], v17\n"
"v_mfma_f32_16x16x32_bf16 v[204:207], v[60:63], a[172:175], v[204:207]\n"
"ds_read_b64_tr_b16 v[54:55], v17 offset:3584\n"
"s_waitcnt lgkmcnt(8)\n"
"v_mfma_f32_16x16x32_bf16 v[208:211], v[32:35], a[168:171], v[208:211]\n"
"ds_read_b64_tr_b16 v[56:57], v16 offset:64\n"
"v_mfma_f32_16x16x32_bf16 v[212:215], v[32:35], a[172:175], v[212:215]\n"
"ds_read_b64_tr_b16 v[58:59], v16 offset:3648\n"
"v_mfma_f32_16x16x32_bf16 v[216:219], v[36:39], a[168:171], v[216:219]\n"
"ds_read_b64_tr_b16 v[60:61], v17 offset:64\n"
"v_mfma_f32_16x16x32_bf16 v[220:223], v[36:39], a[172:175], v[220:223]\n"
"ds_read_b64_tr_b16 v[62:63], v17 offset:3648\n"
"s_waitcnt lgkmcnt(8)\n"
"v_mfma_f32_16x16x32_bf16 v[224:227], v[40:43], a[168:171], v[224:227]\n"
"ds_read_b64_tr_b16 v[32:33], v16 offset:128\n"
"v_mfma_f32_16x16x32_bf16 v[228:231], v[40:43], a[172:175], v[228:231]\n"
"ds_read_b64_tr_b16 v[34:35], v16 offset:3712\n"
"v_mfma_f32_16x16x32_bf16 v[232:235], v[44:47], a[168:171], v[232:235]\n"
"ds_read_b64_tr_b16 v[36:37], v17 offset:128\n"
"v_mfma_f32_16x16x32_bf16 v[236:239], v[44:47], a[172:175], v[236:239]\n"
"ds_read_b64_tr_b16 v[38:39], v17 offset:3712\n"
"s_waitcnt lgkmcnt(8)\n"
"s_waitcnt vmcnt(1)\n"
"v_mfma_f32_16x16x32_bf16 v[128:131], v[48:51], a[176:179], v[128:131]\n"
"ds_read_b64_tr_b16 v[40:41], v16 offset:192\n"
"s_waitcnt vmcnt(0)\n"
"v_mfma_f32_16x16x32_bf16 v[132:135], v[48:51], a[180:183], v[132:135]\n"
"ds_read_b64_tr_b16 v[42:43], v16 offset:3776\n"
"v_mfma_f32_16x16x32_bf16 v[136:139], v[52:55], a[176:179], v[136:139]\n"
"ds_read_b64_tr_b16 v[44:45], v17 offset:192\n"
"v_mfma_f32_16x16x32_bf16 v[140:143], v[52:55], a[180:183], v[140:143]\n"
"ds_read_b64_tr_b16 v[46:47], v17 offset:3776\n"
"s_waitcnt lgkmcnt(8)\n"
"v_mfma_f32_16x16x32_bf16 v[144:147], v[56:59], a[176:179], v[144:147]\n"
"ds_read_b64_tr_b16 v[48:49], v16 offset:256\n"
"v_mfma_f32_16x16x32_bf16 v[148:151], v[56:59], a[180:183], v[148:151]\n"
"ds_read_b64_tr_b16 v[50:51], v16 offset:3840\n"
"v_mfma_f32_16x16x32_bf16 v[152:155], v[60:63], a[176:179], v[152:155]\n"
"ds_read_b64_tr_b16 v[52:53], v17 offset:256\n"
"v_mfma_f32_16x16x32_bf16 v[156:159], v[60:63], a[180:183], v[156:159]\n"
"ds_read_b64_tr_b16 v[54:55], v17 offset:3840\n"
"v_cvt_pk_bf16_f32 v64, v128, v129\n"
"v_cvt_pk_bf16_f32 v65, v130, v131\n"
"v_cvt_pk_bf16_f32 v66, v136, v137\n"
"s_waitcnt lgkmcnt(8)\n"
"v_mfma_f32_16x16x32_bf16 v[160:163], v[32:35], a[176:179], v[160:163]\n"
"ds_read_b64_tr_b16 v[56:57], v16 offset:320\n"
"v_mfma_f32_16x16x32_bf16 v[164:167], v[32:35], a[180:183], v[164:167]\n"
"ds_read_b64_tr_b16 v[58:59], v16 offset:3904\n"
"v_cvt_pk_bf16_f32 v67, v138, v139\n"
"v_cvt_pk_bf16_f32 v92, v132, v133\n"
"v_cvt_pk_bf16_f32 v93, v134, v135\n"
"v_mfma_f32_16x16x32_bf16 v[168:171], v[36:39], a[176:179], v[168:171]\n"
"ds_read_b64_tr_b16 v[60:61], v17 offset:320\n"
"v_mfma_f32_16x16x32_bf16 v[172:175], v[36:39], a[180:183], v[172:175]\n"
"ds_read_b64_tr_b16 v[62:63], v17 offset:3904\n"
"v_cvt_pk_bf16_f32 v94, v140, v141\n"
"v_cvt_pk_bf16_f32 v95, v142, v143\n"
"v_permlane16_swap_b32 v64, v66\n"
"s_waitcnt lgkmcnt(8)\n"
"v_mfma_f32_16x16x32_bf16 v[176:179], v[40:43], a[176:179], v[176:179]\n"
"ds_read_b64_tr_b16 v[32:33], v16 offset:384\n"
"v_mfma_f32_16x16x32_bf16 v[180:183], v[40:43], a[180:183], v[180:183]\n"
"ds_read_b64_tr_b16 v[34:35], v16 offset:3968\n"
"v_permlane16_swap_b32 v65, v67\n"
"v_permlane16_swap_b32 v92, v94\n"
"v_permlane16_swap_b32 v93, v95\n"
"v_mfma_f32_16x16x32_bf16 v[184:187], v[44:47], a[176:179], v[184:187]\n"
"ds_read_b64_tr_b16 v[36:37], v17 offset:384\n"
"v_mfma_f32_16x16x32_bf16 v[188:191], v[44:47], a[180:183], v[188:191]\n"
"ds_read_b64_tr_b16 v[38:39], v17 offset:3968\n"
"s_add_u32 s64, %[pst], 0\n"
"buffer_store_dwordx4 v[64:67], %[vl], %[rsW], s64 offen sc1\n"
"s_add_u32 s64, %[pst], 0xe000\n"
"buffer_store_dwordx4 v[92:95], %[vl], %[rsW], s64 offen sc1\n"
"v_cvt_pk_bf16_f32 v68, v144, v145\n"
"s_waitcnt lgkmcnt(8)\n"
"v_mfma_f32_16x16x32_bf16 v[192:195], v[48:51], a[176:179], v[192:195]\n"
"v_mfma_f32_16x16x32_bf16 v[196:199], v[48:51], a[180:183], v[196:199]\n"
"v_cvt_pk_bf16_f32 v69, v146, v147\n"
"v_cvt_pk_bf16_f32 v70, v152, v153\n"
"v_cvt_pk_bf16_f32 v71, v154, v155\n"
"v_mfma_f32_16x16x32_bf16 v[200:203], v[52:55], a[176:179], v[200:203]\n"
"v_mfma_f32_16x16x32_bf16 v[204:207], v[52:55], a[180:183], v[204:207]\n"
"v_cvt_pk_bf16_f32 v96, v148, v149\n"
"v_cvt_pk_bf16_f32 v97, v150, v151\n"
"v_cvt_pk_bf16_f32 v98, v156, v157\n"
"s_waitcnt lgkmcnt(4)\n"
"v_mfma_f32_16x16x32_bf16 v[208:211], v[56:59], a[176:179], v[208:211]\n"
"v_mfma_f32_16x16x32_bf16 v[212:215], v[56:59], a[180:183], v[212:215]\n"
"v_cvt_pk_bf16_f32 v99, v158, v159\n"
"v_permlane16_swap_b32 v68, v70\n"
"v_permlane16_swap_b32 v69, v71\n"
"v_mfma_f32_16x16x32_bf16 v[216:219], v[60:63], a[176:179], v[216:219]\n"
"v_mfma_f32_16x16x32_bf16 v[220:223], v[60:63], a[180:183], v[220:223]\n"
"v_permlane16_swap_b32 v96, v98\n"
"v_permlane16_swap_b32 v97, v99\n"
"s_add_u32 s64, %[pst], 0\n"
"buffer_store_dwordx4 v[68:71], %[vl], %[rsW], s64 offen offset:1024 sc1\n"
"s_waitcnt lgkmcnt(0)\n"
"v_mfma_f32_16x16x32_bf16 v[224:227], v[32:35], a[176:179], v[224:227]\n"
"v_mfma_f32_16x16x32_bf16 v[228:231], v[32:35], a[180:183], v[228:231]\n"
"s_add_u32 s64, %[pst], 0xe000\n"
"buffer_store_dwordx4 v[96:99], %[vl], %[rsW], s64 offen offset:1024 sc1\n"
"v_cvt_pk_bf16_f32 v72, v160, v161\n"
"v_cvt_pk_bf16_f32 v73, v162, v163\n"
"v_mfma_f32_16x16x32_bf16 v[232:235], v[36:39], a[176:179], v[232:235]\n"
"v_mfma_f32_16x16x32_bf16 v[236:239], v[36:39], a[180:183], v[236:239]\n"
"v_cvt_pk_bf16_f32 v74, v168, v169\n"
"v_cvt_pk_bf16_f32 v75, v170, v171\n"
"v_cvt_pk_bf16_f32 v100, v164, v165\n"
"s_nop 3\n"
"v_cvt_pk_bf16_f32 v101, v166, v167\n"
"v_cvt_pk_bf16_f32 v102, v172, v173\n"
"v_cvt_pk_bf16_f32 v103, v174, v175\n"
"v_permlane16_swap_b32 v72, v74\n"
"v_permlane16_swap_b32 v73, v75\n"
"v_permlane16_swap_b32 v100, v102\n"
"v_permlane16_swap_b32 v101, v103\n"
"s_add_u32 s64, %[pst], 0\n"
"buffer_store_dwordx4 v[72:75], %[vl], %[rsW], s64 offen offset:2048 sc1\n"
"s_add_u32 s64, %[pst], 0xe000\n"
"buffer_store_dwordx4 v[100:103], %[vl], %[rsW], s64 offen offset:2048 sc1\n"
"v_cvt_pk_bf16_f32 v76, v176, v177\n"
"v_cvt_pk_bf16_f32 v77, v178, v179\n"
"v_cvt_pk_bf16_f32 v78, v184, v185\n"
"v_cvt_pk_bf16_f32 v79, v186, v187\n"
"v_cvt_pk_bf16_f32 v104, v180, v181\n"
"v_cvt_pk_bf16_f32 v105, v182, v183\n"
"v_cvt_pk_bf16_f32 v106, v188, v189\n"
"v_cvt_pk_bf16_f32 v107, v190, v191\n"
"v_permlane16_swap_b32 v76, v78\n"
"v_permlane16_swap_b32 v77, v79\n"
"v_permlane16_swap_b32 v104, v106\n"
"v_permlane16_swap_b32 v105, v107\n"
"s_add_u32 s64, %[pst], 0\n"
"buffer_store_dwordx4 v[76:79], %[vl], %[rsW], s64 offen offset:3072 sc1\n"
"s_add_u32 s64, %[pst], 0xe000\n"
"buffer_store_dwordx4 v[104:107], %[vl], %[rsW], s64 offen offset:3072 sc1\n"
"v_cvt_pk_bf16_f32 v80, v192, v193\n"
"v_cvt_pk_bf16_f32 v81, v194, v195\n"
"v_cvt_pk_bf16_f32 v82, v200, v201\n"
"v_cvt_pk_bf16_f32 v83, v202, v203\n"
"v_cvt_pk_bf16_f32 v108, v196, v197\n"
"v_cvt_pk_bf16_f32 v109, v198, v199\n"
"v_cvt_pk_bf16_f32 v110, v204, v205\n"
"v_cvt_pk_bf16_f32 v111, v206, v207\n"
"v_permlane16_swap_b32 v80, v82\n"
"v_permlane16_swap_b32 v81, v83\n"
"v_permlane16_swap_b32 v108, v110\n"
"v_permlane16_swap_b32 v109, v111\n"
"s_add_u32 s64, %[pst], 0x1000\n"
"buffer_store_dwordx4 v[80:83], %[vl], %[rsW], s64 offen sc1\n"
"s_add_u32 s64, %[pst], 0xf000\n"
"buffer_store_dwordx4 v[108:111], %[vl], %[rsW], s64 offen sc1\n"
"v_cvt_pk_bf16_f32 v84, v208, v209\n"
"v_cvt_pk_bf16_f32 v85, v210, v211\n"
"v_cvt_pk_bf16_f32 v86, v216, v217\n"
"v_cvt_pk_bf16_f32 v87, v218, v219\n"
"v_cvt_pk_bf16_f32 v112, v212, v213\n"
"v_cvt_pk_bf16_f32 v113, v214, v215\n"
"v_cvt_pk_bf16_f32 v114, v220, v221\n"
"v_cvt_pk_bf16_f32 v115, v222, v223\n"
"v_permlane16_swap_b32 v84, v86\n"
"v_permlane16_swap_b32 v85, v87\n"
"v_permlane16_swap_b32 v112, v114\n"
"v_permlane16_swap_b32 v113, v115\n"
"s_add_u32 s64, %[pst], 0x1000\n"
"buffer_store_dwordx4 v[84:87], %[vl], %[rsW], s64 offen offset:1024 sc1\n"
"s_add_u32 s64, %[pst], 0xf000\n"
"buffer_store_dwordx4 v[112:115], %[vl], %[rsW], s64 offen offset:1024 sc1\n"
"v_cvt_pk_bf16_f32 v88, v224, v225\n"
"v_cvt_pk_bf16_f32 v89, v226, v227\n"
"v_cvt_pk_bf16_f32 v90, v232, v233\n"
"v_cvt_pk_bf16_f32 v91, v234, v235\n"
"v_cvt_pk_bf16_f32 v116, v228, v229\n"
"v_cvt_pk_bf16_f32 v117, v230, v231\n"
"v_cvt_pk_bf16_f32 v118, v236, v237\n"
"v_cvt_pk_bf16_f32 v119, v238, v239\n"
"v_permlane16_swap_b32 v88, v90\n"
"v_permlane16_swap_b32 v89, v91\n"
"v_permlane16_swap_b32 v116, v118\n"
"v_permlane16_swap_b32 v117, v119\n"
"s_add_u32 s64, %[pst], 0x1000\n"
"buffer_store_dwordx4 v[88:91], %[vl], %[rsW], s64 offen offset:2048 sc1\n"
"s_add_u32 s64, %[pst], 0xf000\n"
"buffer_store_dwordx4 v[116:119], %[vl], %[rsW], s64 offen offset:2048 sc1\n"
"s_waitcnt vmcnt(0)\n"
"buffer_store_dwordx2 v[18:19], off, %[rsW], %[fst] sc1\n"
"v_lshlrev_b32 v192, 16, v20\n"
"v_and_b32 v193, 0xffff0000, v20\n"
"v_lshlrev_b32 v194, 16, v21\n"
"v_and_b32 v195, 0xffff0000, v21\n"
"v_lshlrev_b32 v196, 16, v22\n"
"v_and_b32 v197, 0xffff0000, v22\n"
"v_lshlrev_b32 v198, 16, v23\n"
"v_and_b32 v199, 0xffff0000, v23\n"
"v_lshlrev_b32 v200, 16, v24\n"
"v_and_b32 v201, 0xffff0000, v24\n"
"v_lshlrev_b32 v202, 16, v25\n"
"v_and_b32 v203, 0xffff0000, v25\n"
"v_lshlrev_b32 v204, 16, v26\n"
"v_and_b32 v205, 0xffff0000, v26\n"
"v_lshlrev_b32 v206, 16, v27\n"
"v_and_b32 v207, 0xffff0000, v27\n"
"s_mov_b32 s67, 0\n"
"POLL%=:\n"
"buffer_load_dwordx2 v[28:29], %[vf], %[rsW], %[fld] offen sc1\n"
"s_waitcnt vmcnt(0)\n"
"v_cmp_ne_u64 vcc, %[tag], v[28:29]\n"
"s_add_u32 s67, s67, 1\n"
"s_cbranch_vccz GOT%=\n"
"s_sleep 3\n"
"s_cmp_lt_u32 s67, 0x100000\n"
"s_cbranch_scc1 POLL%=\n"
"GOT%=:\n"
"s_add_u32 s64, %[pl1], 0\n"
"buffer_load_dwordx4 v[128:131], %[vl], %[rsW], s64 offen sc1\n"
"s_add_u32 s64, %[pl2], 0\n"
"buffer_load_dwordx4 v[160:163], %[vl], %[rsW], s64 offen sc1\n"
"s_add_u32 s64, %[pl1], 0x1c00\n"
"buffer_load_dwordx4 v[132:135], %[vl], %[rsW], s64 offen sc1\n"
"s_add_u32 s64, %[pl2], 0x1c00\n"
"buffer_load_dwordx4 v[164:167], %[vl], %[rsW], s64 offen sc1\n"
"s_add_u32 s64, %[pl1], 0x3800\n"
"buffer_load_dwordx4 v[136:139], %[vl], %[rsW], s64 offen sc1\n"
"s_add_u32 s64, %[pl2], 0x3800\n"
"buffer_load_dwordx4 v[168:171], %[vl], %[rsW], s64 offen sc1\n"
"s_add_u32 s64, %[pl1], 0x5400\n"
"buffer_load_dwordx4 v[140:143], %[vl], %[rsW], s64 offen sc1\n"
"s_add_u32 s64, %[pl2], 0x5400\n"
"buffer_load_dwordx4 v[172:175], %[vl], %[rsW], s64 offen sc1\n"
"s_add_u32 s64, %[pl1], 0x7000\n"
"buffer_load_dwordx4 v[144:147], %[vl], %[rsW], s64 offen sc1\n"
"s_add_u32 s64, %[pl2], 0x7000\n"
"buffer_load_dwordx4 v[176:179], %[vl], %[rsW], s64 offen sc1\n"
"s_add_u32 s64, %[pl1], 0x8c00\n"
"buffer_load_dwordx4 v[148:151], %[vl], %[rsW], s64 offen sc1\n"
"s_add_u32 s64, %[pl2], 0x8c00\n"
"buffer_load_dwordx4 v[180:183], %[vl], %[rsW], s64 offen sc1\n"
"s_add_u32 s64, %[pl1], 0xa800\n"
"buffer_load_dwordx4 v[152:155], %[vl], %[rsW], s64 offen sc1\n"
"s_add_u32 s64, %[pl2], 0xa800\n"
"buffer_load_dwordx4 v[184:187], %[vl], %[rsW], s64 offen sc1\n"
"s_add_u32 s64, %[pl1], 0xc400\n"
"buffer_load_dwordx4 v[156:159], %[vl], %[rsW], s64 offen sc1\n"
"s_add_u32 s64, %[pl2], 0xc400\n"
"buffer_load_dwordx4 v[188:191], %[vl], %[rsW], s64 offen sc1\n"
"s_waitcnt vmcnt(15)\n"
"v_lshlrev_b32 v208, 16, v128\n"
"v_and_b32 v209, 0xffff0000, v128\n"
"v_lshlrev_b32 v210, 16, v129\n"
"v_and_b32 v211, 0xffff0000, v129\n"
"v_lshlrev_b32 v212, 16, v130\n"
"v_and_b32 v213, 0xffff0000, v130\n"
"v_lshlrev_b32 v214, 16, v131\n"
"v_and_b32 v215, 0xffff0000, v131\n"
"v_pk_add_f32 v[192:193], v[192:193], v[208:209]\n"
"v_pk_add_f32 v[194:195], v[194:195], v[210:211]\n"
"v_pk_add_f32 v[196:197], v[196:197], v[212:213]\n"
"v_pk_add_f32 v[198:199], v[198:199], v[214:215]\n"
"s_waitcnt vmcnt(14)\n"
"v_lshlrev_b32 v208, 16, v160\n"
"v_and_b32 v209, 0xffff0000, v160\n"
"v_lshlrev_b32 v210, 16, v161\n"
"v_and_b32 v211, 0xffff0000, v161\n"
"v_lshlrev_b32 v212, 16, v162\n"
"v_and_b32 v213, 0xffff0000, v162\n"
"v_lshlrev_b32 v214, 16, v163\n"
"v_and_b32 v215, 0xffff0000, v163\n"
"v_pk_add_f32 v[200:201], v[200:201], v[208:209]\n"
"v_pk_add_f32 v[202:203], v[202:203], v[210:211]\n"
"v_pk_add_f32 v[204:205], v[204:205], v[212:213]\n"
"v_pk_add_f32 v[206:207], v[206:207], v[214:215]\n"
"s_waitcnt vmcnt(13)\n"
"v_lshlrev_b32 v208, 16, v132\n"
"v_and_b32 v209, 0xffff0000, v132\n"
"v_lshlrev_b32 v210, 16, v133\n"
"v_and_b32 v211, 0xffff0000, v133\n"
"v_lshlrev_b32 v212, 16, v134\n"
"v_and_b32 v213, 0xffff0000, v134\n"
"v_lshlrev_b32 v214, 16, v135\n"
"v_and_b32 v215, 0xffff0000, v135\n"
"v_pk_add_f32 v[192:193], v[192:193], v[208:209]\n"
"v_pk_add_f32 v[194:195], v[194:195], v[210:211]\n"
"v_pk_add_f32 v[196:197], v[196:197], v[212:213]\n"
"v_pk_add_f32 v[198:199], v[198:199], v[214:215]\n"
"s_waitcnt vmcnt(12)\n"
"v_lshlrev_b32 v208, 16, v164\n"
"v_and_b32 v209, 0xffff0000, v164\n"
"v_lshlrev_b32 v210, 16, v165\n"
"v_and_b32 v211, 0xffff0000, v165\n"
"v_lshlrev_b32 v212, 16, v166\n"
"v_and_b32 v213, 0xffff0000, v166\n"
"v_lshlrev_b32 v214, 16, v167\n"
"v_and_b32 v215, 0xffff0000, v167\n"
"v_pk_add_f32 v[200:201], v[200:201], v[208:209]\n"
"v_pk_add_f32 v[202:203], v[202:203], v[210:211]\n"
"v_pk_add_f32 v[204:205], v[204:205], v[212:213]\n"
"v_pk_add_f32 v[206:207], v[206:207], v[214:215]\n"
"s_waitcnt vmcnt(11)\n"
"v_lshlrev_b32 v208, 16, v136\n"
"v_and_b32 v209, 0xffff0000, v136\n"
"v_lshlrev_b32 v210, 16, v137\n"
"v_and_b32 v211, 0xffff0000, v137\n"
"v_lshlrev_b32 v212, 16, v138\n"
"v_and_b32 v213, 0xffff0000, v138\n"
"v_lshlrev_b32 v214, 16, v139\n"
"v_and_b32 v215, 0xffff0000, v139\n"
"v_pk_add_f32 v[192:193], v[192:193], v[208:209]\n"
"v_pk_add_f32 v[194:195], v[194:195], v[210:211]\n"
"v_pk_add_f32 v[196:197], v[196:197], v[212:213]\n"
"v_pk_add_f32 v[198:199], v[198:199], v[214:215]\n"
"s_waitcnt vmcnt(10)\n"
"v_lshlrev_b32 v208, 16, v168\n"
"v_and_b32 v209, 0xffff0000, v168\n"
"v_lshlrev_b32 v210, 16, v169\n"
"v_and_b32 v211, 0xffff0000, v169\n"
"v_lshlrev_b32 v212, 16, v170\n"
"v_and_b32 v213, 0xffff0000, v170\n"
"v_lshlrev_b32 v214, 16, v171\n"
"v_and_b32 v215, 0xffff0000, v171\n"
"v_pk_add_f32 v[200:201], v[200:201], v[208:209]\n"
"v_pk_add_f32 v[202:203], v[202:203], v[210:211]\n"
"v_pk_add_f32 v[204:205], v[204:205], v[212:213]\n"
"v_pk_add_f32 v[206:207], v[206:207], v[214:215]\n"
"s_waitcnt vmcnt(9)\n"
"v_lshlrev_b32 v208, 16, v140\n"
"v_and_b32 v209, 0xffff0000, v140\n"
"v_lshlrev_b32 v210, 16, v141\n"
"v_and_b32 v211, 0xffff0000, v141\n"
"v_lshlrev_b32 v212, 16, v142\n"
"v_and_b32 v213, 0xffff0000, v142\n"
"v_lshlrev_b32 v214, 16, v143\n"
"v_and_b32 v215, 0xffff0000, v143\n"
"v_pk_add_f32 v[192:193], v[192:193], v[208:209]\n"
"v_pk_add_f32 v[194:195], v[194:195], v[210:211]\n"
"v_pk_add_f32 v[196:197], v[196:197], v[212:213]\n"
"v_pk_add_f32 v[198:199], v[198:199], v[214:215]\n"
"s_waitcnt vmcnt(8)\n"
"v_lshlrev_b32 v208, 16, v172\n"
"v_and_b32 v209, 0xffff0000, v172\n"
"v_lshlrev_b32 v210, 16, v173\n"
"v_and_b32 v211, 0xffff0000, v173\n"
"v_lshlrev_b32 v212, 16, v174\n"
"v_and_b32 v213, 0xffff0000, v174\n"
"v_lshlrev_b32 v214, 16, v175\n"
"v_and_b32 v215, 0xffff0000, v175\n"
"v_pk_add_f32 v[200:201], v[200:201], v[208:209]\n"
"v_pk_add_f32 v[202:203], v[202:203], v[210:211]\n"
"v_pk_add_f32 v[204:205], v[204:205], v[212:213]\n"
"v_pk_add_f32 v[206:207], v[206:207], v[214:215]\n"
"s_waitcnt vmcnt(7)\n"
"v_lshlrev_b32 v208, 16, v144\n"
"v_and_b32 v209, 0xffff0000, v144\n"
"v_lshlrev_b32 v210, 16, v145\n"
"v_and_b32 v211, 0xffff0000, v145\n"
"v_lshlrev_b32 v212, 16, v146\n"
"v_and_b32 v213, 0xffff0000, v146\n"
"v_lshlrev_b32 v214, 16, v147\n"
"v_and_b32 v215, 0xffff0000, v147\n"
"v_pk_add_f32 v[192:193], v[192:193], v[208:209]\n"
"v_pk_add_f32 v[194:195], v[194:195], v[210:211]\n"
"v_pk_add_f32 v[196:197], v[196:197], v[212:213]\n"
"v_pk_add_f32 v[198:199], v[198:199], v[214:215]\n"
"s_waitcnt vmcnt(6)\n"
"v_lshlrev_b32 v208, 16, v176\n"
"v_and_b32 v209, 0xffff0000, v176\n"
"v_lshlrev_b32 v210, 16, v177\n"
"v_and_b32 v211, 0xffff0000, v177\n"
"v_lshlrev_b32 v212, 16, v178\n"
"v_and_b32 v213, 0xffff0000, v178\n"
"v_lshlrev_b32 v214, 16, v179\n"
"v_and_b32 v215, 0xffff0000, v179\n"
"v_pk_add_f32 v[200:201], v[200:201], v[208:209]\n"
"v_pk_add_f32 v[202:203], v[202:203], v[210:211]\n"
"v_pk_add_f32 v[204:205], v[204:205], v[212:213]\n"
"v_pk_add_f32 v[206:207], v[206:207], v[214:215]\n"
"s_waitcnt vmcnt(5)\n"
"v_lshlrev_b32 v208, 16, v148\n"
"v_and_b32 v209, 0xffff0000, v148\n"
"v_lshlrev_b32 v210, 16, v149\n"
"v_and_b32 v211, 0xffff0000, v149\n"
"v_lshlrev_b32 v212, 16, v150\n"
"v_and_b32 v213, 0xffff0000, v150\n"
"v_lshlrev_b32 v214, 16, v151\n"
"v_and_b32 v215, 0xffff0000, v151\n"
"v_pk_add_f32 v[192:193], v[192:193], v[208:209]\n"
"v_pk_add_f32 v[194:195], v[194:195], v[210:211]\n"
"v_pk_add_f32 v[196:197], v[196:197], v[212:213]\n"
"v_pk_add_f32 v[198:199], v[198:199], v[214:215]\n"
"s_waitcnt vmcnt(4)\n"
"v_lshlrev_b32 v208, 16, v180\n"
"v_and_b32 v209, 0xffff0000, v180\n"
"v_lshlrev_b32 v210, 16, v181\n"
"v_and_b32 v211, 0xffff0000, v181\n"
"v_lshlrev_b32 v212, 16, v182\n"
"v_and_b32 v213, 0xffff0000, v182\n"
"v_lshlrev_b32 v214, 16, v183\n"
"v_and_b32 v215, 0xffff0000, v183\n"
"v_pk_add_f32 v[200:201], v[200:201], v[208:209]\n"
"v_pk_add_f32 v[202:203], v[202:203], v[210:211]\n"
"v_pk_add_f32 v[204:205], v[204:205], v[212:213]\n"
"v_pk_add_f32 v[206:207], v[206:207], v[214:215]\n"
"s_waitcnt vmcnt(3)\n"
"v_lshlrev_b32 v208, 16, v152\n"
"v_and_b32 v209, 0xffff0000, v152\n"
"v_lshlrev_b32 v210, 16, v153\n"
"v_and_b32 v211, 0xffff0000, v153\n"
"v_lshlrev_b32 v212, 16, v154\n"
"v_and_b32 v213, 0xffff0000, v154\n"
"v_lshlrev_b32 v214, 16, v155\n"
"v_and_b32 v215, 0xffff0000, v155\n"
"v_pk_add_f32 v[192:193], v[192:193], v[208:209]\n"
"v_pk_add_f32 v[194:195], v[194:195], v[210:211]\n"
"v_pk_add_f32 v[196:197], v[196:197], v[212:213]\n"
"v_pk_add_f32 v[198:199], v[198:199], v[214:215]\n"
"s_waitcnt vmcnt(2)\n"
"v_lshlrev_b32 v208, 16, v184\n"
"v_and_b32 v209, 0xffff0000, v184\n"
"v_lshlrev_b32 v210, 16, v185\n"
"v_and_b32 v211, 0xffff0000, v185\n"
"v_lshlrev_b32 v212, 16, v186\n"
"v_and_b32 v213, 0xffff0000, v186\n"
"v_lshlrev_b32 v214, 16, v187\n"
"v_and_b32 v215, 0xffff0000, v187\n"
"v_pk_add_f32 v[200:201], v[200:201], v[208:209]\n"
"v_pk_add_f32 v[202:203], v[202:203], v[210:211]\n"
"v_pk_add_f32 v[204:205], v[204:205], v[212:213]\n"
"v_pk_add_f32 v[206:207], v[206:207], v[214:215]\n"
"s_waitcnt vmcnt(1)\n"
"v_lshlrev_b32 v208, 16, v156\n"
"v_and_b32 v209, 0xffff0000, v156\n"
"v_lshlrev_b32 v210, 16, v157\n"
"v_and_b32 v211, 0xffff0000, v157\n"
"v_lshlrev_b32 v212, 16, v158\n"
"v_and_b32 v213, 0xffff0000, v158\n"
"v_lshlrev_b32 v214, 16, v159\n"
"v_and_b32 v215, 0xffff0000, v159\n"
"v_pk_add_f32 v[192:193], v[192:193], v[208:209]\n"
"v_pk_add_f32 v[194:195], v[194:195], v[210:211]\n"
"v_pk_add_f32 v[196:197], v[196:197], v[212:213]\n"
"v_pk_add_f32 v[198:199], v[198:199], v[214:215]\n"
"s_waitcnt vmcnt(0)\n"
"v_lshlrev_b32 v208, 16, v188\n"
"v_and_b32 v209, 0xffff0000, v188\n"
"v_lshlrev_b32 v210, 16, v189\n"
"v_and_b32 v211, 0xffff0000, v189\n"
"v_lshlrev_b32 v212, 16, v190\n"
"v_and_b32 v213, 0xffff0000, v190\n"
"v_lshlrev_b32 v214, 16, v191\n"
"v_and_b32 v215, 0xffff0000, v191\n"
"v_pk_add_f32 v[200:201], v[200:201], v[208:209]\n"
"v_pk_add_f32 v[202:203], v[202:203], v[210:211]\n"
"v_pk_add_f32 v[204:205], v[204:205], v[212:213]\n"
"v_pk_add_f32 v[206:207], v[206:207], v[214:215]\n"
"v_cvt_pk_bf16_f32 v20, v192, v193\n"
"v_cvt_pk_bf16_f32 v21, v194, v195\n"
"v_cvt_pk_bf16_f32 v22, v196, v197\n"
"v_cvt_pk_bf16_f32 v23, v198, v199\n"
"buffer_store_dwordx4 v[20:23], %[vx], %[rsY], %[x1] offen\n"
"v_cvt_pk_bf16_f32 v24, v200, v201\n"
"v_cvt_pk_bf16_f32 v25, v202, v203\n"
"v_cvt_pk_bf16_f32 v26, v204, v205\n"
"v_cvt_pk_bf16_f32 v27, v206, v207\n"
"buffer_store_dwordx4 v[24:27], %[vx], %[rsY], %[x2] offen\n"
"s_cmp_lg_u32 %[ep], 0\n"
"s_cbranch_scc1 NOEP%=\n"
"s_mov_b32 s64, 0x5f00000\n"
"buffer_store_dword v18, off, %[rsW], s64 sc1\n"
"NOEP%=:\n"
"s_branch FIN%=\n"
"AFW%=:\n"
"s_mov_b32 s67, 0\n"
"AFP%=:\n"
"s_sleep 2\n"
"buffer_load_dwordx2 v[28:29], off, %[rsW], %[afl] sc1\n"
"s_waitcnt vmcnt(0)\n"
"v_cmp_ne_u64 vcc, %[tag], v[28:29]\n"
"s_add_u32 s67, s67, 1\n"
"s_cmp_lt_u32 s67, 0x100000\n"
"s_cbranch_vccz AFG%=\n"
"s_cbranch_scc1 AFP%=\n"
"s_branch AFG%=\n"
"FIN%=:\n"
"kf_end:\n"
    :
    : [rsW] "s"(rsW), [rsO] "s"(rsO), [rs2] "s"(rs2), [rsX] "s"(rsX), [rsY] "s"(rsY), [l0] "s"(l0), [a1] "s"(a1), [a2] "s"(a2), [so] "s"(so), [s2] "s"(s2), [s11] "s"(s11),
      [pst] "s"(pst), [pl1] "s"(pl1), [pl2] "s"(pl2), [fst] "s"(fst), [fld] "s"(fld), [x1] "s"(x1), [x2] "s"(x2), [stoff] "s"(stoff),
      [sob] "s"(sob), [a1b] "s"(a1b), [tag] "s"(tag), [aff] "s"(aff), [afl] "s"(afl), [ep] "s"(ep),
      [vo0] "v"(vo[0]), [vo1] "v"(vo[1]), [vo2] "v"(vo[2]), [vo3] "v"(vo[3]), [vo4] "v"(vo[4]), [vo5] "v"(vo[5]), [vo6] "v"(vo[6]),
      [va] "v"(va), [tbe] "v"(tbe), [tbo] "v"(tbo), [vl] "v"(vl), [vx] "v"(vx), [vf] "v"(vf)
    : CL3);
  }
}

extern "C" void kernel_launch(void* const* d_in, const int* in_sizes, int n_in, void* d_out, int out_size, void* d_ws, size_t ws_size, hipStream_t stream) {
  (void)in_sizes; (void)n_in; (void)out_size;
  if (ws_size < WS_END) return;
  (void)hipFuncSetAttribute((const void*)fused, hipFuncAttributeMaxDynamicSharedMemorySize, LDS_BYTES);
  Args a;
  a.x = (const u16*)d_in[0]; a.ckv = (const u16*)d_in[1]; a.wq = (const u16*)d_in[2]; a.wuk = (const u16*)d_in[3]; a.wuv = (const u16*)d_in[4];
  a.wo = (const u16*)d_in[5]; a.w1 = (const u16*)d_in[7]; a.w2 = (const u16*)d_in[8]; a.ws = (char*)d_ws; a.out = (u16*)d_out;
  hipLaunchKernelGGL(fused, dim3(256), dim3(256), LDS_BYTES, stream, a);
}
```
